```python
import torch

def _softmax(x, axis=-1):
    return torch.softmax(x, dim=axis)
def _gelu(x):
    return torch.nn.functional.gelu(x, approximate="tanh")
def _silu(x):
    return torch.nn.functional.silu(x)
def _sigmoid(x):
    return torch.sigmoid(x)
def _layernorm(x, axis=-1, eps=1e-5):
    m = x.mean(dim=axis, keepdim=True)
    v = x.var(dim=axis, keepdim=True, unbiased=False)
    return (x - m) / torch.sqrt(v + eps)
def _scatter_add(tgt, idx, src, axis=0):
    return tgt.clone().index_add_(axis, idx, src)
def _segment_sum(src, seg, n):
    out = torch.zeros((n,) + src.shape[1:], dtype=src.dtype)
    return out.index_add_(0, seg, src)
def _pad(x, pw):
    flat = tuple(v for pair in reversed(pw) for v in pair)
    return torch.nn.functional.pad(x, flat)

kernel_name = 'parallel_sum__compressed_kv_cla__glu_gated_glu__decode'

def setup_inputs():

    _w = (lambda a: (lambda t: t.to(torch.bfloat16) if t.is_floating_point() else t)(torch.as_tensor(a)))
    def _A_setup():
        B, H, Sq, Sk, D, M = 2, 32, 1, 4096, 128, 512
        Dc = 4 * D
        return {
            "x": _w((lambda *s: torch.randn(*s, dtype=torch.bfloat16) * 0.05)(B, Sq, M)),
            "c_kv": _w((lambda *s: torch.randn(*s, dtype=torch.bfloat16) * 1.6)(B, Sk, Dc)),
            "w_q": _w((lambda *s: torch.randn(*s, dtype=torch.bfloat16) * 1.6)(M, H, D) * (M ** -0.5)),
            "w_uk": _w((lambda *s: torch.randn(*s, dtype=torch.bfloat16) * 1.6)(Dc, H, D) * (Dc ** -0.5)),
            "w_uv": _w((lambda *s: torch.randn(*s, dtype=torch.bfloat16) * 1.6)(Dc, H, D) * (Dc ** -0.5)),
            "w_o": _w((lambda *s: torch.randn(*s, dtype=torch.bfloat16) * 1.6)(H, D, M) * ((H * D) ** -0.5)),
        }
    _A_in = _A_setup()

    def _B_setup():
        B, S, D, F = 2, 1, 512, 1376
        return {
            "x": _w((lambda *s: torch.randn(*s, dtype=torch.bfloat16) * 0.05)(B, S, D)),
            "w_gate": _w((lambda *s: torch.randn(*s, dtype=torch.bfloat16) * 0.1)(D, F)),
            "w_up": _w((lambda *s: torch.randn(*s, dtype=torch.bfloat16) * 0.05)(D, F)),
            "w_down": _w((lambda *s: torch.randn(*s, dtype=torch.bfloat16) * 0.05)(F, D)),
        }
    _B_in = _B_setup()

    return {
        "A_x": _A_in["x"],
        "A_c_kv": _A_in["c_kv"],
        "A_w_q": _A_in["w_q"],
        "A_w_uk": _A_in["w_uk"],
        "A_w_uv": _A_in["w_uv"],
        "A_w_o": _A_in["w_o"],
        "B_x": _B_in["x"],
        "B_w_gate": _B_in["w_gate"],
        "B_w_up": _B_in["w_up"],
        "B_w_down": _B_in["w_down"],
    }

def reference(A_x, A_c_kv, A_w_q, A_w_uk, A_w_uv, A_w_o, B_x, B_w_gate, B_w_up, B_w_down):
    def _A_ref(x, c_kv, w_q, w_uk, w_uv, w_o):
        B, Sq, M = x.shape[0], x.shape[1], x.shape[2]
        Sk = c_kv.shape[1]
        H, D = w_q.shape[1], w_q.shape[2]
        q = torch.einsum("bqm,mhd->bhqd", x, w_q)
        k = torch.einsum("bkc,chd->bhkd", c_kv, w_uk)
        v = torch.einsum("bkc,chd->bhkd", c_kv, w_uv)
        scores = torch.einsum("bhqd,bhkd->bhqk", q, k) * (D ** -0.5)
        off = Sk - Sq
        qi = torch.arange(Sq)[:, None]
        kj = torch.arange(Sk)[None, :]
        cmask = kj <= (qi + off)
        scores = torch.where(cmask[None, None], scores, -1e9)
        p = _softmax(scores, axis=-1)
        ctx = torch.einsum("bhqk,bhkd->bhqd", p, v)
        return torch.einsum("bhqd,hdm->bqm", ctx, w_o)

    def _B_ref(x, w_gate, w_up, w_down):
        g = torch.einsum("bsd,df->bsf", x, w_gate)
        u = torch.einsum("bsd,df->bsf", x, w_up)
        h = _sigmoid(g) * u
        return torch.einsum("bsf,fd->bsd", h, w_down)

    x = A_x
    xn = _layernorm(x, axis=-1)
    return x + _A_ref(xn, A_c_kv, A_w_q, A_w_uk, A_w_uv, A_w_o) + _B_ref(xn, B_w_gate, B_w_up, B_w_down)
```

```cpp
#include <hip/hip_runtime.h>
#include <hip/hip_bf16.h>
typedef unsigned short u16;
__device__ __forceinline__ float bf2f(u16 b) { return __uint_as_float(((unsigned)b) << 16); }
__device__ __forceinline__ u16 f2bf(float f) { unsigned u = __float_as_uint(f); u += 0x7FFFu + ((u >> 16) & 1u); return (u16)(u >> 16); }
__device__ __forceinline__ float rbf(float f) { return bf2f(f2bf(f)); }
constexpr int B=2,H=32,SK=4096,D=128,M=512,DC=512,F=1376;

__device__ void ln(const u16* x, float* xn  ) {
    __shared__ float red[2][2];
    int t = threadIdx.x;
    if (t < 2) {
        float s = 0; for (int i = 0; i < M; ++i) s += bf2f(x[t*M+i]);
        float mean = s / M; float v = 0;
        for (int i = 0; i < M; ++i) { float d = bf2f(x[t*M+i]) - mean; v += d*d; }
        v /= M;
        red[t][0] = rbf(mean); red[t][1] = rbf(sqrtf(rbf(rbf(v) + 1e-5f)));
    }
    __syncthreads();
    for (int i = t; i < 2*M; i += blockDim.x) { int b = i / M; xn[i] = rbf(rbf(bf2f(x[i]) - red[b][0]) / red[b][1]); }
    __syncthreads();
}
__global__ void k_q(const u16* x, const u16* wq, const u16* wuk, float* qabs) {
    __shared__ float xn[2*M]; __shared__ float q[2][D];
    ln(x, xn);
    int h = blockIdx.x, t = threadIdx.x; int b = t >> 7, d = t & 127;
    float a = 0; for (int m = 0; m < M; ++m) a += xn[b*M+m] * bf2f(wq[(size_t)m*H*D + h*D + d]);
    q[b][d] = rbf(a);
    __syncthreads();
    for (int i = t; i < 2*DC; i += 256) { int bb = i / DC, c = i % DC; float s = 0;
        for (int dd = 0; dd < D; ++dd) s += q[bb][dd] * bf2f(wuk[(size_t)c*H*D + h*D + dd]);
        qabs[(bb*H + h)*DC + c] = s; }
}
__global__ void k_s(const float* qabs, const u16* ckv, float* S) {
    int b = blockIdx.y, k0 = blockIdx.x * 8; int t = threadIdx.x; int h = t & 31, k = k0 + (t >> 5);
    float s = 0; for (int c = 0; c < DC; ++c) s += qabs[(b*H+h)*DC + c] * bf2f(ckv[((size_t)b*SK + k)*DC + c]);
    S[(b*H+h)*SK + k] = s * 0.08838834764831845f;
}
__global__ void k_o(float* S, const u16* ckv, const u16* wuv, float* ctx) {
    __shared__ float p[SK]; __shared__ float red[512]; __shared__ float o[DC];
    int bh = blockIdx.x, b = bh / H, h = bh % H, t = threadIdx.x;
    float mx = -1e30f; for (int k = t; k < SK; k += 512) mx = fmaxf(mx, S[bh*SK + k]);
    red[t] = mx; __syncthreads();
    for (int s = 256; s > 0; s >>= 1) { if (t < s) red[t] = fmaxf(red[t], red[t+s]); __syncthreads(); }
    mx = red[0]; __syncthreads();
    float sum = 0; for (int k = t; k < SK; k += 512) { float e = __expf(S[bh*SK+k] - mx); p[k] = e; sum += e; }
    red[t] = sum; __syncthreads();
    for (int s = 256; s > 0; s >>= 1) { if (t < s) red[t] += red[t+s]; __syncthreads(); }
    sum = red[0];
    float a = 0; for (int k = 0; k < SK; ++k) a += p[k] * bf2f(ckv[((size_t)b*SK + k)*DC + t]);
    o[t] = a / sum; __syncthreads();
    if (t < D) { float s = 0; for (int c = 0; c < DC; ++c) s += o[c] * bf2f(wuv[(size_t)c*H*D + h*D + t]); ctx[bh*D + t] = rbf(s); }
}
__global__ void k_h(const u16* x, const u16* wg, const u16* wu, float* hh) {
    __shared__ float xn[2*M]; ln(x, xn);
    int t = threadIdx.x; if (t >= 64) return; int b = t >> 5, f = blockIdx.x*32 + (t & 31);
    float g = 0, u = 0; for (int m = 0; m < M; ++m) { g += xn[b*M+m]*bf2f(wg[(size_t)m*F+f]); u += xn[b*M+m]*bf2f(wu[(size_t)m*F+f]); }
    g = rbf(g); u = rbf(u); float s = rbf(1.f/(1.f+__expf(-g))); hh[b*F+f] = rbf(s*u);
}
__global__ void k_out(const u16* x, const float* ctx, const u16* wo, const float* hh, const u16* wd, u16* out) {
    int i = blockIdx.x*64 + threadIdx.x; int b = i / M, m = i % M;
    float a = 0; for (int j = 0; j < H*D; ++j) a += ctx[b*H*D + j] * bf2f(wo[(size_t)j*M + m]);
    float g = 0; for (int f = 0; f < F; ++f) g += hh[b*F+f] * bf2f(wd[(size_t)f*M + m]);
    out[i] = f2bf(rbf(bf2f(x[i]) + rbf(a)) + rbf(g));
}
extern "C" void kernel_launch(void* const* d_in, const int* in_sizes, int n_in,
                              void* d_out, int out_size, void* d_ws, size_t ws_size,
                              hipStream_t stream) {
    const u16* x = (const u16*)d_in[0]; const u16* ckv = (const u16*)d_in[1]; const u16* wq = (const u16*)d_in[2];
    const u16* wuk = (const u16*)d_in[3]; const u16* wuv = (const u16*)d_in[4]; const u16* wo = (const u16*)d_in[5];
    const u16* wg = (const u16*)d_in[7]; const u16* wu = (const u16*)d_in[8]; const u16* wd = (const u16*)d_in[9];
    float* ws = (float*)d_ws; float* qabs = ws; float* S = qabs + B*H*DC; float* ctx = S + B*H*SK; float* hh = ctx + B*H*D;
    k_q<<<H, 256, 0, stream>>>(x, wq, wuk, qabs);
    k_s<<<dim3(SK/8, B), 256, 0, stream>>>(qabs, ckv, S);
    k_o<<<B*H, 512, 0, stream>>>(S, ckv, wuv, ctx);
    k_h<<<F/32, 256, 0, stream>>>(x, wg, wu, hh);
    k_out<<<16, 64, 0, stream>>>(x, ctx, wo, hh, wd, (u16*)d_out);
}
```

```cpp
#include <hip/hip_runtime.h>
#include <hip/hip_bf16.h>
typedef unsigned short u16;
__device__ __forceinline__ float bf2f(u16 b) { return __uint_as_float(((unsigned)b) << 16); }
__device__ __forceinline__ u16 f2bf(float f) { unsigned u = __float_as_uint(f); u += 0x7FFFu + ((u >> 16) & 1u); return (u16)(u >> 16); }
__device__ __forceinline__ float rbf(float f) { return bf2f(f2bf(f)); }
constexpr int B=2,H=32,SK=4096,D=128,M=512,DC=512,F=1376;

__device__ void ln(const u16* x, float* xn  ) {
    __shared__ float red[2][2];
    int t = threadIdx.x;
    if (t < 2) {
        float s = 0; for (int i = 0; i < M; ++i) s += bf2f(x[t*M+i]);
        float mean = s / M; float v = 0;
        for (int i = 0; i < M; ++i) { float d = bf2f(x[t*M+i]) - mean; v += d*d; }
        v /= M;
        red[t][0] = rbf(mean); red[t][1] = rbf(sqrtf(rbf(rbf(v) + 1e-5f)));
    }
    __syncthreads();
    for (int i = t; i < 2*M; i += blockDim.x) { int b = i / M; xn[i] = rbf(rbf(bf2f(x[i]) - red[b][0]) / red[b][1]); }
    __syncthreads();
}
__global__ void k_q(const u16* x, const u16* wq, const u16* wuk, float* qabs) {
    __shared__ float xn[2*M]; __shared__ float q[2][D];
    ln(x, xn);
    int h = blockIdx.x, t = threadIdx.x; int b = t >> 7, d = t & 127;
    float a = 0; for (int m = 0; m < M; ++m) a += xn[b*M+m] * bf2f(wq[(size_t)m*H*D + h*D + d]);
    q[b][d] = rbf(a);
    __syncthreads();
    for (int i = t; i < 2*DC; i += 256) { int bb = i / DC, c = i % DC; float s = 0;
        for (int dd = 0; dd < D; ++dd) s += q[bb][dd] * bf2f(wuk[(size_t)c*H*D + h*D + dd]);
        qabs[(bb*H + h)*DC + c] = s; }
}
__global__ void k_s(const float* qabs, const u16* ckv, float* S) {
    int b = blockIdx.y, k0 = blockIdx.x * 8; int t = threadIdx.x; int h = t & 31, k = k0 + (t >> 5);
    float s = 0; for (int c = 0; c < DC; ++c) s += qabs[(b*H+h)*DC + c] * bf2f(ckv[((size_t)b*SK + k)*DC + c]);
    S[(b*H+h)*SK + k] = s * 0.08838834764831845f;
}
__global__ void k_o(float* S, const u16* ckv, const u16* wuv, float* ctx) {
    __shared__ float p[SK]; __shared__ float red[512]; __shared__ float o[DC];
    int bh = blockIdx.x, b = bh / H, h = bh % H, t = threadIdx.x;
    float mx = -1e30f; for (int k = t; k < SK; k += 512) mx = fmaxf(mx, S[bh*SK + k]);
    red[t] = mx; __syncthreads();
    for (int s = 256; s > 0; s >>= 1) { if (t < s) red[t] = fmaxf(red[t], red[t+s]); __syncthreads(); }
    mx = red[0]; __syncthreads();
    float sum = 0; for (int k = t; k < SK; k += 512) { float e = __expf(S[bh*SK+k] - mx); p[k] = e; sum += e; }
    red[t] = sum; __syncthreads();
    for (int s = 256; s > 0; s >>= 1) { if (t < s) red[t] += red[t+s]; __syncthreads(); }
    sum = red[0];
    float a = 0; for (int k = 0; k < SK; ++k) a += p[k] * bf2f(ckv[((size_t)b*SK + k)*DC + t]);
    o[t] = a / sum; __syncthreads();
    if (t < D) { float s = 0; for (int c = 0; c < DC; ++c) s += o[c] * bf2f(wuv[(size_t)c*H*D + h*D + t]); ctx[bh*D + t] = rbf(s); }
}
__global__ void k_h(const u16* x, const u16* wg, const u16* wu, float* hh) {
    __shared__ float xn[2*M]; ln(x, xn);
    int t = threadIdx.x; if (t >= 64) return; int b = t >> 5, f = blockIdx.x*32 + (t & 31);
    float g = 0, u = 0; for (int m = 0; m < M; ++m) { g += xn[b*M+m]*bf2f(wg[(size_t)m*F+f]); u += xn[b*M+m]*bf2f(wu[(size_t)m*F+f]); }
    g = rbf(g); u = rbf(u); float s = rbf(1.f/(1.f+__expf(-g))); hh[b*F+f] = rbf(s*u);
}
__global__ void k_out(const u16* x, const float* ctx, const u16* wo, const float* hh, const u16* wd, u16* out) {
    int i = blockIdx.x*64 + threadIdx.x; int b = i / M, m = i % M;
    float a = 0; for (int j = 0; j < H*D; ++j) a += ctx[b*H*D + j] * bf2f(wo[(size_t)j*M + m]);
    float g = 0; for (int f = 0; f < F; ++f) g += hh[b*F+f] * bf2f(wd[(size_t)f*M + m]);
    out[i] = f2bf(rbf(bf2f(x[i]) + rbf(a)) + rbf(g));
}

__global__ void k_chase(const unsigned* p, int n, int strideW, unsigned* sink) {
    unsigned off = 0, acc = 0;
    for (int i = 0; i < n; ++i) {
        unsigned v = __builtin_nontemporal_load(p + off);
        asm volatile("" : "+v"(v));
        unsigned z = v; asm volatile("v_and_b32 %0, 0, %1" : "=v"(z) : "v"(v));
        off += strideW + z; acc += v;
    }
    sink[0] = acc;
}
extern "C" void kernel_launch(void* const* d_in, const int* in_sizes, int n_in,
                              void* d_out, int out_size, void* d_ws, size_t ws_size,
                              hipStream_t stream) {
    const u16* x = (const u16*)d_in[0]; const u16* ckv = (const u16*)d_in[1]; const u16* wq = (const u16*)d_in[2];
    const u16* wuk = (const u16*)d_in[3]; const u16* wuv = (const u16*)d_in[4]; const u16* wo = (const u16*)d_in[5];
    const u16* wg = (const u16*)d_in[7]; const u16* wu = (const u16*)d_in[8]; const u16* wd = (const u16*)d_in[9];
    float* ws = (float*)d_ws; float* qabs = ws; float* S = qabs + B*H*DC; float* ctx = S + B*H*SK; float* hh = ctx + B*H*D;
    k_chase<<<1,1,0,stream>>>((const unsigned*)d_in[1], 0, 1056, (unsigned*)d_ws + 10000000);
    k_q<<<H, 256, 0, stream>>>(x, wq, wuk, qabs);
    k_s<<<dim3(SK/8, B), 256, 0, stream>>>(qabs, ckv, S);
    k_o<<<B*H, 512, 0, stream>>>(S, ckv, wuv, ctx);
    k_h<<<F/32, 256, 0, stream>>>(x, wg, wu, hh);
    k_out<<<16, 64, 0, stream>>>(x, ctx, wo, hh, wd, (u16*)d_out);
}
```

```cpp
#include <hip/hip_runtime.h>
#include <hip/hip_bf16.h>
typedef unsigned short u16;
__device__ __forceinline__ float bf2f(u16 b) { return __uint_as_float(((unsigned)b) << 16); }
__device__ __forceinline__ u16 f2bf(float f) { unsigned u = __float_as_uint(f); u += 0x7FFFu + ((u >> 16) & 1u); return (u16)(u >> 16); }
__device__ __forceinline__ float rbf(float f) { return bf2f(f2bf(f)); }
constexpr int B=2,H=32,SK=4096,D=128,M=512,DC=512,F=1376;

__device__ void ln(const u16* x, float* xn  ) {
    __shared__ float red[2][2];
    int t = threadIdx.x;
    if (t < 2) {
        float s = 0; for (int i = 0; i < M; ++i) s += bf2f(x[t*M+i]);
        float mean = s / M; float v = 0;
        for (int i = 0; i < M; ++i) { float d = bf2f(x[t*M+i]) - mean; v += d*d; }
        v /= M;
        red[t][0] = rbf(mean); red[t][1] = rbf(sqrtf(rbf(rbf(v) + 1e-5f)));
    }
    __syncthreads();
    for (int i = t; i < 2*M; i += blockDim.x) { int b = i / M; xn[i] = rbf(rbf(bf2f(x[i]) - red[b][0]) / red[b][1]); }
    __syncthreads();
}
__global__ void k_q(const u16* x, const u16* wq, const u16* wuk, float* qabs) {
    __shared__ float xn[2*M]; __shared__ float q[2][D];
    ln(x, xn);
    int h = blockIdx.x, t = threadIdx.x; int b = t >> 7, d = t & 127;
    float a = 0; for (int m = 0; m < M; ++m) a += xn[b*M+m] * bf2f(wq[(size_t)m*H*D + h*D + d]);
    q[b][d] = rbf(a);
    __syncthreads();
    for (int i = t; i < 2*DC; i += 256) { int bb = i / DC, c = i % DC; float s = 0;
        for (int dd = 0; dd < D; ++dd) s += q[bb][dd] * bf2f(wuk[(size_t)c*H*D + h*D + dd]);
        qabs[(bb*H + h)*DC + c] = s; }
}
__global__ void k_s(const float* qabs, const u16* ckv, float* S) {
    int b = blockIdx.y, k0 = blockIdx.x * 8; int t = threadIdx.x; int h = t & 31, k = k0 + (t >> 5);
    float s = 0; for (int c = 0; c < DC; ++c) s += qabs[(b*H+h)*DC + c] * bf2f(ckv[((size_t)b*SK + k)*DC + c]);
    S[(b*H+h)*SK + k] = s * 0.08838834764831845f;
}
__global__ void k_o(float* S, const u16* ckv, const u16* wuv, float* ctx) {
    __shared__ float p[SK]; __shared__ float red[512]; __shared__ float o[DC];
    int bh = blockIdx.x, b = bh / H, h = bh % H, t = threadIdx.x;
    float mx = -1e30f; for (int k = t; k < SK; k += 512) mx = fmaxf(mx, S[bh*SK + k]);
    red[t] = mx; __syncthreads();
    for (int s = 256; s > 0; s >>= 1) { if (t < s) red[t] = fmaxf(red[t], red[t+s]); __syncthreads(); }
    mx = red[0]; __syncthreads();
    float sum = 0; for (int k = t; k < SK; k += 512) { float e = __expf(S[bh*SK+k] - mx); p[k] = e; sum += e; }
    red[t] = sum; __syncthreads();
    for (int s = 256; s > 0; s >>= 1) { if (t < s) red[t] += red[t+s]; __syncthreads(); }
    sum = red[0];
    float a = 0; for (int k = 0; k < SK; ++k) a += p[k] * bf2f(ckv[((size_t)b*SK + k)*DC + t]);
    o[t] = a / sum; __syncthreads();
    if (t < D) { float s = 0; for (int c = 0; c < DC; ++c) s += o[c] * bf2f(wuv[(size_t)c*H*D + h*D + t]); ctx[bh*D + t] = rbf(s); }
}
__global__ void k_h(const u16* x, const u16* wg, const u16* wu, float* hh) {
    __shared__ float xn[2*M]; ln(x, xn);
    int t = threadIdx.x; if (t >= 64) return; int b = t >> 5, f = blockIdx.x*32 + (t & 31);
    float g = 0, u = 0; for (int m = 0; m < M; ++m) { g += xn[b*M+m]*bf2f(wg[(size_t)m*F+f]); u += xn[b*M+m]*bf2f(wu[(size_t)m*F+f]); }
    g = rbf(g); u = rbf(u); float s = rbf(1.f/(1.f+__expf(-g))); hh[b*F+f] = rbf(s*u);
}
__global__ void k_out(const u16* x, const float* ctx, const u16* wo, const float* hh, const u16* wd, u16* out) {
    int i = blockIdx.x*64 + threadIdx.x; int b = i / M, m = i % M;
    float a = 0; for (int j = 0; j < H*D; ++j) a += ctx[b*H*D + j] * bf2f(wo[(size_t)j*M + m]);
    float g = 0; for (int f = 0; f < F; ++f) g += hh[b*F+f] * bf2f(wd[(size_t)f*M + m]);
    out[i] = f2bf(rbf(bf2f(x[i]) + rbf(a)) + rbf(g));
}

typedef unsigned v4u __attribute__((ext_vector_type(4)));
#define GAS __attribute__((address_space(1)))
__device__ __forceinline__ unsigned long long rt() { return __builtin_amdgcn_s_memrealtime(); }
__global__ void k_report(const unsigned long long* res, int n, int mode, int scale) {
    if (threadIdx.x) return;
    unsigned long long mna = ~0ull, mxa = 0, mxb = 0, mxd = 0, mnd = ~0ull, sum = 0;
    for (int i = 0; i < n; ++i) { unsigned long long a = res[2*i], b = res[2*i+1]; mna = a < mna ? a : mna; mxa = a > mxa ? a : mxa; mxb = b > mxb ? b : mxb; unsigned long long d = b - a; mxd = d > mxd ? d : mxd; mnd = d < mnd ? d : mnd; sum += d; }
    unsigned long long q = mode == 0 ? mxa - mna : mode == 1 ? mxd : mode == 2 ? mxb - mna : mode == 3 ? mnd : sum / n;
    if (q > 100000) q = 100000;
    unsigned long long t = rt() + q * scale;
    while (rt() < t) __builtin_amdgcn_s_sleep(1);
}

template <bool NT>
__global__ void __launch_bounds__(512) k_probe(unsigned long long* out, const v4u* a0, const v4u* a1, const v4u* a2, const v4u* a3, const v4u* a4) {
    extern __shared__ unsigned char smem[];
    unsigned long long t0, t1;
    asm volatile("s_memrealtime %0\n\ts_waitcnt lgkmcnt(0)" : "=s"(t0) :: "memory");
    const int i = blockIdx.x * 512 + threadIdx.x; const int N = 256 * 512;
    v4u r[12];
#define LD(p) (NT ? __builtin_nontemporal_load((const GAS v4u*)(p)) : *(const GAS v4u*)(p))
    r[0] = LD(a1 + i); r[1] = LD(a1 + i + N); r[2] = LD(a2 + i); r[3] = LD(a2 + i + N);
    r[4] = LD(a0 + i); r[5] = LD(a0 + i + N); r[6] = LD(a0 + i + 2*N); r[7] = LD(a0 + i + 3*N);
    r[8] = LD(a3 + i); r[9] = LD(a3 + i + N); r[10] = LD(a4 + i); r[11] = LD(a4 + i + N);
    __builtin_amdgcn_sched_barrier(0);
    asm volatile("s_waitcnt vmcnt(0)" ::: "memory");
    asm volatile("s_memrealtime %0\n\ts_waitcnt lgkmcnt(0)" : "=s"(t1) :: "memory");
    v4u x = r[0];
    for (int k = 1; k < 12; ++k) x ^= r[k];
    if ((threadIdx.x & 63) == 0) { unsigned long long* s = out + ((size_t)blockIdx.x * 8 + (threadIdx.x >> 6)) * 2; s[0] = t0; s[1] = t1 + ((x.x ^ x.y ^ x.z ^ x.w) == 0x12345678u); }
}
extern "C" void kernel_launch(void* const* d_in, const int* in_sizes, int n_in,
                              void* d_out, int out_size, void* d_ws, size_t ws_size,
                              hipStream_t stream) {
    const u16* x = (const u16*)d_in[0]; const u16* ckv = (const u16*)d_in[1]; const u16* wq = (const u16*)d_in[2];
    const u16* wuk = (const u16*)d_in[3]; const u16* wuv = (const u16*)d_in[4]; const u16* wo = (const u16*)d_in[5];
    const u16* wg = (const u16*)d_in[7]; const u16* wu = (const u16*)d_in[8]; const u16* wd = (const u16*)d_in[9];
    float* ws = (float*)d_ws; float* qabs = ws; float* S = qabs + B*H*DC; float* ctx = S + B*H*SK; float* hh = ctx + B*H*D;
    unsigned long long* res = (unsigned long long*)((char*)d_ws + (64<<20));
    hipFuncSetAttribute((const void*)k_probe<false>, hipFuncAttributeMaxDynamicSharedMemorySize, 160*1024); k_probe<false><<<256,512,100*1024,stream>>>(res, (const v4u*)d_in[1], (const v4u*)d_in[2], (const v4u*)d_in[3], (const v4u*)d_in[4], (const v4u*)d_in[5]);
    k_report<<<1,64,0,stream>>>(res, 2048, 2, 20);
    k_q<<<H, 256, 0, stream>>>(x, wq, wuk, qabs);
    k_s<<<dim3(SK/8, B), 256, 0, stream>>>(qabs, ckv, S);
    k_o<<<B*H, 512, 0, stream>>>(S, ckv, wuv, ctx);
    k_h<<<F/32, 256, 0, stream>>>(x, wg, wu, hh);
    k_out<<<16, 64, 0, stream>>>(x, ctx, wo, hh, wd, (u16*)d_out);
}
```

```cpp
#include <hip/hip_runtime.h>
#include <hip/hip_bf16.h>
typedef unsigned short u16;
__device__ __forceinline__ float bf2f(u16 b) { return __uint_as_float(((unsigned)b) << 16); }
__device__ __forceinline__ u16 f2bf(float f) { unsigned u = __float_as_uint(f); u += 0x7FFFu + ((u >> 16) & 1u); return (u16)(u >> 16); }
__device__ __forceinline__ float rbf(float f) { return bf2f(f2bf(f)); }
constexpr int B=2,H=32,SK=4096,D=128,M=512,DC=512,F=1376;

__device__ void ln(const u16* x, float* xn  ) {
    __shared__ float red[2][2];
    int t = threadIdx.x;
    if (t < 2) {
        float s = 0; for (int i = 0; i < M; ++i) s += bf2f(x[t*M+i]);
        float mean = s / M; float v = 0;
        for (int i = 0; i < M; ++i) { float d = bf2f(x[t*M+i]) - mean; v += d*d; }
        v /= M;
        red[t][0] = rbf(mean); red[t][1] = rbf(sqrtf(rbf(rbf(v) + 1e-5f)));
    }
    __syncthreads();
    for (int i = t; i < 2*M; i += blockDim.x) { int b = i / M; xn[i] = rbf(rbf(bf2f(x[i]) - red[b][0]) / red[b][1]); }
    __syncthreads();
}
__global__ void k_q(const u16* x, const u16* wq, const u16* wuk, float* qabs) {
    __shared__ float xn[2*M]; __shared__ float q[2][D];
    ln(x, xn);
    int h = blockIdx.x, t = threadIdx.x; int b = t >> 7, d = t & 127;
    float a = 0; for (int m = 0; m < M; ++m) a += xn[b*M+m] * bf2f(wq[(size_t)m*H*D + h*D + d]);
    q[b][d] = rbf(a);
    __syncthreads();
    for (int i = t; i < 2*DC; i += 256) { int bb = i / DC, c = i % DC; float s = 0;
        for (int dd = 0; dd < D; ++dd) s += q[bb][dd] * bf2f(wuk[(size_t)c*H*D + h*D + dd]);
        qabs[(bb*H + h)*DC + c] = s; }
}
__global__ void k_s(const float* qabs, const u16* ckv, float* S) {
    int b = blockIdx.y, k0 = blockIdx.x * 8; int t = threadIdx.x; int h = t & 31, k = k0 + (t >> 5);
    float s = 0; for (int c = 0; c < DC; ++c) s += qabs[(b*H+h)*DC + c] * bf2f(ckv[((size_t)b*SK + k)*DC + c]);
    S[(b*H+h)*SK + k] = s * 0.08838834764831845f;
}
__global__ void k_o(float* S, const u16* ckv, const u16* wuv, float* ctx) {
    __shared__ float p[SK]; __shared__ float red[512]; __shared__ float o[DC];
    int bh = blockIdx.x, b = bh / H, h = bh % H, t = threadIdx.x;
    float mx = -1e30f; for (int k = t; k < SK; k += 512) mx = fmaxf(mx, S[bh*SK + k]);
    red[t] = mx; __syncthreads();
    for (int s = 256; s > 0; s >>= 1) { if (t < s) red[t] = fmaxf(red[t], red[t+s]); __syncthreads(); }
    mx = red[0]; __syncthreads();
    float sum = 0; for (int k = t; k < SK; k += 512) { float e = __expf(S[bh*SK+k] - mx); p[k] = e; sum += e; }
    red[t] = sum; __syncthreads();
    for (int s = 256; s > 0; s >>= 1) { if (t < s) red[t] += red[t+s]; __syncthreads(); }
    sum = red[0];
    float a = 0; for (int k = 0; k < SK; ++k) a += p[k] * bf2f(ckv[((size_t)b*SK + k)*DC + t]);
    o[t] = a / sum; __syncthreads();
    if (t < D) { float s = 0; for (int c = 0; c < DC; ++c) s += o[c] * bf2f(wuv[(size_t)c*H*D + h*D + t]); ctx[bh*D + t] = rbf(s); }
}
__global__ void k_h(const u16* x, const u16* wg, const u16* wu, float* hh) {
    __shared__ float xn[2*M]; ln(x, xn);
    int t = threadIdx.x; if (t >= 64) return; int b = t >> 5, f = blockIdx.x*32 + (t & 31);
    float g = 0, u = 0; for (int m = 0; m < M; ++m) { g += xn[b*M+m]*bf2f(wg[(size_t)m*F+f]); u += xn[b*M+m]*bf2f(wu[(size_t)m*F+f]); }
    g = rbf(g); u = rbf(u); float s = rbf(1.f/(1.f+__expf(-g))); hh[b*F+f] = rbf(s*u);
}
__global__ void k_out(const u16* x, const float* ctx, const u16* wo, const float* hh, const u16* wd, u16* out) {
    int i = blockIdx.x*64 + threadIdx.x; int b = i / M, m = i % M;
    float a = 0; for (int j = 0; j < H*D; ++j) a += ctx[b*H*D + j] * bf2f(wo[(size_t)j*M + m]);
    float g = 0; for (int f = 0; f < F; ++f) g += hh[b*F+f] * bf2f(wd[(size_t)f*M + m]);
    out[i] = f2bf(rbf(bf2f(x[i]) + rbf(a)) + rbf(g));
}

typedef unsigned v4u __attribute__((ext_vector_type(4)));
#define GAS __attribute__((address_space(1)))
__device__ __forceinline__ unsigned long long rt() { return __builtin_amdgcn_s_memrealtime(); }
__global__ void k_report(const unsigned long long* res, int n, int mode, int scale) {
    if (threadIdx.x) return;
    unsigned long long mna = ~0ull, mxa = 0, mxb = 0, mxd = 0, mnd = ~0ull, sum = 0;
    for (int i = 0; i < n; ++i) { unsigned long long a = res[2*i], b = res[2*i+1]; mna = a < mna ? a : mna; mxa = a > mxa ? a : mxa; mxb = b > mxb ? b : mxb; unsigned long long d = b - a; mxd = d > mxd ? d : mxd; mnd = d < mnd ? d : mnd; sum += d; }
    unsigned long long q = mode == 0 ? mxa - mna : mode == 1 ? mxd : mode == 2 ? mxb - mna : mode == 3 ? mnd : sum / n;
    if (q > 100000) q = 100000;
    unsigned long long t = rt() + q * scale;
    while (rt() < t) __builtin_amdgcn_s_sleep(1);
}

__global__ void __launch_bounds__(64) k_probe(unsigned long long* out, const unsigned* p) {
    unsigned long long t0, t1;
    unsigned v = __hip_atomic_load((const GAS unsigned*)p + 1024 * 8 + blockIdx.x * 64, __ATOMIC_RELAXED, __HIP_MEMORY_SCOPE_AGENT);
    asm volatile("s_waitcnt vmcnt(0)" ::: "memory"); asm volatile("" :: "v"(v));
    asm volatile("s_memrealtime %0\n\ts_waitcnt lgkmcnt(0)" : "=s"(t0) :: "memory");
    unsigned off = 0;
    for (int i = 0; i < 100; ++i) { unsigned w = __hip_atomic_load((const GAS unsigned*)p + off, __ATOMIC_RELAXED, __HIP_MEMORY_SCOPE_AGENT); unsigned z; asm volatile("v_and_b32 %0, 0, %1" : "=v"(z) : "v"(w)); off += 64 + z; }
    asm volatile("s_memrealtime %0\n\ts_waitcnt lgkmcnt(0)" : "=s"(t1) :: "memory");
    if (threadIdx.x == 0) { out[0] = t0; out[1] = t1 + (off == 1); }
}
extern "C" void kernel_launch(void* const* d_in, const int* in_sizes, int n_in,
                              void* d_out, int out_size, void* d_ws, size_t ws_size,
                              hipStream_t stream) {
    const u16* x = (const u16*)d_in[0]; const u16* ckv = (const u16*)d_in[1]; const u16* wq = (const u16*)d_in[2];
    const u16* wuk = (const u16*)d_in[3]; const u16* wuv = (const u16*)d_in[4]; const u16* wo = (const u16*)d_in[5];
    const u16* wg = (const u16*)d_in[7]; const u16* wu = (const u16*)d_in[8]; const u16* wd = (const u16*)d_in[9];
    float* ws = (float*)d_ws; float* qabs = ws; float* S = qabs + B*H*DC; float* ctx = S + B*H*SK; float* hh = ctx + B*H*D;
    unsigned long long* res = (unsigned long long*)((char*)d_ws + (64<<20));
    k_probe<<<1,64,0,stream>>>(res, (const unsigned*)((char*)d_ws + 0));
    k_report<<<1,64,0,stream>>>(res, 1, 1, 10);
    k_q<<<H, 256, 0, stream>>>(x, wq, wuk, qabs);
    k_s<<<dim3(SK/8, B), 256, 0, stream>>>(qabs, ckv, S);
    k_o<<<B*H, 512, 0, stream>>>(S, ckv, wuv, ctx);
    k_h<<<F/32, 256, 0, stream>>>(x, wg, wu, hh);
    k_out<<<16, 64, 0, stream>>>(x, ctx, wo, hh, wd, (u16*)d_out);
}
```

```cpp
#include <hip/hip_runtime.h>
#include <hip/hip_bf16.h>
typedef unsigned short u16;
__device__ __forceinline__ float bf2f(u16 b) { return __uint_as_float(((unsigned)b) << 16); }
__device__ __forceinline__ u16 f2bf(float f) { unsigned u = __float_as_uint(f); u += 0x7FFFu + ((u >> 16) & 1u); return (u16)(u >> 16); }
__device__ __forceinline__ float rbf(float f) { return bf2f(f2bf(f)); }
constexpr int B=2,H=32,SK=4096,D=128,M=512,DC=512,F=1376;

__device__ void ln(const u16* x, float* xn  ) {
    __shared__ float red[2][2];
    int t = threadIdx.x;
    if (t < 2) {
        float s = 0; for (int i = 0; i < M; ++i) s += bf2f(x[t*M+i]);
        float mean = s / M; float v = 0;
        for (int i = 0; i < M; ++i) { float d = bf2f(x[t*M+i]) - mean; v += d*d; }
        v /= M;
        red[t][0] = rbf(mean); red[t][1] = rbf(sqrtf(rbf(rbf(v) + 1e-5f)));
    }
    __syncthreads();
    for (int i = t; i < 2*M; i += blockDim.x) { int b = i / M; xn[i] = rbf(rbf(bf2f(x[i]) - red[b][0]) / red[b][1]); }
    __syncthreads();
}
__global__ void k_q(const u16* x, const u16* wq, const u16* wuk, float* qabs) {
    __shared__ float xn[2*M]; __shared__ float q[2][D];
    ln(x, xn);
    int h = blockIdx.x, t = threadIdx.x; int b = t >> 7, d = t & 127;
    float a = 0; for (int m = 0; m < M; ++m) a += xn[b*M+m] * bf2f(wq[(size_t)m*H*D + h*D + d]);
    q[b][d] = rbf(a);
    __syncthreads();
    for (int i = t; i < 2*DC; i += 256) { int bb = i / DC, c = i % DC; float s = 0;
        for (int dd = 0; dd < D; ++dd) s += q[bb][dd] * bf2f(wuk[(size_t)c*H*D + h*D + dd]);
        qabs[(bb*H + h)*DC + c] = s; }
}
__global__ void k_s(const float* qabs, const u16* ckv, float* S) {
    int b = blockIdx.y, k0 = blockIdx.x * 8; int t = threadIdx.x; int h = t & 31, k = k0 + (t >> 5);
    float s = 0; for (int c = 0; c < DC; ++c) s += qabs[(b*H+h)*DC + c] * bf2f(ckv[((size_t)b*SK + k)*DC + c]);
    S[(b*H+h)*SK + k] = s * 0.08838834764831845f;
}
__global__ void k_o(float* S, const u16* ckv, const u16* wuv, float* ctx) {
    __shared__ float p[SK]; __shared__ float red[512]; __shared__ float o[DC];
    int bh = blockIdx.x, b = bh / H, h = bh % H, t = threadIdx.x;
    float mx = -1e30f; for (int k = t; k < SK; k += 512) mx = fmaxf(mx, S[bh*SK + k]);
    red[t] = mx; __syncthreads();
    for (int s = 256; s > 0; s >>= 1) { if (t < s) red[t] = fmaxf(red[t], red[t+s]); __syncthreads(); }
    mx = red[0]; __syncthreads();
    float sum = 0; for (int k = t; k < SK; k += 512) { float e = __expf(S[bh*SK+k] - mx); p[k] = e; sum += e; }
    red[t] = sum; __syncthreads();
    for (int s = 256; s > 0; s >>= 1) { if (t < s) red[t] += red[t+s]; __syncthreads(); }
    sum = red[0];
    float a = 0; for (int k = 0; k < SK; ++k) a += p[k] * bf2f(ckv[((size_t)b*SK + k)*DC + t]);
    o[t] = a / sum; __syncthreads();
    if (t < D) { float s = 0; for (int c = 0; c < DC; ++c) s += o[c] * bf2f(wuv[(size_t)c*H*D + h*D + t]); ctx[bh*D + t] = rbf(s); }
}
__global__ void k_h(const u16* x, const u16* wg, const u16* wu, float* hh) {
    __shared__ float xn[2*M]; ln(x, xn);
    int t = threadIdx.x; if (t >= 64) return; int b = t >> 5, f = blockIdx.x*32 + (t & 31);
    float g = 0, u = 0; for (int m = 0; m < M; ++m) { g += xn[b*M+m]*bf2f(wg[(size_t)m*F+f]); u += xn[b*M+m]*bf2f(wu[(size_t)m*F+f]); }
    g = rbf(g); u = rbf(u); float s = rbf(1.f/(1.f+__expf(-g))); hh[b*F+f] = rbf(s*u);
}
__global__ void k_out(const u16* x, const float* ctx, const u16* wo, const float* hh, const u16* wd, u16* out) {
    int i = blockIdx.x*64 + threadIdx.x; int b = i / M, m = i % M;
    float a = 0; for (int j = 0; j < H*D; ++j) a += ctx[b*H*D + j] * bf2f(wo[(size_t)j*M + m]);
    float g = 0; for (int f = 0; f < F; ++f) g += hh[b*F+f] * bf2f(wd[(size_t)f*M + m]);
    out[i] = f2bf(rbf(bf2f(x[i]) + rbf(a)) + rbf(g));
}

typedef unsigned v4u __attribute__((ext_vector_type(4)));
#define GAS __attribute__((address_space(1)))
__device__ __forceinline__ unsigned long long rt() { return __builtin_amdgcn_s_memrealtime(); }
__global__ void k_report(const unsigned long long* res, int n, int mode, int scale) {
    if (threadIdx.x) return;
    unsigned long long mna = ~0ull, mxa = 0, mxb = 0, mxd = 0, mnd = ~0ull, sum = 0;
    for (int i = 0; i < n; ++i) { unsigned long long a = res[2*i], b = res[2*i+1]; mna = a < mna ? a : mna; mxa = a > mxa ? a : mxa; mxb = b > mxb ? b : mxb; unsigned long long d = b - a; mxd = d > mxd ? d : mxd; mnd = d < mnd ? d : mnd; sum += d; }
    unsigned long long q = mode == 0 ? mxa - mna : mode == 1 ? mxd : mode == 2 ? mxb - mna : mode == 3 ? mnd : sum / n;
    if (q > 100000) q = 100000;
    unsigned long long t = rt() + q * scale;
    while (rt() < t) __builtin_amdgcn_s_sleep(1);
}

template <bool NT>
__global__ void __launch_bounds__(512) k_probe(unsigned long long* out, const v4u* a0, const v4u* a1, const v4u* a2, const v4u* a3, const v4u* a4) {
    extern __shared__ unsigned char smem[];
    unsigned long long t0, t1;
    asm volatile("s_memrealtime %0\n\ts_waitcnt lgkmcnt(0)" : "=s"(t0) :: "memory");
    const int i = blockIdx.x * 512 + threadIdx.x; const int N = 256 * 512;
    v4u r[12];
#define LD(p) (NT ? __builtin_nontemporal_load((const GAS v4u*)(p)) : *(const GAS v4u*)(p))
    r[0] = LD(a1 + i); r[1] = LD(a1 + i + N); r[2] = LD(a2 + i); r[3] = LD(a2 + i + N);

    __builtin_amdgcn_sched_barrier(0);
    asm volatile("s_waitcnt vmcnt(0)" ::: "memory");
    asm volatile("s_memrealtime %0\n\ts_waitcnt lgkmcnt(0)" : "=s"(t1) :: "memory");
    v4u x = r[0];
    for (int k = 1; k < 4; ++k) x ^= r[k];
    if ((threadIdx.x & 63) == 0) { unsigned long long* s = out + ((size_t)blockIdx.x * 8 + (threadIdx.x >> 6)) * 2; s[0] = t0; s[1] = t1 + ((x.x ^ x.y ^ x.z ^ x.w) == 0x12345678u); }
}
extern "C" void kernel_launch(void* const* d_in, const int* in_sizes, int n_in,
                              void* d_out, int out_size, void* d_ws, size_t ws_size,
                              hipStream_t stream) {
    const u16* x = (const u16*)d_in[0]; const u16* ckv = (const u16*)d_in[1]; const u16* wq = (const u16*)d_in[2];
    const u16* wuk = (const u16*)d_in[3]; const u16* wuv = (const u16*)d_in[4]; const u16* wo = (const u16*)d_in[5];
    const u16* wg = (const u16*)d_in[7]; const u16* wu = (const u16*)d_in[8]; const u16* wd = (const u16*)d_in[9];
    float* ws = (float*)d_ws; float* qabs = ws; float* S = qabs + B*H*DC; float* ctx = S + B*H*SK; float* hh = ctx + B*H*D;
    unsigned long long* res = (unsigned long long*)((char*)d_ws + (64<<20));
    hipFuncSetAttribute((const void*)k_probe<true>, hipFuncAttributeMaxDynamicSharedMemorySize, 160*1024); k_probe<true><<<256,512,100*1024,stream>>>(res, (const v4u*)d_in[1], (const v4u*)d_in[2], (const v4u*)d_in[3], (const v4u*)d_in[4], (const v4u*)d_in[5]);
    k_report<<<1,64,0,stream>>>(res, 2048, 2, 300);
    k_q<<<H, 256, 0, stream>>>(x, wq, wuk, qabs);
    k_s<<<dim3(SK/8, B), 256, 0, stream>>>(qabs, ckv, S);
    k_o<<<B*H, 512, 0, stream>>>(S, ckv, wuv, ctx);
    k_h<<<F/32, 256, 0, stream>>>(x, wg, wu, hh);
    k_out<<<16, 64, 0, stream>>>(x, ctx, wo, hh, wd, (u16*)d_out);
}
```

```cpp
#include <hip/hip_runtime.h>
#include <hip/hip_bf16.h>

typedef unsigned short u16;
typedef unsigned v4u __attribute__((ext_vector_type(4)));
typedef unsigned v2u __attribute__((ext_vector_type(2)));
typedef float f32x4 __attribute__((ext_vector_type(4)));
typedef short s16x4 __attribute__((ext_vector_type(4)));
typedef __bf16 bf16x8 __attribute__((ext_vector_type(8)));
typedef __bf16 bf16x2 __attribute__((ext_vector_type(2)));
#define LAS __attribute__((address_space(3)))
#define GAS __attribute__((address_space(1)))
#define DEVI __device__ __forceinline__

#define WNT ""
#define BDELAY 32
#define PREW 1
#define D1 3
#define D4 3
#define D2 0
#define D3 4
#define D5 0
#define HSL 1
#define STOPAT 99

constexpr int HREP = 8;
constexpr int WS_T2 = 4096;
constexpr int WS_T3 = WS_T2 + HREP * 128 * 128;
constexpr int WS_T5 = WS_T3 + HREP * 128 * 128;
constexpr int WS_H1 = WS_T5 + HREP * 128 * 128;
constexpr int WS_H4 = WS_H1 + 32 * 4 * 128 * 16;
constexpr int WS_H2 = WS_H4 + 32 * 8 * 128 * 16;
constexpr int WS_HG = WS_H2 + 2 * 32 * 128 * 16;
constexpr int WS_H5 = WS_HG + 2 * 16 * 1376 * 16;
constexpr int WS_H3 = WS_H5 + 118 * 512 * 16;
constexpr int WS_END = WS_H3 + 2 * 16 * 8 * 32 * 384;
constexpr int WS_STAMP = 16 << 20;
constexpr int L_Q = 0;
constexpr int L_XN = 0;
constexpr int L_P = 16384;
constexpr int L_QS = L_P + 4096;
constexpr int L_MX = L_QS + 1024;
constexpr int L_LS = L_MX + 512;
constexpr int L_OW = L_LS + 512;
constexpr int L_MW = L_OW + 2304;
constexpr int L_LW = L_MW + 64;
constexpr int L_HH = L_LW + 64;
constexpr int L_TAG = L_HH + 64;
constexpr int L_JUNK = L_TAG + 64;
constexpr int L_RED = L_JUNK + 256;
constexpr int L_I0 = 32768;
constexpr int L_I1 = 65536;
constexpr int L_I2 = 98304;
constexpr int L_END = 163840;
static_assert(L_RED + 1024 <= L_I0, "lds");
constexpr int SPIN_CAP = 1 << 16;

DEVI unsigned pk(float lo, float hi) { bf16x2 r = {(__bf16)lo, (__bf16)hi}; return __builtin_bit_cast(unsigned, r); }
DEVI float rbf(float f) { return (float)(__bf16)f; }
DEVI float lo16(unsigned w) { return __builtin_bit_cast(float, w << 16); }
DEVI float hi16(unsigned w) { return __builtin_bit_cast(float, w & 0xffff0000u); }
DEVI float asf(unsigned w) { return __builtin_bit_cast(float, w); }
DEVI unsigned asu(float w) { return __builtin_bit_cast(unsigned, w); }
DEVI float mx(float a, float b) { float r; asm("v_max_f32 %0, %1, %2" : "=v"(r) : "v"(a), "v"(b)); return r; }
DEVI float mx3(float a, float b, float c) { float r; asm("v_max3_f32 %0, %1, %2, %3" : "=v"(r) : "v"(a), "v"(b), "v"(c)); return r; }
DEVI f32x4 mfma32(v4u a, v4u b, f32x4 c) { return __builtin_amdgcn_mfma_f32_16x16x32_bf16(__builtin_bit_cast(bf16x8, a), __builtin_bit_cast(bf16x8, b), c, 0, 0, 0); }
DEVI f32x4 mfma16(v2u a, v2u b, f32x4 c) { return __builtin_amdgcn_mfma_f32_16x16x16bf16_1k(__builtin_bit_cast(s16x4, a), __builtin_bit_cast(s16x4, b), c, 0, 0, 0); }
template <class T> DEVI T lrd(unsigned off) { return *(LAS T*)(size_t)off; }
template <class T> DEVI void lwr(unsigned off, T v) { *(LAS T*)(size_t)off = v; }
DEVI v2u trd(unsigned off) { return __builtin_bit_cast(v2u, __builtin_amdgcn_ds_read_tr16_b64_v4i16((LAS s16x4*)(size_t)off)); }
DEVI v4u trd2(unsigned o0, unsigned o1) { v2u a = trd(o0), b = trd(o1); return v4u{a.x, a.y, b.x, b.y}; }
DEVI float sum16(float x) { auto r = __builtin_amdgcn_permlane16_swap(asu(x), asu(x), false, false); return asf(r[0]) + asf(r[1]); }
DEVI float sum32(float x) { auto r = __builtin_amdgcn_permlane32_swap(asu(x), asu(x), false, false); return asf(r[0]) + asf(r[1]); }
DEVI float max16(float x) { auto r = __builtin_amdgcn_permlane16_swap(asu(x), asu(x), false, false); return mx(asf(r[0]), asf(r[1])); }
DEVI float max32(float x) { auto r = __builtin_amdgcn_permlane32_swap(asu(x), asu(x), false, false); return mx(asf(r[0]), asf(r[1])); }
DEVI float sum16x2(float x, float y) { auto r = __builtin_amdgcn_permlane16_swap(asu(x), asu(y), false, false); return asf(r[0]) + asf(r[1]); }
DEVI float sum32x2(float x, float y) { auto r = __builtin_amdgcn_permlane32_swap(asu(x), asu(y), false, false); return asf(r[0]) + asf(r[1]); }
template <int CTRL> DEVI float dpp(float x) { return asf((unsigned)__builtin_amdgcn_update_dpp(0, (int)asu(x), CTRL, 0xf, 0xf, true)); }
DEVI float wavesum(float x) {
    x += dpp<0xB1>(x);
    x += dpp<0x4E>(x);
    x += dpp<0x141>(x);
    x += dpp<0x140>(x);
    return sum32(sum16(x));
}
#define STR_(x) #x
#define STR(x) STR_(x)
#define WAITVM(n) asm volatile("s_waitcnt vmcnt(" STR(n) ")" ::: "memory")
#define PIN(x) asm volatile("" : "+v"(x))
#define FENCE() __builtin_amdgcn_sched_barrier(0)
DEVI void bar() { asm volatile("s_waitcnt lgkmcnt(0)\n\ts_barrier" ::: "memory"); }
DEVI void bar0() { asm volatile("s_barrier" ::: "memory"); }
DEVI void glds_s(unsigned voff, const void* sbase, unsigned lds) {
    unsigned keep;
    asm volatile("s_mov_b32 %0, m0\n\ts_mov_b32 m0, %3\n\ts_nop 0\n\tglobal_load_lds_dwordx4 %1, %2" WNT "\n\ts_mov_b32 m0, %0" : "=&s"(keep) : "v"(voff), "s"(sbase), "s"(lds) : "memory");
}
typedef __amdgpu_buffer_rsrc_t rsrc_t;
DEVI v4u ldx(rsrc_t rs, unsigned off) { return __builtin_amdgcn_raw_buffer_load_b128(rs, off, 0, 16); }
DEVI void stx(rsrc_t rs, unsigned off, v4u v) { __builtin_amdgcn_raw_buffer_store_b128(v, rs, off, 0, 16); }
DEVI bool none(bool bad) { asm volatile("" ::: "memory"); return __builtin_amdgcn_ballot_w64(bad) == 0ull; }
DEVI unsigned tagx(v4u a, unsigned tag) { return (a[1] ^ tag) | (a[3] ^ tag); }
constexpr unsigned OOB = 0x80000000u;
DEVI void hint_put(rsrc_t rs, unsigned off, unsigned tag) { __builtin_amdgcn_raw_buffer_store_b32(tag, rs, off, 0, 16); }
DEVI void hint_wait(rsrc_t rs, unsigned off, unsigned tag) {
#pragma nounroll
    for (int it = SPIN_CAP; it > 0; --it) { const unsigned hv = __builtin_amdgcn_raw_buffer_load_b32(rs, off, 0, 16); if (none(hv != tag)) break; if (HSL) __builtin_amdgcn_s_sleep(HSL); }
}

#define STAMP(i)

DEVI void frags128(unsigned a0, unsigned a1, v4u (&f)[4]) {
#pragma unroll
    for (int s = 0; s < 4; ++s) f[s] = trd2(a0 + s * 8192, a1 + s * 8192);
}
DEVI f32x4 gemv4(const v4u (&a)[4], const v4u (&b)[4]) {
    f32x4 c = {0.f, 0.f, 0.f, 0.f};
#pragma unroll
    for (int s = 0; s < 4; ++s) c = mfma32(a[s], b[s], c);
    return c;
}

__global__ void __launch_bounds__(512, 2)
mla_glu_decode(const u16* __restrict__ x, const u16* __restrict__ ckv, const u16* __restrict__ wq, const u16* __restrict__ wuk,
               const u16* __restrict__ wuv, const u16* __restrict__ wo, const u16* __restrict__ wg, const u16* __restrict__ wu,
               const u16* __restrict__ wd, u16* __restrict__ out, unsigned char* __restrict__ ws) {
    STAMP(0);
    const int tid = threadIdx.x, l = tid & 63, w = __builtin_amdgcn_readfirstlane(tid >> 6);
    const int id = blockIdx.x, xc = id & 7, y = id >> 3, yy = y & 15;
    const int un = yy * 8 + xc;
    const unsigned g = l >> 4, l15 = l & 15;
    asm volatile("" :: "s"(x), "s"(ckv), "s"(wq), "s"(wuk), "s"(wuv), "s"(wo), "s"(wg), "s"(wu), "s"(wd), "s"(out), "s"(ws));
    const rsrc_t rs = __builtin_amdgcn_make_buffer_rsrc(ws, 0, WS_END, 0x00020000);
    const v4u Z = {0u, 0u, 0u, 0u};
    const f32x4 ZF = {0.f, 0.f, 0.f, 0.f};

    if (y < 16) {
        const int h = 4 * xc + (yy >> 2), cq = yy & 3;
        v4u xv; unsigned ep;
        if (w < 2) {
            asm volatile("global_load_dwordx4 %0, %1, %2" : "=v"(xv) : "v"(w * 1024 + l * 16), "s"(x) : "memory");
            asm volatile("global_load_dword %0, %1, %2 sc1" : "=v"(ep) : "v"(0), "s"(ws) : "memory");
        }
        const unsigned vb = (128 * cq + 16 * w + g) * 8192 + h * 256;
#pragma unroll
        for (int i = 0; i < 4; ++i) glds_s(vb + i * 32768 + ((l15 ^ ((g << 2) | i)) << 4), wq, L_I0 + (16 * w + 4 * i) * 256);
        bar0();
#pragma unroll
        for (int i = 0; i < 4; ++i) glds_s(vb + i * 32768 + ((l15 ^ (4 * i + g)) << 4), wuk, L_I1 + (16 * w + 4 * i) * 256);
        unsigned o_p1 = l < 16 ? WS_H1 + ((h * 4 + cq) * 128 + 16 * w + l) * 16 : OOB;
        unsigned o_g1 = WS_H1 + ((h * 4 + g) * 128 + 16 * w + l15) * 16;
        unsigned o_h2 = l15 < 2 ? WS_H2 + ((l15 * 32 + h) * 128 + 32 * cq + 4 * w + g) * 16 : OOB;
        unsigned o_t2 = tid < HREP ? WS_T2 + (tid * 128 + h * 4 + cq) * 128 : OOB;
        if (PREW) stx(rs, o_p1, Z);
        STAMP(1);
        if (w < 2) {
            if (PREW) asm volatile("s_waitcnt vmcnt(9)" : "+v"(xv), "+v"(ep) :: "memory");
            else asm volatile("s_waitcnt vmcnt(8)" : "+v"(xv), "+v"(ep) :: "memory");
            float v[8];
#pragma unroll
            for (int j = 0; j < 4; ++j) { v[2 * j] = lo16(xv[j]); v[2 * j + 1] = hi16(xv[j]); }
            float s = ((v[0] + v[1]) + (v[2] + v[3])) + ((v[4] + v[5]) + (v[6] + v[7]));
            const float mean = wavesum(s) * (1.f / 512.f);
            float sq = 0.f;
#pragma unroll
            for (int j = 0; j < 8; ++j) { const float d = v[j] - mean; sq += d * d; }
            const float var = wavesum(sq) * (1.f / 512.f);
            const float mb = rbf(mean), ri = __builtin_amdgcn_rcpf(rbf(__builtin_amdgcn_sqrtf(rbf(rbf(var) + 1e-5f))));
            v4u o;
#pragma unroll
            for (int j = 0; j < 4; ++j) o[j] = pk(rbf(v[2 * j] - mb) * ri, rbf(v[2 * j + 1] - mb) * ri);
            lwr<v4u>(L_XN + w * 1024 + l * 16, o);
            unsigned t = ep + 1u; t += (t & 0xffffu) == 0u;
            lwr<unsigned>(L_TAG, t);
        }
        bar();
        const unsigned tag = lrd<unsigned>(L_TAG);
        unsigned a0, a1;
        {
            const unsigned q = l15 >> 2, p = l & 3, ch = 2 * w + (p >> 1);
            a0 = L_I0 + (8 * g + q) * 256 + 16 * (ch ^ ((q << 2) | ((2 * g) & 3))) + 8 * (p & 1);
            a1 = L_I0 + (8 * g + q + 4) * 256 + 16 * (ch ^ ((q << 2) | ((2 * g + 1) & 3))) + 8 * (p & 1);
        }
        unsigned a_qw = L_QS + (g & 1) * 272 + (16 * w + l15) * 2;
        unsigned a_qr = L_QS + (l & 1) * 272 + 16 * g;
        unsigned a_uk[4];
        v4u xf[4];
#pragma unroll
        for (int s = 0; s < 4; ++s) {
            xf[s] = lrd<v4u>(L_XN + (l & 1) * 1024 + (128 * cq + 32 * s + 8 * g) * 2);
            a_uk[s] = L_I1 + (16 * w + l15) * 256 + (((4 * s + g) ^ l15) << 4);
            PIN(xf[s]); PIN(a_uk[s]);
        }
        v4u pb = {0u, tag, 0u, tag};
        PIN(a0); PIN(a1); PIN(o_p1); PIN(o_g1); PIN(o_h2); PIN(o_t2); PIN(a_qw); PIN(a_qr); PIN(pb);
        if (PREW) WAITVM(5); else WAITVM(4);
        bar0();
        STAMP(2);
        {
            v4u f[4]; frags128(a0, a1, f);
            FENCE();
            const f32x4 acc = gemv4(xf, f);
            pb[0] = asu(acc[0]); pb[2] = asu(acc[1]);
            stx(rs, o_p1, pb);
        }
        STAMP(3);
        if (STOPAT <= 3) { WAITVM(0); STAMP(14); goto done; }
        if (D1) __builtin_amdgcn_s_sleep(D1);
        v4u fu[4];
        {
            v4u a = ldx(rs, o_g1);
            WAITVM(0);
            STAMP(14);
#pragma unroll
            for (int s = 0; s < 4; ++s) fu[s] = lrd<v4u>(a_uk[s]);
#pragma nounroll
            for (int it = SPIN_CAP; it > 0 && !none(tagx(a, tag) != 0u); --it) a = ldx(rs, o_g1);
            hint_put(rs, o_t2, tag);
            lwr<u16>(a_qw, (u16)pk(sum32(sum16x2(asf(a[0]), asf(a[2]))), 0.f));
        }
        STAMP(4);
        bar();
        {
            v4u qf[4];
#pragma unroll
            for (int s = 0; s < 4; ++s) qf[s] = lrd<v4u>(a_qr + 64 * s);
            FENCE();
            const f32x4 acc = gemv4(fu, qf);
            constexpr float SC = 0.08838834764831845f * 1.4426950408889634f;
            pb[0] = pk(acc[0] * SC, acc[1] * SC); pb[2] = pk(acc[2] * SC, acc[3] * SC);
            stx(rs, o_h2, pb);
        }
        STAMP(5);
        if (STOPAT <= 5) goto done;
        const int hp = h >> 1, e = yy & 7, mat = un >> 6, mp = (un >> 2) & 15, fp = un & 3;
#pragma unroll
        for (int i = 0; i < 3; ++i) {
            const unsigned pc = 8 * i + w, idx = min(64 * pc + l, 1375u), row = idx / 43u, ch = idx - row * 43u;
            if (pc < 22) glds_s((32 * mp + row) * 2752 + fp * 688 + ch * 16, mat ? wu : wg, L_I0 + pc * 1024);
        }
#pragma unroll
        for (int i = 0; i < 4; ++i)
            glds_s((64 * e + 16 * (w & 3) + 4 * i + g) * 8192 + (2 * hp + (w >> 2)) * 256 + ((l15 ^ ((g << 2) | i)) << 4), wuv, L_I1 + (16 * w + 4 * i) * 256);
#pragma unroll
        for (int i = 0; i < 4; ++i) glds_s((h * 128 + 16 * w + 4 * i + g) * 1024 + cq * 256 + ((l15 ^ ((g << 2) | i)) << 4), wo, L_I2 + (16 * w + 4 * i) * 256);
        unsigned o_h5 = l < 16 ? WS_H5 + (h * 512 + 128 * cq + 16 * w + l) * 16 : OOB;
        unsigned o_h4 = l < 32 ? WS_H4 + (((2 * hp + (w >> 2)) * 8 + e) * 128 + 32 * (w & 3) + l) * 16 : OOB;
        {
            const v4u xg = lrd<v4u>(L_XN + (l & 1) * 1024 + (32 * mp + 8 * g) * 2);
            WAITVM(8);
            bar();
            const unsigned ag = L_I0 + (8 * g + (l15 >> 2)) * 688 + (l & 3) * 8;
            f32x4 o[3];
#pragma unroll
            for (int t = 0; t < 3; ++t) {
                const unsigned tl = min(8 * t + w, 21);
                o[t] = mfma32(xg, trd2(ag + tl * 32, ag + tl * 32 + 4 * 688), ZF);
            }
            const float o0 = g == 0 ? o[0][0] : g == 1 ? o[1][0] : o[2][0], o1 = g == 0 ? o[0][1] : g == 1 ? o[1][1] : o[2][1];
            const unsigned f = 16 * (8 * g + w) + l15;
            stx(rs, (g < 3 && f < 344) ? WS_HG + ((mat * 16 + mp) * 1376 + 344 * fp + f) * 16 : OOB, v4u{asu(o0), tag, asu(o1), tag});
            if (PREW) stx(rs, o_h4, Z);
        }
        STAMP(6);
        WAITVM(0);
        bar();
        v4u fv[2][2], fw[4];
        {
            const unsigned q = l15 >> 2, p = l & 3;
#pragma unroll
            for (int t = 0; t < 2; ++t) {
                const unsigned ch = 4 * (w & 3) + 2 * t + (p >> 1), base = L_I1 + (w >> 2) * 16384 + (8 * g + q) * 256 + 8 * (p & 1);
#pragma unroll
                for (int s = 0; s < 2; ++s)
                    fv[t][s] = trd2(base + s * 8192 + 16 * (ch ^ ((q << 2) | ((2 * g) & 3))), base + s * 8192 + 1024 + 16 * (ch ^ ((q << 2) | ((2 * g + 1) & 3))));
            }
        }
        frags128(a0 + (L_I2 - L_I0), a1 + (L_I2 - L_I0), fw);
        const unsigned u = l15 < 12 ? l15 : l15 - 4;
        unsigned o_h3 = WS_H3 + ((((w >> 2) * 16 + hp) * 8 + e) * 32 + 8 * (w & 3) + (g >> 1)) * 384 + (u >> 2) * 128 + (g & 1) * 64 + (u & 3) * 16;
        unsigned o_t3 = WS_T3 + (xc * 128 + (h >> 4) * 64 + 8 * w + (l & 7)) * 128;
        unsigned o_t5 = tid < HREP ? WS_T5 + (tid * 128 + cq * 32 + h) * 128 : OOB;
        unsigned o_g4 = WS_H4 + ((h * 8 + g) * 128 + 16 * w + l15) * 16;
        unsigned a_ow, a_ow2, a_ms;
        {
            const unsigned n = 6 * (l15 >> 2) + 2 * (g >> 1), n2 = 6 * (l15 >> 2) + 4, gg = l15 & 3;
            a_ow = l15 < 12 ? L_OW + w * 272 + (64 * (g & 1) + 16 * gg + 4 * ((n >> 2) ^ (gg & 1)) + (n & 3)) * 2 : L_JUNK + l * 4;
            a_ow2 = (l15 < 8 && l < 32) ? L_OW + w * 272 + (64 * (g & 1) + 16 * gg + 4 * ((n2 >> 2) ^ (gg & 1)) + (n2 & 3)) * 2 : L_JUNK + l * 4;
            a_ms = L_MW + (g >> 1) * 64 + (g & 1) * 32 + w * 4;
        }
        unsigned a_or = L_OW + (l & 7) * 272 + (w >> 2) * 128 + 16 * g, a_mw = L_MW + (w >> 2) * 32 + (g & 1) * 16;
        unsigned TT = tag << 16;
        PIN(o_h3); PIN(o_t3); PIN(o_t5); PIN(o_g4); PIN(o_h4); PIN(a_ow); PIN(a_ow2); PIN(a_or); PIN(a_mw); PIN(a_ms); PIN(o_h5); PIN(TT);
        PIN(fv[0][0]); PIN(fv[0][1]); PIN(fv[1][0]); PIN(fv[1][1]); PIN(fw[0]); PIN(fw[1]); PIN(fw[2]); PIN(fw[3]);
        STAMP(7);
        if (STOPAT <= 7) goto done;
        hint_wait(rs, o_t3, tag);
        if (D3) __builtin_amdgcn_s_sleep(D3);
        STAMP(15);
        {
            v4u a[4];
#pragma nounroll
            for (int it = SPIN_CAP; it > 0; --it) {
                unsigned bad = 0;
#pragma unroll
                for (int k = 0; k < 4; ++k) a[k] = ldx(rs, o_h3 + k * 768);
#pragma unroll
                for (int k = 0; k < 4; ++k) bad |= tagx(a[k], TT);
                if (none(bad > 0xffffu)) break;
            }
            STAMP(8);
            float m[4], ls[4];
#pragma unroll
            for (int k = 0; k < 4; ++k) { const float sv = asf(__builtin_amdgcn_alignbit(a[k][3], a[k][2], 16)); m[k] = dpp<0x158>(sv); ls[k] = dpp<0x159>(sv); }
            const float M = max32(mx(mx3(m[0], m[1], m[2]), m[3]));
            float acc[6], Ls;
#pragma unroll
            for (int k = 0; k < 4; ++k) {
                const float al = __builtin_amdgcn_exp2f(m[k] - M);
                const float v[7] = {lo16(a[k][0]), hi16(a[k][0]), lo16(a[k][1]), lo16(a[k][2]), hi16(a[k][2]), lo16(a[k][3]), ls[k]};
                if (k == 0) { for (int j = 0; j < 6; ++j) acc[j] = al * v[j]; Ls = al * v[6]; }
                else { for (int j = 0; j < 6; ++j) acc[j] = __builtin_fmaf(al, v[j], acc[j]); Ls = __builtin_fmaf(al, v[6], Ls); }
            }
            const float v4 = sum32x2(acc[4], Ls);
            lwr<unsigned>(a_ow, pk(sum32x2(acc[0], acc[2]), sum32x2(acc[1], acc[3])));
            lwr<unsigned>(a_ow2, pk(v4, sum32(acc[5])));
            lwr<float>(a_ms, l < 32 ? M : v4);
        }
        bar();
        {
            const v4u of0 = lrd<v4u>(a_or), of1 = lrd<v4u>(a_or + 64);
            const f32x4 mw = lrd<f32x4>(a_mw), lw = lrd<f32x4>(a_mw + 64);
            FENCE();
            const f32x4 c0 = mfma32(of1, fv[0][1], mfma32(of0, fv[0][0], ZF)), c1 = mfma32(of1, fv[1][1], mfma32(of0, fv[1][0], ZF));
            const float M = mx(mx3(mw[0], mw[1], mw[2]), mw[3]);
            float be[4];
#pragma unroll
            for (int r = 0; r < 4; ++r) be[r] = __builtin_amdgcn_exp2f(mw[r] - M);
            const float rd = __builtin_amdgcn_rcpf((be[0] * lw[0] + be[1] * lw[1]) + (be[2] * lw[2] + be[3] * lw[3]));
            const float v0 = ((be[0] * c0[0] + be[1] * c0[1]) + (be[2] * c0[2] + be[3] * c0[3])) * rd;
            const float v1 = ((be[0] * c1[0] + be[1] * c1[1]) + (be[2] * c1[2] + be[3] * c1[3])) * rd;
            auto r = __builtin_amdgcn_permlane16_swap(asu(v0), asu(v1), false, false);
            pb[0] = r[0]; pb[2] = r[1];
            stx(rs, o_h4, pb);
        }
        STAMP(9);
        if (D4) __builtin_amdgcn_s_sleep(D4);
        {
            v4u a, b;
#pragma nounroll
            for (int it = SPIN_CAP; it > 0; --it) { a = ldx(rs, o_g4); b = ldx(rs, o_g4 + 4 * 2048); if (none((tagx(a, tag) | tagx(b, tag)) != 0u)) break; }
            hint_put(rs, o_t5, tag);
            lwr<u16>(a_qw, (u16)pk(sum32(sum16x2(asf(a[0]) + asf(b[0]), asf(a[2]) + asf(b[2]))), 0.f));
        }
        STAMP(10);
        bar();
        {
            v4u cf[4];
#pragma unroll
            for (int s = 0; s < 4; ++s) cf[s] = lrd<v4u>(a_qr + 64 * s);
            FENCE();
            const f32x4 acc = gemv4(cf, fw);
            pb[0] = asu(acc[0]); pb[2] = asu(acc[1]);
            stx(rs, o_h5, pb);
        }
        STAMP(11);
    } else {
        const int pr = xc * 8 + (yy >> 1), hg = yy & 1, b = pr >> 5, sp = pr & 31;
        unsigned ep;
        if (w == 0) asm volatile("global_load_dword %0, %1, %2 sc1" : "=v"(ep) : "v"(0), "s"(ws) : "memory");
        if (BDELAY) __builtin_amdgcn_s_sleep(BDELAY);
#pragma unroll
        for (int i = 0; i < 16; ++i) {
            if (i == 8) bar0();
            glds_s((pr * 128 + 64 * ((i >> 3) ^ hg) + 8 * w + (i & 7)) * 1024 + ((l ^ i) << 4), ckv, L_I0 + (16 * w + i) * 1024);
        }
        STAMP(1);
        if (w == 0) {
            asm volatile("s_waitcnt vmcnt(16)" : "+v"(ep) :: "memory");
            unsigned t = ep + 1u; t += (t & 0xffffu) == 0u;
            lwr<unsigned>(L_TAG, t);
        }
        unsigned qoff = WS_H2 + ((b * 32 + 16 * hg + 2 * w) * 128 + l) * 16;
        unsigned o_t2 = WS_T2 + (xc * 128 + (16 * hg + 2 * w) * 4 + (l & 7)) * 128;
        unsigned o_t3 = tid < HREP ? WS_T3 + (tid * 128 + hg * 64 + pr) * 128 : OOB;
        unsigned o_h3 = WS_H3 + (((b * 16 + 8 * hg + (l15 >> 1)) * 8 + w) * 32 + sp) * 384 + (l & 1) * 64 + g * 16;
        unsigned a_r[4], a_t[4];
#pragma unroll
        for (int j = 0; j < 4; ++j) { a_r[j] = l15 * 1024 + (((4 * j + g) ^ l15) << 4); PIN(a_r[j]); }
        {
            const unsigned q = l15 >> 2, p = l & 3;
#pragma unroll
            for (int j = 0; j < 4; ++j) {
                const unsigned r0 = 8 * g + 4 * (j & 1) + q, ch = 8 * w + 2 * p + (j >> 1);
                a_t[j] = L_I0 + r0 * 1024 + ((ch ^ (r0 & 15)) << 4) + (p & 1) * 8;
                PIN(a_t[j]);
            }
        }
        unsigned a_q0 = L_Q + 2 * w * 1024 + (((l >> 1) ^ (2 * w)) << 4) + (l & 1) * 8, a_q1 = L_Q + (2 * w + 1) * 1024 + (((l >> 1) ^ (2 * w + 1)) << 4) + (l & 1) * 8;
        unsigned a_mxw = L_MX + l15 * 32 + w * 4, a_mxr = L_MX + l15 * 32;
        unsigned a_pw = L_P + l15 * 256 + (((2 * w + (g >> 1)) ^ l15) << 4) + (g & 1) * 8;
        unsigned a_pr[4];
#pragma unroll
        for (int j = 0; j < 4; ++j) { a_pr[j] = L_P + l15 * 256 + (((4 * j + g) ^ l15) << 4); PIN(a_pr[j]); }
        PIN(qoff); PIN(o_t2); PIN(o_t3); PIN(o_h3); PIN(a_q0); PIN(a_q1); PIN(a_mxw); PIN(a_mxr); PIN(a_pw);
        WAITVM(0);
        STAMP(14);
        v4u cr[16], ct[4][4];
#pragma unroll
        for (int s = 0; s < 16; ++s) cr[s] = lrd<v4u>(a_r[s & 3] + (L_I0 + w * 16384) + (s >> 2) * 256);
        bar();
        STAMP(2);
        if (STOPAT <= 5) goto done;
        const unsigned tag = lrd<unsigned>(L_TAG);
#pragma unroll
        for (int t = 0; t < 4; ++t)
#pragma unroll
            for (int s = 0; s < 4; ++s) {
                const unsigned x8 = (t & 1) * 8;
                ct[t][s] = trd2((a_t[2 * (t >> 1)] ^ x8) + s * 32768, (a_t[2 * (t >> 1) + 1] ^ x8) + s * 32768);
            }
        FENCE();
        hint_wait(rs, o_t2, tag);
        if (D2) __builtin_amdgcn_s_sleep(D2);
        STAMP(15);
        {
            v4u qa[4];
#pragma nounroll
            for (int it = SPIN_CAP; it > 0; --it) {
#pragma unroll
                for (int k = 0; k < 4; ++k) qa[k] = ldx(rs, qoff + k * 1024);
                if (none(((tagx(qa[0], tag) | tagx(qa[1], tag)) | (tagx(qa[2], tag) | tagx(qa[3], tag))) != 0u)) break;
            }
            STAMP(3);
            lwr<v2u>(a_q0, v2u{qa[0][0], qa[0][2]}); lwr<v2u>(a_q0 + 512, v2u{qa[1][0], qa[1][2]});
            lwr<v2u>(a_q1, v2u{qa[2][0], qa[2][2]}); lwr<v2u>(a_q1 + 512, v2u{qa[3][0], qa[3][2]});
        }
        bar();
        STAMP(4);
        f32x4 sa = ZF, sb = ZF;
        {
            v4u qf[16];
#pragma unroll
            for (int s = 0; s < 16; ++s) qf[s] = lrd<v4u>(a_r[s & 3] + L_Q + (s >> 2) * 256);
            FENCE();
#pragma unroll
            for (int s = 0; s < 16; s += 2) { sa = mfma32(cr[s], qf[s], sa); sb = mfma32(cr[s + 1], qf[s + 1], sb); }
        }
        sa += sb;
        lwr<float>(a_mxw, max32(max16(mx(mx3(sa[0], sa[1], sa[2]), sa[3]))));
        bar();
        STAMP(12);
        float mrow;
        {
            const f32x4 m0 = lrd<f32x4>(a_mxr), m1 = lrd<f32x4>(a_mxr + 16);
            mrow = mx(mx3(mx3(m0[0], m0[1], m0[2]), m0[3], m1[0]), mx3(m1[1], m1[2], m1[3]));
            float p[4];
#pragma unroll
            for (int r = 0; r < 4; ++r) p[r] = __builtin_amdgcn_exp2f(sa[r] - mrow);
            lwr<v2u>(a_pw, v2u{pk(p[0], p[1]), pk(p[2], p[3])});
            lwr<float>(a_mxw + (L_LS - L_MX), sum32(sum16((p[0] + p[1]) + (p[2] + p[3]))));
        }
        bar();
        STAMP(5);
        {
            const unsigned T16 = tag & 0xffffu;
            v4u pf[4];
#pragma unroll
            for (int s = 0; s < 4; ++s) pf[s] = lrd<v4u>(a_pr[s]);
            const f32x4 l0 = lrd<f32x4>(a_mxr + (L_LS - L_MX)), l1 = lrd<f32x4>(a_mxr + (L_LS - L_MX) + 16);
            FENCE();
            f32x4 o[4];
#pragma unroll
            for (int t = 0; t < 4; ++t) {
                o[t] = ZF;
#pragma unroll
                for (int s = 0; s < 4; ++s) o[t] = mfma32(ct[t][s], pf[s], o[t]);
            }
            const float lsum = ((l0[0] + l0[1]) + (l0[2] + l0[3])) + ((l1[0] + l1[1]) + (l1[2] + l1[3]));
            unsigned d[9];
#pragma unroll
            for (int t = 0; t < 4; ++t) { d[2 * t] = pk(o[t][0], o[t][1]); d[2 * t + 1] = pk(o[t][2], o[t][3]); }
            d[8] = asu((g & 1) ? lsum : mrow);
            hint_put(rs, o_t3, tag);
#pragma unroll
            for (int i = 0; i < 3; ++i) {
                const unsigned A = d[3 * i], B = d[3 * i + 1], C = d[3 * i + 2];
                stx(rs, o_h3 + i * 128, v4u{A, __builtin_amdgcn_perm(T16, B, 0x05040100u), __builtin_amdgcn_alignbit(C, B, 16), __builtin_amdgcn_perm(T16, C, 0x05040302u)});
            }
        }
        STAMP(6);
        if (STOPAT <= 7) goto done;
        if (un < 86) {
#pragma unroll
            for (int i = 0; i < 2; ++i) glds_s((16 * un + 2 * w + i) * 1024 + ((l ^ (((2 * w + i) & 7) << 1)) << 4), wd, L_I0 + (2 * w + i) * 1024);
            {
                const unsigned off = WS_HG + (((w >> 2) * 16 + 4 * (w & 3) + g) * 1376 + 16 * un + l15) * 16;
                v4u ga;
#pragma nounroll
                for (int it = SPIN_CAP; it > 0; --it) { ga = ldx(rs, off); if (none(tagx(ga, tag) != 0u)) break; __builtin_amdgcn_s_sleep(4); }
                lwr<float>(L_RED + (l & 31) * 32 + w * 4, sum32(sum16x2(asf(ga[0]), asf(ga[2]))));
            }
            bar();
            if (w == 0) {
                const f32x4 gp = lrd<f32x4>(L_RED + (l & 31) * 32), up = lrd<f32x4>(L_RED + (l & 31) * 32 + 16);
                const float gg = rbf((gp[0] + gp[1]) + (gp[2] + gp[3])), uu = rbf((up[0] + up[1]) + (up[2] + up[3]));
                const float sg = rbf(__builtin_amdgcn_rcpf(1.f + __builtin_amdgcn_exp2f(gg * -1.4426950408889634f)));
                lwr<u16>(L_HH + (l & 31) * 2, (u16)pk(sg * uu, 0.f));
            }
            bar();
            const v2u ha = lrd<v2u>(L_HH + (l & 1) * 32 + g * 8);
            const unsigned q = l15 >> 2, p = l & 3, row = 4 * g + q;
            f32x4 o[4];
#pragma unroll
            for (int t = 0; t < 4; ++t) {
                const unsigned ch = 8 * w + 2 * t + (p >> 1);
                o[t] = mfma16(ha, trd(L_I0 + row * 1024 + ((ch ^ ((row & 7) << 1)) << 4) + (p & 1) * 8), ZF);
            }
            const float o0 = g == 0 ? o[0][0] : g == 1 ? o[1][0] : g == 2 ? o[2][0] : o[3][0];
            const float o1 = g == 0 ? o[0][1] : g == 1 ? o[1][1] : g == 2 ? o[2][1] : o[3][1];
            stx(rs, WS_H5 + ((32 + un) * 512 + 64 * w + l) * 16, v4u{asu(o0), tag, asu(o1), tag});
        }
        STAMP(7);
        if (hg == 0) {
            const int r = pr;
            unsigned o0 = WS_H5 + ((tid >> 3) * 512 + 8 * r + (tid & 7)) * 16;
            const bool two = tid < 432;
            const unsigned o1 = two ? o0 + 64 * 512 * 16 : o0 + 32 * 512 * 16;
            float xin = lo16(x[((tid >> 3) & 1) * 512 + 8 * r + (tid & 7)]);
            v4u a0, a1;
            __builtin_amdgcn_s_sleep(8);
#pragma nounroll
            for (int it = SPIN_CAP; it > 0; --it) { a1 = ldx(rs, o1); if (none(tagx(a1, tag) != 0u)) break; __builtin_amdgcn_s_sleep(4); }
            float s0 = two ? asf(a1[0]) : 0.f, s1 = two ? asf(a1[2]) : 0.f;
            unsigned a_rw = l15 < 8 ? L_RED + (((g & 1) * 8 + l15) * 2 + (g >> 1)) * 32 + w * 4 : L_JUNK + l * 4;
            unsigned a_rr = L_RED + (tid & 15) * 64;
            unsigned o_out = (((tid >> 3) & 1) * 512 + 8 * r + (tid & 7)) * 2;
            PIN(s0); PIN(s1); PIN(xin); PIN(a_rw); PIN(a_rr); PIN(o0); PIN(o_out);
            if (w < 4) { hint_wait(rs, WS_T5 + (xc * 128 + (r >> 4) * 32 + 8 * w + (l & 7)) * 128, tag); if (D5) __builtin_amdgcn_s_sleep(D5); }
            STAMP(10);
#pragma nounroll
            for (int it = SPIN_CAP; it > 0; --it) { a0 = ldx(rs, o0); if (none(tagx(a0, tag) != 0u)) break; }
            STAMP(8);
            float v0 = asf(a0[0]), v1 = asf(a0[2]);
            if (w >= 4) { s0 += v0; s1 += v1; v0 = 0.f; v1 = 0.f; }
            float p0 = sum16x2(v0, v1), p1 = sum16x2(s0, s1);
            float t = sum32x2(p0, p1);
            t += dpp<0x128>(t);
            lwr<float>(a_rw, t);
            bar();
            if (tid < 16) {
                const f32x4 t0 = lrd<f32x4>(a_rr), t2 = lrd<f32x4>(a_rr + 32), t3 = lrd<f32x4>(a_rr + 48);
                const float att = (t0[0] + t0[1]) + (t0[2] + t0[3]);
                const float glu = ((t2[0] + t2[1]) + (t2[2] + t2[3])) + ((t3[0] + t3[1]) + (t3[2] + t3[3]));
                *(GAS u16*)((GAS unsigned char*)out + o_out) = (u16)pk(rbf(xin + rbf(att)) + rbf(glu), 0.f);
            }
            if (r == 0 && tid == 0) __hip_atomic_store((GAS unsigned*)ws, tag, __ATOMIC_RELAXED, __HIP_MEMORY_SCOPE_AGENT);
            STAMP(9);
        }
    }
done:
    STAMP(13);
}

extern "C" void kernel_launch(void* const* d_in, const int* in_sizes, int n_in, void* d_out, int out_size, void* d_ws, size_t ws_size,
                              hipStream_t stream) {
    (void)in_sizes; (void)n_in; (void)out_size; (void)ws_size;
    (void)hipFuncSetAttribute((const void*)mla_glu_decode, hipFuncAttributeMaxDynamicSharedMemorySize, L_END);
    mla_glu_decode<<<256, 512, L_END, stream>>>((const u16*)d_in[0], (const u16*)d_in[1], (const u16*)d_in[2], (const u16*)d_in[3], (const u16*)d_in[4],
                                               (const u16*)d_in[5], (const u16*)d_in[7], (const u16*)d_in[8], (const u16*)d_in[9], (u16*)d_out,
                                               (unsigned char*)d_ws);
}
```

```cpp
#define STAMPS 1
#define BDELAY 32
#include <hip/hip_runtime.h>
#include <hip/hip_bf16.h>

typedef unsigned short u16;
typedef unsigned v4u __attribute__((ext_vector_type(4)));
typedef unsigned v2u __attribute__((ext_vector_type(2)));
typedef float f32x4 __attribute__((ext_vector_type(4)));
typedef short s16x4 __attribute__((ext_vector_type(4)));
typedef __bf16 bf16x8 __attribute__((ext_vector_type(8)));
typedef __bf16 bf16x2 __attribute__((ext_vector_type(2)));
#define LAS __attribute__((address_space(3)))
#define GAS __attribute__((address_space(1)))
#define DEVI __device__ __forceinline__

#define WNT ""
#define PREW 1
#define D1 3
#define D4 3
#define D2 0
#define D3 4
#define D5 0
#define HSL 1
#define STOPAT 99

constexpr int HREP = 8;
constexpr int WS_T2 = 4096;
constexpr int WS_T3 = WS_T2 + HREP * 128 * 128;
constexpr int WS_T5 = WS_T3 + HREP * 128 * 128;
constexpr int WS_H1 = WS_T5 + HREP * 128 * 128;
constexpr int WS_H4 = WS_H1 + 32 * 4 * 128 * 16;
constexpr int WS_H2 = WS_H4 + 32 * 8 * 128 * 16;
constexpr int WS_HG = WS_H2 + 2 * 32 * 128 * 16;
constexpr int WS_H5 = WS_HG + 2 * 16 * 1376 * 16;
constexpr int WS_H3 = WS_H5 + 118 * 512 * 16;
constexpr int WS_END = WS_H3 + 2 * 16 * 8 * 32 * 384;
constexpr int WS_STAMP = 16 << 20;
constexpr int L_Q = 0;
constexpr int L_XN = 0;
constexpr int L_P = 16384;
constexpr int L_QS = L_P + 4096;
constexpr int L_MX = L_QS + 1024;
constexpr int L_LS = L_MX + 512;
constexpr int L_OW = L_LS + 512;
constexpr int L_MW = L_OW + 2304;
constexpr int L_LW = L_MW + 64;
constexpr int L_HH = L_LW + 64;
constexpr int L_TAG = L_HH + 64;
constexpr int L_JUNK = L_TAG + 64;
constexpr int L_RED = L_JUNK + 256;
constexpr int L_I0 = 32768;
constexpr int L_I1 = 65536;
constexpr int L_I2 = 98304;
constexpr int L_END = 163840;
static_assert(L_RED + 1024 <= L_I0, "lds");
constexpr int SPIN_CAP = 1 << 16;

DEVI unsigned pk(float lo, float hi) { bf16x2 r = {(__bf16)lo, (__bf16)hi}; return __builtin_bit_cast(unsigned, r); }
DEVI float rbf(float f) { return (float)(__bf16)f; }
DEVI float lo16(unsigned w) { return __builtin_bit_cast(float, w << 16); }
DEVI float hi16(unsigned w) { return __builtin_bit_cast(float, w & 0xffff0000u); }
DEVI float asf(unsigned w) { return __builtin_bit_cast(float, w); }
DEVI unsigned asu(float w) { return __builtin_bit_cast(unsigned, w); }
DEVI float mx(float a, float b) { float r; asm("v_max_f32 %0, %1, %2" : "=v"(r) : "v"(a), "v"(b)); return r; }
DEVI float mx3(float a, float b, float c) { float r; asm("v_max3_f32 %0, %1, %2, %3" : "=v"(r) : "v"(a), "v"(b), "v"(c)); return r; }
DEVI f32x4 mfma32(v4u a, v4u b, f32x4 c) { return __builtin_amdgcn_mfma_f32_16x16x32_bf16(__builtin_bit_cast(bf16x8, a), __builtin_bit_cast(bf16x8, b), c, 0, 0, 0); }
DEVI f32x4 mfma16(v2u a, v2u b, f32x4 c) { return __builtin_amdgcn_mfma_f32_16x16x16bf16_1k(__builtin_bit_cast(s16x4, a), __builtin_bit_cast(s16x4, b), c, 0, 0, 0); }
template <class T> DEVI T lrd(unsigned off) { return *(LAS T*)(size_t)off; }
template <class T> DEVI void lwr(unsigned off, T v) { *(LAS T*)(size_t)off = v; }
DEVI v2u trd(unsigned off) { return __builtin_bit_cast(v2u, __builtin_amdgcn_ds_read_tr16_b64_v4i16((LAS s16x4*)(size_t)off)); }
DEVI v4u trd2(unsigned o0, unsigned o1) { v2u a = trd(o0), b = trd(o1); return v4u{a.x, a.y, b.x, b.y}; }
DEVI float sum16(float x) { auto r = __builtin_amdgcn_permlane16_swap(asu(x), asu(x), false, false); return asf(r[0]) + asf(r[1]); }
DEVI float sum32(float x) { auto r = __builtin_amdgcn_permlane32_swap(asu(x), asu(x), false, false); return asf(r[0]) + asf(r[1]); }
DEVI float max16(float x) { auto r = __builtin_amdgcn_permlane16_swap(asu(x), asu(x), false, false); return mx(asf(r[0]), asf(r[1])); }
DEVI float max32(float x) { auto r = __builtin_amdgcn_permlane32_swap(asu(x), asu(x), false, false); return mx(asf(r[0]), asf(r[1])); }
DEVI float sum16x2(float x, float y) { auto r = __builtin_amdgcn_permlane16_swap(asu(x), asu(y), false, false); return asf(r[0]) + asf(r[1]); }
DEVI float sum32x2(float x, float y) { auto r = __builtin_amdgcn_permlane32_swap(asu(x), asu(y), false, false); return asf(r[0]) + asf(r[1]); }
template <int CTRL> DEVI float dpp(float x) { return asf((unsigned)__builtin_amdgcn_update_dpp(0, (int)asu(x), CTRL, 0xf, 0xf, true)); }
DEVI float wavesum(float x) {
    x += dpp<0xB1>(x);
    x += dpp<0x4E>(x);
    x += dpp<0x141>(x);
    x += dpp<0x140>(x);
    return sum32(sum16(x));
}
#define STR_(x) #x
#define STR(x) STR_(x)
#define WAITVM(n) asm volatile("s_waitcnt vmcnt(" STR(n) ")" ::: "memory")
#define PIN(x) asm volatile("" : "+v"(x))
#define FENCE() __builtin_amdgcn_sched_barrier(0)
DEVI void bar() { asm volatile("s_waitcnt lgkmcnt(0)\n\ts_barrier" ::: "memory"); }
DEVI void bar0() { asm volatile("s_barrier" ::: "memory"); }
DEVI void glds_s(unsigned voff, const void* sbase, unsigned lds) {
    unsigned keep;
    asm volatile("s_mov_b32 %0, m0\n\ts_mov_b32 m0, %3\n\ts_nop 0\n\tglobal_load_lds_dwordx4 %1, %2" WNT "\n\ts_mov_b32 m0, %0" : "=&s"(keep) : "v"(voff), "s"(sbase), "s"(lds) : "memory");
}
typedef __amdgpu_buffer_rsrc_t rsrc_t;
DEVI v4u ldx(rsrc_t rs, unsigned off) { return __builtin_amdgcn_raw_buffer_load_b128(rs, off, 0, 16); }
DEVI void stx(rsrc_t rs, unsigned off, v4u v) { __builtin_amdgcn_raw_buffer_store_b128(v, rs, off, 0, 16); }
DEVI bool none(bool bad) { asm volatile("" ::: "memory"); return __builtin_amdgcn_ballot_w64(bad) == 0ull; }
DEVI unsigned tagx(v4u a, unsigned tag) { return (a[1] ^ tag) | (a[3] ^ tag); }
constexpr unsigned OOB = 0x80000000u;
DEVI void hint_put(rsrc_t rs, unsigned off, unsigned tag) { __builtin_amdgcn_raw_buffer_store_b32(tag, rs, off, 0, 16); }
DEVI void hint_wait(rsrc_t rs, unsigned off, unsigned tag) {
#pragma nounroll
    for (int it = SPIN_CAP; it > 0; --it) { const unsigned hv = __builtin_amdgcn_raw_buffer_load_b32(rs, off, 0, 16); if (none(hv != tag)) break; if (HSL) __builtin_amdgcn_s_sleep(HSL); }
}

#define NST 16
#define STAMP(i) do { FENCE(); asm volatile("s_memrealtime %0\n\ts_waitcnt lgkmcnt(0)" : "=s"(st[i]) :: "memory"); FENCE(); } while (0)

DEVI void frags128(unsigned a0, unsigned a1, v4u (&f)[4]) {
#pragma unroll
    for (int s = 0; s < 4; ++s) f[s] = trd2(a0 + s * 8192, a1 + s * 8192);
}
DEVI f32x4 gemv4(const v4u (&a)[4], const v4u (&b)[4]) {
    f32x4 c = {0.f, 0.f, 0.f, 0.f};
#pragma unroll
    for (int s = 0; s < 4; ++s) c = mfma32(a[s], b[s], c);
    return c;
}

__global__ void __launch_bounds__(512, 2)
mla_glu_decode(const u16* __restrict__ x, const u16* __restrict__ ckv, const u16* __restrict__ wq, const u16* __restrict__ wuk,
               const u16* __restrict__ wuv, const u16* __restrict__ wo, const u16* __restrict__ wg, const u16* __restrict__ wu,
               const u16* __restrict__ wd, u16* __restrict__ out, unsigned char* __restrict__ ws) {
    unsigned long long st[NST];
#pragma unroll
    for (int i = 0; i < NST; ++i) st[i] = 0;
    STAMP(0);
    const int tid = threadIdx.x, l = tid & 63, w = __builtin_amdgcn_readfirstlane(tid >> 6);
    const int id = blockIdx.x, xc = id & 7, y = id >> 3, yy = y & 15;
    const int un = yy * 8 + xc;
    const unsigned g = l >> 4, l15 = l & 15;
    asm volatile("" :: "s"(x), "s"(ckv), "s"(wq), "s"(wuk), "s"(wuv), "s"(wo), "s"(wg), "s"(wu), "s"(wd), "s"(out), "s"(ws));
    const rsrc_t rs = __builtin_amdgcn_make_buffer_rsrc(ws, 0, WS_END, 0x00020000);
    const v4u Z = {0u, 0u, 0u, 0u};
    const f32x4 ZF = {0.f, 0.f, 0.f, 0.f};

    if (y < 16) {
        const int h = 4 * xc + (yy >> 2), cq = yy & 3;
        v4u xv; unsigned ep;
        if (w < 2) {
            asm volatile("global_load_dwordx4 %0, %1, %2" : "=v"(xv) : "v"(w * 1024 + l * 16), "s"(x) : "memory");
            asm volatile("global_load_dword %0, %1, %2 sc1" : "=v"(ep) : "v"(0), "s"(ws) : "memory");
        }
        const unsigned vb = (128 * cq + 16 * w + g) * 8192 + h * 256;
#pragma unroll
        for (int i = 0; i < 4; ++i) glds_s(vb + i * 32768 + ((l15 ^ ((g << 2) | i)) << 4), wq, L_I0 + (16 * w + 4 * i) * 256);
        bar0();
#pragma unroll
        for (int i = 0; i < 4; ++i) glds_s(vb + i * 32768 + ((l15 ^ (4 * i + g)) << 4), wuk, L_I1 + (16 * w + 4 * i) * 256);
        unsigned o_p1 = l < 16 ? WS_H1 + ((h * 4 + cq) * 128 + 16 * w + l) * 16 : OOB;
        unsigned o_g1 = WS_H1 + ((h * 4 + g) * 128 + 16 * w + l15) * 16;
        unsigned o_h2 = l15 < 2 ? WS_H2 + ((l15 * 32 + h) * 128 + 32 * cq + 4 * w + g) * 16 : OOB;
        unsigned o_t2 = tid < HREP ? WS_T2 + (tid * 128 + h * 4 + cq) * 128 : OOB;
        if (PREW) stx(rs, o_p1, Z);
        STAMP(1);
        if (w < 2) {
            if (PREW) asm volatile("s_waitcnt vmcnt(9)" : "+v"(xv), "+v"(ep) :: "memory");
            else asm volatile("s_waitcnt vmcnt(8)" : "+v"(xv), "+v"(ep) :: "memory");
            float v[8];
#pragma unroll
            for (int j = 0; j < 4; ++j) { v[2 * j] = lo16(xv[j]); v[2 * j + 1] = hi16(xv[j]); }
            float s = ((v[0] + v[1]) + (v[2] + v[3])) + ((v[4] + v[5]) + (v[6] + v[7]));
            const float mean = wavesum(s) * (1.f / 512.f);
            float sq = 0.f;
#pragma unroll
            for (int j = 0; j < 8; ++j) { const float d = v[j] - mean; sq += d * d; }
            const float var = wavesum(sq) * (1.f / 512.f);
            const float mb = rbf(mean), ri = __builtin_amdgcn_rcpf(rbf(__builtin_amdgcn_sqrtf(rbf(rbf(var) + 1e-5f))));
            v4u o;
#pragma unroll
            for (int j = 0; j < 4; ++j) o[j] = pk(rbf(v[2 * j] - mb) * ri, rbf(v[2 * j + 1] - mb) * ri);
            lwr<v4u>(L_XN + w * 1024 + l * 16, o);
            unsigned t = ep + 1u; t += (t & 0xffffu) == 0u;
            lwr<unsigned>(L_TAG, t);
        }
        bar();
        const unsigned tag = lrd<unsigned>(L_TAG);
        unsigned a0, a1;
        {
            const unsigned q = l15 >> 2, p = l & 3, ch = 2 * w + (p >> 1);
            a0 = L_I0 + (8 * g + q) * 256 + 16 * (ch ^ ((q << 2) | ((2 * g) & 3))) + 8 * (p & 1);
            a1 = L_I0 + (8 * g + q + 4) * 256 + 16 * (ch ^ ((q << 2) | ((2 * g + 1) & 3))) + 8 * (p & 1);
        }
        unsigned a_qw = L_QS + (g & 1) * 272 + (16 * w + l15) * 2;
        unsigned a_qr = L_QS + (l & 1) * 272 + 16 * g;
        unsigned a_uk[4];
        v4u xf[4];
#pragma unroll
        for (int s = 0; s < 4; ++s) {
            xf[s] = lrd<v4u>(L_XN + (l & 1) * 1024 + (128 * cq + 32 * s + 8 * g) * 2);
            a_uk[s] = L_I1 + (16 * w + l15) * 256 + (((4 * s + g) ^ l15) << 4);
            PIN(xf[s]); PIN(a_uk[s]);
        }
        v4u pb = {0u, tag, 0u, tag};
        PIN(a0); PIN(a1); PIN(o_p1); PIN(o_g1); PIN(o_h2); PIN(o_t2); PIN(a_qw); PIN(a_qr); PIN(pb);
        if (PREW) WAITVM(5); else WAITVM(4);
        bar0();
        STAMP(2);
        {
            v4u f[4]; frags128(a0, a1, f);
            FENCE();
            const f32x4 acc = gemv4(xf, f);
            pb[0] = asu(acc[0]); pb[2] = asu(acc[1]);
            stx(rs, o_p1, pb);
        }
        STAMP(3);
        if (STOPAT <= 3) { WAITVM(0); STAMP(14); goto done; }
        if (D1) __builtin_amdgcn_s_sleep(D1);
        v4u fu[4];
        {
            v4u a = ldx(rs, o_g1);
            WAITVM(0);
            STAMP(14);
#pragma unroll
            for (int s = 0; s < 4; ++s) fu[s] = lrd<v4u>(a_uk[s]);
#pragma nounroll
            for (int it = SPIN_CAP; it > 0 && !none(tagx(a, tag) != 0u); --it) a = ldx(rs, o_g1);
            hint_put(rs, o_t2, tag);
            lwr<u16>(a_qw, (u16)pk(sum32(sum16x2(asf(a[0]), asf(a[2]))), 0.f));
        }
        STAMP(4);
        bar();
        {
            v4u qf[4];
#pragma unroll
            for (int s = 0; s < 4; ++s) qf[s] = lrd<v4u>(a_qr + 64 * s);
            FENCE();
            const f32x4 acc = gemv4(fu, qf);
            constexpr float SC = 0.08838834764831845f * 1.4426950408889634f;
            pb[0] = pk(acc[0] * SC, acc[1] * SC); pb[2] = pk(acc[2] * SC, acc[3] * SC);
            stx(rs, o_h2, pb);
        }
        STAMP(5);
        if (STOPAT <= 5) goto done;
        const int hp = h >> 1, e = yy & 7, mat = un >> 6, mp = (un >> 2) & 15, fp = un & 3;
#pragma unroll
        for (int i = 0; i < 3; ++i) {
            const unsigned pc = 8 * i + w, idx = min(64 * pc + l, 1375u), row = idx / 43u, ch = idx - row * 43u;
            if (pc < 22) glds_s((32 * mp + row) * 2752 + fp * 688 + ch * 16, mat ? wu : wg, L_I0 + pc * 1024);
        }
#pragma unroll
        for (int i = 0; i < 4; ++i)
            glds_s((64 * e + 16 * (w & 3) + 4 * i + g) * 8192 + (2 * hp + (w >> 2)) * 256 + ((l15 ^ ((g << 2) | i)) << 4), wuv, L_I1 + (16 * w + 4 * i) * 256);
#pragma unroll
        for (int i = 0; i < 4; ++i) glds_s((h * 128 + 16 * w + 4 * i + g) * 1024 + cq * 256 + ((l15 ^ ((g << 2) | i)) << 4), wo, L_I2 + (16 * w + 4 * i) * 256);
        unsigned o_h5 = l < 16 ? WS_H5 + (h * 512 + 128 * cq + 16 * w + l) * 16 : OOB;
        unsigned o_h4 = l < 32 ? WS_H4 + (((2 * hp + (w >> 2)) * 8 + e) * 128 + 32 * (w & 3) + l) * 16 : OOB;
        {
            const v4u xg = lrd<v4u>(L_XN + (l & 1) * 1024 + (32 * mp + 8 * g) * 2);
            WAITVM(8);
            bar();
            const unsigned ag = L_I0 + (8 * g + (l15 >> 2)) * 688 + (l & 3) * 8;
            f32x4 o[3];
#pragma unroll
            for (int t = 0; t < 3; ++t) {
                const unsigned tl = min(8 * t + w, 21);
                o[t] = mfma32(xg, trd2(ag + tl * 32, ag + tl * 32 + 4 * 688), ZF);
            }
            const float o0 = g == 0 ? o[0][0] : g == 1 ? o[1][0] : o[2][0], o1 = g == 0 ? o[0][1] : g == 1 ? o[1][1] : o[2][1];
            const unsigned f = 16 * (8 * g + w) + l15;
            stx(rs, (g < 3 && f < 344) ? WS_HG + ((mat * 16 + mp) * 1376 + 344 * fp + f) * 16 : OOB, v4u{asu(o0), tag, asu(o1), tag});
            if (PREW) stx(rs, o_h4, Z);
        }
        STAMP(6);
        WAITVM(0);
        bar();
        v4u fv[2][2], fw[4];
        {
            const unsigned q = l15 >> 2, p = l & 3;
#pragma unroll
            for (int t = 0; t < 2; ++t) {
                const unsigned ch = 4 * (w & 3) + 2 * t + (p >> 1), base = L_I1 + (w >> 2) * 16384 + (8 * g + q) * 256 + 8 * (p & 1);
#pragma unroll
                for (int s = 0; s < 2; ++s)
                    fv[t][s] = trd2(base + s * 8192 + 16 * (ch ^ ((q << 2) | ((2 * g) & 3))), base + s * 8192 + 1024 + 16 * (ch ^ ((q << 2) | ((2 * g + 1) & 3))));
            }
        }
        frags128(a0 + (L_I2 - L_I0), a1 + (L_I2 - L_I0), fw);
        const unsigned u = l15 < 12 ? l15 : l15 - 4;
        unsigned o_h3 = WS_H3 + ((((w >> 2) * 16 + hp) * 8 + e) * 32 + 8 * (w & 3) + (g >> 1)) * 384 + (u >> 2) * 128 + (g & 1) * 64 + (u & 3) * 16;
        unsigned o_t3 = WS_T3 + (xc * 128 + (h >> 4) * 64 + 8 * w + (l & 7)) * 128;
        unsigned o_t5 = tid < HREP ? WS_T5 + (tid * 128 + cq * 32 + h) * 128 : OOB;
        unsigned o_g4 = WS_H4 + ((h * 8 + g) * 128 + 16 * w + l15) * 16;
        unsigned a_ow, a_ow2, a_ms;
        {
            const unsigned n = 6 * (l15 >> 2) + 2 * (g >> 1), n2 = 6 * (l15 >> 2) + 4, gg = l15 & 3;
            a_ow = l15 < 12 ? L_OW + w * 272 + (64 * (g & 1) + 16 * gg + 4 * ((n >> 2) ^ (gg & 1)) + (n & 3)) * 2 : L_JUNK + l * 4;
            a_ow2 = (l15 < 8 && l < 32) ? L_OW + w * 272 + (64 * (g & 1) + 16 * gg + 4 * ((n2 >> 2) ^ (gg & 1)) + (n2 & 3)) * 2 : L_JUNK + l * 4;
            a_ms = L_MW + (g >> 1) * 64 + (g & 1) * 32 + w * 4;
        }
        unsigned a_or = L_OW + (l & 7) * 272 + (w >> 2) * 128 + 16 * g, a_mw = L_MW + (w >> 2) * 32 + (g & 1) * 16;
        unsigned TT = tag << 16;
        PIN(o_h3); PIN(o_t3); PIN(o_t5); PIN(o_g4); PIN(o_h4); PIN(a_ow); PIN(a_ow2); PIN(a_or); PIN(a_mw); PIN(a_ms); PIN(o_h5); PIN(TT);
        PIN(fv[0][0]); PIN(fv[0][1]); PIN(fv[1][0]); PIN(fv[1][1]); PIN(fw[0]); PIN(fw[1]); PIN(fw[2]); PIN(fw[3]);
        STAMP(7);
        if (STOPAT <= 7) goto done;
        hint_wait(rs, o_t3, tag);
        if (D3) __builtin_amdgcn_s_sleep(D3);
        STAMP(15);
        {
            v4u a[4];
#pragma nounroll
            for (int it = SPIN_CAP; it > 0; --it) {
                unsigned bad = 0;
#pragma unroll
                for (int k = 0; k < 4; ++k) a[k] = ldx(rs, o_h3 + k * 768);
#pragma unroll
                for (int k = 0; k < 4; ++k) bad |= tagx(a[k], TT);
                if (none(bad > 0xffffu)) break;
            }
            STAMP(8);
            float m[4], ls[4];
#pragma unroll
            for (int k = 0; k < 4; ++k) { const float sv = asf(__builtin_amdgcn_alignbit(a[k][3], a[k][2], 16)); m[k] = dpp<0x158>(sv); ls[k] = dpp<0x159>(sv); }
            const float M = max32(mx(mx3(m[0], m[1], m[2]), m[3]));
            float acc[6], Ls;
#pragma unroll
            for (int k = 0; k < 4; ++k) {
                const float al = __builtin_amdgcn_exp2f(m[k] - M);
                const float v[7] = {lo16(a[k][0]), hi16(a[k][0]), lo16(a[k][1]), lo16(a[k][2]), hi16(a[k][2]), lo16(a[k][3]), ls[k]};
                if (k == 0) { for (int j = 0; j < 6; ++j) acc[j] = al * v[j]; Ls = al * v[6]; }
                else { for (int j = 0; j < 6; ++j) acc[j] = __builtin_fmaf(al, v[j], acc[j]); Ls = __builtin_fmaf(al, v[6], Ls); }
            }
            const float v4 = sum32x2(acc[4], Ls);
            lwr<unsigned>(a_ow, pk(sum32x2(acc[0], acc[2]), sum32x2(acc[1], acc[3])));
            lwr<unsigned>(a_ow2, pk(v4, sum32(acc[5])));
            lwr<float>(a_ms, l < 32 ? M : v4);
        }
        bar();
        {
            const v4u of0 = lrd<v4u>(a_or), of1 = lrd<v4u>(a_or + 64);
            const f32x4 mw = lrd<f32x4>(a_mw), lw = lrd<f32x4>(a_mw + 64);
            FENCE();
            const f32x4 c0 = mfma32(of1, fv[0][1], mfma32(of0, fv[0][0], ZF)), c1 = mfma32(of1, fv[1][1], mfma32(of0, fv[1][0], ZF));
            const float M = mx(mx3(mw[0], mw[1], mw[2]), mw[3]);
            float be[4];
#pragma unroll
            for (int r = 0; r < 4; ++r) be[r] = __builtin_amdgcn_exp2f(mw[r] - M);
            const float rd = __builtin_amdgcn_rcpf((be[0] * lw[0] + be[1] * lw[1]) + (be[2] * lw[2] + be[3] * lw[3]));
            const float v0 = ((be[0] * c0[0] + be[1] * c0[1]) + (be[2] * c0[2] + be[3] * c0[3])) * rd;
            const float v1 = ((be[0] * c1[0] + be[1] * c1[1]) + (be[2] * c1[2] + be[3] * c1[3])) * rd;
            auto r = __builtin_amdgcn_permlane16_swap(asu(v0), asu(v1), false, false);
            pb[0] = r[0]; pb[2] = r[1];
            stx(rs, o_h4, pb);
        }
        STAMP(9);
        if (D4) __builtin_amdgcn_s_sleep(D4);
        {
            v4u a, b;
#pragma nounroll
            for (int it = SPIN_CAP; it > 0; --it) { a = ldx(rs, o_g4); b = ldx(rs, o_g4 + 4 * 2048); if (none((tagx(a, tag) | tagx(b, tag)) != 0u)) break; }
            hint_put(rs, o_t5, tag);
            lwr<u16>(a_qw, (u16)pk(sum32(sum16x2(asf(a[0]) + asf(b[0]), asf(a[2]) + asf(b[2]))), 0.f));
        }
        STAMP(10);
        bar();
        {
            v4u cf[4];
#pragma unroll
            for (int s = 0; s < 4; ++s) cf[s] = lrd<v4u>(a_qr + 64 * s);
            FENCE();
            const f32x4 acc = gemv4(cf, fw);
            pb[0] = asu(acc[0]); pb[2] = asu(acc[1]);
            stx(rs, o_h5, pb);
        }
        STAMP(11);
    } else {
        const int pr = xc * 8 + (yy >> 1), hg = yy & 1, b = pr >> 5, sp = pr & 31;
        unsigned ep;
        if (w == 0) asm volatile("global_load_dword %0, %1, %2 sc1" : "=v"(ep) : "v"(0), "s"(ws) : "memory");
        if (BDELAY) __builtin_amdgcn_s_sleep(BDELAY);
#pragma unroll
        for (int i = 0; i < 16; ++i) {
            if (i == 8) bar0();
            glds_s((pr * 128 + 64 * ((i >> 3) ^ hg) + 8 * w + (i & 7)) * 1024 + ((l ^ i) << 4), ckv, L_I0 + (16 * w + i) * 1024);
        }
        STAMP(1);
        if (w == 0) {
            asm volatile("s_waitcnt vmcnt(16)" : "+v"(ep) :: "memory");
            unsigned t = ep + 1u; t += (t & 0xffffu) == 0u;
            lwr<unsigned>(L_TAG, t);
        }
        unsigned qoff = WS_H2 + ((b * 32 + 16 * hg + 2 * w) * 128 + l) * 16;
        unsigned o_t2 = WS_T2 + (xc * 128 + (16 * hg + 2 * w) * 4 + (l & 7)) * 128;
        unsigned o_t3 = tid < HREP ? WS_T3 + (tid * 128 + hg * 64 + pr) * 128 : OOB;
        unsigned o_h3 = WS_H3 + (((b * 16 + 8 * hg + (l15 >> 1)) * 8 + w) * 32 + sp) * 384 + (l & 1) * 64 + g * 16;
        unsigned a_r[4], a_t[4];
#pragma unroll
        for (int j = 0; j < 4; ++j) { a_r[j] = l15 * 1024 + (((4 * j + g) ^ l15) << 4); PIN(a_r[j]); }
        {
            const unsigned q = l15 >> 2, p = l & 3;
#pragma unroll
            for (int j = 0; j < 4; ++j) {
                const unsigned r0 = 8 * g + 4 * (j & 1) + q, ch = 8 * w + 2 * p + (j >> 1);
                a_t[j] = L_I0 + r0 * 1024 + ((ch ^ (r0 & 15)) << 4) + (p & 1) * 8;
                PIN(a_t[j]);
            }
        }
        unsigned a_q0 = L_Q + 2 * w * 1024 + (((l >> 1) ^ (2 * w)) << 4) + (l & 1) * 8, a_q1 = L_Q + (2 * w + 1) * 1024 + (((l >> 1) ^ (2 * w + 1)) << 4) + (l & 1) * 8;
        unsigned a_mxw = L_MX + l15 * 32 + w * 4, a_mxr = L_MX + l15 * 32;
        unsigned a_pw = L_P + l15 * 256 + (((2 * w + (g >> 1)) ^ l15) << 4) + (g & 1) * 8;
        unsigned a_pr[4];
#pragma unroll
        for (int j = 0; j < 4; ++j) { a_pr[j] = L_P + l15 * 256 + (((4 * j + g) ^ l15) << 4); PIN(a_pr[j]); }
        PIN(qoff); PIN(o_t2); PIN(o_t3); PIN(o_h3); PIN(a_q0); PIN(a_q1); PIN(a_mxw); PIN(a_mxr); PIN(a_pw);
        WAITVM(0);
        STAMP(14);
        v4u cr[16], ct[4][4];
#pragma unroll
        for (int s = 0; s < 16; ++s) cr[s] = lrd<v4u>(a_r[s & 3] + (L_I0 + w * 16384) + (s >> 2) * 256);
        bar();
        STAMP(2);
        if (STOPAT <= 5) goto done;
        const unsigned tag = lrd<unsigned>(L_TAG);
#pragma unroll
        for (int t = 0; t < 4; ++t)
#pragma unroll
            for (int s = 0; s < 4; ++s) {
                const unsigned x8 = (t & 1) * 8;
                ct[t][s] = trd2((a_t[2 * (t >> 1)] ^ x8) + s * 32768, (a_t[2 * (t >> 1) + 1] ^ x8) + s * 32768);
            }
        FENCE();
        hint_wait(rs, o_t2, tag);
        if (D2) __builtin_amdgcn_s_sleep(D2);
        STAMP(15);
        {
            v4u qa[4];
#pragma nounroll
            for (int it = SPIN_CAP; it > 0; --it) {
#pragma unroll
                for (int k = 0; k < 4; ++k) qa[k] = ldx(rs, qoff + k * 1024);
                if (none(((tagx(qa[0], tag) | tagx(qa[1], tag)) | (tagx(qa[2], tag) | tagx(qa[3], tag))) != 0u)) break;
            }
            STAMP(3);
            lwr<v2u>(a_q0, v2u{qa[0][0], qa[0][2]}); lwr<v2u>(a_q0 + 512, v2u{qa[1][0], qa[1][2]});
            lwr<v2u>(a_q1, v2u{qa[2][0], qa[2][2]}); lwr<v2u>(a_q1 + 512, v2u{qa[3][0], qa[3][2]});
        }
        bar();
        STAMP(4);
        f32x4 sa = ZF, sb = ZF;
        {
            v4u qf[16];
#pragma unroll
            for (int s = 0; s < 16; ++s) qf[s] = lrd<v4u>(a_r[s & 3] + L_Q + (s >> 2) * 256);
            FENCE();
#pragma unroll
            for (int s = 0; s < 16; s += 2) { sa = mfma32(cr[s], qf[s], sa); sb = mfma32(cr[s + 1], qf[s + 1], sb); }
        }
        sa += sb;
        lwr<float>(a_mxw, max32(max16(mx(mx3(sa[0], sa[1], sa[2]), sa[3]))));
        bar();
        STAMP(12);
        float mrow;
        {
            const f32x4 m0 = lrd<f32x4>(a_mxr), m1 = lrd<f32x4>(a_mxr + 16);
            mrow = mx(mx3(mx3(m0[0], m0[1], m0[2]), m0[3], m1[0]), mx3(m1[1], m1[2], m1[3]));
            float p[4];
#pragma unroll
            for (int r = 0; r < 4; ++r) p[r] = __builtin_amdgcn_exp2f(sa[r] - mrow);
            lwr<v2u>(a_pw, v2u{pk(p[0], p[1]), pk(p[2], p[3])});
            lwr<float>(a_mxw + (L_LS - L_MX), sum32(sum16((p[0] + p[1]) + (p[2] + p[3]))));
        }
        bar();
        STAMP(5);
        {
            const unsigned T16 = tag & 0xffffu;
            v4u pf[4];
#pragma unroll
            for (int s = 0; s < 4; ++s) pf[s] = lrd<v4u>(a_pr[s]);
            const f32x4 l0 = lrd<f32x4>(a_mxr + (L_LS - L_MX)), l1 = lrd<f32x4>(a_mxr + (L_LS - L_MX) + 16);
            FENCE();
            f32x4 o[4];
#pragma unroll
            for (int t = 0; t < 4; ++t) {
                o[t] = ZF;
#pragma unroll
                for (int s = 0; s < 4; ++s) o[t] = mfma32(ct[t][s], pf[s], o[t]);
            }
            const float lsum = ((l0[0] + l0[1]) + (l0[2] + l0[3])) + ((l1[0] + l1[1]) + (l1[2] + l1[3]));
            unsigned d[9];
#pragma unroll
            for (int t = 0; t < 4; ++t) { d[2 * t] = pk(o[t][0], o[t][1]); d[2 * t + 1] = pk(o[t][2], o[t][3]); }
            d[8] = asu((g & 1) ? lsum : mrow);
            hint_put(rs, o_t3, tag);
#pragma unroll
            for (int i = 0; i < 3; ++i) {
                const unsigned A = d[3 * i], B = d[3 * i + 1], C = d[3 * i + 2];
                stx(rs, o_h3 + i * 128, v4u{A, __builtin_amdgcn_perm(T16, B, 0x05040100u), __builtin_amdgcn_alignbit(C, B, 16), __builtin_amdgcn_perm(T16, C, 0x05040302u)});
            }
        }
        STAMP(6);
        if (STOPAT <= 7) goto done;
        if (un < 86) {
#pragma unroll
            for (int i = 0; i < 2; ++i) glds_s((16 * un + 2 * w + i) * 1024 + ((l ^ (((2 * w + i) & 7) << 1)) << 4), wd, L_I0 + (2 * w + i) * 1024);
            {
                const unsigned off = WS_HG + (((w >> 2) * 16 + 4 * (w & 3) + g) * 1376 + 16 * un + l15) * 16;
                v4u ga;
#pragma nounroll
                for (int it = SPIN_CAP; it > 0; --it) { ga = ldx(rs, off); if (none(tagx(ga, tag) != 0u)) break; __builtin_amdgcn_s_sleep(4); }
                lwr<float>(L_RED + (l & 31) * 32 + w * 4, sum32(sum16x2(asf(ga[0]), asf(ga[2]))));
            }
            bar();
            if (w == 0) {
                const f32x4 gp = lrd<f32x4>(L_RED + (l & 31) * 32), up = lrd<f32x4>(L_RED + (l & 31) * 32 + 16);
                const float gg = rbf((gp[0] + gp[1]) + (gp[2] + gp[3])), uu = rbf((up[0] + up[1]) + (up[2] + up[3]));
                const float sg = rbf(__builtin_amdgcn_rcpf(1.f + __builtin_amdgcn_exp2f(gg * -1.4426950408889634f)));
                lwr<u16>(L_HH + (l & 31) * 2, (u16)pk(sg * uu, 0.f));
            }
            bar();
            const v2u ha = lrd<v2u>(L_HH + (l & 1) * 32 + g * 8);
            const unsigned q = l15 >> 2, p = l & 3, row = 4 * g + q;
            f32x4 o[4];
#pragma unroll
            for (int t = 0; t < 4; ++t) {
                const unsigned ch = 8 * w + 2 * t + (p >> 1);
                o[t] = mfma16(ha, trd(L_I0 + row * 1024 + ((ch ^ ((row & 7) << 1)) << 4) + (p & 1) * 8), ZF);
            }
            const float o0 = g == 0 ? o[0][0] : g == 1 ? o[1][0] : g == 2 ? o[2][0] : o[3][0];
            const float o1 = g == 0 ? o[0][1] : g == 1 ? o[1][1] : g == 2 ? o[2][1] : o[3][1];
            stx(rs, WS_H5 + ((32 + un) * 512 + 64 * w + l) * 16, v4u{asu(o0), tag, asu(o1), tag});
        }
        STAMP(7);
        if (hg == 0) {
            const int r = pr;
            unsigned o0 = WS_H5 + ((tid >> 3) * 512 + 8 * r + (tid & 7)) * 16;
            const bool two = tid < 432;
            const unsigned o1 = two ? o0 + 64 * 512 * 16 : o0 + 32 * 512 * 16;
            float xin = lo16(x[((tid >> 3) & 1) * 512 + 8 * r + (tid & 7)]);
            v4u a0, a1;
            __builtin_amdgcn_s_sleep(8);
#pragma nounroll
            for (int it = SPIN_CAP; it > 0; --it) { a1 = ldx(rs, o1); if (none(tagx(a1, tag) != 0u)) break; __builtin_amdgcn_s_sleep(4); }
            float s0 = two ? asf(a1[0]) : 0.f, s1 = two ? asf(a1[2]) : 0.f;
            unsigned a_rw = l15 < 8 ? L_RED + (((g & 1) * 8 + l15) * 2 + (g >> 1)) * 32 + w * 4 : L_JUNK + l * 4;
            unsigned a_rr = L_RED + (tid & 15) * 64;
            unsigned o_out = (((tid >> 3) & 1) * 512 + 8 * r + (tid & 7)) * 2;
            PIN(s0); PIN(s1); PIN(xin); PIN(a_rw); PIN(a_rr); PIN(o0); PIN(o_out);
            if (w < 4) { hint_wait(rs, WS_T5 + (xc * 128 + (r >> 4) * 32 + 8 * w + (l & 7)) * 128, tag); if (D5) __builtin_amdgcn_s_sleep(D5); }
            STAMP(10);
#pragma nounroll
            for (int it = SPIN_CAP; it > 0; --it) { a0 = ldx(rs, o0); if (none(tagx(a0, tag) != 0u)) break; }
            STAMP(8);
            float v0 = asf(a0[0]), v1 = asf(a0[2]);
            if (w >= 4) { s0 += v0; s1 += v1; v0 = 0.f; v1 = 0.f; }
            float p0 = sum16x2(v0, v1), p1 = sum16x2(s0, s1);
            float t = sum32x2(p0, p1);
            t += dpp<0x128>(t);
            lwr<float>(a_rw, t);
            bar();
            if (tid < 16) {
                const f32x4 t0 = lrd<f32x4>(a_rr), t2 = lrd<f32x4>(a_rr + 32), t3 = lrd<f32x4>(a_rr + 48);
                const float att = (t0[0] + t0[1]) + (t0[2] + t0[3]);
                const float glu = ((t2[0] + t2[1]) + (t2[2] + t2[3])) + ((t3[0] + t3[1]) + (t3[2] + t3[3]));
                *(GAS u16*)((GAS unsigned char*)out + o_out) = (u16)pk(rbf(xin + rbf(att)) + rbf(glu), 0.f);
            }
            if (r == 0 && tid == 0) __hip_atomic_store((GAS unsigned*)ws, tag, __ATOMIC_RELAXED, __HIP_MEMORY_SCOPE_AGENT);
            STAMP(9);
        }
    }
done:
    STAMP(13);
    if (l == 0) {
        GAS unsigned long long* so = (GAS unsigned long long*)(ws + WS_STAMP) + ((size_t)id * 8 + w) * NST;
#pragma unroll
        for (int i = 0; i < NST; ++i) so[i] = st[i];
    }
}

__global__ void __launch_bounds__(256) k_report(const unsigned long long* stp, int cls, int k, int mode, int scale) {
    __shared__ unsigned long long r0[256], r1[256], r2[256]; __shared__ unsigned cn[256];
    const int id = threadIdx.x; unsigned long long mn = ~0ull, mx = 0, sm = 0; unsigned c = 0;
    for (int w = 0; w < 8; ++w) { const unsigned long long a = stp[((size_t)id * 8 + w) * NST], v = stp[((size_t)id * 8 + w) * NST + k]; mn = a < mn ? a : mn;
        if (v && (cls == 2 || (cls == 0) == ((id >> 3) < 16))) { mx = v > mx ? v : mx; sm += v & 0xffffffffull; ++c; } }
    r0[id] = mn; r1[id] = mx; r2[id] = sm; cn[id] = c; __syncthreads();
    if (id == 0) { for (int i = 1; i < 256; ++i) { mn = r0[i] < mn ? r0[i] : mn; mx = r1[i] > mx ? r1[i] : mx; sm += r2[i]; c += cn[i]; }
        unsigned long long q = mode == 0 ? mx - mn : (sm / c - (mn & 0xffffffffull)); if (q > 20000) q = 20000;
        const unsigned long long t = __builtin_amdgcn_s_memrealtime() + q * scale; while (__builtin_amdgcn_s_memrealtime() < t) __builtin_amdgcn_s_sleep(1); }
}
extern "C" void kernel_launch(void* const* d_in, const int* in_sizes, int n_in, void* d_out, int out_size, void* d_ws, size_t ws_size,
                              hipStream_t stream) {
    (void)in_sizes; (void)n_in; (void)out_size; (void)ws_size;
    (void)hipFuncSetAttribute((const void*)mla_glu_decode, hipFuncAttributeMaxDynamicSharedMemorySize, L_END);
    mla_glu_decode<<<256, 512, L_END, stream>>>((const u16*)d_in[0], (const u16*)d_in[1], (const u16*)d_in[2], (const u16*)d_in[3], (const u16*)d_in[4],
                                               (const u16*)d_in[5], (const u16*)d_in[7], (const u16*)d_in[8], (const u16*)d_in[9], (u16*)d_out,
                                               (unsigned char*)d_ws);
    k_report<<<1, 256, 0, stream>>>((const unsigned long long*)((char*)d_ws + WS_STAMP), 2, 13, 0, 0);
}
```

```cpp
#include <hip/hip_runtime.h>
#include <hip/hip_bf16.h>

typedef unsigned short u16;
typedef unsigned v4u __attribute__((ext_vector_type(4)));
typedef unsigned v2u __attribute__((ext_vector_type(2)));
typedef float f32x4 __attribute__((ext_vector_type(4)));
typedef short s16x4 __attribute__((ext_vector_type(4)));
typedef __bf16 bf16x8 __attribute__((ext_vector_type(8)));
typedef __bf16 bf16x2 __attribute__((ext_vector_type(2)));
#define LAS __attribute__((address_space(3)))
#define GAS __attribute__((address_space(1)))
#define DEVI __device__ __forceinline__

#define WNT ""
#define BDELAY 32
#define PREW 1
#define D1 3
#define D4 3
#define D2 0
#define D3 4
#define D5 0
#define HSL 1
#define STOPAT 99

constexpr int HREP = 8;
constexpr int WS_T2 = 4096;
constexpr int WS_T3 = WS_T2 + HREP * 128 * 128;
constexpr int WS_T5 = WS_T3 + HREP * 128 * 128;
constexpr int WS_H1 = WS_T5 + HREP * 128 * 128;
constexpr int WS_H4 = WS_H1 + 32 * 4 * 128 * 16;
constexpr int WS_H2 = WS_H4 + 32 * 8 * 128 * 16;
constexpr int WS_HG = WS_H2 + 2 * 32 * 128 * 16;
constexpr int WS_H5 = WS_HG + 2 * 16 * 1376 * 16;
constexpr int WS_H3 = WS_H5 + 118 * 512 * 16;
constexpr int WS_END = WS_H3 + 2 * 16 * 8 * 32 * 384;
constexpr int WS_STAMP = 16 << 20;
constexpr int L_Q = 0;
constexpr int L_XN = 0;
constexpr int L_P = 16384;
constexpr int L_QS = L_P + 4096;
constexpr int L_MX = L_QS + 1024;
constexpr int L_LS = L_MX + 512;
constexpr int L_OW = L_LS + 512;
constexpr int L_MW = L_OW + 2304;
constexpr int L_LW = L_MW + 64;
constexpr int L_HH = L_LW + 64;
constexpr int L_TAG = L_HH + 64;
constexpr int L_JUNK = L_TAG + 64;
constexpr int L_RED = L_JUNK + 256;
constexpr int L_I0 = 32768;
constexpr int L_I1 = 65536;
constexpr int L_I2 = 98304;
constexpr int L_END = 163840;
static_assert(L_RED + 1024 <= L_I0, "lds");
constexpr int SPIN_CAP = 1 << 16;

DEVI unsigned pk(float lo, float hi) { bf16x2 r = {(__bf16)lo, (__bf16)hi}; return __builtin_bit_cast(unsigned, r); }
DEVI float rbf(float f) { return (float)(__bf16)f; }
DEVI float lo16(unsigned w) { return __builtin_bit_cast(float, w << 16); }
DEVI float hi16(unsigned w) { return __builtin_bit_cast(float, w & 0xffff0000u); }
DEVI float asf(unsigned w) { return __builtin_bit_cast(float, w); }
DEVI unsigned asu(float w) { return __builtin_bit_cast(unsigned, w); }
DEVI float mx(float a, float b) { float r; asm("v_max_f32 %0, %1, %2" : "=v"(r) : "v"(a), "v"(b)); return r; }
DEVI float mx3(float a, float b, float c) { float r; asm("v_max3_f32 %0, %1, %2, %3" : "=v"(r) : "v"(a), "v"(b), "v"(c)); return r; }
DEVI f32x4 mfma32(v4u a, v4u b, f32x4 c) { return __builtin_amdgcn_mfma_f32_16x16x32_bf16(__builtin_bit_cast(bf16x8, a), __builtin_bit_cast(bf16x8, b), c, 0, 0, 0); }
DEVI f32x4 mfma16(v2u a, v2u b, f32x4 c) { return __builtin_amdgcn_mfma_f32_16x16x16bf16_1k(__builtin_bit_cast(s16x4, a), __builtin_bit_cast(s16x4, b), c, 0, 0, 0); }
template <class T> DEVI T lrd(unsigned off) { return *(LAS T*)(size_t)off; }
template <class T> DEVI void lwr(unsigned off, T v) { *(LAS T*)(size_t)off = v; }
DEVI v2u trd(unsigned off) { return __builtin_bit_cast(v2u, __builtin_amdgcn_ds_read_tr16_b64_v4i16((LAS s16x4*)(size_t)off)); }
DEVI v4u trd2(unsigned o0, unsigned o1) { v2u a = trd(o0), b = trd(o1); return v4u{a.x, a.y, b.x, b.y}; }
DEVI float sum16(float x) { auto r = __builtin_amdgcn_permlane16_swap(asu(x), asu(x), false, false); return asf(r[0]) + asf(r[1]); }
DEVI float sum32(float x) { auto r = __builtin_amdgcn_permlane32_swap(asu(x), asu(x), false, false); return asf(r[0]) + asf(r[1]); }
DEVI float max16(float x) { auto r = __builtin_amdgcn_permlane16_swap(asu(x), asu(x), false, false); return mx(asf(r[0]), asf(r[1])); }
DEVI float max32(float x) { auto r = __builtin_amdgcn_permlane32_swap(asu(x), asu(x), false, false); return mx(asf(r[0]), asf(r[1])); }
DEVI float sum16x2(float x, float y) { auto r = __builtin_amdgcn_permlane16_swap(asu(x), asu(y), false, false); return asf(r[0]) + asf(r[1]); }
DEVI float sum32x2(float x, float y) { auto r = __builtin_amdgcn_permlane32_swap(asu(x), asu(y), false, false); return asf(r[0]) + asf(r[1]); }
template <int CTRL> DEVI float dpp(float x) { return asf((unsigned)__builtin_amdgcn_update_dpp(0, (int)asu(x), CTRL, 0xf, 0xf, true)); }
DEVI float wavesum(float x) {
    x += dpp<0xB1>(x);
    x += dpp<0x4E>(x);
    x += dpp<0x141>(x);
    x += dpp<0x140>(x);
    return sum32(sum16(x));
}
#define STR_(x) #x
#define STR(x) STR_(x)
#define WAITVM(n) asm volatile("s_waitcnt vmcnt(" STR(n) ")" ::: "memory")
#define PIN(x) asm volatile("" : "+v"(x))
#define FENCE() __builtin_amdgcn_sched_barrier(0)
DEVI void bar() { asm volatile("s_waitcnt lgkmcnt(0)\n\ts_barrier" ::: "memory"); }
DEVI void bar0() { asm volatile("s_barrier" ::: "memory"); }
DEVI void glds_s(unsigned voff, const void* sbase, unsigned lds) {
    unsigned keep;
    asm volatile("s_mov_b32 %0, m0\n\ts_mov_b32 m0, %3\n\ts_nop 0\n\tglobal_load_lds_dwordx4 %1, %2" WNT "\n\ts_mov_b32 m0, %0" : "=&s"(keep) : "v"(voff), "s"(sbase), "s"(lds) : "memory");
}
typedef __amdgpu_buffer_rsrc_t rsrc_t;
DEVI v4u ldx(rsrc_t rs, unsigned off) { return __builtin_amdgcn_raw_buffer_load_b128(rs, off, 0, 16); }
DEVI void stx(rsrc_t rs, unsigned off, v4u v) { __builtin_amdgcn_raw_buffer_store_b128(v, rs, off, 0, 16); }
DEVI bool none(bool bad) { asm volatile("" ::: "memory"); return __builtin_amdgcn_ballot_w64(bad) == 0ull; }
DEVI unsigned tagx(v4u a, unsigned tag) { return (a[1] ^ tag) | (a[3] ^ tag); }
constexpr unsigned OOB = 0x80000000u;
DEVI void hint_put(rsrc_t rs, unsigned off, unsigned tag) { __builtin_amdgcn_raw_buffer_store_b32(tag, rs, off, 0, 16); }
DEVI void hint_wait(rsrc_t rs, unsigned off, unsigned tag) {
#pragma nounroll
    for (int it = SPIN_CAP; it > 0; --it) { const unsigned hv = __builtin_amdgcn_raw_buffer_load_b32(rs, off, 0, 16); if (none(hv != tag)) break; if (HSL) __builtin_amdgcn_s_sleep(HSL); }
}

#define STAMP(i)

DEVI void frags128(unsigned a0, unsigned a1, v4u (&f)[4]) {
#pragma unroll
    for (int s = 0; s < 4; ++s) f[s] = trd2(a0 + s * 8192, a1 + s * 8192);
}
DEVI f32x4 gemv4(const v4u (&a)[4], const v4u (&b)[4]) {
    f32x4 c = {0.f, 0.f, 0.f, 0.f};
#pragma unroll
    for (int s = 0; s < 4; ++s) c = mfma32(a[s], b[s], c);
    return c;
}

__global__ void __launch_bounds__(512, 2)
mla_glu_decode(const u16* __restrict__ x, const u16* __restrict__ ckv, const u16* __restrict__ wq, const u16* __restrict__ wuk,
               const u16* __restrict__ wuv, const u16* __restrict__ wo, const u16* __restrict__ wg, const u16* __restrict__ wu,
               const u16* __restrict__ wd, u16* __restrict__ out, unsigned char* __restrict__ ws) {
    STAMP(0);
    const int tid = threadIdx.x, l = tid & 63, w = __builtin_amdgcn_readfirstlane(tid >> 6);
    const int id = blockIdx.x, xc = id & 7, y = id >> 3, yy = y & 15;
    const int un = yy * 8 + xc;
    const unsigned g = l >> 4, l15 = l & 15;
    asm volatile("" :: "s"(x), "s"(ckv), "s"(wq), "s"(wuk), "s"(wuv), "s"(wo), "s"(wg), "s"(wu), "s"(wd), "s"(out), "s"(ws));
    const rsrc_t rs = __builtin_amdgcn_make_buffer_rsrc(ws, 0, WS_END, 0x00020000);
    const v4u Z = {0u, 0u, 0u, 0u};
    const f32x4 ZF = {0.f, 0.f, 0.f, 0.f};

    if (y < 16) {
        const int h = 4 * xc + (yy >> 2), cq = yy & 3;
        v4u xv; unsigned ep;
        if (w < 2) {
            asm volatile("global_load_dwordx4 %0, %1, %2" : "=v"(xv) : "v"(w * 1024 + l * 16), "s"(x) : "memory");
            asm volatile("global_load_dword %0, %1, %2 sc1" : "=v"(ep) : "v"(0), "s"(ws) : "memory");
        }
        const unsigned vb = (128 * cq + 16 * w + g) * 8192 + h * 256;
#pragma unroll
        for (int i = 0; i < 4; ++i) glds_s(vb + i * 32768 + ((l15 ^ ((g << 2) | i)) << 4), wq, L_I0 + (16 * w + 4 * i) * 256);
        bar0();
#pragma unroll
        for (int i = 0; i < 4; ++i) glds_s(vb + i * 32768 + ((l15 ^ (4 * i + g)) << 4), wuk, L_I1 + (16 * w + 4 * i) * 256);
        unsigned o_p1 = l < 16 ? WS_H1 + ((h * 4 + cq) * 128 + 16 * w + l) * 16 : OOB;
        unsigned o_g1 = WS_H1 + ((h * 4 + g) * 128 + 16 * w + l15) * 16;
        unsigned o_h2 = l15 < 2 ? WS_H2 + ((l15 * 32 + h) * 128 + 32 * cq + 4 * w + g) * 16 : OOB;
        unsigned o_t2 = tid < HREP ? WS_T2 + (tid * 128 + h * 4 + cq) * 128 : OOB;
        if (PREW) stx(rs, o_p1, Z);
        STAMP(1);
        if (w < 2) {
            if (PREW) asm volatile("s_waitcnt vmcnt(9)" : "+v"(xv), "+v"(ep) :: "memory");
            else asm volatile("s_waitcnt vmcnt(8)" : "+v"(xv), "+v"(ep) :: "memory");
            float v[8];
#pragma unroll
            for (int j = 0; j < 4; ++j) { v[2 * j] = lo16(xv[j]); v[2 * j + 1] = hi16(xv[j]); }
            float s = ((v[0] + v[1]) + (v[2] + v[3])) + ((v[4] + v[5]) + (v[6] + v[7]));
            const float mean = wavesum(s) * (1.f / 512.f);
            float sq = 0.f;
#pragma unroll
            for (int j = 0; j < 8; ++j) { const float d = v[j] - mean; sq += d * d; }
            const float var = wavesum(sq) * (1.f / 512.f);
            const float mb = rbf(mean), ri = __builtin_amdgcn_rcpf(rbf(__builtin_amdgcn_sqrtf(rbf(rbf(var) + 1e-5f))));
            v4u o;
#pragma unroll
            for (int j = 0; j < 4; ++j) o[j] = pk(rbf(v[2 * j] - mb) * ri, rbf(v[2 * j + 1] - mb) * ri);
            lwr<v4u>(L_XN + w * 1024 + l * 16, o);
            unsigned t = ep + 1u; t += (t & 0xffffu) == 0u;
            lwr<unsigned>(L_TAG, t);
        }
        bar();
        const unsigned tag = lrd<unsigned>(L_TAG);
        unsigned a0, a1;
        {
            const unsigned q = l15 >> 2, p = l & 3, ch = 2 * w + (p >> 1);
            a0 = L_I0 + (8 * g + q) * 256 + 16 * (ch ^ ((q << 2) | ((2 * g) & 3))) + 8 * (p & 1);
            a1 = L_I0 + (8 * g + q + 4) * 256 + 16 * (ch ^ ((q << 2) | ((2 * g + 1) & 3))) + 8 * (p & 1);
        }
        unsigned a_qw = L_QS + (g & 1) * 272 + (16 * w + l15) * 2;
        unsigned a_qr = L_QS + (l & 1) * 272 + 16 * g;
        unsigned a_uk[4];
        v4u xf[4];
#pragma unroll
        for (int s = 0; s < 4; ++s) {
            xf[s] = lrd<v4u>(L_XN + (l & 1) * 1024 + (128 * cq + 32 * s + 8 * g) * 2);
            a_uk[s] = L_I1 + (16 * w + l15) * 256 + (((4 * s + g) ^ l15) << 4);
            PIN(xf[s]); PIN(a_uk[s]);
        }
        v4u pb = {0u, tag, 0u, tag};
        PIN(a0); PIN(a1); PIN(o_p1); PIN(o_g1); PIN(o_h2); PIN(o_t2); PIN(a_qw); PIN(a_qr); PIN(pb);
        if (PREW) WAITVM(5); else WAITVM(4);
        bar0();
        STAMP(2);
        {
            v4u f[4]; frags128(a0, a1, f);
            FENCE();
            const f32x4 acc = gemv4(xf, f);
            pb[0] = asu(acc[0]); pb[2] = asu(acc[1]);
            stx(rs, o_p1, pb);
        }
        STAMP(3);
        if (STOPAT <= 3) { WAITVM(0); STAMP(14); goto done; }
        if (D1) __builtin_amdgcn_s_sleep(D1);
        v4u fu[4];
        {
            v4u a = ldx(rs, o_g1);
            WAITVM(0);
            STAMP(14);
#pragma unroll
            for (int s = 0; s < 4; ++s) fu[s] = lrd<v4u>(a_uk[s]);
#pragma nounroll
            for (int it = SPIN_CAP; it > 0 && !none(tagx(a, tag) != 0u); --it) a = ldx(rs, o_g1);
            hint_put(rs, o_t2, tag);
            lwr<u16>(a_qw, (u16)pk(sum32(sum16x2(asf(a[0]), asf(a[2]))), 0.f));
        }
        STAMP(4);
        bar();
        {
            v4u qf[4];
#pragma unroll
            for (int s = 0; s < 4; ++s) qf[s] = lrd<v4u>(a_qr + 64 * s);
            FENCE();
            const f32x4 acc = gemv4(fu, qf);
            constexpr float SC = 0.08838834764831845f * 1.4426950408889634f;
            pb[0] = pk(acc[0] * SC, acc[1] * SC); pb[2] = pk(acc[2] * SC, acc[3] * SC);
            stx(rs, o_h2, pb);
        }
        STAMP(5);
        if (STOPAT <= 5) goto done;
        const int hp = h >> 1, e = yy & 7, mat = un >> 6, mp = (un >> 2) & 15, fp = un & 3;
#pragma unroll
        for (int i = 0; i < 3; ++i) {
            const unsigned pc = 8 * i + w, idx = min(64 * pc + l, 1375u), row = idx / 43u, ch = idx - row * 43u;
            if (pc < 22) glds_s((32 * mp + row) * 2752 + fp * 688 + ch * 16, mat ? wu : wg, L_I0 + pc * 1024);
        }
#pragma unroll
        for (int i = 0; i < 4; ++i)
            glds_s((64 * e + 16 * (w & 3) + 4 * i + g) * 8192 + (2 * hp + (w >> 2)) * 256 + ((l15 ^ ((g << 2) | i)) << 4), wuv, L_I1 + (16 * w + 4 * i) * 256);
#pragma unroll
        for (int i = 0; i < 4; ++i) glds_s((h * 128 + 16 * w + 4 * i + g) * 1024 + cq * 256 + ((l15 ^ ((g << 2) | i)) << 4), wo, L_I2 + (16 * w + 4 * i) * 256);
        unsigned o_h5 = l < 16 ? WS_H5 + (h * 512 + 128 * cq + 16 * w + l) * 16 : OOB;
        unsigned o_h4 = l < 32 ? WS_H4 + (((2 * hp + (w >> 2)) * 8 + e) * 128 + 32 * (w & 3) + l) * 16 : OOB;
        {
            const v4u xg = lrd<v4u>(L_XN + (l & 1) * 1024 + (32 * mp + 8 * g) * 2);
            WAITVM(8);
            bar();
            const unsigned ag = L_I0 + (8 * g + (l15 >> 2)) * 688 + (l & 3) * 8;
            f32x4 o[3];
#pragma unroll
            for (int t = 0; t < 3; ++t) {
                const unsigned tl = min(8 * t + w, 21);
                o[t] = mfma32(xg, trd2(ag + tl * 32, ag + tl * 32 + 4 * 688), ZF);
            }
            const float o0 = g == 0 ? o[0][0] : g == 1 ? o[1][0] : o[2][0], o1 = g == 0 ? o[0][1] : g == 1 ? o[1][1] : o[2][1];
            const unsigned f = 16 * (8 * g + w) + l15;
            stx(rs, (g < 3 && f < 344) ? WS_HG + ((mat * 16 + mp) * 1376 + 344 * fp + f) * 16 : OOB, v4u{asu(o0), tag, asu(o1), tag});
            if (PREW) stx(rs, o_h4, Z);
        }
        STAMP(6);
        WAITVM(0);
        bar();
        v4u fv[2][2], fw[4];
        {
            const unsigned q = l15 >> 2, p = l & 3;
#pragma unroll
            for (int t = 0; t < 2; ++t) {
                const unsigned ch = 4 * (w & 3) + 2 * t + (p >> 1), base = L_I1 + (w >> 2) * 16384 + (8 * g + q) * 256 + 8 * (p & 1);
#pragma unroll
                for (int s = 0; s < 2; ++s)
                    fv[t][s] = trd2(base + s * 8192 + 16 * (ch ^ ((q << 2) | ((2 * g) & 3))), base + s * 8192 + 1024 + 16 * (ch ^ ((q << 2) | ((2 * g + 1) & 3))));
            }
        }
        frags128(a0 + (L_I2 - L_I0), a1 + (L_I2 - L_I0), fw);
        const unsigned u = l15 < 12 ? l15 : l15 - 4;
        unsigned o_h3 = WS_H3 + ((((w >> 2) * 16 + hp) * 8 + e) * 32 + 8 * (w & 3) + (g >> 1)) * 384 + (u >> 2) * 128 + (g & 1) * 64 + (u & 3) * 16;
        unsigned o_t3 = WS_T3 + (xc * 128 + (h >> 4) * 64 + 8 * w + (l & 7)) * 128;
        unsigned o_t5 = tid < HREP ? WS_T5 + (tid * 128 + cq * 32 + h) * 128 : OOB;
        unsigned o_g4 = WS_H4 + ((h * 8 + g) * 128 + 16 * w + l15) * 16;
        unsigned a_ow, a_ow2, a_ms;
        {
            const unsigned n = 6 * (l15 >> 2) + 2 * (g >> 1), n2 = 6 * (l15 >> 2) + 4, gg = l15 & 3;
            a_ow = l15 < 12 ? L_OW + w * 272 + (64 * (g & 1) + 16 * gg + 4 * ((n >> 2) ^ (gg & 1)) + (n & 3)) * 2 : L_JUNK + l * 4;
            a_ow2 = (l15 < 8 && l < 32) ? L_OW + w * 272 + (64 * (g & 1) + 16 * gg + 4 * ((n2 >> 2) ^ (gg & 1)) + (n2 & 3)) * 2 : L_JUNK + l * 4;
            a_ms = L_MW + (g >> 1) * 64 + (g & 1) * 32 + w * 4;
        }
        unsigned a_or = L_OW + (l & 7) * 272 + (w >> 2) * 128 + 16 * g, a_mw = L_MW + (w >> 2) * 32 + (g & 1) * 16;
        unsigned TT = tag << 16;
        PIN(o_h3); PIN(o_t3); PIN(o_t5); PIN(o_g4); PIN(o_h4); PIN(a_ow); PIN(a_ow2); PIN(a_or); PIN(a_mw); PIN(a_ms); PIN(o_h5); PIN(TT);
        PIN(fv[0][0]); PIN(fv[0][1]); PIN(fv[1][0]); PIN(fv[1][1]); PIN(fw[0]); PIN(fw[1]); PIN(fw[2]); PIN(fw[3]);
        STAMP(7);
        if (STOPAT <= 7) goto done;
        hint_wait(rs, o_t3, tag);
        if (D3) __builtin_amdgcn_s_sleep(D3);
        STAMP(15);
        {
            v4u a[4];
#pragma nounroll
            for (int it = SPIN_CAP; it > 0; --it) {
                unsigned bad = 0;
#pragma unroll
                for (int k = 0; k < 4; ++k) a[k] = ldx(rs, o_h3 + k * 768);
#pragma unroll
                for (int k = 0; k < 4; ++k) bad |= tagx(a[k], TT);
                if (none(bad > 0xffffu)) break;
            }
            STAMP(8);
            float m[4], ls[4];
#pragma unroll
            for (int k = 0; k < 4; ++k) { const float sv = asf(__builtin_amdgcn_alignbit(a[k][3], a[k][2], 16)); m[k] = dpp<0x158>(sv); ls[k] = dpp<0x159>(sv); }
            const float M = max32(mx(mx3(m[0], m[1], m[2]), m[3]));
            float acc[6], Ls;
#pragma unroll
            for (int k = 0; k < 4; ++k) {
                const float al = __builtin_amdgcn_exp2f(m[k] - M);
                const float v[7] = {lo16(a[k][0]), hi16(a[k][0]), lo16(a[k][1]), lo16(a[k][2]), hi16(a[k][2]), lo16(a[k][3]), ls[k]};
                if (k == 0) { for (int j = 0; j < 6; ++j) acc[j] = al * v[j]; Ls = al * v[6]; }
                else { for (int j = 0; j < 6; ++j) acc[j] = __builtin_fmaf(al, v[j], acc[j]); Ls = __builtin_fmaf(al, v[6], Ls); }
            }
            const float v4 = sum32x2(acc[4], Ls);
            lwr<unsigned>(a_ow, pk(sum32x2(acc[0], acc[2]), sum32x2(acc[1], acc[3])));
            lwr<unsigned>(a_ow2, pk(v4, sum32(acc[5])));
            lwr<float>(a_ms, l < 32 ? M : v4);
        }
        bar();
        {
            const v4u of0 = lrd<v4u>(a_or), of1 = lrd<v4u>(a_or + 64);
            const f32x4 mw = lrd<f32x4>(a_mw), lw = lrd<f32x4>(a_mw + 64);
            FENCE();
            const f32x4 c0 = mfma32(of1, fv[0][1], mfma32(of0, fv[0][0], ZF)), c1 = mfma32(of1, fv[1][1], mfma32(of0, fv[1][0], ZF));
            const float M = mx(mx3(mw[0], mw[1], mw[2]), mw[3]);
            float be[4];
#pragma unroll
            for (int r = 0; r < 4; ++r) be[r] = __builtin_amdgcn_exp2f(mw[r] - M);
            const float rd = __builtin_amdgcn_rcpf((be[0] * lw[0] + be[1] * lw[1]) + (be[2] * lw[2] + be[3] * lw[3]));
            const float v0 = ((be[0] * c0[0] + be[1] * c0[1]) + (be[2] * c0[2] + be[3] * c0[3])) * rd;
            const float v1 = ((be[0] * c1[0] + be[1] * c1[1]) + (be[2] * c1[2] + be[3] * c1[3])) * rd;
            auto r = __builtin_amdgcn_permlane16_swap(asu(v0), asu(v1), false, false);
            pb[0] = r[0]; pb[2] = r[1];
            stx(rs, o_h4, pb);
        }
        STAMP(9);
        if (D4) __builtin_amdgcn_s_sleep(D4);
        {
            v4u a, b;
#pragma nounroll
            for (int it = SPIN_CAP; it > 0; --it) { a = ldx(rs, o_g4); b = ldx(rs, o_g4 + 4 * 2048); if (none((tagx(a, tag) | tagx(b, tag)) != 0u)) break; }
            hint_put(rs, o_t5, tag);
            lwr<u16>(a_qw, (u16)pk(sum32(sum16x2(asf(a[0]) + asf(b[0]), asf(a[2]) + asf(b[2]))), 0.f));
        }
        STAMP(10);
        bar();
        {
            v4u cf[4];
#pragma unroll
            for (int s = 0; s < 4; ++s) cf[s] = lrd<v4u>(a_qr + 64 * s);
            FENCE();
            const f32x4 acc = gemv4(cf, fw);
            pb[0] = asu(acc[0]); pb[2] = asu(acc[1]);
            stx(rs, o_h5, pb);
        }
        STAMP(11);
    } else {
        const int pr = xc * 8 + (yy >> 1), hg = yy & 1, b = pr >> 5, sp = pr & 31;
        unsigned ep;
        if (w == 0) asm volatile("global_load_dword %0, %1, %2 sc1" : "=v"(ep) : "v"(0), "s"(ws) : "memory");
        if (BDELAY) __builtin_amdgcn_s_sleep(BDELAY);
#pragma unroll
        for (int i = 0; i < 16; ++i) {
            if (i == 8) bar0();
            glds_s((pr * 128 + 64 * ((i >> 3) ^ hg) + 8 * w + (i & 7)) * 1024 + ((l ^ i) << 4), ckv, L_I0 + (16 * w + i) * 1024);
        }
        STAMP(1);
        if (w == 0) {
            asm volatile("s_waitcnt vmcnt(16)" : "+v"(ep) :: "memory");
            unsigned t = ep + 1u; t += (t & 0xffffu) == 0u;
            lwr<unsigned>(L_TAG, t);
        }
        unsigned qoff = WS_H2 + ((b * 32 + 16 * hg + 2 * w) * 128 + l) * 16;
        unsigned o_t2 = WS_T2 + (xc * 128 + (16 * hg + 2 * w) * 4 + (l & 7)) * 128;
        unsigned o_t3 = tid < HREP ? WS_T3 + (tid * 128 + hg * 64 + pr) * 128 : OOB;
        unsigned o_h3 = WS_H3 + (((b * 16 + 8 * hg + (l15 >> 1)) * 8 + w) * 32 + sp) * 384 + (l & 1) * 64 + g * 16;
        unsigned a_r[4], a_t[4];
#pragma unroll
        for (int j = 0; j < 4; ++j) { a_r[j] = l15 * 1024 + (((4 * j + g) ^ l15) << 4); PIN(a_r[j]); }
        {
            const unsigned q = l15 >> 2, p = l & 3;
#pragma unroll
            for (int j = 0; j < 4; ++j) {
                const unsigned r0 = 8 * g + 4 * (j & 1) + q, ch = 8 * w + 2 * p + (j >> 1);
                a_t[j] = L_I0 + r0 * 1024 + ((ch ^ (r0 & 15)) << 4) + (p & 1) * 8;
                PIN(a_t[j]);
            }
        }
        unsigned a_q0 = L_Q + 2 * w * 1024 + (((l >> 1) ^ (2 * w)) << 4) + (l & 1) * 8, a_q1 = L_Q + (2 * w + 1) * 1024 + (((l >> 1) ^ (2 * w + 1)) << 4) + (l & 1) * 8;
        unsigned a_mxw = L_MX + l15 * 32 + w * 4, a_mxr = L_MX + l15 * 32;
        unsigned a_pw = L_P + l15 * 256 + (((2 * w + (g >> 1)) ^ l15) << 4) + (g & 1) * 8;
        unsigned a_pr[4];
#pragma unroll
        for (int j = 0; j < 4; ++j) { a_pr[j] = L_P + l15 * 256 + (((4 * j + g) ^ l15) << 4); PIN(a_pr[j]); }
        PIN(qoff); PIN(o_t2); PIN(o_t3); PIN(o_h3); PIN(a_q0); PIN(a_q1); PIN(a_mxw); PIN(a_mxr); PIN(a_pw);
        WAITVM(0);
        STAMP(14);
        v4u cr[16], ct[4][4];
#pragma unroll
        for (int s = 0; s < 16; ++s) cr[s] = lrd<v4u>(a_r[s & 3] + (L_I0 + w * 16384) + (s >> 2) * 256);
        bar();
        STAMP(2);
        if (STOPAT <= 5) goto done;
        const unsigned tag = lrd<unsigned>(L_TAG);
#pragma unroll
        for (int t = 0; t < 4; ++t)
#pragma unroll
            for (int s = 0; s < 4; ++s) {
                const unsigned x8 = (t & 1) * 8;
                ct[t][s] = trd2((a_t[2 * (t >> 1)] ^ x8) + s * 32768, (a_t[2 * (t >> 1) + 1] ^ x8) + s * 32768);
            }
        FENCE();
        hint_wait(rs, o_t2, tag);
        if (D2) __builtin_amdgcn_s_sleep(D2);
        STAMP(15);
        {
            v4u qa[4];
#pragma nounroll
            for (int it = SPIN_CAP; it > 0; --it) {
#pragma unroll
                for (int k = 0; k < 4; ++k) qa[k] = ldx(rs, qoff + k * 1024);
                if (none(((tagx(qa[0], tag) | tagx(qa[1], tag)) | (tagx(qa[2], tag) | tagx(qa[3], tag))) != 0u)) break;
            }
            STAMP(3);
            lwr<v2u>(a_q0, v2u{qa[0][0], qa[0][2]}); lwr<v2u>(a_q0 + 512, v2u{qa[1][0], qa[1][2]});
            lwr<v2u>(a_q1, v2u{qa[2][0], qa[2][2]}); lwr<v2u>(a_q1 + 512, v2u{qa[3][0], qa[3][2]});
        }
        bar();
        STAMP(4);
        f32x4 sa = ZF, sb = ZF;
        {
            v4u qf[16];
#pragma unroll
            for (int s = 0; s < 16; ++s) qf[s] = lrd<v4u>(a_r[s & 3] + L_Q + (s >> 2) * 256);
            FENCE();
#pragma unroll
            for (int s = 0; s < 16; s += 2) { sa = mfma32(cr[s], qf[s], sa); sb = mfma32(cr[s + 1], qf[s + 1], sb); }
        }
        sa += sb;
        lwr<float>(a_mxw, max32(max16(mx(mx3(sa[0], sa[1], sa[2]), sa[3]))));
        bar();
        STAMP(12);
        float mrow;
        {
            const f32x4 m0 = lrd<f32x4>(a_mxr), m1 = lrd<f32x4>(a_mxr + 16);
            mrow = mx(mx3(mx3(m0[0], m0[1], m0[2]), m0[3], m1[0]), mx3(m1[1], m1[2], m1[3]));
            float p[4];
#pragma unroll
            for (int r = 0; r < 4; ++r) p[r] = __builtin_amdgcn_exp2f(sa[r] - mrow);
            lwr<v2u>(a_pw, v2u{pk(p[0], p[1]), pk(p[2], p[3])});
            lwr<float>(a_mxw + (L_LS - L_MX), sum32(sum16((p[0] + p[1]) + (p[2] + p[3]))));
        }
        bar();
        STAMP(5);
        {
            const unsigned T16 = tag & 0xffffu;
            v4u pf[4];
#pragma unroll
            for (int s = 0; s < 4; ++s) pf[s] = lrd<v4u>(a_pr[s]);
            const f32x4 l0 = lrd<f32x4>(a_mxr + (L_LS - L_MX)), l1 = lrd<f32x4>(a_mxr + (L_LS - L_MX) + 16);
            FENCE();
            f32x4 o[4];
#pragma unroll
            for (int t = 0; t < 4; ++t) {
                o[t] = ZF;
#pragma unroll
                for (int s = 0; s < 4; ++s) o[t] = mfma32(ct[t][s], pf[s], o[t]);
            }
            const float lsum = ((l0[0] + l0[1]) + (l0[2] + l0[3])) + ((l1[0] + l1[1]) + (l1[2] + l1[3]));
            unsigned d[9];
#pragma unroll
            for (int t = 0; t < 4; ++t) { d[2 * t] = pk(o[t][0], o[t][1]); d[2 * t + 1] = pk(o[t][2], o[t][3]); }
            d[8] = asu((g & 1) ? lsum : mrow);
            hint_put(rs, o_t3, tag);
#pragma unroll
            for (int i = 0; i < 3; ++i) {
                const unsigned A = d[3 * i], B = d[3 * i + 1], C = d[3 * i + 2];
                stx(rs, o_h3 + i * 128, v4u{A, __builtin_amdgcn_perm(T16, B, 0x05040100u), __builtin_amdgcn_alignbit(C, B, 16), __builtin_amdgcn_perm(T16, C, 0x05040302u)});
            }
        }
        STAMP(6);
        if (STOPAT <= 7) goto done;
        if (un < 86) {
#pragma unroll
            for (int i = 0; i < 2; ++i) glds_s((16 * un + 2 * w + i) * 1024 + ((l ^ (((2 * w + i) & 7) << 1)) << 4), wd, L_I0 + (2 * w + i) * 1024);
            {
                const unsigned off = WS_HG + (((w >> 2) * 16 + 4 * (w & 3) + g) * 1376 + 16 * un + l15) * 16;
                v4u ga;
#pragma nounroll
                for (int it = SPIN_CAP; it > 0; --it) { ga = ldx(rs, off); if (none(tagx(ga, tag) != 0u)) break; __builtin_amdgcn_s_sleep(4); }
                lwr<float>(L_RED + (l & 31) * 32 + w * 4, sum32(sum16x2(asf(ga[0]), asf(ga[2]))));
            }
            bar();
            if (w == 0) {
                const f32x4 gp = lrd<f32x4>(L_RED + (l & 31) * 32), up = lrd<f32x4>(L_RED + (l & 31) * 32 + 16);
                const float gg = rbf((gp[0] + gp[1]) + (gp[2] + gp[3])), uu = rbf((up[0] + up[1]) + (up[2] + up[3]));
                const float sg = rbf(__builtin_amdgcn_rcpf(1.f + __builtin_amdgcn_exp2f(gg * -1.4426950408889634f)));
                lwr<u16>(L_HH + (l & 31) * 2, (u16)pk(sg * uu, 0.f));
            }
            bar();
            const v2u ha = lrd<v2u>(L_HH + (l & 1) * 32 + g * 8);
            const unsigned q = l15 >> 2, p = l & 3, row = 4 * g + q;
            f32x4 o[4];
#pragma unroll
            for (int t = 0; t < 4; ++t) {
                const unsigned ch = 8 * w + 2 * t + (p >> 1);
                o[t] = mfma16(ha, trd(L_I0 + row * 1024 + ((ch ^ ((row & 7) << 1)) << 4) + (p & 1) * 8), ZF);
            }
            const float o0 = g == 0 ? o[0][0] : g == 1 ? o[1][0] : g == 2 ? o[2][0] : o[3][0];
            const float o1 = g == 0 ? o[0][1] : g == 1 ? o[1][1] : g == 2 ? o[2][1] : o[3][1];
            stx(rs, WS_H5 + ((32 + un) * 512 + 64 * w + l) * 16, v4u{asu(o0), tag, asu(o1), tag});
        }
        STAMP(7);
        if (hg == 0) {
            const int r = pr;
            unsigned o0 = WS_H5 + ((tid >> 3) * 512 + 8 * r + (tid & 7)) * 16;
            const bool two = tid < 432;
            const unsigned o1 = two ? o0 + 64 * 512 * 16 : o0 + 32 * 512 * 16;
            float xin = lo16(x[((tid >> 3) & 1) * 512 + 8 * r + (tid & 7)]);
            v4u a0, a1;
            __builtin_amdgcn_s_sleep(8);
#pragma nounroll
            for (int it = SPIN_CAP; it > 0; --it) { a1 = ldx(rs, o1); if (none(tagx(a1, tag) != 0u)) break; __builtin_amdgcn_s_sleep(4); }
            float s0 = two ? asf(a1[0]) : 0.f, s1 = two ? asf(a1[2]) : 0.f;
            unsigned a_rw = l15 < 8 ? L_RED + (((g & 1) * 8 + l15) * 2 + (g >> 1)) * 32 + w * 4 : L_JUNK + l * 4;
            unsigned a_rr = L_RED + (tid & 15) * 64;
            unsigned o_out = (((tid >> 3) & 1) * 512 + 8 * r + (tid & 7)) * 2;
            PIN(s0); PIN(s1); PIN(xin); PIN(a_rw); PIN(a_rr); PIN(o0); PIN(o_out);
            if (w < 4) { hint_wait(rs, WS_T5 + (xc * 128 + (r >> 4) * 32 + 8 * w + (l & 7)) * 128, tag); if (D5) __builtin_amdgcn_s_sleep(D5); }
            STAMP(10);
#pragma nounroll
            for (int it = SPIN_CAP; it > 0; --it) { a0 = ldx(rs, o0); if (none(tagx(a0, tag) != 0u)) break; }
            STAMP(8);
            float v0 = asf(a0[0]), v1 = asf(a0[2]);
            if (w >= 4) { s0 += v0; s1 += v1; v0 = 0.f; v1 = 0.f; }
            float p0 = sum16x2(v0, v1), p1 = sum16x2(s0, s1);
            float t = sum32x2(p0, p1);
            t += dpp<0x128>(t);
            lwr<float>(a_rw, t);
            bar();
            if (tid < 16) {
                const f32x4 t0 = lrd<f32x4>(a_rr), t2 = lrd<f32x4>(a_rr + 32), t3 = lrd<f32x4>(a_rr + 48);
                const float att = (t0[0] + t0[1]) + (t0[2] + t0[3]);
                const float glu = ((t2[0] + t2[1]) + (t2[2] + t2[3])) + ((t3[0] + t3[1]) + (t3[2] + t3[3]));
                *(GAS u16*)((GAS unsigned char*)out + o_out) = (u16)pk(rbf(xin + rbf(att)) + rbf(glu), 0.f);
            }
            if (r == 0 && tid == 0) __hip_atomic_store((GAS unsigned*)ws, tag, __ATOMIC_RELAXED, __HIP_MEMORY_SCOPE_AGENT);
            STAMP(9);
        }
    }
done:
    STAMP(13);
}

__global__ void k_nop(unsigned* p) { if (threadIdx.x == 12345) p[0] = 1; }
extern "C" void kernel_launch(void* const* d_in, const int* in_sizes, int n_in, void* d_out, int out_size, void* d_ws, size_t ws_size,
                              hipStream_t stream) {
    (void)in_sizes; (void)n_in; (void)out_size; (void)ws_size;
    (void)hipFuncSetAttribute((const void*)mla_glu_decode, hipFuncAttributeMaxDynamicSharedMemorySize, L_END);
    mla_glu_decode<<<256, 512, L_END, stream>>>((const u16*)d_in[0], (const u16*)d_in[1], (const u16*)d_in[2], (const u16*)d_in[3], (const u16*)d_in[4],
                                               (const u16*)d_in[5], (const u16*)d_in[7], (const u16*)d_in[8], (const u16*)d_in[9], (u16*)d_out,
                                               (unsigned char*)d_ws);
    k_nop<<<1, 64, 0, stream>>>((unsigned*)d_ws + (8<<20));
}
```

```cpp
#define STAMPS 1
#define SMASK 0x2001
#define BDELAY 32
#include <hip/hip_runtime.h>
#include <hip/hip_bf16.h>

typedef unsigned short u16;
typedef unsigned v4u __attribute__((ext_vector_type(4)));
typedef unsigned v2u __attribute__((ext_vector_type(2)));
typedef float f32x4 __attribute__((ext_vector_type(4)));
typedef short s16x4 __attribute__((ext_vector_type(4)));
typedef __bf16 bf16x8 __attribute__((ext_vector_type(8)));
typedef __bf16 bf16x2 __attribute__((ext_vector_type(2)));
#define LAS __attribute__((address_space(3)))
#define GAS __attribute__((address_space(1)))
#define DEVI __device__ __forceinline__

#define WNT ""
#define PREW 1
#define D1 3
#define D4 3
#define D2 0
#define D3 4
#define D5 0
#define HSL 1
#define STOPAT 99

constexpr int HREP = 8;
constexpr int WS_T2 = 4096;
constexpr int WS_T3 = WS_T2 + HREP * 128 * 128;
constexpr int WS_T5 = WS_T3 + HREP * 128 * 128;
constexpr int WS_H1 = WS_T5 + HREP * 128 * 128;
constexpr int WS_H4 = WS_H1 + 32 * 4 * 128 * 16;
constexpr int WS_H2 = WS_H4 + 32 * 8 * 128 * 16;
constexpr int WS_HG = WS_H2 + 2 * 32 * 128 * 16;
constexpr int WS_H5 = WS_HG + 2 * 16 * 1376 * 16;
constexpr int WS_H3 = WS_H5 + 118 * 512 * 16;
constexpr int WS_END = WS_H3 + 2 * 16 * 8 * 32 * 384;
constexpr int WS_STAMP = 16 << 20;
constexpr int L_Q = 0;
constexpr int L_XN = 0;
constexpr int L_P = 16384;
constexpr int L_QS = L_P + 4096;
constexpr int L_MX = L_QS + 1024;
constexpr int L_LS = L_MX + 512;
constexpr int L_OW = L_LS + 512;
constexpr int L_MW = L_OW + 2304;
constexpr int L_LW = L_MW + 64;
constexpr int L_HH = L_LW + 64;
constexpr int L_TAG = L_HH + 64;
constexpr int L_JUNK = L_TAG + 64;
constexpr int L_RED = L_JUNK + 256;
constexpr int L_I0 = 32768;
constexpr int L_I1 = 65536;
constexpr int L_I2 = 98304;
constexpr int L_END = 163840;
static_assert(L_RED + 1024 <= L_I0, "lds");
constexpr int SPIN_CAP = 1 << 16;

DEVI unsigned pk(float lo, float hi) { bf16x2 r = {(__bf16)lo, (__bf16)hi}; return __builtin_bit_cast(unsigned, r); }
DEVI float rbf(float f) { return (float)(__bf16)f; }
DEVI float lo16(unsigned w) { return __builtin_bit_cast(float, w << 16); }
DEVI float hi16(unsigned w) { return __builtin_bit_cast(float, w & 0xffff0000u); }
DEVI float asf(unsigned w) { return __builtin_bit_cast(float, w); }
DEVI unsigned asu(float w) { return __builtin_bit_cast(unsigned, w); }
DEVI float mx(float a, float b) { float r; asm("v_max_f32 %0, %1, %2" : "=v"(r) : "v"(a), "v"(b)); return r; }
DEVI float mx3(float a, float b, float c) { float r; asm("v_max3_f32 %0, %1, %2, %3" : "=v"(r) : "v"(a), "v"(b), "v"(c)); return r; }
DEVI f32x4 mfma32(v4u a, v4u b, f32x4 c) { return __builtin_amdgcn_mfma_f32_16x16x32_bf16(__builtin_bit_cast(bf16x8, a), __builtin_bit_cast(bf16x8, b), c, 0, 0, 0); }
DEVI f32x4 mfma16(v2u a, v2u b, f32x4 c) { return __builtin_amdgcn_mfma_f32_16x16x16bf16_1k(__builtin_bit_cast(s16x4, a), __builtin_bit_cast(s16x4, b), c, 0, 0, 0); }
template <class T> DEVI T lrd(unsigned off) { return *(LAS T*)(size_t)off; }
template <class T> DEVI void lwr(unsigned off, T v) { *(LAS T*)(size_t)off = v; }
DEVI v2u trd(unsigned off) { return __builtin_bit_cast(v2u, __builtin_amdgcn_ds_read_tr16_b64_v4i16((LAS s16x4*)(size_t)off)); }
DEVI v4u trd2(unsigned o0, unsigned o1) { v2u a = trd(o0), b = trd(o1); return v4u{a.x, a.y, b.x, b.y}; }
DEVI float sum16(float x) { auto r = __builtin_amdgcn_permlane16_swap(asu(x), asu(x), false, false); return asf(r[0]) + asf(r[1]); }
DEVI float sum32(float x) { auto r = __builtin_amdgcn_permlane32_swap(asu(x), asu(x), false, false); return asf(r[0]) + asf(r[1]); }
DEVI float max16(float x) { auto r = __builtin_amdgcn_permlane16_swap(asu(x), asu(x), false, false); return mx(asf(r[0]), asf(r[1])); }
DEVI float max32(float x) { auto r = __builtin_amdgcn_permlane32_swap(asu(x), asu(x), false, false); return mx(asf(r[0]), asf(r[1])); }
DEVI float sum16x2(float x, float y) { auto r = __builtin_amdgcn_permlane16_swap(asu(x), asu(y), false, false); return asf(r[0]) + asf(r[1]); }
DEVI float sum32x2(float x, float y) { auto r = __builtin_amdgcn_permlane32_swap(asu(x), asu(y), false, false); return asf(r[0]) + asf(r[1]); }
template <int CTRL> DEVI float dpp(float x) { return asf((unsigned)__builtin_amdgcn_update_dpp(0, (int)asu(x), CTRL, 0xf, 0xf, true)); }
DEVI float wavesum(float x) {
    x += dpp<0xB1>(x);
    x += dpp<0x4E>(x);
    x += dpp<0x141>(x);
    x += dpp<0x140>(x);
    return sum32(sum16(x));
}
#define STR_(x) #x
#define STR(x) STR_(x)
#define WAITVM(n) asm volatile("s_waitcnt vmcnt(" STR(n) ")" ::: "memory")
#define PIN(x) asm volatile("" : "+v"(x))
#define FENCE() __builtin_amdgcn_sched_barrier(0)
DEVI void bar() { asm volatile("s_waitcnt lgkmcnt(0)\n\ts_barrier" ::: "memory"); }
DEVI void bar0() { asm volatile("s_barrier" ::: "memory"); }
DEVI void glds_s(unsigned voff, const void* sbase, unsigned lds) {
    unsigned keep;
    asm volatile("s_mov_b32 %0, m0\n\ts_mov_b32 m0, %3\n\ts_nop 0\n\tglobal_load_lds_dwordx4 %1, %2" WNT "\n\ts_mov_b32 m0, %0" : "=&s"(keep) : "v"(voff), "s"(sbase), "s"(lds) : "memory");
}
typedef __amdgpu_buffer_rsrc_t rsrc_t;
DEVI v4u ldx(rsrc_t rs, unsigned off) { return __builtin_amdgcn_raw_buffer_load_b128(rs, off, 0, 16); }
DEVI void stx(rsrc_t rs, unsigned off, v4u v) { __builtin_amdgcn_raw_buffer_store_b128(v, rs, off, 0, 16); }
DEVI bool none(bool bad) { asm volatile("" ::: "memory"); return __builtin_amdgcn_ballot_w64(bad) == 0ull; }
DEVI unsigned tagx(v4u a, unsigned tag) { return (a[1] ^ tag) | (a[3] ^ tag); }
constexpr unsigned OOB = 0x80000000u;
DEVI void hint_put(rsrc_t rs, unsigned off, unsigned tag) { __builtin_amdgcn_raw_buffer_store_b32(tag, rs, off, 0, 16); }
DEVI void hint_wait(rsrc_t rs, unsigned off, unsigned tag) {
#pragma nounroll
    for (int it = SPIN_CAP; it > 0; --it) { const unsigned hv = __builtin_amdgcn_raw_buffer_load_b32(rs, off, 0, 16); if (none(hv != tag)) break; if (HSL) __builtin_amdgcn_s_sleep(HSL); }
}

#define NST 16
#define STAMP(i) do { if (!((SMASK >> (i)) & 1)) break; FENCE(); asm volatile("s_memrealtime %0\n\ts_waitcnt lgkmcnt(0)" : "=s"(st[i]) :: "memory"); FENCE(); } while (0)

DEVI void frags128(unsigned a0, unsigned a1, v4u (&f)[4]) {
#pragma unroll
    for (int s = 0; s < 4; ++s) f[s] = trd2(a0 + s * 8192, a1 + s * 8192);
}
DEVI f32x4 gemv4(const v4u (&a)[4], const v4u (&b)[4]) {
    f32x4 c = {0.f, 0.f, 0.f, 0.f};
#pragma unroll
    for (int s = 0; s < 4; ++s) c = mfma32(a[s], b[s], c);
    return c;
}

__global__ void __launch_bounds__(512, 2)
mla_glu_decode(const u16* __restrict__ x, const u16* __restrict__ ckv, const u16* __restrict__ wq, const u16* __restrict__ wuk,
               const u16* __restrict__ wuv, const u16* __restrict__ wo, const u16* __restrict__ wg, const u16* __restrict__ wu,
               const u16* __restrict__ wd, u16* __restrict__ out, unsigned char* __restrict__ ws) {
    unsigned long long st[NST];
#pragma unroll
    for (int i = 0; i < NST; ++i) st[i] = 0;
    STAMP(0);
    const int tid = threadIdx.x, l = tid & 63, w = __builtin_amdgcn_readfirstlane(tid >> 6);
    const int id = blockIdx.x, xc = id & 7, y = id >> 3, yy = y & 15;
    const int un = yy * 8 + xc;
    const unsigned g = l >> 4, l15 = l & 15;
    asm volatile("" :: "s"(x), "s"(ckv), "s"(wq), "s"(wuk), "s"(wuv), "s"(wo), "s"(wg), "s"(wu), "s"(wd), "s"(out), "s"(ws));
    const rsrc_t rs = __builtin_amdgcn_make_buffer_rsrc(ws, 0, WS_END, 0x00020000);
    const v4u Z = {0u, 0u, 0u, 0u};
    const f32x4 ZF = {0.f, 0.f, 0.f, 0.f};

    if (y < 16) {
        const int h = 4 * xc + (yy >> 2), cq = yy & 3;
        v4u xv; unsigned ep;
        if (w < 2) {
            asm volatile("global_load_dwordx4 %0, %1, %2" : "=v"(xv) : "v"(w * 1024 + l * 16), "s"(x) : "memory");
            asm volatile("global_load_dword %0, %1, %2 sc1" : "=v"(ep) : "v"(0), "s"(ws) : "memory");
        }
        const unsigned vb = (128 * cq + 16 * w + g) * 8192 + h * 256;
#pragma unroll
        for (int i = 0; i < 4; ++i) glds_s(vb + i * 32768 + ((l15 ^ ((g << 2) | i)) << 4), wq, L_I0 + (16 * w + 4 * i) * 256);
        bar0();
#pragma unroll
        for (int i = 0; i < 4; ++i) glds_s(vb + i * 32768 + ((l15 ^ (4 * i + g)) << 4), wuk, L_I1 + (16 * w + 4 * i) * 256);
        unsigned o_p1 = l < 16 ? WS_H1 + ((h * 4 + cq) * 128 + 16 * w + l) * 16 : OOB;
        unsigned o_g1 = WS_H1 + ((h * 4 + g) * 128 + 16 * w + l15) * 16;
        unsigned o_h2 = l15 < 2 ? WS_H2 + ((l15 * 32 + h) * 128 + 32 * cq + 4 * w + g) * 16 : OOB;
        unsigned o_t2 = tid < HREP ? WS_T2 + (tid * 128 + h * 4 + cq) * 128 : OOB;
        if (PREW) stx(rs, o_p1, Z);
        STAMP(1);
        if (w < 2) {
            if (PREW) asm volatile("s_waitcnt vmcnt(9)" : "+v"(xv), "+v"(ep) :: "memory");
            else asm volatile("s_waitcnt vmcnt(8)" : "+v"(xv), "+v"(ep) :: "memory");
            float v[8];
#pragma unroll
            for (int j = 0; j < 4; ++j) { v[2 * j] = lo16(xv[j]); v[2 * j + 1] = hi16(xv[j]); }
            float s = ((v[0] + v[1]) + (v[2] + v[3])) + ((v[4] + v[5]) + (v[6] + v[7]));
            const float mean = wavesum(s) * (1.f / 512.f);
            float sq = 0.f;
#pragma unroll
            for (int j = 0; j < 8; ++j) { const float d = v[j] - mean; sq += d * d; }
            const float var = wavesum(sq) * (1.f / 512.f);
            const float mb = rbf(mean), ri = __builtin_amdgcn_rcpf(rbf(__builtin_amdgcn_sqrtf(rbf(rbf(var) + 1e-5f))));
            v4u o;
#pragma unroll
            for (int j = 0; j < 4; ++j) o[j] = pk(rbf(v[2 * j] - mb) * ri, rbf(v[2 * j + 1] - mb) * ri);
            lwr<v4u>(L_XN + w * 1024 + l * 16, o);
            unsigned t = ep + 1u; t += (t & 0xffffu) == 0u;
            lwr<unsigned>(L_TAG, t);
        }
        bar();
        const unsigned tag = lrd<unsigned>(L_TAG);
        unsigned a0, a1;
        {
            const unsigned q = l15 >> 2, p = l & 3, ch = 2 * w + (p >> 1);
            a0 = L_I0 + (8 * g + q) * 256 + 16 * (ch ^ ((q << 2) | ((2 * g) & 3))) + 8 * (p & 1);
            a1 = L_I0 + (8 * g + q + 4) * 256 + 16 * (ch ^ ((q << 2) | ((2 * g + 1) & 3))) + 8 * (p & 1);
        }
        unsigned a_qw = L_QS + (g & 1) * 272 + (16 * w + l15) * 2;
        unsigned a_qr = L_QS + (l & 1) * 272 + 16 * g;
        unsigned a_uk[4];
        v4u xf[4];
#pragma unroll
        for (int s = 0; s < 4; ++s) {
            xf[s] = lrd<v4u>(L_XN + (l & 1) * 1024 + (128 * cq + 32 * s + 8 * g) * 2);
            a_uk[s] = L_I1 + (16 * w + l15) * 256 + (((4 * s + g) ^ l15) << 4);
            PIN(xf[s]); PIN(a_uk[s]);
        }
        v4u pb = {0u, tag, 0u, tag};
        PIN(a0); PIN(a1); PIN(o_p1); PIN(o_g1); PIN(o_h2); PIN(o_t2); PIN(a_qw); PIN(a_qr); PIN(pb);
        if (PREW) WAITVM(5); else WAITVM(4);
        bar0();
        STAMP(2);
        {
            v4u f[4]; frags128(a0, a1, f);
            FENCE();
            const f32x4 acc = gemv4(xf, f);
            pb[0] = asu(acc[0]); pb[2] = asu(acc[1]);
            stx(rs, o_p1, pb);
        }
        STAMP(3);
        if (STOPAT <= 3) { WAITVM(0); STAMP(14); goto done; }
        if (D1) __builtin_amdgcn_s_sleep(D1);
        v4u fu[4];
        {
            v4u a = ldx(rs, o_g1);
            WAITVM(0);
            STAMP(14);
#pragma unroll
            for (int s = 0; s < 4; ++s) fu[s] = lrd<v4u>(a_uk[s]);
#pragma nounroll
            for (int it = SPIN_CAP; it > 0 && !none(tagx(a, tag) != 0u); --it) a = ldx(rs, o_g1);
            hint_put(rs, o_t2, tag);
            lwr<u16>(a_qw, (u16)pk(sum32(sum16x2(asf(a[0]), asf(a[2]))), 0.f));
        }
        STAMP(4);
        bar();
        {
            v4u qf[4];
#pragma unroll
            for (int s = 0; s < 4; ++s) qf[s] = lrd<v4u>(a_qr + 64 * s);
            FENCE();
            const f32x4 acc = gemv4(fu, qf);
            constexpr float SC = 0.08838834764831845f * 1.4426950408889634f;
            pb[0] = pk(acc[0] * SC, acc[1] * SC); pb[2] = pk(acc[2] * SC, acc[3] * SC);
            stx(rs, o_h2, pb);
        }
        STAMP(5);
        if (STOPAT <= 5) goto done;
        const int hp = h >> 1, e = yy & 7, mat = un >> 6, mp = (un >> 2) & 15, fp = un & 3;
#pragma unroll
        for (int i = 0; i < 3; ++i) {
            const unsigned pc = 8 * i + w, idx = min(64 * pc + l, 1375u), row = idx / 43u, ch = idx - row * 43u;
            if (pc < 22) glds_s((32 * mp + row) * 2752 + fp * 688 + ch * 16, mat ? wu : wg, L_I0 + pc * 1024);
        }
#pragma unroll
        for (int i = 0; i < 4; ++i)
            glds_s((64 * e + 16 * (w & 3) + 4 * i + g) * 8192 + (2 * hp + (w >> 2)) * 256 + ((l15 ^ ((g << 2) | i)) << 4), wuv, L_I1 + (16 * w + 4 * i) * 256);
#pragma unroll
        for (int i = 0; i < 4; ++i) glds_s((h * 128 + 16 * w + 4 * i + g) * 1024 + cq * 256 + ((l15 ^ ((g << 2) | i)) << 4), wo, L_I2 + (16 * w + 4 * i) * 256);
        unsigned o_h5 = l < 16 ? WS_H5 + (h * 512 + 128 * cq + 16 * w + l) * 16 : OOB;
        unsigned o_h4 = l < 32 ? WS_H4 + (((2 * hp + (w >> 2)) * 8 + e) * 128 + 32 * (w & 3) + l) * 16 : OOB;
        {
            const v4u xg = lrd<v4u>(L_XN + (l & 1) * 1024 + (32 * mp + 8 * g) * 2);
            WAITVM(8);
            bar();
            const unsigned ag = L_I0 + (8 * g + (l15 >> 2)) * 688 + (l & 3) * 8;
            f32x4 o[3];
#pragma unroll
            for (int t = 0; t < 3; ++t) {
                const unsigned tl = min(8 * t + w, 21);
                o[t] = mfma32(xg, trd2(ag + tl * 32, ag + tl * 32 + 4 * 688), ZF);
            }
            const float o0 = g == 0 ? o[0][0] : g == 1 ? o[1][0] : o[2][0], o1 = g == 0 ? o[0][1] : g == 1 ? o[1][1] : o[2][1];
            const unsigned f = 16 * (8 * g + w) + l15;
            stx(rs, (g < 3 && f < 344) ? WS_HG + ((mat * 16 + mp) * 1376 + 344 * fp + f) * 16 : OOB, v4u{asu(o0), tag, asu(o1), tag});
            if (PREW) stx(rs, o_h4, Z);
        }
        STAMP(6);
        WAITVM(0);
        bar();
        v4u fv[2][2], fw[4];
        {
            const unsigned q = l15 >> 2, p = l & 3;
#pragma unroll
            for (int t = 0; t < 2; ++t) {
                const unsigned ch = 4 * (w & 3) + 2 * t + (p >> 1), base = L_I1 + (w >> 2) * 16384 + (8 * g + q) * 256 + 8 * (p & 1);
#pragma unroll
                for (int s = 0; s < 2; ++s)
                    fv[t][s] = trd2(base + s * 8192 + 16 * (ch ^ ((q << 2) | ((2 * g) & 3))), base + s * 8192 + 1024 + 16 * (ch ^ ((q << 2) | ((2 * g + 1) & 3))));
            }
        }
        frags128(a0 + (L_I2 - L_I0), a1 + (L_I2 - L_I0), fw);
        const unsigned u = l15 < 12 ? l15 : l15 - 4;
        unsigned o_h3 = WS_H3 + ((((w >> 2) * 16 + hp) * 8 + e) * 32 + 8 * (w & 3) + (g >> 1)) * 384 + (u >> 2) * 128 + (g & 1) * 64 + (u & 3) * 16;
        unsigned o_t3 = WS_T3 + (xc * 128 + (h >> 4) * 64 + 8 * w + (l & 7)) * 128;
        unsigned o_t5 = tid < HREP ? WS_T5 + (tid * 128 + cq * 32 + h) * 128 : OOB;
        unsigned o_g4 = WS_H4 + ((h * 8 + g) * 128 + 16 * w + l15) * 16;
        unsigned a_ow, a_ow2, a_ms;
        {
            const unsigned n = 6 * (l15 >> 2) + 2 * (g >> 1), n2 = 6 * (l15 >> 2) + 4, gg = l15 & 3;
            a_ow = l15 < 12 ? L_OW + w * 272 + (64 * (g & 1) + 16 * gg + 4 * ((n >> 2) ^ (gg & 1)) + (n & 3)) * 2 : L_JUNK + l * 4;
            a_ow2 = (l15 < 8 && l < 32) ? L_OW + w * 272 + (64 * (g & 1) + 16 * gg + 4 * ((n2 >> 2) ^ (gg & 1)) + (n2 & 3)) * 2 : L_JUNK + l * 4;
            a_ms = L_MW + (g >> 1) * 64 + (g & 1) * 32 + w * 4;
        }
        unsigned a_or = L_OW + (l & 7) * 272 + (w >> 2) * 128 + 16 * g, a_mw = L_MW + (w >> 2) * 32 + (g & 1) * 16;
        unsigned TT = tag << 16;
        PIN(o_h3); PIN(o_t3); PIN(o_t5); PIN(o_g4); PIN(o_h4); PIN(a_ow); PIN(a_ow2); PIN(a_or); PIN(a_mw); PIN(a_ms); PIN(o_h5); PIN(TT);
        PIN(fv[0][0]); PIN(fv[0][1]); PIN(fv[1][0]); PIN(fv[1][1]); PIN(fw[0]); PIN(fw[1]); PIN(fw[2]); PIN(fw[3]);
        STAMP(7);
        if (STOPAT <= 7) goto done;
        hint_wait(rs, o_t3, tag);
        if (D3) __builtin_amdgcn_s_sleep(D3);
        STAMP(15);
        {
            v4u a[4];
#pragma nounroll
            for (int it = SPIN_CAP; it > 0; --it) {
                unsigned bad = 0;
#pragma unroll
                for (int k = 0; k < 4; ++k) a[k] = ldx(rs, o_h3 + k * 768);
#pragma unroll
                for (int k = 0; k < 4; ++k) bad |= tagx(a[k], TT);
                if (none(bad > 0xffffu)) break;
            }
            STAMP(8);
            float m[4], ls[4];
#pragma unroll
            for (int k = 0; k < 4; ++k) { const float sv = asf(__builtin_amdgcn_alignbit(a[k][3], a[k][2], 16)); m[k] = dpp<0x158>(sv); ls[k] = dpp<0x159>(sv); }
            const float M = max32(mx(mx3(m[0], m[1], m[2]), m[3]));
            float acc[6], Ls;
#pragma unroll
            for (int k = 0; k < 4; ++k) {
                const float al = __builtin_amdgcn_exp2f(m[k] - M);
                const float v[7] = {lo16(a[k][0]), hi16(a[k][0]), lo16(a[k][1]), lo16(a[k][2]), hi16(a[k][2]), lo16(a[k][3]), ls[k]};
                if (k == 0) { for (int j = 0; j < 6; ++j) acc[j] = al * v[j]; Ls = al * v[6]; }
                else { for (int j = 0; j < 6; ++j) acc[j] = __builtin_fmaf(al, v[j], acc[j]); Ls = __builtin_fmaf(al, v[6], Ls); }
            }
            const float v4 = sum32x2(acc[4], Ls);
            lwr<unsigned>(a_ow, pk(sum32x2(acc[0], acc[2]), sum32x2(acc[1], acc[3])));
            lwr<unsigned>(a_ow2, pk(v4, sum32(acc[5])));
            lwr<float>(a_ms, l < 32 ? M : v4);
        }
        bar();
        {
            const v4u of0 = lrd<v4u>(a_or), of1 = lrd<v4u>(a_or + 64);
            const f32x4 mw = lrd<f32x4>(a_mw), lw = lrd<f32x4>(a_mw + 64);
            FENCE();
            const f32x4 c0 = mfma32(of1, fv[0][1], mfma32(of0, fv[0][0], ZF)), c1 = mfma32(of1, fv[1][1], mfma32(of0, fv[1][0], ZF));
            const float M = mx(mx3(mw[0], mw[1], mw[2]), mw[3]);
            float be[4];
#pragma unroll
            for (int r = 0; r < 4; ++r) be[r] = __builtin_amdgcn_exp2f(mw[r] - M);
            const float rd = __builtin_amdgcn_rcpf((be[0] * lw[0] + be[1] * lw[1]) + (be[2] * lw[2] + be[3] * lw[3]));
            const float v0 = ((be[0] * c0[0] + be[1] * c0[1]) + (be[2] * c0[2] + be[3] * c0[3])) * rd;
            const float v1 = ((be[0] * c1[0] + be[1] * c1[1]) + (be[2] * c1[2] + be[3] * c1[3])) * rd;
            auto r = __builtin_amdgcn_permlane16_swap(asu(v0), asu(v1), false, false);
            pb[0] = r[0]; pb[2] = r[1];
            stx(rs, o_h4, pb);
        }
        STAMP(9);
        if (D4) __builtin_amdgcn_s_sleep(D4);
        {
            v4u a, b;
#pragma nounroll
            for (int it = SPIN_CAP; it > 0; --it) { a = ldx(rs, o_g4); b = ldx(rs, o_g4 + 4 * 2048); if (none((tagx(a, tag) | tagx(b, tag)) != 0u)) break; }
            hint_put(rs, o_t5, tag);
            lwr<u16>(a_qw, (u16)pk(sum32(sum16x2(asf(a[0]) + asf(b[0]), asf(a[2]) + asf(b[2]))), 0.f));
        }
        STAMP(10);
        bar();
        {
            v4u cf[4];
#pragma unroll
            for (int s = 0; s < 4; ++s) cf[s] = lrd<v4u>(a_qr + 64 * s);
            FENCE();
            const f32x4 acc = gemv4(cf, fw);
            pb[0] = asu(acc[0]); pb[2] = asu(acc[1]);
            stx(rs, o_h5, pb);
        }
        STAMP(11);
    } else {
        const int pr = xc * 8 + (yy >> 1), hg = yy & 1, b = pr >> 5, sp = pr & 31;
        unsigned ep;
        if (w == 0) asm volatile("global_load_dword %0, %1, %2 sc1" : "=v"(ep) : "v"(0), "s"(ws) : "memory");
        if (BDELAY) __builtin_amdgcn_s_sleep(BDELAY);
#pragma unroll
        for (int i = 0; i < 16; ++i) {
            if (i == 8) bar0();
            glds_s((pr * 128 + 64 * ((i >> 3) ^ hg) + 8 * w + (i & 7)) * 1024 + ((l ^ i) << 4), ckv, L_I0 + (16 * w + i) * 1024);
        }
        STAMP(1);
        if (w == 0) {
            asm volatile("s_waitcnt vmcnt(16)" : "+v"(ep) :: "memory");
            unsigned t = ep + 1u; t += (t & 0xffffu) == 0u;
            lwr<unsigned>(L_TAG, t);
        }
        unsigned qoff = WS_H2 + ((b * 32 + 16 * hg + 2 * w) * 128 + l) * 16;
        unsigned o_t2 = WS_T2 + (xc * 128 + (16 * hg + 2 * w) * 4 + (l & 7)) * 128;
        unsigned o_t3 = tid < HREP ? WS_T3 + (tid * 128 + hg * 64 + pr) * 128 : OOB;
        unsigned o_h3 = WS_H3 + (((b * 16 + 8 * hg + (l15 >> 1)) * 8 + w) * 32 + sp) * 384 + (l & 1) * 64 + g * 16;
        unsigned a_r[4], a_t[4];
#pragma unroll
        for (int j = 0; j < 4; ++j) { a_r[j] = l15 * 1024 + (((4 * j + g) ^ l15) << 4); PIN(a_r[j]); }
        {
            const unsigned q = l15 >> 2, p = l & 3;
#pragma unroll
            for (int j = 0; j < 4; ++j) {
                const unsigned r0 = 8 * g + 4 * (j & 1) + q, ch = 8 * w + 2 * p + (j >> 1);
                a_t[j] = L_I0 + r0 * 1024 + ((ch ^ (r0 & 15)) << 4) + (p & 1) * 8;
                PIN(a_t[j]);
            }
        }
        unsigned a_q0 = L_Q + 2 * w * 1024 + (((l >> 1) ^ (2 * w)) << 4) + (l & 1) * 8, a_q1 = L_Q + (2 * w + 1) * 1024 + (((l >> 1) ^ (2 * w + 1)) << 4) + (l & 1) * 8;
        unsigned a_mxw = L_MX + l15 * 32 + w * 4, a_mxr = L_MX + l15 * 32;
        unsigned a_pw = L_P + l15 * 256 + (((2 * w + (g >> 1)) ^ l15) << 4) + (g & 1) * 8;
        unsigned a_pr[4];
#pragma unroll
        for (int j = 0; j < 4; ++j) { a_pr[j] = L_P + l15 * 256 + (((4 * j + g) ^ l15) << 4); PIN(a_pr[j]); }
        PIN(qoff); PIN(o_t2); PIN(o_t3); PIN(o_h3); PIN(a_q0); PIN(a_q1); PIN(a_mxw); PIN(a_mxr); PIN(a_pw);
        WAITVM(0);
        STAMP(14);
        v4u cr[16], ct[4][4];
#pragma unroll
        for (int s = 0; s < 16; ++s) cr[s] = lrd<v4u>(a_r[s & 3] + (L_I0 + w * 16384) + (s >> 2) * 256);
        bar();
        STAMP(2);
        if (STOPAT <= 5) goto done;
        const unsigned tag = lrd<unsigned>(L_TAG);
#pragma unroll
        for (int t = 0; t < 4; ++t)
#pragma unroll
            for (int s = 0; s < 4; ++s) {
                const unsigned x8 = (t & 1) * 8;
                ct[t][s] = trd2((a_t[2 * (t >> 1)] ^ x8) + s * 32768, (a_t[2 * (t >> 1) + 1] ^ x8) + s * 32768);
            }
        FENCE();
        hint_wait(rs, o_t2, tag);
        if (D2) __builtin_amdgcn_s_sleep(D2);
        STAMP(15);
        {
            v4u qa[4];
#pragma nounroll
            for (int it = SPIN_CAP; it > 0; --it) {
#pragma unroll
                for (int k = 0; k < 4; ++k) qa[k] = ldx(rs, qoff + k * 1024);
                if (none(((tagx(qa[0], tag) | tagx(qa[1], tag)) | (tagx(qa[2], tag) | tagx(qa[3], tag))) != 0u)) break;
            }
            STAMP(3);
            lwr<v2u>(a_q0, v2u{qa[0][0], qa[0][2]}); lwr<v2u>(a_q0 + 512, v2u{qa[1][0], qa[1][2]});
            lwr<v2u>(a_q1, v2u{qa[2][0], qa[2][2]}); lwr<v2u>(a_q1 + 512, v2u{qa[3][0], qa[3][2]});
        }
        bar();
        STAMP(4);
        f32x4 sa = ZF, sb = ZF;
        {
            v4u qf[16];
#pragma unroll
            for (int s = 0; s < 16; ++s) qf[s] = lrd<v4u>(a_r[s & 3] + L_Q + (s >> 2) * 256);
            FENCE();
#pragma unroll
            for (int s = 0; s < 16; s += 2) { sa = mfma32(cr[s], qf[s], sa); sb = mfma32(cr[s + 1], qf[s + 1], sb); }
        }
        sa += sb;
        lwr<float>(a_mxw, max32(max16(mx(mx3(sa[0], sa[1], sa[2]), sa[3]))));
        bar();
        STAMP(12);
        float mrow;
        {
            const f32x4 m0 = lrd<f32x4>(a_mxr), m1 = lrd<f32x4>(a_mxr + 16);
            mrow = mx(mx3(mx3(m0[0], m0[1], m0[2]), m0[3], m1[0]), mx3(m1[1], m1[2], m1[3]));
            float p[4];
#pragma unroll
            for (int r = 0; r < 4; ++r) p[r] = __builtin_amdgcn_exp2f(sa[r] - mrow);
            lwr<v2u>(a_pw, v2u{pk(p[0], p[1]), pk(p[2], p[3])});
            lwr<float>(a_mxw + (L_LS - L_MX), sum32(sum16((p[0] + p[1]) + (p[2] + p[3]))));
        }
        bar();
        STAMP(5);
        {
            const unsigned T16 = tag & 0xffffu;
            v4u pf[4];
#pragma unroll
            for (int s = 0; s < 4; ++s) pf[s] = lrd<v4u>(a_pr[s]);
            const f32x4 l0 = lrd<f32x4>(a_mxr + (L_LS - L_MX)), l1 = lrd<f32x4>(a_mxr + (L_LS - L_MX) + 16);
            FENCE();
            f32x4 o[4];
#pragma unroll
            for (int t = 0; t < 4; ++t) {
                o[t] = ZF;
#pragma unroll
                for (int s = 0; s < 4; ++s) o[t] = mfma32(ct[t][s], pf[s], o[t]);
            }
            const float lsum = ((l0[0] + l0[1]) + (l0[2] + l0[3])) + ((l1[0] + l1[1]) + (l1[2] + l1[3]));
            unsigned d[9];
#pragma unroll
            for (int t = 0; t < 4; ++t) { d[2 * t] = pk(o[t][0], o[t][1]); d[2 * t + 1] = pk(o[t][2], o[t][3]); }
            d[8] = asu((g & 1) ? lsum : mrow);
            hint_put(rs, o_t3, tag);
#pragma unroll
            for (int i = 0; i < 3; ++i) {
                const unsigned A = d[3 * i], B = d[3 * i + 1], C = d[3 * i + 2];
                stx(rs, o_h3 + i * 128, v4u{A, __builtin_amdgcn_perm(T16, B, 0x05040100u), __builtin_amdgcn_alignbit(C, B, 16), __builtin_amdgcn_perm(T16, C, 0x05040302u)});
            }
        }
        STAMP(6);
        if (STOPAT <= 7) goto done;
        if (un < 86) {
#pragma unroll
            for (int i = 0; i < 2; ++i) glds_s((16 * un + 2 * w + i) * 1024 + ((l ^ (((2 * w + i) & 7) << 1)) << 4), wd, L_I0 + (2 * w + i) * 1024);
            {
                const unsigned off = WS_HG + (((w >> 2) * 16 + 4 * (w & 3) + g) * 1376 + 16 * un + l15) * 16;
                v4u ga;
#pragma nounroll
                for (int it = SPIN_CAP; it > 0; --it) { ga = ldx(rs, off); if (none(tagx(ga, tag) != 0u)) break; __builtin_amdgcn_s_sleep(4); }
                lwr<float>(L_RED + (l & 31) * 32 + w * 4, sum32(sum16x2(asf(ga[0]), asf(ga[2]))));
            }
            bar();
            if (w == 0) {
                const f32x4 gp = lrd<f32x4>(L_RED + (l & 31) * 32), up = lrd<f32x4>(L_RED + (l & 31) * 32 + 16);
                const float gg = rbf((gp[0] + gp[1]) + (gp[2] + gp[3])), uu = rbf((up[0] + up[1]) + (up[2] + up[3]));
                const float sg = rbf(__builtin_amdgcn_rcpf(1.f + __builtin_amdgcn_exp2f(gg * -1.4426950408889634f)));
                lwr<u16>(L_HH + (l & 31) * 2, (u16)pk(sg * uu, 0.f));
            }
            bar();
            const v2u ha = lrd<v2u>(L_HH + (l & 1) * 32 + g * 8);
            const unsigned q = l15 >> 2, p = l & 3, row = 4 * g + q;
            f32x4 o[4];
#pragma unroll
            for (int t = 0; t < 4; ++t) {
                const unsigned ch = 8 * w + 2 * t + (p >> 1);
                o[t] = mfma16(ha, trd(L_I0 + row * 1024 + ((ch ^ ((row & 7) << 1)) << 4) + (p & 1) * 8), ZF);
            }
            const float o0 = g == 0 ? o[0][0] : g == 1 ? o[1][0] : g == 2 ? o[2][0] : o[3][0];
            const float o1 = g == 0 ? o[0][1] : g == 1 ? o[1][1] : g == 2 ? o[2][1] : o[3][1];
            stx(rs, WS_H5 + ((32 + un) * 512 + 64 * w + l) * 16, v4u{asu(o0), tag, asu(o1), tag});
        }
        STAMP(7);
        if (hg == 0) {
            const int r = pr;
            unsigned o0 = WS_H5 + ((tid >> 3) * 512 + 8 * r + (tid & 7)) * 16;
            const bool two = tid < 432;
            const unsigned o1 = two ? o0 + 64 * 512 * 16 : o0 + 32 * 512 * 16;
            float xin = lo16(x[((tid >> 3) & 1) * 512 + 8 * r + (tid & 7)]);
            v4u a0, a1;
            __builtin_amdgcn_s_sleep(8);
#pragma nounroll
            for (int it = SPIN_CAP; it > 0; --it) { a1 = ldx(rs, o1); if (none(tagx(a1, tag) != 0u)) break; __builtin_amdgcn_s_sleep(4); }
            float s0 = two ? asf(a1[0]) : 0.f, s1 = two ? asf(a1[2]) : 0.f;
            unsigned a_rw = l15 < 8 ? L_RED + (((g & 1) * 8 + l15) * 2 + (g >> 1)) * 32 + w * 4 : L_JUNK + l * 4;
            unsigned a_rr = L_RED + (tid & 15) * 64;
            unsigned o_out = (((tid >> 3) & 1) * 512 + 8 * r + (tid & 7)) * 2;
            PIN(s0); PIN(s1); PIN(xin); PIN(a_rw); PIN(a_rr); PIN(o0); PIN(o_out);
            if (w < 4) { hint_wait(rs, WS_T5 + (xc * 128 + (r >> 4) * 32 + 8 * w + (l & 7)) * 128, tag); if (D5) __builtin_amdgcn_s_sleep(D5); }
            STAMP(10);
#pragma nounroll
            for (int it = SPIN_CAP; it > 0; --it) { a0 = ldx(rs, o0); if (none(tagx(a0, tag) != 0u)) break; }
            STAMP(8);
            float v0 = asf(a0[0]), v1 = asf(a0[2]);
            if (w >= 4) { s0 += v0; s1 += v1; v0 = 0.f; v1 = 0.f; }
            float p0 = sum16x2(v0, v1), p1 = sum16x2(s0, s1);
            float t = sum32x2(p0, p1);
            t += dpp<0x128>(t);
            lwr<float>(a_rw, t);
            bar();
            if (tid < 16) {
                const f32x4 t0 = lrd<f32x4>(a_rr), t2 = lrd<f32x4>(a_rr + 32), t3 = lrd<f32x4>(a_rr + 48);
                const float att = (t0[0] + t0[1]) + (t0[2] + t0[3]);
                const float glu = ((t2[0] + t2[1]) + (t2[2] + t2[3])) + ((t3[0] + t3[1]) + (t3[2] + t3[3]));
                *(GAS u16*)((GAS unsigned char*)out + o_out) = (u16)pk(rbf(xin + rbf(att)) + rbf(glu), 0.f);
            }
            if (r == 0 && tid == 0) __hip_atomic_store((GAS unsigned*)ws, tag, __ATOMIC_RELAXED, __HIP_MEMORY_SCOPE_AGENT);
            STAMP(9);
        }
    }
done:
    STAMP(13);
    if (l == 0) {
        GAS unsigned long long* so = (GAS unsigned long long*)(ws + WS_STAMP) + ((size_t)id * 8 + w) * NST;
#pragma unroll
        for (int i = 0; i < NST; ++i) so[i] = st[i];
    }
}

__global__ void k_mark(unsigned long long* p) { p[0] = __builtin_amdgcn_s_memrealtime(); }
__global__ void __launch_bounds__(512) k_probe(unsigned long long* p) {
    const unsigned long long t = __builtin_amdgcn_s_memrealtime();

    if (threadIdx.x == 0) p[(size_t)blockIdx.x * 8 * NST] = t;
}
__global__ void __launch_bounds__(256) k_report(const unsigned long long* mark, const unsigned long long* stp, int n, int scale) {
    __shared__ unsigned long long r0[256];
    const int id = threadIdx.x; unsigned long long mn = id < n ? stp[(size_t)id * 8 * NST] : ~0ull;
    r0[id] = mn; __syncthreads();
    if (id == 0) { for (int i = 1; i < 256; ++i) mn = r0[i] < mn ? r0[i] : mn;
        unsigned long long q = mn - mark[0]; if (q > 20000) q = 20000;
        const unsigned long long t = __builtin_amdgcn_s_memrealtime() + q * scale; while (__builtin_amdgcn_s_memrealtime() < t) __builtin_amdgcn_s_sleep(1); }
}
extern "C" void kernel_launch(void* const* d_in, const int* in_sizes, int n_in, void* d_out, int out_size, void* d_ws, size_t ws_size,
                              hipStream_t stream) {
    (void)in_sizes; (void)n_in; (void)out_size; (void)ws_size;
    (void)hipFuncSetAttribute((const void*)mla_glu_decode, hipFuncAttributeMaxDynamicSharedMemorySize, L_END);
    unsigned long long* mk = (unsigned long long*)((char*)d_ws + (20 << 20)); unsigned long long* ps = (unsigned long long*)((char*)d_ws + (24 << 20));
    k_mark<<<1, 1, 0, stream>>>(mk);
    (void)hipFuncSetAttribute((const void*)k_probe, hipFuncAttributeMaxDynamicSharedMemorySize, 163840);
    k_probe<<<256, 512, 0, stream>>>(ps);
    mla_glu_decode<<<256, 512, L_END, stream>>>((const u16*)d_in[0], (const u16*)d_in[1], (const u16*)d_in[2], (const u16*)d_in[3], (const u16*)d_in[4],
                                               (const u16*)d_in[5], (const u16*)d_in[7], (const u16*)d_in[8], (const u16*)d_in[9], (u16*)d_out,
                                               (unsigned char*)d_ws);
    k_report<<<1, 256, 0, stream>>>(mk, ps, 256, 100);
}
```

```cpp
#define STAMPS 1
#define SMASK 0x0001
#define BDELAY 32
#include <hip/hip_runtime.h>
#include <hip/hip_bf16.h>

typedef unsigned short u16;
typedef unsigned v4u __attribute__((ext_vector_type(4)));
typedef unsigned v2u __attribute__((ext_vector_type(2)));
typedef float f32x4 __attribute__((ext_vector_type(4)));
typedef short s16x4 __attribute__((ext_vector_type(4)));
typedef __bf16 bf16x8 __attribute__((ext_vector_type(8)));
typedef __bf16 bf16x2 __attribute__((ext_vector_type(2)));
#define LAS __attribute__((address_space(3)))
#define GAS __attribute__((address_space(1)))
#define DEVI __device__ __forceinline__

#define WNT ""
#define PREW 1
#define D1 3
#define D4 3
#define D2 0
#define D3 4
#define D5 0
#define HSL 1
#define STOPAT 99

constexpr int HREP = 8;
constexpr int WS_T2 = 4096;
constexpr int WS_T3 = WS_T2 + HREP * 128 * 128;
constexpr int WS_T5 = WS_T3 + HREP * 128 * 128;
constexpr int WS_H1 = WS_T5 + HREP * 128 * 128;
constexpr int WS_H4 = WS_H1 + 32 * 4 * 128 * 16;
constexpr int WS_H2 = WS_H4 + 32 * 8 * 128 * 16;
constexpr int WS_HG = WS_H2 + 2 * 32 * 128 * 16;
constexpr int WS_H5 = WS_HG + 2 * 16 * 1376 * 16;
constexpr int WS_H3 = WS_H5 + 118 * 512 * 16;
constexpr int WS_END = WS_H3 + 2 * 16 * 8 * 32 * 384;
constexpr int WS_STAMP = 16 << 20;
constexpr int L_Q = 0;
constexpr int L_XN = 0;
constexpr int L_P = 16384;
constexpr int L_QS = L_P + 4096;
constexpr int L_MX = L_QS + 1024;
constexpr int L_LS = L_MX + 512;
constexpr int L_OW = L_LS + 512;
constexpr int L_MW = L_OW + 2304;
constexpr int L_LW = L_MW + 64;
constexpr int L_HH = L_LW + 64;
constexpr int L_TAG = L_HH + 64;
constexpr int L_JUNK = L_TAG + 64;
constexpr int L_RED = L_JUNK + 256;
constexpr int L_I0 = 32768;
constexpr int L_I1 = 65536;
constexpr int L_I2 = 98304;
constexpr int L_END = 163840;
static_assert(L_RED + 1024 <= L_I0, "lds");
constexpr int SPIN_CAP = 1 << 16;

DEVI unsigned pk(float lo, float hi) { bf16x2 r = {(__bf16)lo, (__bf16)hi}; return __builtin_bit_cast(unsigned, r); }
DEVI float rbf(float f) { return (float)(__bf16)f; }
DEVI float lo16(unsigned w) { return __builtin_bit_cast(float, w << 16); }
DEVI float hi16(unsigned w) { return __builtin_bit_cast(float, w & 0xffff0000u); }
DEVI float asf(unsigned w) { return __builtin_bit_cast(float, w); }
DEVI unsigned asu(float w) { return __builtin_bit_cast(unsigned, w); }
DEVI float mx(float a, float b) { float r; asm("v_max_f32 %0, %1, %2" : "=v"(r) : "v"(a), "v"(b)); return r; }
DEVI float mx3(float a, float b, float c) { float r; asm("v_max3_f32 %0, %1, %2, %3" : "=v"(r) : "v"(a), "v"(b), "v"(c)); return r; }
DEVI f32x4 mfma32(v4u a, v4u b, f32x4 c) { return __builtin_amdgcn_mfma_f32_16x16x32_bf16(__builtin_bit_cast(bf16x8, a), __builtin_bit_cast(bf16x8, b), c, 0, 0, 0); }
DEVI f32x4 mfma16(v2u a, v2u b, f32x4 c) { return __builtin_amdgcn_mfma_f32_16x16x16bf16_1k(__builtin_bit_cast(s16x4, a), __builtin_bit_cast(s16x4, b), c, 0, 0, 0); }
template <class T> DEVI T lrd(unsigned off) { return *(LAS T*)(size_t)off; }
template <class T> DEVI void lwr(unsigned off, T v) { *(LAS T*)(size_t)off = v; }
DEVI v2u trd(unsigned off) { return __builtin_bit_cast(v2u, __builtin_amdgcn_ds_read_tr16_b64_v4i16((LAS s16x4*)(size_t)off)); }
DEVI v4u trd2(unsigned o0, unsigned o1) { v2u a = trd(o0), b = trd(o1); return v4u{a.x, a.y, b.x, b.y}; }
DEVI float sum16(float x) { auto r = __builtin_amdgcn_permlane16_swap(asu(x), asu(x), false, false); return asf(r[0]) + asf(r[1]); }
DEVI float sum32(float x) { auto r = __builtin_amdgcn_permlane32_swap(asu(x), asu(x), false, false); return asf(r[0]) + asf(r[1]); }
DEVI float max16(float x) { auto r = __builtin_amdgcn_permlane16_swap(asu(x), asu(x), false, false); return mx(asf(r[0]), asf(r[1])); }
DEVI float max32(float x) { auto r = __builtin_amdgcn_permlane32_swap(asu(x), asu(x), false, false); return mx(asf(r[0]), asf(r[1])); }
DEVI float sum16x2(float x, float y) { auto r = __builtin_amdgcn_permlane16_swap(asu(x), asu(y), false, false); return asf(r[0]) + asf(r[1]); }
DEVI float sum32x2(float x, float y) { auto r = __builtin_amdgcn_permlane32_swap(asu(x), asu(y), false, false); return asf(r[0]) + asf(r[1]); }
template <int CTRL> DEVI float dpp(float x) { return asf((unsigned)__builtin_amdgcn_update_dpp(0, (int)asu(x), CTRL, 0xf, 0xf, true)); }
DEVI float wavesum(float x) {
    x += dpp<0xB1>(x);
    x += dpp<0x4E>(x);
    x += dpp<0x141>(x);
    x += dpp<0x140>(x);
    return sum32(sum16(x));
}
#define STR_(x) #x
#define STR(x) STR_(x)
#define WAITVM(n) asm volatile("s_waitcnt vmcnt(" STR(n) ")" ::: "memory")
#define PIN(x) asm volatile("" : "+v"(x))
#define FENCE() __builtin_amdgcn_sched_barrier(0)
DEVI void bar() { asm volatile("s_waitcnt lgkmcnt(0)\n\ts_barrier" ::: "memory"); }
DEVI void bar0() { asm volatile("s_barrier" ::: "memory"); }
DEVI void glds_s(unsigned voff, const void* sbase, unsigned lds) {
    unsigned keep;
    asm volatile("s_mov_b32 %0, m0\n\ts_mov_b32 m0, %3\n\ts_nop 0\n\tglobal_load_lds_dwordx4 %1, %2" WNT "\n\ts_mov_b32 m0, %0" : "=&s"(keep) : "v"(voff), "s"(sbase), "s"(lds) : "memory");
}
typedef __amdgpu_buffer_rsrc_t rsrc_t;
DEVI v4u ldx(rsrc_t rs, unsigned off) { return __builtin_amdgcn_raw_buffer_load_b128(rs, off, 0, 16); }
DEVI void stx(rsrc_t rs, unsigned off, v4u v) { __builtin_amdgcn_raw_buffer_store_b128(v, rs, off, 0, 16); }
DEVI bool none(bool bad) { asm volatile("" ::: "memory"); return __builtin_amdgcn_ballot_w64(bad) == 0ull; }
DEVI unsigned tagx(v4u a, unsigned tag) { return (a[1] ^ tag) | (a[3] ^ tag); }
constexpr unsigned OOB = 0x80000000u;
DEVI void hint_put(rsrc_t rs, unsigned off, unsigned tag) { __builtin_amdgcn_raw_buffer_store_b32(tag, rs, off, 0, 16); }
DEVI void hint_wait(rsrc_t rs, unsigned off, unsigned tag) {
#pragma nounroll
    for (int it = SPIN_CAP; it > 0; --it) { const unsigned hv = __builtin_amdgcn_raw_buffer_load_b32(rs, off, 0, 16); if (none(hv != tag)) break; if (HSL) __builtin_amdgcn_s_sleep(HSL); }
}

#define NST 16
#define STAMP(i) do { if (!((SMASK >> (i)) & 1)) break; FENCE(); asm volatile("s_memrealtime %0\n\ts_waitcnt lgkmcnt(0)" : "=s"(st[i]) :: "memory"); FENCE(); } while (0)

DEVI void frags128(unsigned a0, unsigned a1, v4u (&f)[4]) {
#pragma unroll
    for (int s = 0; s < 4; ++s) f[s] = trd2(a0 + s * 8192, a1 + s * 8192);
}
DEVI f32x4 gemv4(const v4u (&a)[4], const v4u (&b)[4]) {
    f32x4 c = {0.f, 0.f, 0.f, 0.f};
#pragma unroll
    for (int s = 0; s < 4; ++s) c = mfma32(a[s], b[s], c);
    return c;
}

__global__ void __launch_bounds__(512, 2)
mla_glu_decode(const u16* __restrict__ x, const u16* __restrict__ ckv, const u16* __restrict__ wq, const u16* __restrict__ wuk,
               const u16* __restrict__ wuv, const u16* __restrict__ wo, const u16* __restrict__ wg, const u16* __restrict__ wu,
               const u16* __restrict__ wd, u16* __restrict__ out, unsigned char* __restrict__ ws) {
    unsigned long long st[NST];
#pragma unroll
    for (int i = 0; i < NST; ++i) st[i] = 0;
    STAMP(0);
    const int tid = threadIdx.x, l = tid & 63, w = __builtin_amdgcn_readfirstlane(tid >> 6);
    const int id = blockIdx.x, xc = id & 7, y = id >> 3, yy = y & 15;
    const int un = yy * 8 + xc;
    const unsigned g = l >> 4, l15 = l & 15;
    asm volatile("" :: "s"(x), "s"(ckv), "s"(wq), "s"(wuk), "s"(wuv), "s"(wo), "s"(wg), "s"(wu), "s"(wd), "s"(out), "s"(ws));
    const rsrc_t rs = __builtin_amdgcn_make_buffer_rsrc(ws, 0, WS_END, 0x00020000);
    const v4u Z = {0u, 0u, 0u, 0u};
    const f32x4 ZF = {0.f, 0.f, 0.f, 0.f};

    if (y < 16) {
        const int h = 4 * xc + (yy >> 2), cq = yy & 3;
        v4u xv; unsigned ep;
        if (w < 2) {
            asm volatile("global_load_dwordx4 %0, %1, %2" : "=v"(xv) : "v"(w * 1024 + l * 16), "s"(x) : "memory");
            asm volatile("global_load_dword %0, %1, %2 sc1" : "=v"(ep) : "v"(0), "s"(ws) : "memory");
        }
        const unsigned vb = (128 * cq + 16 * w + g) * 8192 + h * 256;
#pragma unroll
        for (int i = 0; i < 4; ++i) glds_s(vb + i * 32768 + ((l15 ^ ((g << 2) | i)) << 4), wq, L_I0 + (16 * w + 4 * i) * 256);
        bar0();
#pragma unroll
        for (int i = 0; i < 4; ++i) glds_s(vb + i * 32768 + ((l15 ^ (4 * i + g)) << 4), wuk, L_I1 + (16 * w + 4 * i) * 256);
        unsigned o_p1 = l < 16 ? WS_H1 + ((h * 4 + cq) * 128 + 16 * w + l) * 16 : OOB;
        unsigned o_g1 = WS_H1 + ((h * 4 + g) * 128 + 16 * w + l15) * 16;
        unsigned o_h2 = l15 < 2 ? WS_H2 + ((l15 * 32 + h) * 128 + 32 * cq + 4 * w + g) * 16 : OOB;
        unsigned o_t2 = tid < HREP ? WS_T2 + (tid * 128 + h * 4 + cq) * 128 : OOB;
        if (PREW) stx(rs, o_p1, Z);
        STAMP(1);
        if (w < 2) {
            if (PREW) asm volatile("s_waitcnt vmcnt(9)" : "+v"(xv), "+v"(ep) :: "memory");
            else asm volatile("s_waitcnt vmcnt(8)" : "+v"(xv), "+v"(ep) :: "memory");
            float v[8];
#pragma unroll
            for (int j = 0; j < 4; ++j) { v[2 * j] = lo16(xv[j]); v[2 * j + 1] = hi16(xv[j]); }
            float s = ((v[0] + v[1]) + (v[2] + v[3])) + ((v[4] + v[5]) + (v[6] + v[7]));
            const float mean = wavesum(s) * (1.f / 512.f);
            float sq = 0.f;
#pragma unroll
            for (int j = 0; j < 8; ++j) { const float d = v[j] - mean; sq += d * d; }
            const float var = wavesum(sq) * (1.f / 512.f);
            const float mb = rbf(mean), ri = __builtin_amdgcn_rcpf(rbf(__builtin_amdgcn_sqrtf(rbf(rbf(var) + 1e-5f))));
            v4u o;
#pragma unroll
            for (int j = 0; j < 4; ++j) o[j] = pk(rbf(v[2 * j] - mb) * ri, rbf(v[2 * j + 1] - mb) * ri);
            lwr<v4u>(L_XN + w * 1024 + l * 16, o);
            unsigned t = ep + 1u; t += (t & 0xffffu) == 0u;
            lwr<unsigned>(L_TAG, t);
        }
        bar();
        const unsigned tag = lrd<unsigned>(L_TAG);
        unsigned a0, a1;
        {
            const unsigned q = l15 >> 2, p = l & 3, ch = 2 * w + (p >> 1);
            a0 = L_I0 + (8 * g + q) * 256 + 16 * (ch ^ ((q << 2) | ((2 * g) & 3))) + 8 * (p & 1);
            a1 = L_I0 + (8 * g + q + 4) * 256 + 16 * (ch ^ ((q << 2) | ((2 * g + 1) & 3))) + 8 * (p & 1);
        }
        unsigned a_qw = L_QS + (g & 1) * 272 + (16 * w + l15) * 2;
        unsigned a_qr = L_QS + (l & 1) * 272 + 16 * g;
        unsigned a_uk[4];
        v4u xf[4];
#pragma unroll
        for (int s = 0; s < 4; ++s) {
            xf[s] = lrd<v4u>(L_XN + (l & 1) * 1024 + (128 * cq + 32 * s + 8 * g) * 2);
            a_uk[s] = L_I1 + (16 * w + l15) * 256 + (((4 * s + g) ^ l15) << 4);
            PIN(xf[s]); PIN(a_uk[s]);
        }
        v4u pb = {0u, tag, 0u, tag};
        PIN(a0); PIN(a1); PIN(o_p1); PIN(o_g1); PIN(o_h2); PIN(o_t2); PIN(a_qw); PIN(a_qr); PIN(pb);
        if (PREW) WAITVM(5); else WAITVM(4);
        bar0();
        STAMP(2);
        {
            v4u f[4]; frags128(a0, a1, f);
            FENCE();
            const f32x4 acc = gemv4(xf, f);
            pb[0] = asu(acc[0]); pb[2] = asu(acc[1]);
            stx(rs, o_p1, pb);
        }
        STAMP(3);
        if (STOPAT <= 3) { WAITVM(0); STAMP(14); goto done; }
        if (D1) __builtin_amdgcn_s_sleep(D1);
        v4u fu[4];
        {
            v4u a = ldx(rs, o_g1);
            WAITVM(0);
            STAMP(14);
#pragma unroll
            for (int s = 0; s < 4; ++s) fu[s] = lrd<v4u>(a_uk[s]);
#pragma nounroll
            for (int it = SPIN_CAP; it > 0 && !none(tagx(a, tag) != 0u); --it) a = ldx(rs, o_g1);
            hint_put(rs, o_t2, tag);
            lwr<u16>(a_qw, (u16)pk(sum32(sum16x2(asf(a[0]), asf(a[2]))), 0.f));
        }
        STAMP(4);
        bar();
        {
            v4u qf[4];
#pragma unroll
            for (int s = 0; s < 4; ++s) qf[s] = lrd<v4u>(a_qr + 64 * s);
            FENCE();
            const f32x4 acc = gemv4(fu, qf);
            constexpr float SC = 0.08838834764831845f * 1.4426950408889634f;
            pb[0] = pk(acc[0] * SC, acc[1] * SC); pb[2] = pk(acc[2] * SC, acc[3] * SC);
            stx(rs, o_h2, pb);
        }
        STAMP(5);
        if (STOPAT <= 5) goto done;
        const int hp = h >> 1, e = yy & 7, mat = un >> 6, mp = (un >> 2) & 15, fp = un & 3;
#pragma unroll
        for (int i = 0; i < 3; ++i) {
            const unsigned pc = 8 * i + w, idx = min(64 * pc + l, 1375u), row = idx / 43u, ch = idx - row * 43u;
            if (pc < 22) glds_s((32 * mp + row) * 2752 + fp * 688 + ch * 16, mat ? wu : wg, L_I0 + pc * 1024);
        }
#pragma unroll
        for (int i = 0; i < 4; ++i)
            glds_s((64 * e + 16 * (w & 3) + 4 * i + g) * 8192 + (2 * hp + (w >> 2)) * 256 + ((l15 ^ ((g << 2) | i)) << 4), wuv, L_I1 + (16 * w + 4 * i) * 256);
#pragma unroll
        for (int i = 0; i < 4; ++i) glds_s((h * 128 + 16 * w + 4 * i + g) * 1024 + cq * 256 + ((l15 ^ ((g << 2) | i)) << 4), wo, L_I2 + (16 * w + 4 * i) * 256);
        unsigned o_h5 = l < 16 ? WS_H5 + (h * 512 + 128 * cq + 16 * w + l) * 16 : OOB;
        unsigned o_h4 = l < 32 ? WS_H4 + (((2 * hp + (w >> 2)) * 8 + e) * 128 + 32 * (w & 3) + l) * 16 : OOB;
        {
            const v4u xg = lrd<v4u>(L_XN + (l & 1) * 1024 + (32 * mp + 8 * g) * 2);
            WAITVM(8);
            bar();
            const unsigned ag = L_I0 + (8 * g + (l15 >> 2)) * 688 + (l & 3) * 8;
            f32x4 o[3];
#pragma unroll
            for (int t = 0; t < 3; ++t) {
                const unsigned tl = min(8 * t + w, 21);
                o[t] = mfma32(xg, trd2(ag + tl * 32, ag + tl * 32 + 4 * 688), ZF);
            }
            const float o0 = g == 0 ? o[0][0] : g == 1 ? o[1][0] : o[2][0], o1 = g == 0 ? o[0][1] : g == 1 ? o[1][1] : o[2][1];
            const unsigned f = 16 * (8 * g + w) + l15;
            stx(rs, (g < 3 && f < 344) ? WS_HG + ((mat * 16 + mp) * 1376 + 344 * fp + f) * 16 : OOB, v4u{asu(o0), tag, asu(o1), tag});
            if (PREW) stx(rs, o_h4, Z);
        }
        STAMP(6);
        WAITVM(0);
        bar();
        v4u fv[2][2], fw[4];
        {
            const unsigned q = l15 >> 2, p = l & 3;
#pragma unroll
            for (int t = 0; t < 2; ++t) {
                const unsigned ch = 4 * (w & 3) + 2 * t + (p >> 1), base = L_I1 + (w >> 2) * 16384 + (8 * g + q) * 256 + 8 * (p & 1);
#pragma unroll
                for (int s = 0; s < 2; ++s)
                    fv[t][s] = trd2(base + s * 8192 + 16 * (ch ^ ((q << 2) | ((2 * g) & 3))), base + s * 8192 + 1024 + 16 * (ch ^ ((q << 2) | ((2 * g + 1) & 3))));
            }
        }
        frags128(a0 + (L_I2 - L_I0), a1 + (L_I2 - L_I0), fw);
        const unsigned u = l15 < 12 ? l15 : l15 - 4;
        unsigned o_h3 = WS_H3 + ((((w >> 2) * 16 + hp) * 8 + e) * 32 + 8 * (w & 3) + (g >> 1)) * 384 + (u >> 2) * 128 + (g & 1) * 64 + (u & 3) * 16;
        unsigned o_t3 = WS_T3 + (xc * 128 + (h >> 4) * 64 + 8 * w + (l & 7)) * 128;
        unsigned o_t5 = tid < HREP ? WS_T5 + (tid * 128 + cq * 32 + h) * 128 : OOB;
        unsigned o_g4 = WS_H4 + ((h * 8 + g) * 128 + 16 * w + l15) * 16;
        unsigned a_ow, a_ow2, a_ms;
        {
            const unsigned n = 6 * (l15 >> 2) + 2 * (g >> 1), n2 = 6 * (l15 >> 2) + 4, gg = l15 & 3;
            a_ow = l15 < 12 ? L_OW + w * 272 + (64 * (g & 1) + 16 * gg + 4 * ((n >> 2) ^ (gg & 1)) + (n & 3)) * 2 : L_JUNK + l * 4;
            a_ow2 = (l15 < 8 && l < 32) ? L_OW + w * 272 + (64 * (g & 1) + 16 * gg + 4 * ((n2 >> 2) ^ (gg & 1)) + (n2 & 3)) * 2 : L_JUNK + l * 4;
            a_ms = L_MW + (g >> 1) * 64 + (g & 1) * 32 + w * 4;
        }
        unsigned a_or = L_OW + (l & 7) * 272 + (w >> 2) * 128 + 16 * g, a_mw = L_MW + (w >> 2) * 32 + (g & 1) * 16;
        unsigned TT = tag << 16;
        PIN(o_h3); PIN(o_t3); PIN(o_t5); PIN(o_g4); PIN(o_h4); PIN(a_ow); PIN(a_ow2); PIN(a_or); PIN(a_mw); PIN(a_ms); PIN(o_h5); PIN(TT);
        PIN(fv[0][0]); PIN(fv[0][1]); PIN(fv[1][0]); PIN(fv[1][1]); PIN(fw[0]); PIN(fw[1]); PIN(fw[2]); PIN(fw[3]);
        STAMP(7);
        if (STOPAT <= 7) goto done;
        hint_wait(rs, o_t3, tag);
        if (D3) __builtin_amdgcn_s_sleep(D3);
        STAMP(15);
        {
            v4u a[4];
#pragma nounroll
            for (int it = SPIN_CAP; it > 0; --it) {
                unsigned bad = 0;
#pragma unroll
                for (int k = 0; k < 4; ++k) a[k] = ldx(rs, o_h3 + k * 768);
#pragma unroll
                for (int k = 0; k < 4; ++k) bad |= tagx(a[k], TT);
                if (none(bad > 0xffffu)) break;
            }
            STAMP(8);
            float m[4], ls[4];
#pragma unroll
            for (int k = 0; k < 4; ++k) { const float sv = asf(__builtin_amdgcn_alignbit(a[k][3], a[k][2], 16)); m[k] = dpp<0x158>(sv); ls[k] = dpp<0x159>(sv); }
            const float M = max32(mx(mx3(m[0], m[1], m[2]), m[3]));
            float acc[6], Ls;
#pragma unroll
            for (int k = 0; k < 4; ++k) {
                const float al = __builtin_amdgcn_exp2f(m[k] - M);
                const float v[7] = {lo16(a[k][0]), hi16(a[k][0]), lo16(a[k][1]), lo16(a[k][2]), hi16(a[k][2]), lo16(a[k][3]), ls[k]};
                if (k == 0) { for (int j = 0; j < 6; ++j) acc[j] = al * v[j]; Ls = al * v[6]; }
                else { for (int j = 0; j < 6; ++j) acc[j] = __builtin_fmaf(al, v[j], acc[j]); Ls = __builtin_fmaf(al, v[6], Ls); }
            }
            const float v4 = sum32x2(acc[4], Ls);
            lwr<unsigned>(a_ow, pk(sum32x2(acc[0], acc[2]), sum32x2(acc[1], acc[3])));
            lwr<unsigned>(a_ow2, pk(v4, sum32(acc[5])));
            lwr<float>(a_ms, l < 32 ? M : v4);
        }
        bar();
        {
            const v4u of0 = lrd<v4u>(a_or), of1 = lrd<v4u>(a_or + 64);
            const f32x4 mw = lrd<f32x4>(a_mw), lw = lrd<f32x4>(a_mw + 64);
            FENCE();
            const f32x4 c0 = mfma32(of1, fv[0][1], mfma32(of0, fv[0][0], ZF)), c1 = mfma32(of1, fv[1][1], mfma32(of0, fv[1][0], ZF));
            const float M = mx(mx3(mw[0], mw[1], mw[2]), mw[3]);
            float be[4];
#pragma unroll
            for (int r = 0; r < 4; ++r) be[r] = __builtin_amdgcn_exp2f(mw[r] - M);
            const float rd = __builtin_amdgcn_rcpf((be[0] * lw[0] + be[1] * lw[1]) + (be[2] * lw[2] + be[3] * lw[3]));
            const float v0 = ((be[0] * c0[0] + be[1] * c0[1]) + (be[2] * c0[2] + be[3] * c0[3])) * rd;
            const float v1 = ((be[0] * c1[0] + be[1] * c1[1]) + (be[2] * c1[2] + be[3] * c1[3])) * rd;
            auto r = __builtin_amdgcn_permlane16_swap(asu(v0), asu(v1), false, false);
            pb[0] = r[0]; pb[2] = r[1];
            stx(rs, o_h4, pb);
        }
        STAMP(9);
        if (D4) __builtin_amdgcn_s_sleep(D4);
        {
            v4u a, b;
#pragma nounroll
            for (int it = SPIN_CAP; it > 0; --it) { a = ldx(rs, o_g4); b = ldx(rs, o_g4 + 4 * 2048); if (none((tagx(a, tag) | tagx(b, tag)) != 0u)) break; }
            hint_put(rs, o_t5, tag);
            lwr<u16>(a_qw, (u16)pk(sum32(sum16x2(asf(a[0]) + asf(b[0]), asf(a[2]) + asf(b[2]))), 0.f));
        }
        STAMP(10);
        bar();
        {
            v4u cf[4];
#pragma unroll
            for (int s = 0; s < 4; ++s) cf[s] = lrd<v4u>(a_qr + 64 * s);
            FENCE();
            const f32x4 acc = gemv4(cf, fw);
            pb[0] = asu(acc[0]); pb[2] = asu(acc[1]);
            stx(rs, o_h5, pb);
        }
        STAMP(11);
    } else {
        const int pr = xc * 8 + (yy >> 1), hg = yy & 1, b = pr >> 5, sp = pr & 31;
        unsigned ep;
        if (w == 0) asm volatile("global_load_dword %0, %1, %2 sc1" : "=v"(ep) : "v"(0), "s"(ws) : "memory");
        if (BDELAY) __builtin_amdgcn_s_sleep(BDELAY);
#pragma unroll
        for (int i = 0; i < 16; ++i) {
            if (i == 8) bar0();
            glds_s((pr * 128 + 64 * ((i >> 3) ^ hg) + 8 * w + (i & 7)) * 1024 + ((l ^ i) << 4), ckv, L_I0 + (16 * w + i) * 1024);
        }
        STAMP(1);
        if (w == 0) {
            asm volatile("s_waitcnt vmcnt(16)" : "+v"(ep) :: "memory");
            unsigned t = ep + 1u; t += (t & 0xffffu) == 0u;
            lwr<unsigned>(L_TAG, t);
        }
        unsigned qoff = WS_H2 + ((b * 32 + 16 * hg + 2 * w) * 128 + l) * 16;
        unsigned o_t2 = WS_T2 + (xc * 128 + (16 * hg + 2 * w) * 4 + (l & 7)) * 128;
        unsigned o_t3 = tid < HREP ? WS_T3 + (tid * 128 + hg * 64 + pr) * 128 : OOB;
        unsigned o_h3 = WS_H3 + (((b * 16 + 8 * hg + (l15 >> 1)) * 8 + w) * 32 + sp) * 384 + (l & 1) * 64 + g * 16;
        unsigned a_r[4], a_t[4];
#pragma unroll
        for (int j = 0; j < 4; ++j) { a_r[j] = l15 * 1024 + (((4 * j + g) ^ l15) << 4); PIN(a_r[j]); }
        {
            const unsigned q = l15 >> 2, p = l & 3;
#pragma unroll
            for (int j = 0; j < 4; ++j) {
                const unsigned r0 = 8 * g + 4 * (j & 1) + q, ch = 8 * w + 2 * p + (j >> 1);
                a_t[j] = L_I0 + r0 * 1024 + ((ch ^ (r0 & 15)) << 4) + (p & 1) * 8;
                PIN(a_t[j]);
            }
        }
        unsigned a_q0 = L_Q + 2 * w * 1024 + (((l >> 1) ^ (2 * w)) << 4) + (l & 1) * 8, a_q1 = L_Q + (2 * w + 1) * 1024 + (((l >> 1) ^ (2 * w + 1)) << 4) + (l & 1) * 8;
        unsigned a_mxw = L_MX + l15 * 32 + w * 4, a_mxr = L_MX + l15 * 32;
        unsigned a_pw = L_P + l15 * 256 + (((2 * w + (g >> 1)) ^ l15) << 4) + (g & 1) * 8;
        unsigned a_pr[4];
#pragma unroll
        for (int j = 0; j < 4; ++j) { a_pr[j] = L_P + l15 * 256 + (((4 * j + g) ^ l15) << 4); PIN(a_pr[j]); }
        PIN(qoff); PIN(o_t2); PIN(o_t3); PIN(o_h3); PIN(a_q0); PIN(a_q1); PIN(a_mxw); PIN(a_mxr); PIN(a_pw);
        WAITVM(0);
        STAMP(14);
        v4u cr[16], ct[4][4];
#pragma unroll
        for (int s = 0; s < 16; ++s) cr[s] = lrd<v4u>(a_r[s & 3] + (L_I0 + w * 16384) + (s >> 2) * 256);
        bar();
        STAMP(2);
        if (STOPAT <= 5) goto done;
        const unsigned tag = lrd<unsigned>(L_TAG);
#pragma unroll
        for (int t = 0; t < 4; ++t)
#pragma unroll
            for (int s = 0; s < 4; ++s) {
                const unsigned x8 = (t & 1) * 8;
                ct[t][s] = trd2((a_t[2 * (t >> 1)] ^ x8) + s * 32768, (a_t[2 * (t >> 1) + 1] ^ x8) + s * 32768);
            }
        FENCE();
        hint_wait(rs, o_t2, tag);
        if (D2) __builtin_amdgcn_s_sleep(D2);
        STAMP(15);
        {
            v4u qa[4];
#pragma nounroll
            for (int it = SPIN_CAP; it > 0; --it) {
#pragma unroll
                for (int k = 0; k < 4; ++k) qa[k] = ldx(rs, qoff + k * 1024);
                if (none(((tagx(qa[0], tag) | tagx(qa[1], tag)) | (tagx(qa[2], tag) | tagx(qa[3], tag))) != 0u)) break;
            }
            STAMP(3);
            lwr<v2u>(a_q0, v2u{qa[0][0], qa[0][2]}); lwr<v2u>(a_q0 + 512, v2u{qa[1][0], qa[1][2]});
            lwr<v2u>(a_q1, v2u{qa[2][0], qa[2][2]}); lwr<v2u>(a_q1 + 512, v2u{qa[3][0], qa[3][2]});
        }
        bar();
        STAMP(4);
        f32x4 sa = ZF, sb = ZF;
        {
            v4u qf[16];
#pragma unroll
            for (int s = 0; s < 16; ++s) qf[s] = lrd<v4u>(a_r[s & 3] + L_Q + (s >> 2) * 256);
            FENCE();
#pragma unroll
            for (int s = 0; s < 16; s += 2) { sa = mfma32(cr[s], qf[s], sa); sb = mfma32(cr[s + 1], qf[s + 1], sb); }
        }
        sa += sb;
        lwr<float>(a_mxw, max32(max16(mx(mx3(sa[0], sa[1], sa[2]), sa[3]))));
        bar();
        STAMP(12);
        float mrow;
        {
            const f32x4 m0 = lrd<f32x4>(a_mxr), m1 = lrd<f32x4>(a_mxr + 16);
            mrow = mx(mx3(mx3(m0[0], m0[1], m0[2]), m0[3], m1[0]), mx3(m1[1], m1[2], m1[3]));
            float p[4];
#pragma unroll
            for (int r = 0; r < 4; ++r) p[r] = __builtin_amdgcn_exp2f(sa[r] - mrow);
            lwr<v2u>(a_pw, v2u{pk(p[0], p[1]), pk(p[2], p[3])});
            lwr<float>(a_mxw + (L_LS - L_MX), sum32(sum16((p[0] + p[1]) + (p[2] + p[3]))));
        }
        bar();
        STAMP(5);
        {
            const unsigned T16 = tag & 0xffffu;
            v4u pf[4];
#pragma unroll
            for (int s = 0; s < 4; ++s) pf[s] = lrd<v4u>(a_pr[s]);
            const f32x4 l0 = lrd<f32x4>(a_mxr + (L_LS - L_MX)), l1 = lrd<f32x4>(a_mxr + (L_LS - L_MX) + 16);
            FENCE();
            f32x4 o[4];
#pragma unroll
            for (int t = 0; t < 4; ++t) {
                o[t] = ZF;
#pragma unroll
                for (int s = 0; s < 4; ++s) o[t] = mfma32(ct[t][s], pf[s], o[t]);
            }
            const float lsum = ((l0[0] + l0[1]) + (l0[2] + l0[3])) + ((l1[0] + l1[1]) + (l1[2] + l1[3]));
            unsigned d[9];
#pragma unroll
            for (int t = 0; t < 4; ++t) { d[2 * t] = pk(o[t][0], o[t][1]); d[2 * t + 1] = pk(o[t][2], o[t][3]); }
            d[8] = asu((g & 1) ? lsum : mrow);
            hint_put(rs, o_t3, tag);
#pragma unroll
            for (int i = 0; i < 3; ++i) {
                const unsigned A = d[3 * i], B = d[3 * i + 1], C = d[3 * i + 2];
                stx(rs, o_h3 + i * 128, v4u{A, __builtin_amdgcn_perm(T16, B, 0x05040100u), __builtin_amdgcn_alignbit(C, B, 16), __builtin_amdgcn_perm(T16, C, 0x05040302u)});
            }
        }
        STAMP(6);
        if (STOPAT <= 7) goto done;
        if (un < 86) {
#pragma unroll
            for (int i = 0; i < 2; ++i) glds_s((16 * un + 2 * w + i) * 1024 + ((l ^ (((2 * w + i) & 7) << 1)) << 4), wd, L_I0 + (2 * w + i) * 1024);
            {
                const unsigned off = WS_HG + (((w >> 2) * 16 + 4 * (w & 3) + g) * 1376 + 16 * un + l15) * 16;
                v4u ga;
#pragma nounroll
                for (int it = SPIN_CAP; it > 0; --it) { ga = ldx(rs, off); if (none(tagx(ga, tag) != 0u)) break; __builtin_amdgcn_s_sleep(4); }
                lwr<float>(L_RED + (l & 31) * 32 + w * 4, sum32(sum16x2(asf(ga[0]), asf(ga[2]))));
            }
            bar();
            if (w == 0) {
                const f32x4 gp = lrd<f32x4>(L_RED + (l & 31) * 32), up = lrd<f32x4>(L_RED + (l & 31) * 32 + 16);
                const float gg = rbf((gp[0] + gp[1]) + (gp[2] + gp[3])), uu = rbf((up[0] + up[1]) + (up[2] + up[3]));
                const float sg = rbf(__builtin_amdgcn_rcpf(1.f + __builtin_amdgcn_exp2f(gg * -1.4426950408889634f)));
                lwr<u16>(L_HH + (l & 31) * 2, (u16)pk(sg * uu, 0.f));
            }
            bar();
            const v2u ha = lrd<v2u>(L_HH + (l & 1) * 32 + g * 8);
            const unsigned q = l15 >> 2, p = l & 3, row = 4 * g + q;
            f32x4 o[4];
#pragma unroll
            for (int t = 0; t < 4; ++t) {
                const unsigned ch = 8 * w + 2 * t + (p >> 1);
                o[t] = mfma16(ha, trd(L_I0 + row * 1024 + ((ch ^ ((row & 7) << 1)) << 4) + (p & 1) * 8), ZF);
            }
            const float o0 = g == 0 ? o[0][0] : g == 1 ? o[1][0] : g == 2 ? o[2][0] : o[3][0];
            const float o1 = g == 0 ? o[0][1] : g == 1 ? o[1][1] : g == 2 ? o[2][1] : o[3][1];
            stx(rs, WS_H5 + ((32 + un) * 512 + 64 * w + l) * 16, v4u{asu(o0), tag, asu(o1), tag});
        }
        STAMP(7);
        if (hg == 0) {
            const int r = pr;
            unsigned o0 = WS_H5 + ((tid >> 3) * 512 + 8 * r + (tid & 7)) * 16;
            const bool two = tid < 432;
            const unsigned o1 = two ? o0 + 64 * 512 * 16 : o0 + 32 * 512 * 16;
            float xin = lo16(x[((tid >> 3) & 1) * 512 + 8 * r + (tid & 7)]);
            v4u a0, a1;
            __builtin_amdgcn_s_sleep(8);
#pragma nounroll
            for (int it = SPIN_CAP; it > 0; --it) { a1 = ldx(rs, o1); if (none(tagx(a1, tag) != 0u)) break; __builtin_amdgcn_s_sleep(4); }
            float s0 = two ? asf(a1[0]) : 0.f, s1 = two ? asf(a1[2]) : 0.f;
            unsigned a_rw = l15 < 8 ? L_RED + (((g & 1) * 8 + l15) * 2 + (g >> 1)) * 32 + w * 4 : L_JUNK + l * 4;
            unsigned a_rr = L_RED + (tid & 15) * 64;
            unsigned o_out = (((tid >> 3) & 1) * 512 + 8 * r + (tid & 7)) * 2;
            PIN(s0); PIN(s1); PIN(xin); PIN(a_rw); PIN(a_rr); PIN(o0); PIN(o_out);
            if (w < 4) { hint_wait(rs, WS_T5 + (xc * 128 + (r >> 4) * 32 + 8 * w + (l & 7)) * 128, tag); if (D5) __builtin_amdgcn_s_sleep(D5); }
            STAMP(10);
#pragma nounroll
            for (int it = SPIN_CAP; it > 0; --it) { a0 = ldx(rs, o0); if (none(tagx(a0, tag) != 0u)) break; }
            STAMP(8);
            float v0 = asf(a0[0]), v1 = asf(a0[2]);
            if (w >= 4) { s0 += v0; s1 += v1; v0 = 0.f; v1 = 0.f; }
            float p0 = sum16x2(v0, v1), p1 = sum16x2(s0, s1);
            float t = sum32x2(p0, p1);
            t += dpp<0x128>(t);
            lwr<float>(a_rw, t);
            bar();
            if (tid < 16) {
                const f32x4 t0 = lrd<f32x4>(a_rr), t2 = lrd<f32x4>(a_rr + 32), t3 = lrd<f32x4>(a_rr + 48);
                const float att = (t0[0] + t0[1]) + (t0[2] + t0[3]);
                const float glu = ((t2[0] + t2[1]) + (t2[2] + t2[3])) + ((t3[0] + t3[1]) + (t3[2] + t3[3]));
                *(GAS u16*)((GAS unsigned char*)out + o_out) = (u16)pk(rbf(xin + rbf(att)) + rbf(glu), 0.f);
            }
            if (r == 0 && tid == 0) __hip_atomic_store((GAS unsigned*)ws, tag, __ATOMIC_RELAXED, __HIP_MEMORY_SCOPE_AGENT);
            STAMP(9);
        }
    }
done:
    STAMP(13);
    if (l == 0) {
        GAS unsigned long long* so = (GAS unsigned long long*)(ws + WS_STAMP) + ((size_t)id * 8 + w) * NST;
#pragma unroll
        for (int i = 0; i < NST; ++i) so[i] = st[i];
    }
}

__global__ void k_report(const unsigned long long* stp, int scale) {
    const unsigned long long tin = __builtin_amdgcn_s_memrealtime();
    unsigned long long mn = ~0ull;
    for (int k = 0; k < 4; ++k) { unsigned long long v = stp[(size_t)(threadIdx.x + 64 * k) * 8 * NST]; mn = v < mn ? v : mn; }
    for (int o = 32; o; o >>= 1) { unsigned long long t = __shfl_xor(mn, o); mn = t < mn ? t : mn; }
    const unsigned long long t = tin + (tin - mn) * scale; while (__builtin_amdgcn_s_memrealtime() < t) __builtin_amdgcn_s_sleep(1);
}
extern "C" void kernel_launch(void* const* d_in, const int* in_sizes, int n_in, void* d_out, int out_size, void* d_ws, size_t ws_size,
                              hipStream_t stream) {
    (void)in_sizes; (void)n_in; (void)out_size; (void)ws_size;
    (void)hipFuncSetAttribute((const void*)mla_glu_decode, hipFuncAttributeMaxDynamicSharedMemorySize, L_END);
    mla_glu_decode<<<256, 512, L_END, stream>>>((const u16*)d_in[0], (const u16*)d_in[1], (const u16*)d_in[2], (const u16*)d_in[3], (const u16*)d_in[4],
                                               (const u16*)d_in[5], (const u16*)d_in[7], (const u16*)d_in[8], (const u16*)d_in[9], (u16*)d_out,
                                               (unsigned char*)d_ws);
    k_report<<<1, 64, 0, stream>>>((const unsigned long long*)((char*)d_ws + WS_STAMP), 0);
}
```

```cpp
#include <hip/hip_runtime.h>
#include <hip/hip_bf16.h>

typedef unsigned short u16;
typedef unsigned v4u __attribute__((ext_vector_type(4)));
typedef unsigned v2u __attribute__((ext_vector_type(2)));
typedef float f32x4 __attribute__((ext_vector_type(4)));
typedef short s16x4 __attribute__((ext_vector_type(4)));
typedef __bf16 bf16x8 __attribute__((ext_vector_type(8)));
typedef __bf16 bf16x2 __attribute__((ext_vector_type(2)));
#define LAS __attribute__((address_space(3)))
#define GAS __attribute__((address_space(1)))
#define DEVI __device__ __forceinline__

#define WNT ""
#define BDELAY 40
#define PREW 0
#define D1 10
#define D4 0
#define D2 0
#define D3 28
#define D5 0
#define HSL 1
#define ADELAY 0
#define PROG3 0
#define NOGLU 0
#define HAFTER 0
#define ROT 0
#define GUGAP 16
#define DG 0
#define NLAG 3
#define NOGU 0
#define UNMAP 0
#define W1 3
#define NOHG 0
#define TLBW 0
#define HPOS 0
#define H2POS 3
#define RSL 4
#define P2 90
#define P3 180
#define P4 65
#define P5 70
#define PG 270
#define WDPOS 0
#define STOPAT 99

constexpr int HREP = 8;
constexpr int WS_T2 = 4096;
constexpr int WS_T3 = WS_T2 + HREP * 128 * 128;
constexpr int WS_T5 = WS_T3 + HREP * 128 * 128;
constexpr int WS_T4 = WS_T5 + 64 * 32 * 128;
constexpr int WS_H1 = WS_T4 + 16 * 64 * 128;
constexpr int WS_H4 = WS_H1 + 32 * 4 * 128 * 16;
constexpr int WS_H2 = WS_H4 + 32 * 8 * 128 * 16;
constexpr int WS_HG = WS_H2 + 2 * 32 * 128 * 16;
constexpr int WS_H5 = WS_HG + 2 * 16 * 1376 * 16;
constexpr int WS_H3 = WS_H5 + 118 * 512 * 16;
constexpr int WS_END = WS_H3 + 2 * 16 * 8 * 32 * 384;
constexpr int WS_STAMP = 16 << 20;
constexpr int L_Q = 0;
constexpr int L_GU = 0;
constexpr int L_P = 16384;
constexpr int L_QS = L_P + 4096;
constexpr int L_OW = L_QS + 1024;
constexpr int L_MW = L_OW + 2304;
constexpr int L_LW = L_MW + 64;
constexpr int L_MX = 24576;
constexpr int L_LS = L_MX + 512;
constexpr int L_HH = L_LS + 512;
constexpr int L_TAG = L_HH + 64;
constexpr int L_JUNK = L_TAG + 64;
constexpr int L_RED = L_JUNK + 256;
constexpr int L_XN = 28672;
constexpr int L_I0 = 32768;
constexpr int L_I1 = 65536;
constexpr int L_I2 = 98304;
constexpr int L_END = 163840;
static_assert(L_LW + 64 <= L_MX && L_RED + 1024 <= L_XN && L_XN + 2048 <= L_I0, "lds");
constexpr int SPIN_CAP = 1 << 16;

DEVI unsigned pk(float lo, float hi) { bf16x2 r = {(__bf16)lo, (__bf16)hi}; return __builtin_bit_cast(unsigned, r); }
DEVI float rbf(float f) { return (float)(__bf16)f; }
DEVI float lo16(unsigned w) { return __builtin_bit_cast(float, w << 16); }
DEVI float hi16(unsigned w) { return __builtin_bit_cast(float, w & 0xffff0000u); }
DEVI float asf(unsigned w) { return __builtin_bit_cast(float, w); }
DEVI unsigned asu(float w) { return __builtin_bit_cast(unsigned, w); }
DEVI float mx(float a, float b) { float r; asm("v_max_f32 %0, %1, %2" : "=v"(r) : "v"(a), "v"(b)); return r; }
DEVI float mx3(float a, float b, float c) { float r; asm("v_max3_f32 %0, %1, %2, %3" : "=v"(r) : "v"(a), "v"(b), "v"(c)); return r; }
DEVI f32x4 mfma32(v4u a, v4u b, f32x4 c) { return __builtin_amdgcn_mfma_f32_16x16x32_bf16(__builtin_bit_cast(bf16x8, a), __builtin_bit_cast(bf16x8, b), c, 0, 0, 0); }
DEVI f32x4 mfma16(v2u a, v2u b, f32x4 c) { return __builtin_amdgcn_mfma_f32_16x16x16bf16_1k(__builtin_bit_cast(s16x4, a), __builtin_bit_cast(s16x4, b), c, 0, 0, 0); }
template <class T> DEVI T lrd(unsigned off) { return *(LAS T*)(size_t)off; }
template <class T> DEVI void lwr(unsigned off, T v) { *(LAS T*)(size_t)off = v; }
DEVI v2u trd(unsigned off) { return __builtin_bit_cast(v2u, __builtin_amdgcn_ds_read_tr16_b64_v4i16((LAS s16x4*)(size_t)off)); }
DEVI v4u trd2(unsigned o0, unsigned o1) { v2u a = trd(o0), b = trd(o1); return v4u{a.x, a.y, b.x, b.y}; }
DEVI float sum16(float x) { auto r = __builtin_amdgcn_permlane16_swap(asu(x), asu(x), false, false); return asf(r[0]) + asf(r[1]); }
DEVI float sum32(float x) { auto r = __builtin_amdgcn_permlane32_swap(asu(x), asu(x), false, false); return asf(r[0]) + asf(r[1]); }
DEVI float max16(float x) { auto r = __builtin_amdgcn_permlane16_swap(asu(x), asu(x), false, false); return mx(asf(r[0]), asf(r[1])); }
DEVI float max32(float x) { auto r = __builtin_amdgcn_permlane32_swap(asu(x), asu(x), false, false); return mx(asf(r[0]), asf(r[1])); }
DEVI float sum16x2(float x, float y) { auto r = __builtin_amdgcn_permlane16_swap(asu(x), asu(y), false, false); return asf(r[0]) + asf(r[1]); }
DEVI float sum32x2(float x, float y) { auto r = __builtin_amdgcn_permlane32_swap(asu(x), asu(y), false, false); return asf(r[0]) + asf(r[1]); }
template <int CTRL> DEVI float dpp(float x) { return asf((unsigned)__builtin_amdgcn_update_dpp(0, (int)asu(x), CTRL, 0xf, 0xf, true)); }
DEVI float wavesum(float x) {
    x += dpp<0xB1>(x);
    x += dpp<0x4E>(x);
    x += dpp<0x141>(x);
    x += dpp<0x140>(x);
    return sum32(sum16(x));
}
#define STR_(x) #x
#define STR(x) STR_(x)
#define WAITVM(n) asm volatile("s_waitcnt vmcnt(" STR(n) ")" ::: "memory")
#define PIN(x) asm volatile("" : "+v"(x))
#define FENCE() __builtin_amdgcn_sched_barrier(0)
DEVI void bar() { asm volatile("s_waitcnt lgkmcnt(0)\n\ts_barrier" ::: "memory"); }
DEVI void bar0() { asm volatile("s_barrier" ::: "memory"); }
DEVI void glds_s(unsigned voff, const void* sbase, unsigned lds) {
    unsigned keep;
    asm volatile("s_mov_b32 %0, m0\n\ts_mov_b32 m0, %3\n\ts_nop 0\n\tglobal_load_lds_dwordx4 %1, %2" WNT "\n\ts_mov_b32 m0, %0" : "=&s"(keep) : "v"(voff), "s"(sbase), "s"(lds) : "memory");
}
typedef __amdgpu_buffer_rsrc_t rsrc_t;
DEVI v4u ldx(rsrc_t rs, unsigned off) { return __builtin_amdgcn_raw_buffer_load_b128(rs, off, 0, 16); }
DEVI void stx(rsrc_t rs, unsigned off, v4u v) { __builtin_amdgcn_raw_buffer_store_b128(v, rs, off, 0, 16); }
DEVI bool none(bool bad) { asm volatile("" ::: "memory"); return __builtin_amdgcn_ballot_w64(bad) == 0ull; }
DEVI unsigned tagx(v4u a, unsigned tag) { unsigned r = (a[1] ^ tag) | (a[3] ^ tag); asm("" : "+v"(r)); return r; }
constexpr unsigned OOB = 0x80000000u;
DEVI void get1(v4u& a, unsigned off, v4u rq) { asm volatile("buffer_load_dwordx4 %0, %1, %2, 0 offen sc1\n\ts_waitcnt vmcnt(0)" : "+&v"(a) : "v"(off), "s"(rq) : "memory"); }
DEVI void get2(v4u& a, v4u& b, unsigned oa, unsigned ob, v4u rq) {
    asm volatile("buffer_load_dwordx4 %0, %2, %4, 0 offen sc1\n\tbuffer_load_dwordx4 %1, %3, %4, 0 offen sc1\n\ts_waitcnt vmcnt(0)" : "+&v"(a), "+&v"(b) : "v"(oa), "v"(ob), "s"(rq) : "memory");
}
template <int ST> DEVI void get4(v4u (&a)[4], unsigned off, v4u rq) {
    asm volatile("buffer_load_dwordx4 %0, %4, %5, 0 offen sc1\n\tbuffer_load_dwordx4 %1, %4, %5, 0 offen offset:%6 sc1\n\t"
                 "buffer_load_dwordx4 %2, %4, %5, 0 offen offset:%7 sc1\n\tbuffer_load_dwordx4 %3, %4, %5, 0 offen offset:%8 sc1\n\ts_waitcnt vmcnt(0)"
                 : "+&v"(a[0]), "+&v"(a[1]), "+&v"(a[2]), "+&v"(a[3]) : "v"(off), "s"(rq), "i"(ST), "i"(2 * ST), "i"(3 * ST) : "memory");
}
DEVI void get4s(v4u (&a)[4], unsigned off, v4u rq, unsigned s1, unsigned s2, unsigned s3) {
    asm volatile("buffer_load_dwordx4 %0, %4, %5, 0 offen sc1\n\tbuffer_load_dwordx4 %1, %4, %5, %6 offen sc1\n\t"
                 "buffer_load_dwordx4 %2, %4, %5, %7 offen sc1\n\tbuffer_load_dwordx4 %3, %4, %5, %8 offen sc1\n\ts_waitcnt vmcnt(0)"
                 : "+&v"(a[0]), "+&v"(a[1]), "+&v"(a[2]), "+&v"(a[3]) : "v"(off), "s"(rq), "s"(s1), "s"(s2), "s"(s3) : "memory");
}
DEVI unsigned tagx4(const v4u (&a)[4], unsigned tag) { return (tagx(a[0], tag) | tagx(a[1], tag)) | (tagx(a[2], tag) | tagx(a[3], tag)); }
#define SPIN for (int it_ = SPIN_CAP; it_ > 0; --it_)
DEVI unsigned now() { return (unsigned)__builtin_amdgcn_s_memrealtime(); }
DEVI void hint_put(rsrc_t rs, unsigned off, unsigned tag) { __builtin_amdgcn_raw_buffer_store_b64(v2u{tag, now()}, rs, off, 0, 16); }
DEVI v2u hint_get(v4u rq, unsigned off) { v2u v; asm volatile("buffer_load_dwordx2 %0, %1, %2, 0 offen sc1\n\ts_waitcnt vmcnt(0)" : "=&v"(v) : "v"(off), "s"(rq) : "memory"); return v; }
DEVI bool last_wave(unsigned cnt) { return __builtin_amdgcn_readfirstlane(__hip_atomic_fetch_add((LAS unsigned*)(size_t)cnt, 1u, __ATOMIC_RELAXED, __HIP_MEMORY_SCOPE_WORKGROUP)) == 7u * 64u; }
DEVI void pace(unsigned t, unsigned lead) {
#pragma nounroll
    for (int it = 256; it > 0; --it) { if (none(now() - t < lead)) break; }
}
DEVI unsigned hint_wait(v4u rq, unsigned off, unsigned tag, unsigned lead) {
    v2u v;
#pragma nounroll
    SPIN { v = hint_get(rq, off); if (none(v[0] != tag)) break; if (HSL) __builtin_amdgcn_s_sleep(HSL); }
    pace(v[1], lead);
    return v[1];
}

#define STAMP(i)

DEVI void frags128(unsigned a0, unsigned a1, v4u (&f)[4]) {
#pragma unroll
    for (int s = 0; s < 4; ++s) f[s] = trd2(a0 + s * 8192, a1 + s * 8192);
}
DEVI f32x4 gemv4(const v4u (&a)[4], const v4u (&b)[4]) {
    f32x4 c = {0.f, 0.f, 0.f, 0.f};
#pragma unroll
    for (int s = 0; s < 4; ++s) c = mfma32(a[s], b[s], c);
    return c;
}

DEVI void layernorm(v4u xv, int w, int l) {
    float v[8];
#pragma unroll
    for (int j = 0; j < 4; ++j) { v[2 * j] = lo16(xv[j]); v[2 * j + 1] = hi16(xv[j]); }
    float s = ((v[0] + v[1]) + (v[2] + v[3])) + ((v[4] + v[5]) + (v[6] + v[7]));
    const float mean = wavesum(s) * (1.f / 512.f);
    float sq = 0.f;
#pragma unroll
    for (int j = 0; j < 8; ++j) { const float d = v[j] - mean; sq += d * d; }
    const float var = wavesum(sq) * (1.f / 512.f);
    const float mb = rbf(mean), ri = __builtin_amdgcn_rcpf(rbf(__builtin_amdgcn_sqrtf(rbf(rbf(var) + 1e-5f))));
    v4u o;
#pragma unroll
    for (int j = 0; j < 4; ++j) o[j] = pk(rbf(v[2 * j] - mb) * ri, rbf(v[2 * j + 1] - mb) * ri);
    lwr<v4u>(L_XN + w * 1024 + l * 16, o);
}

__global__ void __launch_bounds__(512, 2)
mla_glu_decode(const u16* __restrict__ x, const u16* __restrict__ ckv, const u16* __restrict__ wq, const u16* __restrict__ wuk,
               const u16* __restrict__ wuv, const u16* __restrict__ wo, const u16* __restrict__ wg, const u16* __restrict__ wu,
               const u16* __restrict__ wd, u16* __restrict__ out, unsigned char* __restrict__ ws) {
    STAMP(0);
    const int tid = threadIdx.x, l = tid & 63, w = __builtin_amdgcn_readfirstlane(tid >> 6);
    const int id = blockIdx.x, xc = id & 7, y = id >> 3, yy = y & 15;
    const int un = UNMAP ? xc * 16 + yy : yy * 8 + xc;
    const unsigned g = l >> 4, l15 = l & 15;
    asm volatile("" :: "s"(x), "s"(ckv), "s"(wq), "s"(wuk), "s"(wuv), "s"(wo), "s"(wg), "s"(wu), "s"(wd), "s"(out), "s"(ws));
    const rsrc_t rs = __builtin_amdgcn_make_buffer_rsrc(ws, 0, WS_END, 0x00020000);
    const v4u rsq = {(unsigned)(size_t)ws, (unsigned)((size_t)ws >> 32) & 0xffffu, (unsigned)WS_END, 0x00020000u};
    const v4u Z = {0u, 0u, 0u, 0u};
    const f32x4 ZF = {0.f, 0.f, 0.f, 0.f};

    if (__builtin_expect(y < 16, 1)) {
        const int h = 4 * xc + (yy >> 2), cq = yy & 3;
        const int hu = (h & ~3) | ((h + ROT) & 3);
        v4u xv; unsigned ep;
        if (w < 2) {
            asm volatile("global_load_dwordx4 %0, %1, %2" : "=v"(xv) : "v"(w * 1024 + l * 16), "s"(x) : "memory");
            asm volatile("global_load_dword %0, %1, %2 sc1" : "=v"(ep) : "v"(0), "s"(ws) : "memory");
        }
        const unsigned vb = (128 * cq + 16 * w + g) * 8192 + h * 256;
#pragma unroll
        for (int i = 0; i < 4; ++i) glds_s(vb + i * 32768 + ((l15 ^ ((g << 2) | i)) << 4), wq, L_I0 + (16 * w + 4 * i) * 256);
        bar0();
#pragma unroll
        for (int i = 0; i < 4; ++i) glds_s(vb + (hu - h) * 256 + i * 32768 + ((l15 ^ (4 * i + g)) << 4), wuk, L_I1 + (16 * w + 4 * i) * 256);
        unsigned o_p1 = l < 16 ? WS_H1 + ((h * 4 + cq) * 128 + 16 * w + l) * 16 : OOB;
        unsigned o_g1 = WS_H1 + ((hu * 4 + g) * 128 + 16 * w + l15) * 16;
        unsigned o_h2 = l15 < 2 ? WS_H2 + ((l15 * 32 + hu) * 128 + 32 * cq + 4 * w + g) * 16 : OOB;
        unsigned o_t2 = l < HREP ? WS_T2 + (l * 128 + hu * 4 + cq) * 128 : OOB;
        unsigned tw0, tw1, tw2, tw3;
        if (TLBW && w == 2) {
            asm volatile("global_load_dword %0, %4, %8\n\tglobal_load_dword %1, %5, %9\n\tglobal_load_dword %2, %6, %10\n\tglobal_load_dword %3, %7, %11 sc1"
                         : "=&v"(tw0), "=&v"(tw1), "=&v"(tw2), "=&v"(tw3)
                         : "v"((64 * (yy & 7) + (l & 1) * 63) * 8192 + (h & ~1) * 256), "v"(h * 131072 + cq * 256), "v"((32 * ((un >> 2) & 15) + (l & 1) * 31) * 2752 + (un & 3) * 688), "v"((l & 7) * (1 << 20) + 2048),
                           "s"(wuv), "s"(wo), "s"(un >> 6 ? wu : wg), "s"(ws) : "memory");
        }
        STAMP(1);
        if (w < 2) {
            asm volatile("s_waitcnt vmcnt(8)" : "+v"(xv), "+v"(ep) :: "memory");
            layernorm(xv, w, l);
            unsigned t = ep + 1u; t += (t & 0xffffu) == 0u;
            lwr<v4u>(L_TAG, v4u{t, 0u, 0u, 0u});
        }
        bar();
        const unsigned tag = lrd<unsigned>(L_TAG);
        unsigned a0, a1;
        {
            const unsigned q = l15 >> 2, p = l & 3, ch = 2 * w + (p >> 1);
            a0 = L_I0 + (8 * g + q) * 256 + 16 * (ch ^ ((q << 2) | ((2 * g) & 3))) + 8 * (p & 1);
            a1 = L_I0 + (8 * g + q + 4) * 256 + 16 * (ch ^ ((q << 2) | ((2 * g + 1) & 3))) + 8 * (p & 1);
        }
        unsigned a_qw = L_QS + (g & 1) * 272 + (16 * w + l15) * 2;
        unsigned a_qr = L_QS + (l & 1) * 272 + 16 * g;
        unsigned a_uk[4];
        v4u xf[4];
#pragma unroll
        for (int s = 0; s < 4; ++s) {
            xf[s] = lrd<v4u>(L_XN + (l & 1) * 1024 + (128 * cq + 32 * s + 8 * g) * 2);
            a_uk[s] = L_I1 + (16 * w + l15) * 256 + (((4 * s + g) ^ l15) << 4);
            PIN(xf[s]); PIN(a_uk[s]);
        }
        const int hp = h >> 1, e = yy & 7;
        unsigned o_gu[3];
#pragma unroll
        for (int i = 0; i < 3; ++i) {
            const unsigned pc = min(8 * i + w, 21), idx = min(64 * pc + l, 1375u), row = idx / 43u, ch = idx - row * 43u;
            o_gu[i] = (32 * ((un >> 2) & 15) + row) * 2752 + (un & 3) * 688 + ch * 16; PIN(o_gu[i]);
        }
        v4u pb = {0u, tag, 0u, tag};
        PIN(a0); PIN(a1); PIN(o_p1); PIN(o_g1); PIN(o_h2); PIN(o_t2); PIN(a_qw); PIN(a_qr); PIN(pb);
        if (TLBW && w == 2) WAITVM(8); else WAITVM(4);
        bar0();
        STAMP(2);
        {
            v4u f[4]; frags128(a0, a1, f);
            FENCE();
            const f32x4 acc = gemv4(xf, f);
            pb[0] = asu(acc[0]); pb[2] = asu(acc[1]);
            stx(rs, o_p1, pb);
        }
        STAMP(3);
        if (STOPAT <= 3) { WAITVM(0); STAMP(14); goto done; }
        if (D1) __builtin_amdgcn_s_sleep(D1);
        if (TLBW && w == 2) asm volatile("s_waitcnt vmcnt(1)" : "+v"(tw0), "+v"(tw1), "+v"(tw2), "+v"(tw3) :: "memory"); else WAITVM(W1);
        v4u fu[4];
        {
            v4u a = Z;
            if (H2POS == 3 && last_wave(L_TAG + 8)) hint_put(rs, o_t2, tag);
            get1(a, o_g1, rsq);
            STAMP(14);
#pragma unroll
            for (int s = 0; s < 4; ++s) fu[s] = lrd<v4u>(a_uk[s]);
            if (__builtin_expect(!none(tagx(a, tag) != 0u), 0)) {
#pragma nounroll
                SPIN { __builtin_amdgcn_s_sleep(4); get1(a, o_g1, rsq); if (none(tagx(a, tag) != 0u)) break; }
            }
            if (H2POS == 0 && last_wave(L_TAG + 8)) hint_put(rs, o_t2, tag);
            lwr<u16>(a_qw, (u16)pk(sum32(sum16x2(asf(a[0]), asf(a[2]))), 0.f));
        }
        STAMP(4);
        bar();
        {
            v4u qf[4];
#pragma unroll
            for (int s = 0; s < 4; ++s) qf[s] = lrd<v4u>(a_qr + 64 * s);
            FENCE();
            const f32x4 acc = gemv4(fu, qf);
            constexpr float SC = 0.08838834764831845f * 1.4426950408889634f;
            pb[0] = pk(acc[0] * SC, acc[1] * SC); pb[2] = pk(acc[2] * SC, acc[3] * SC);
            stx(rs, o_h2, pb);
        }
        STAMP(5);
        if (STOPAT <= 5) goto done;
        unsigned o_t3 = WS_T3 + (xc * 128 + (h >> 4) * 64 + 8 * w + (l & 7)) * 128;
        const int r = xc * 8 + (yy >> 1);
        unsigned o_t5 = WS_T5 + (r * 32 + (l & 31)) * 128;
        unsigned j0, j1;
        {
            asm volatile("buffer_load_dword %0, %2, %4, 0 offen sc1\n\tbuffer_load_dword %1, %3, %4, 0 offen sc1" : "=&v"(j0), "=&v"(j1) : "v"(o_t3), "v"((w == 0 && !(yy & 1)) ? o_t5 : o_t3), "s"(rsq) : "memory");
        }
        if (ADELAY) __builtin_amdgcn_s_sleep(ADELAY);
        const int mat = un >> 6, mp = (un >> 2) & 15, fp = un & 3;
#pragma unroll
        for (int i = 0; i < 3; ++i) glds_s(o_gu[i], mat ? wu : wg, L_I2 + min(8 * i + w, 21) * 1024);
#define V_ISSUE(i) glds_s((64 * e + 16 * (w & 3) + 4 * (i) + g) * 8192 + (2 * hp + (w >> 2)) * 256 + ((l15 ^ ((g << 2) | (i))) << 4), wuv, L_I1 + (16 * w + 4 * (i)) * 256)
        V_ISSUE(0); V_ISSUE(1);
        unsigned o_h4 = l < 32 ? WS_H4 + (((2 * hp + (w >> 2)) * 8 + e) * 128 + 32 * (w & 3) + l) * 16 : OOB;
        {
            const v4u xg = lrd<v4u>(L_XN + (l & 1) * 1024 + (32 * mp + 8 * g) * 2);
            const unsigned ag = L_I2 + (8 * g + (l15 >> 2)) * 688 + (l & 3) * 8;
            const unsigned f = 16 * (8 * g + w) + l15;
            const unsigned o_hg = (g < 3 && f < 344) ? WS_HG + ((mat * 16 + mp) * 1376 + 344 * fp + f) * 16 : OOB;
            WAITVM(2);
            bar();
            V_ISSUE(2); V_ISSUE(3);
            f32x4 o[3];
#pragma unroll
            for (int t = 0; t < 3; ++t) {
                const unsigned tl = min(8 * t + w, 21);
                o[t] = mfma32(xg, trd2(ag + tl * 32, ag + tl * 32 + 4 * 688), ZF);
            }
            const float o0 = g == 0 ? o[0][0] : g == 1 ? o[1][0] : o[2][0], o1 = g == 0 ? o[0][1] : g == 1 ? o[1][1] : o[2][1];
            stx(rs, o_hg, v4u{asu(o0), tag, asu(o1), tag});
        }
        STAMP(6);
        asm volatile("s_waitcnt vmcnt(0)" : "+v"(j0), "+v"(j1) :: "memory");
        bar();
        v4u fv[2][2];
        {
            const unsigned q = l15 >> 2, p = l & 3;
#pragma unroll
            for (int t = 0; t < 2; ++t) {
                const unsigned ch = 4 * (w & 3) + 2 * t + (p >> 1), base = L_I1 + (w >> 2) * 16384 + (8 * g + q) * 256 + 8 * (p & 1);
#pragma unroll
                for (int s = 0; s < 2; ++s)
                    fv[t][s] = trd2(base + s * 8192 + 16 * (ch ^ ((q << 2) | ((2 * g) & 3))), base + s * 8192 + 1024 + 16 * (ch ^ ((q << 2) | ((2 * g + 1) & 3))));
            }
        }
        const unsigned u = l15 < 12 ? l15 : l15 - 4;
        unsigned o_h3 = WS_H3 + ((((w >> 2) * 16 + hp) * 8 + e) * 32 + 8 * (w & 3) + (g >> 1)) * 384 + (u >> 2) * 128 + (g & 1) * 64 + (u & 3) * 16;
        unsigned o_p4 = l < 8 ? WS_T4 + ((hp * 8 + l) * 8 + e) * 128 : OOB;
        unsigned a_ow, a_ow2, a_ms;
        {
            const unsigned n = 6 * (l15 >> 2) + 2 * (g >> 1), n2 = 6 * (l15 >> 2) + 4, gg = l15 & 3;
            a_ow = l15 < 12 ? L_OW + w * 272 + (64 * (g & 1) + 16 * gg + 4 * ((n >> 2) ^ (gg & 1)) + (n & 3)) * 2 : L_JUNK + l * 4;
            a_ow2 = (l15 < 8 && l < 32) ? L_OW + w * 272 + (64 * (g & 1) + 16 * gg + 4 * ((n2 >> 2) ^ (gg & 1)) + (n2 & 3)) * 2 : L_JUNK + l * 4;
            a_ms = L_MW + (g >> 1) * 64 + (g & 1) * 32 + w * 4;
        }
        unsigned a_or = L_OW + (l & 7) * 272 + (w >> 2) * 128 + 16 * g, a_mw = L_MW + (w >> 2) * 32 + (g & 1) * 16;
        unsigned TT = tag << 16, a_cnt = l == 0 ? L_TAG + 12 : L_JUNK + l * 4, one = 1u;
        PIN(a_cnt); PIN(one);
        PIN(o_h3); PIN(o_t3); PIN(o_p4); PIN(o_h4); PIN(a_ow); PIN(a_ow2); PIN(a_or); PIN(a_mw); PIN(a_ms); PIN(TT);
        PIN(fv[0][0]); PIN(fv[0][1]); PIN(fv[1][0]); PIN(fv[1][1]);
        STAMP(7);
        if (STOPAT <= 7) goto done;
        unsigned t3;
        {
            v4u a[4] = {Z, Z, Z, Z};
            t3 = hint_wait(rsq, o_t3, tag, P3);
            STAMP(15);
#pragma nounroll
            SPIN { get4<768>(a, o_h3, rsq); if (none(tagx4(a, TT) > 0xffffu)) break; __builtin_amdgcn_s_sleep(4); }
            STAMP(8);
            unsigned arr;
            asm volatile("ds_add_rtn_u32 %0, %1, %2" : "=&v"(arr) : "v"(a_cnt), "v"(one) : "memory");
            float m[4], ls[4];
#pragma unroll
            for (int k = 0; k < 4; ++k) { const float sv = asf(__builtin_amdgcn_alignbit(a[k][3], a[k][2], 16)); m[k] = dpp<0x158>(sv); ls[k] = dpp<0x159>(sv); }
            const float M = max32(mx(mx3(m[0], m[1], m[2]), m[3]));
            FENCE();
            asm volatile("s_waitcnt lgkmcnt(0)" : "+v"(arr) :: "memory");
            if (__builtin_amdgcn_readfirstlane(arr) == 7u) hint_put(rs, o_p4, tag);
            float acc[6], Ls;
#pragma unroll
            for (int k = 0; k < 4; ++k) {
                const float al = __builtin_amdgcn_exp2f(m[k] - M);
                const float v[7] = {lo16(a[k][0]), hi16(a[k][0]), lo16(a[k][1]), lo16(a[k][2]), hi16(a[k][2]), lo16(a[k][3]), ls[k]};
                if (k == 0) { for (int j = 0; j < 6; ++j) acc[j] = al * v[j]; Ls = al * v[6]; }
                else { for (int j = 0; j < 6; ++j) acc[j] = __builtin_fmaf(al, v[j], acc[j]); Ls = __builtin_fmaf(al, v[6], Ls); }
            }
            const float v4 = sum32x2(acc[4], Ls);
            lwr<unsigned>(a_ow, pk(sum32x2(acc[0], acc[2]), sum32x2(acc[1], acc[3])));
            lwr<unsigned>(a_ow2, pk(v4, sum32(acc[5])));
            lwr<float>(a_ms, l < 32 ? M : v4);
        }
        bar();
        {
            const v4u of0 = lrd<v4u>(a_or), of1 = lrd<v4u>(a_or + 64);
            const f32x4 mw = lrd<f32x4>(a_mw), lw = lrd<f32x4>(a_mw + 64);
            FENCE();
            const f32x4 c0 = mfma32(of1, fv[0][1], mfma32(of0, fv[0][0], ZF)), c1 = mfma32(of1, fv[1][1], mfma32(of0, fv[1][0], ZF));
            const float M = mx(mx3(mw[0], mw[1], mw[2]), mw[3]);
            float be[4];
#pragma unroll
            for (int r = 0; r < 4; ++r) be[r] = __builtin_amdgcn_exp2f(mw[r] - M);
            const float rd = __builtin_amdgcn_rcpf((be[0] * lw[0] + be[1] * lw[1]) + (be[2] * lw[2] + be[3] * lw[3]));
            const float v0 = ((be[0] * c0[0] + be[1] * c0[1]) + (be[2] * c0[2] + be[3] * c0[3])) * rd;
            const float v1 = ((be[0] * c1[0] + be[1] * c1[1]) + (be[2] * c1[2] + be[3] * c1[3])) * rd;
            auto r = __builtin_amdgcn_permlane16_swap(asu(v0), asu(v1), false, false);
            pb[0] = r[0]; pb[2] = r[1];
            stx(rs, o_h4, pb);
        }
        STAMP(9);
        if ((yy & 1) == 0) {
            const unsigned o1 = WS_H5 + ((32 + (tid >> 3)) * 512 + 8 * r + (tid & 7)) * 16;
            const bool two = tid < 176;
            const unsigned o2 = two ? o1 + 64 * 512 * 16 : o1;
            v4u a1 = Z, a2 = Z;
            pace(t3, PG);
#pragma nounroll
            SPIN { get2(a1, a2, o1, o2, rsq); if (none((tagx(a1, tag) | tagx(a2, tag)) != 0u)) break; __builtin_amdgcn_s_sleep(12); }
            {
                float t = sum32(sum16x2(asf(a1[0]) + (two ? asf(a2[0]) : 0.f), asf(a1[2]) + (two ? asf(a2[2]) : 0.f)));
                t += dpp<0x128>(t);
                lwr<float>(L_RED + ((g & 1) * 8 + (l & 7)) * 32 + w * 4, t);
            }
            bar();
            STAMP(10);
            if (w == 0) {
                const f32x4 t2 = lrd<f32x4>(L_RED + ((g & 1) * 8 + (l & 7)) * 32), t3 = lrd<f32x4>(L_RED + ((g & 1) * 8 + (l & 7)) * 32 + 16);
                float glu = rbf(((t2[0] + t2[1]) + (t2[2] + t2[3])) + ((t3[0] + t3[1]) + (t3[2] + t3[3])));
                float xin = lo16(x[(g & 1) * 512 + 8 * r + (l & 7)]);
                unsigned o0 = WS_H5 + ((l >> 3) * 512 + 8 * r + (l & 7)) * 16;
                unsigned o_out = ((g & 1) * 512 + 8 * r + (l & 7)) * 2;
                PIN(glu); PIN(xin); PIN(o0); PIN(o_out);
                hint_wait(rsq, o_t5, tag, P5);
                STAMP(11);
                v4u a[4] = {Z, Z, Z, Z};
#pragma nounroll
                SPIN { get4s(a, o0, rsq, 65536, 131072, 196608); if (none(tagx4(a, tag) != 0u)) break; }
                STAMP(12);
                float t = sum32(sum16x2((asf(a[0][0]) + asf(a[1][0])) + (asf(a[2][0]) + asf(a[3][0])), (asf(a[0][2]) + asf(a[1][2])) + (asf(a[2][2]) + asf(a[3][2]))));
                t += dpp<0x128>(t);
                if (l < 32 && l15 < 8) __hip_atomic_store((GAS u16*)((GAS unsigned char*)out + o_out), (u16)pk(rbf(xin + rbf(t)) + glu, 0.f), __ATOMIC_RELAXED, __HIP_MEMORY_SCOPE_AGENT);
                if (r == 0 && l == 0) __hip_atomic_store((GAS unsigned*)ws, tag, __ATOMIC_RELAXED, __HIP_MEMORY_SCOPE_AGENT);
            }
        }
    } else {
        const int pr = xc * 8 + (yy >> 1), hg = yy & 1, b = pr >> 5, sp = pr & 31;
        unsigned ep, j0;
        unsigned o_t2 = WS_T2 + (xc * 128 + 64 * hg + l) * 128;
        if (w == 0) {
            asm volatile("global_load_dword %0, %1, %2 sc1" : "=v"(ep) : "v"(0), "s"(ws) : "memory");
            asm volatile("buffer_load_dword %0, %1, %2, 0 offen sc1" : "=v"(j0) : "v"(o_t2), "s"(rsq) : "memory");
        }
        if (TLBW && w == 1) {
            unsigned t0, t1, t2;
            asm volatile("global_load_dword %0, %3, %5\n\tglobal_load_dword %1, %4, %6 sc1\n\tglobal_load_dword %2, %7, %8\n\ts_waitcnt vmcnt(0)"
                         : "=&v"(t0), "=&v"(t1), "=&v"(t2) : "v"(min(un, 85) * 16384), "v"((l & 7) * (1 << 20) + 2048), "s"(wd), "s"(ws), "v"(0), "s"(out) : "memory");
        }
        if (BDELAY) __builtin_amdgcn_s_sleep(BDELAY);
#pragma unroll
        for (int i = 0; i < 8; ++i) glds_s((pr * 128 + 64 * hg + 8 * w + i) * 1024 + ((l ^ i) << 4), ckv, L_I0 + (16 * w + i) * 1024);
        if (w == 0) {
            asm volatile("s_waitcnt vmcnt(8)" : "+v"(ep), "+v"(j0) :: "memory");
            unsigned t = ep + 1u; t += (t & 0xffffu) == 0u;
            lwr<unsigned>(L_TAG, t);
        }
        unsigned qoff = WS_H2 + ((b * 32 + 16 * hg + 2 * w) * 128 + l) * 16;
        unsigned o_t3 = tid < 4 ? WS_T3 + ((4 * hg + tid) * 128 + hg * 64 + pr) * 128 : OOB;
        unsigned o_h3 = WS_H3 + (((b * 16 + 8 * hg + (l15 >> 1)) * 8 + w) * 32 + sp) * 384 + (l & 1) * 64 + g * 16;
        unsigned a_r[4], a_t[4];
#pragma unroll
        for (int j = 0; j < 4; ++j) { a_r[j] = l15 * 1024 + (((4 * j + g) ^ l15) << 4); PIN(a_r[j]); }
        {
            const unsigned q = l15 >> 2, p = l & 3;
#pragma unroll
            for (int j = 0; j < 4; ++j) {
                const unsigned r0 = 8 * g + 4 * (j & 1) + q, ch = 8 * w + 2 * p + (j >> 1);
                a_t[j] = L_I0 + r0 * 1024 + ((ch ^ (r0 & 15)) << 4) + (p & 1) * 8;
                PIN(a_t[j]);
            }
        }
        unsigned a_q0 = L_Q + 2 * w * 1024 + (((l >> 1) ^ (2 * w)) << 4) + (l & 1) * 8, a_q1 = L_Q + (2 * w + 1) * 1024 + (((l >> 1) ^ (2 * w + 1)) << 4) + (l & 1) * 8;
        unsigned a_mxw = L_MX + l15 * 32 + w * 4, a_mxr = L_MX + l15 * 32;
        unsigned a_pw = L_P + l15 * 256 + (((2 * w + (g >> 1)) ^ l15) << 4) + (g & 1) * 8;
        unsigned a_pr[4];
#pragma unroll
        for (int j = 0; j < 4; ++j) { a_pr[j] = L_P + l15 * 256 + (((4 * j + g) ^ l15) << 4); PIN(a_pr[j]); }
        const int h = 4 * xc + (yy >> 2), cq = yy & 3;
        unsigned o_wd[2];
#pragma unroll
        for (int i = 0; i < 2; ++i) { o_wd[i] = (16 * min(un, 85) + 2 * w + i) * 1024 + ((l ^ (((2 * w + i) & 7) << 1)) << 4); PIN(o_wd[i]); }
        unsigned o_wo[4];
#pragma unroll
        for (int i = 0; i < 4; ++i) {
            const unsigned ch = l15 ^ ((g << 2) | i);
            o_wo[i] = (h * 128 + 16 * w + 4 * i + g) * 1024 + cq * 128 + (ch >> 3) * 512 + (ch & 7) * 16; PIN(o_wo[i]);
        }
        unsigned o_t4 = WS_T4 + (((h >> 1) * 8 + (yy & 7)) * 8 + (l & 7)) * 128;
        unsigned o_hg = un < 86 ? WS_HG + (((w >> 2) * 16 + 4 * (w & 3) + g) * 1376 + 16 * un + l15) * 16 : o_t4;
        v4u ga; unsigned j4;
        PIN(o_t4); PIN(o_hg);
        PIN(qoff); PIN(o_t2); PIN(o_t3); PIN(o_h3); PIN(a_q0); PIN(a_q1); PIN(a_mxw); PIN(a_mxr); PIN(a_pw);
        bar();
#pragma unroll
        for (int i = 8; i < 16; ++i) glds_s((pr * 128 + 64 * (1 - hg) + 8 * w + (i & 7)) * 1024 + ((l ^ i) << 4), ckv, L_I0 + (16 * w + i) * 1024);
        STAMP(1);
        const unsigned tag = lrd<unsigned>(L_TAG);
        if (w == 0) {
            WAITVM(2);
            const v2u hv = hint_get(rsq, o_t2);
            lwr<unsigned>(L_TAG + 4, none(hv[0] != tag || now() - hv[1] < P2 - 10) ? 1u : 0u);
        }
        WAITVM(0);
        STAMP(14);
        v4u cr[16], ct[4][4], wst[4];
#pragma unroll
        for (int s = 0; s < 16; ++s) cr[s] = lrd<v4u>(a_r[s & 3] + (L_I0 + w * 16384) + (s >> 2) * 256);
        bar();
        STAMP(2);
        if (STOPAT <= 5) goto done;
        {
            if (__builtin_amdgcn_readfirstlane(lrd<unsigned>(L_TAG + 4)) == 0u) {
                if (w == 0) hint_wait(rsq, o_t2, tag, P2);
                bar0();
            }
            STAMP(15);
            v4u qa[4];
            asm volatile("buffer_load_dwordx4 %0, %4, %5, 0 offen sc1\n\tbuffer_load_dwordx4 %1, %4, %5, 0 offen offset:1024 sc1\n\t"
                         "buffer_load_dwordx4 %2, %4, %5, 0 offen offset:2048 sc1\n\tbuffer_load_dwordx4 %3, %4, %5, 0 offen offset:3072 sc1"
                         : "=&v"(qa[0]), "=&v"(qa[1]), "=&v"(qa[2]), "=&v"(qa[3]) : "v"(qoff), "s"(rsq) : "memory");
            bar0();
            asm volatile("global_load_dwordx4 %0, %4, %8" WNT "\n\tglobal_load_dwordx4 %1, %5, %8" WNT "\n\tglobal_load_dwordx4 %2, %6, %8" WNT "\n\tglobal_load_dwordx4 %3, %7, %8" WNT
                         : "=&v"(wst[0]), "=&v"(wst[1]), "=&v"(wst[2]), "=&v"(wst[3]) : "v"(o_wo[0]), "v"(o_wo[1]), "v"(o_wo[2]), "v"(o_wo[3]), "s"(wo) : "memory");
#pragma unroll
            for (int t = 0; t < 4; ++t)
#pragma unroll
                for (int s = 0; s < 4; ++s) {
                    const unsigned x8 = (t & 1) * 8;
                    ct[t][s] = trd2((a_t[2 * (t >> 1)] ^ x8) + s * 32768, (a_t[2 * (t >> 1) + 1] ^ x8) + s * 32768);
                }
            FENCE();
            asm volatile("s_waitcnt vmcnt(4)" : "+v"(qa[0]), "+v"(qa[1]), "+v"(qa[2]), "+v"(qa[3]) :: "memory");
            if (__builtin_expect(!none(tagx4(qa, tag) != 0u), 0)) {
#pragma nounroll
                SPIN { if (RSL) __builtin_amdgcn_s_sleep(RSL); get4<1024>(qa, qoff, rsq); if (none(tagx4(qa, tag) != 0u)) break; }
            }
            STAMP(3);
            asm volatile("ds_write2_b32 %0, %2, %3 offset1:1\n\tds_write2_b32 %0, %4, %5 offset0:128 offset1:129\n\tds_write2_b32 %1, %6, %7 offset1:1\n\tds_write2_b32 %1, %8, %9 offset0:128 offset1:129"
                         :: "v"(a_q0), "v"(a_q1), "v"(qa[0][0]), "v"(qa[0][2]), "v"(qa[1][0]), "v"(qa[1][2]), "v"(qa[2][0]), "v"(qa[2][2]), "v"(qa[3][0]), "v"(qa[3][2]) : "memory");
        }
        bar();
        STAMP(4);
        f32x4 sa = ZF, sb = ZF;
        {
            v4u qf[16];
#pragma unroll
            for (int s = 0; s < 16; ++s) qf[s] = lrd<v4u>(a_r[s & 3] + L_Q + (s >> 2) * 256);
            FENCE();
            hint_put(rs, o_t3, tag);
#pragma unroll
            for (int i = 0; i < 2; ++i) glds_s(o_wd[i], wd, L_I0 + (2 * w + i) * 1024);
            FENCE();
#pragma unroll
            for (int s = 0; s < 16; s += 2) { sa = mfma32(cr[s], qf[s], sa); sb = mfma32(cr[s + 1], qf[s + 1], sb); }
        }
        sa += sb;
        lwr<float>(a_mxw, max32(max16(mx(mx3(sa[0], sa[1], sa[2]), sa[3]))));
        bar();
        STAMP(12);
        float mrow;
        {
            const f32x4 m0 = lrd<f32x4>(a_mxr), m1 = lrd<f32x4>(a_mxr + 16);
            mrow = mx(mx3(mx3(m0[0], m0[1], m0[2]), m0[3], m1[0]), mx3(m1[1], m1[2], m1[3]));
            float p[4];
#pragma unroll
            for (int r = 0; r < 4; ++r) p[r] = __builtin_amdgcn_exp2f(sa[r] - mrow);
            lwr<v2u>(a_pw, v2u{pk(p[0], p[1]), pk(p[2], p[3])});
        }
        bar();
        STAMP(5);
        {
            const unsigned T16 = tag & 0xffffu;
            v4u pf[4];
#pragma unroll
            for (int s = 0; s < 4; ++s) pf[s] = lrd<v4u>(a_pr[s]);
            FENCE();
            f32x4 o[4];
#define TILE(t) do { o[t] = ZF; _Pragma("unroll") for (int s = 0; s < 4; ++s) o[t] = mfma32(ct[t][s], pf[s], o[t]); FENCE(); } while (0)
#define UNIT(i, A, B, C) stx(rs, o_h3 + (i) * 128, v4u{A, __builtin_amdgcn_perm(T16, B, 0x05040100u), __builtin_amdgcn_alignbit(C, B, 16), __builtin_amdgcn_perm(T16, C, 0x05040302u)})
            TILE(0); TILE(1);
            float ls = 0.f, ls2 = 0.f;
#pragma unroll
            for (int s = 0; s < 4; ++s) {
                const unsigned p0 = pf[s][0], p1 = pf[s][1], p2 = pf[s][2], p3 = pf[s][3], one = 0x3f803f80u;
                ls = __builtin_amdgcn_fdot2_f32_bf16(__builtin_bit_cast(bf16x2, p0), __builtin_bit_cast(bf16x2, one), ls, false);
                ls2 = __builtin_amdgcn_fdot2_f32_bf16(__builtin_bit_cast(bf16x2, p1), __builtin_bit_cast(bf16x2, one), ls2, false);
                ls = __builtin_amdgcn_fdot2_f32_bf16(__builtin_bit_cast(bf16x2, p2), __builtin_bit_cast(bf16x2, one), ls, false);
                ls2 = __builtin_amdgcn_fdot2_f32_bf16(__builtin_bit_cast(bf16x2, p3), __builtin_bit_cast(bf16x2, one), ls2, false);
            }
            asm volatile("buffer_load_dwordx4 %0, %2, %4, 0 offen sc1\n\tbuffer_load_dword %1, %3, %4, 0 offen sc1" : "=&v"(ga), "=&v"(j4) : "v"(o_hg), "v"(o_t4), "s"(rsq) : "memory");
            FENCE();
            TILE(2);
            const unsigned d0 = pk(o[0][0], o[0][1]), d1 = pk(o[0][2], o[0][3]), d2 = pk(o[1][0], o[1][1]), d3 = pk(o[1][2], o[1][3]);
            UNIT(0, d0, d1, d2);
            FENCE();
            TILE(3);
            float lsum = sum32(sum16(ls + ls2)); PIN(lsum);
            const unsigned d8 = asu((g & 1) ? lsum : mrow);
            const unsigned d4 = pk(o[2][0], o[2][1]), d5 = pk(o[2][2], o[2][3]);
            UNIT(1, d3, d4, d5);
            FENCE();
            const unsigned d6 = pk(o[3][0], o[3][1]), d7 = pk(o[3][2], o[3][3]);
            UNIT(2, d6, d7, d8);
        }
        STAMP(6);
        if (STOPAT <= 7 || NOGLU) goto done;
        asm volatile("s_waitcnt vmcnt(3)" : "+v"(ga), "+v"(j4), "+v"(wst[0]), "+v"(wst[1]), "+v"(wst[2]), "+v"(wst[3]) :: "memory");
#pragma unroll
        for (int i = 0; i < 4; ++i) lwr<v4u>(L_I1 + (16 * w + 4 * i) * 256 + l * 16, wst[i]);
        if (un < 86) {
            {
                if (__builtin_expect(!none(tagx(ga, tag) != 0u), 0)) {
#pragma nounroll
                    SPIN { __builtin_amdgcn_s_sleep(16); get1(ga, o_hg, rsq); if (none(tagx(ga, tag) != 0u)) break; }
                }
                lwr<float>(L_RED + (l & 31) * 32 + w * 4, sum32(sum16x2(asf(ga[0]), asf(ga[2]))));
            }
            bar();
            if (w == 0) {
                const f32x4 gp = lrd<f32x4>(L_RED + (l & 31) * 32), up = lrd<f32x4>(L_RED + (l & 31) * 32 + 16);
                const float gg = rbf((gp[0] + gp[1]) + (gp[2] + gp[3])), uu = rbf((up[0] + up[1]) + (up[2] + up[3]));
                const float sg = rbf(__builtin_amdgcn_rcpf(1.f + __builtin_amdgcn_exp2f(gg * -1.4426950408889634f)));
                lwr<u16>(L_HH + (l & 31) * 2, (u16)pk(sg * uu, 0.f));
            }
            bar();
            const v2u ha = lrd<v2u>(L_HH + (l & 1) * 32 + g * 8);
            const unsigned q = l15 >> 2, p = l & 3, row = 4 * g + q;
            f32x4 o[4];
#pragma unroll
            for (int t = 0; t < 4; ++t) {
                const unsigned ch = 8 * w + 2 * t + (p >> 1);
                o[t] = mfma16(ha, trd(L_I0 + row * 1024 + ((ch ^ ((row & 7) << 1)) << 4) + (p & 1) * 8), ZF);
            }
            const float o0 = g == 0 ? o[0][0] : g == 1 ? o[1][0] : g == 2 ? o[2][0] : o[3][0];
            const float o1 = g == 0 ? o[0][1] : g == 1 ? o[1][1] : g == 2 ? o[2][1] : o[3][1];
            stx(rs, WS_H5 + ((32 + un) * 512 + 64 * w + l) * 16, v4u{asu(o0), tag, asu(o1), tag});
        } else bar();
        STAMP(7);
        {
            v4u fw[4];
            {
                const unsigned q = l15 >> 2, p = l & 3, ch = 2 * w + (p >> 1);
                frags128(L_I1 + (8 * g + q) * 256 + 16 * (ch ^ ((q << 2) | ((2 * g) & 3))) + 8 * (p & 1),
                         L_I1 + (8 * g + q + 4) * 256 + 16 * (ch ^ ((q << 2) | ((2 * g + 1) & 3))) + 8 * (p & 1), fw);
            }
            unsigned o_g4 = WS_H4 + ((h * 8 + g) * 128 + 16 * w + l15) * 16, o_g4b = o_g4 + 8192;
            unsigned o_t5 = tid < 16 ? WS_T5 + ((8 * (cq + 4 * (tid >> 3)) + (tid & 7)) * 32 + h) * 128 : OOB;
            unsigned o_h5 = l < 16 ? WS_H5 + (h * 512 + 64 * (cq + 4 * (w >> 2)) + 16 * (w & 3) + l) * 16 : OOB;
            unsigned a_qw = L_QS + (g & 1) * 272 + (16 * w + l15) * 2, a_qr = L_QS + (l & 1) * 272 + 16 * g;
            v4u pb = {0u, tag, 0u, tag}, a = Z, c = Z;
            PIN(fw[0]); PIN(fw[1]); PIN(fw[2]); PIN(fw[3]); PIN(o_g4); PIN(o_g4b); PIN(o_t5); PIN(o_h5); PIN(a_qw); PIN(a_qr); PIN(pb);
            STAMP(10);
            if (w == 0) hint_wait(rsq, o_t4, tag, P4);
            bar0();
            hint_put(rs, o_t5, tag);
            STAMP(11);
#pragma nounroll
            SPIN { get2(a, c, o_g4, o_g4b, rsq); if (none((tagx(a, tag) | tagx(c, tag)) != 0u)) break; __builtin_amdgcn_s_sleep(2); }
            STAMP(8);
            lwr<u16>(a_qw, (u16)pk(sum32(sum16x2(asf(a[0]) + asf(c[0]), asf(a[2]) + asf(c[2]))), 0.f));
            bar();
            v4u cf[4];
#pragma unroll
            for (int s = 0; s < 4; ++s) cf[s] = lrd<v4u>(a_qr + 64 * s);
            FENCE();
            const f32x4 acc = gemv4(cf, fw);
            pb[0] = asu(acc[0]); pb[2] = asu(acc[1]);
            stx(rs, o_h5, pb);
        }
        STAMP(9);
    }
done:
    STAMP(13);
}

extern "C" void kernel_launch(void* const* d_in, const int* in_sizes, int n_in, void* d_out, int out_size, void* d_ws, size_t ws_size,
                              hipStream_t stream) {
    (void)in_sizes; (void)n_in; (void)out_size; (void)ws_size;
    (void)hipFuncSetAttribute((const void*)mla_glu_decode, hipFuncAttributeMaxDynamicSharedMemorySize, L_END);
    mla_glu_decode<<<256, 512, L_END, stream>>>((const u16*)d_in[0], (const u16*)d_in[1], (const u16*)d_in[2], (const u16*)d_in[3], (const u16*)d_in[4],
                                               (const u16*)d_in[5], (const u16*)d_in[7], (const u16*)d_in[8], (const u16*)d_in[9], (u16*)d_out,
                                               (unsigned char*)d_ws);
}
```

```cpp
#define STAMPS 1
#define SMASK 0x2001
#include <hip/hip_runtime.h>
#include <hip/hip_bf16.h>

typedef unsigned short u16;
typedef unsigned v4u __attribute__((ext_vector_type(4)));
typedef unsigned v2u __attribute__((ext_vector_type(2)));
typedef float f32x4 __attribute__((ext_vector_type(4)));
typedef short s16x4 __attribute__((ext_vector_type(4)));
typedef __bf16 bf16x8 __attribute__((ext_vector_type(8)));
typedef __bf16 bf16x2 __attribute__((ext_vector_type(2)));
#define LAS __attribute__((address_space(3)))
#define GAS __attribute__((address_space(1)))
#define DEVI __device__ __forceinline__

#define WNT ""
#define BDELAY 40
#define PREW 0
#define D1 10
#define D4 0
#define D2 0
#define D3 28
#define D5 0
#define HSL 1
#define ADELAY 0
#define PROG3 0
#define NOGLU 0
#define HAFTER 0
#define ROT 0
#define GUGAP 16
#define DG 0
#define NLAG 3
#define NOGU 0
#define UNMAP 0
#define W1 3
#define NOHG 0
#define TLBW 0
#define HPOS 0
#define H2POS 3
#define RSL 4
#define P2 90
#define P3 180
#define P4 65
#define P5 70
#define PG 270
#define WDPOS 0
#define STOPAT 99

constexpr int HREP = 8;
constexpr int WS_T2 = 4096;
constexpr int WS_T3 = WS_T2 + HREP * 128 * 128;
constexpr int WS_T5 = WS_T3 + HREP * 128 * 128;
constexpr int WS_T4 = WS_T5 + 64 * 32 * 128;
constexpr int WS_H1 = WS_T4 + 16 * 64 * 128;
constexpr int WS_H4 = WS_H1 + 32 * 4 * 128 * 16;
constexpr int WS_H2 = WS_H4 + 32 * 8 * 128 * 16;
constexpr int WS_HG = WS_H2 + 2 * 32 * 128 * 16;
constexpr int WS_H5 = WS_HG + 2 * 16 * 1376 * 16;
constexpr int WS_H3 = WS_H5 + 118 * 512 * 16;
constexpr int WS_END = WS_H3 + 2 * 16 * 8 * 32 * 384;
constexpr int WS_STAMP = 16 << 20;
constexpr int L_Q = 0;
constexpr int L_GU = 0;
constexpr int L_P = 16384;
constexpr int L_QS = L_P + 4096;
constexpr int L_OW = L_QS + 1024;
constexpr int L_MW = L_OW + 2304;
constexpr int L_LW = L_MW + 64;
constexpr int L_MX = 24576;
constexpr int L_LS = L_MX + 512;
constexpr int L_HH = L_LS + 512;
constexpr int L_TAG = L_HH + 64;
constexpr int L_JUNK = L_TAG + 64;
constexpr int L_RED = L_JUNK + 256;
constexpr int L_XN = 28672;
constexpr int L_I0 = 32768;
constexpr int L_I1 = 65536;
constexpr int L_I2 = 98304;
constexpr int L_END = 163840;
static_assert(L_LW + 64 <= L_MX && L_RED + 1024 <= L_XN && L_XN + 2048 <= L_I0, "lds");
constexpr int SPIN_CAP = 1 << 16;

DEVI unsigned pk(float lo, float hi) { bf16x2 r = {(__bf16)lo, (__bf16)hi}; return __builtin_bit_cast(unsigned, r); }
DEVI float rbf(float f) { return (float)(__bf16)f; }
DEVI float lo16(unsigned w) { return __builtin_bit_cast(float, w << 16); }
DEVI float hi16(unsigned w) { return __builtin_bit_cast(float, w & 0xffff0000u); }
DEVI float asf(unsigned w) { return __builtin_bit_cast(float, w); }
DEVI unsigned asu(float w) { return __builtin_bit_cast(unsigned, w); }
DEVI float mx(float a, float b) { float r; asm("v_max_f32 %0, %1, %2" : "=v"(r) : "v"(a), "v"(b)); return r; }
DEVI float mx3(float a, float b, float c) { float r; asm("v_max3_f32 %0, %1, %2, %3" : "=v"(r) : "v"(a), "v"(b), "v"(c)); return r; }
DEVI f32x4 mfma32(v4u a, v4u b, f32x4 c) { return __builtin_amdgcn_mfma_f32_16x16x32_bf16(__builtin_bit_cast(bf16x8, a), __builtin_bit_cast(bf16x8, b), c, 0, 0, 0); }
DEVI f32x4 mfma16(v2u a, v2u b, f32x4 c) { return __builtin_amdgcn_mfma_f32_16x16x16bf16_1k(__builtin_bit_cast(s16x4, a), __builtin_bit_cast(s16x4, b), c, 0, 0, 0); }
template <class T> DEVI T lrd(unsigned off) { return *(LAS T*)(size_t)off; }
template <class T> DEVI void lwr(unsigned off, T v) { *(LAS T*)(size_t)off = v; }
DEVI v2u trd(unsigned off) { return __builtin_bit_cast(v2u, __builtin_amdgcn_ds_read_tr16_b64_v4i16((LAS s16x4*)(size_t)off)); }
DEVI v4u trd2(unsigned o0, unsigned o1) { v2u a = trd(o0), b = trd(o1); return v4u{a.x, a.y, b.x, b.y}; }
DEVI float sum16(float x) { auto r = __builtin_amdgcn_permlane16_swap(asu(x), asu(x), false, false); return asf(r[0]) + asf(r[1]); }
DEVI float sum32(float x) { auto r = __builtin_amdgcn_permlane32_swap(asu(x), asu(x), false, false); return asf(r[0]) + asf(r[1]); }
DEVI float max16(float x) { auto r = __builtin_amdgcn_permlane16_swap(asu(x), asu(x), false, false); return mx(asf(r[0]), asf(r[1])); }
DEVI float max32(float x) { auto r = __builtin_amdgcn_permlane32_swap(asu(x), asu(x), false, false); return mx(asf(r[0]), asf(r[1])); }
DEVI float sum16x2(float x, float y) { auto r = __builtin_amdgcn_permlane16_swap(asu(x), asu(y), false, false); return asf(r[0]) + asf(r[1]); }
DEVI float sum32x2(float x, float y) { auto r = __builtin_amdgcn_permlane32_swap(asu(x), asu(y), false, false); return asf(r[0]) + asf(r[1]); }
template <int CTRL> DEVI float dpp(float x) { return asf((unsigned)__builtin_amdgcn_update_dpp(0, (int)asu(x), CTRL, 0xf, 0xf, true)); }
DEVI float wavesum(float x) {
    x += dpp<0xB1>(x);
    x += dpp<0x4E>(x);
    x += dpp<0x141>(x);
    x += dpp<0x140>(x);
    return sum32(sum16(x));
}
#define STR_(x) #x
#define STR(x) STR_(x)
#define WAITVM(n) asm volatile("s_waitcnt vmcnt(" STR(n) ")" ::: "memory")
#define PIN(x) asm volatile("" : "+v"(x))
#define FENCE() __builtin_amdgcn_sched_barrier(0)
DEVI void bar() { asm volatile("s_waitcnt lgkmcnt(0)\n\ts_barrier" ::: "memory"); }
DEVI void bar0() { asm volatile("s_barrier" ::: "memory"); }
DEVI void glds_s(unsigned voff, const void* sbase, unsigned lds) {
    unsigned keep;
    asm volatile("s_mov_b32 %0, m0\n\ts_mov_b32 m0, %3\n\ts_nop 0\n\tglobal_load_lds_dwordx4 %1, %2" WNT "\n\ts_mov_b32 m0, %0" : "=&s"(keep) : "v"(voff), "s"(sbase), "s"(lds) : "memory");
}
typedef __amdgpu_buffer_rsrc_t rsrc_t;
DEVI v4u ldx(rsrc_t rs, unsigned off) { return __builtin_amdgcn_raw_buffer_load_b128(rs, off, 0, 16); }
DEVI void stx(rsrc_t rs, unsigned off, v4u v) { __builtin_amdgcn_raw_buffer_store_b128(v, rs, off, 0, 16); }
DEVI bool none(bool bad) { asm volatile("" ::: "memory"); return __builtin_amdgcn_ballot_w64(bad) == 0ull; }
DEVI unsigned tagx(v4u a, unsigned tag) { unsigned r = (a[1] ^ tag) | (a[3] ^ tag); asm("" : "+v"(r)); return r; }
constexpr unsigned OOB = 0x80000000u;
DEVI void get1(v4u& a, unsigned off, v4u rq) { asm volatile("buffer_load_dwordx4 %0, %1, %2, 0 offen sc1\n\ts_waitcnt vmcnt(0)" : "+&v"(a) : "v"(off), "s"(rq) : "memory"); }
DEVI void get2(v4u& a, v4u& b, unsigned oa, unsigned ob, v4u rq) {
    asm volatile("buffer_load_dwordx4 %0, %2, %4, 0 offen sc1\n\tbuffer_load_dwordx4 %1, %3, %4, 0 offen sc1\n\ts_waitcnt vmcnt(0)" : "+&v"(a), "+&v"(b) : "v"(oa), "v"(ob), "s"(rq) : "memory");
}
template <int ST> DEVI void get4(v4u (&a)[4], unsigned off, v4u rq) {
    asm volatile("buffer_load_dwordx4 %0, %4, %5, 0 offen sc1\n\tbuffer_load_dwordx4 %1, %4, %5, 0 offen offset:%6 sc1\n\t"
                 "buffer_load_dwordx4 %2, %4, %5, 0 offen offset:%7 sc1\n\tbuffer_load_dwordx4 %3, %4, %5, 0 offen offset:%8 sc1\n\ts_waitcnt vmcnt(0)"
                 : "+&v"(a[0]), "+&v"(a[1]), "+&v"(a[2]), "+&v"(a[3]) : "v"(off), "s"(rq), "i"(ST), "i"(2 * ST), "i"(3 * ST) : "memory");
}
DEVI void get4s(v4u (&a)[4], unsigned off, v4u rq, unsigned s1, unsigned s2, unsigned s3) {
    asm volatile("buffer_load_dwordx4 %0, %4, %5, 0 offen sc1\n\tbuffer_load_dwordx4 %1, %4, %5, %6 offen sc1\n\t"
                 "buffer_load_dwordx4 %2, %4, %5, %7 offen sc1\n\tbuffer_load_dwordx4 %3, %4, %5, %8 offen sc1\n\ts_waitcnt vmcnt(0)"
                 : "+&v"(a[0]), "+&v"(a[1]), "+&v"(a[2]), "+&v"(a[3]) : "v"(off), "s"(rq), "s"(s1), "s"(s2), "s"(s3) : "memory");
}
DEVI unsigned tagx4(const v4u (&a)[4], unsigned tag) { return (tagx(a[0], tag) | tagx(a[1], tag)) | (tagx(a[2], tag) | tagx(a[3], tag)); }
#define SPIN for (int it_ = SPIN_CAP; it_ > 0; --it_)
DEVI unsigned now() { return (unsigned)__builtin_amdgcn_s_memrealtime(); }
DEVI void hint_put(rsrc_t rs, unsigned off, unsigned tag) { __builtin_amdgcn_raw_buffer_store_b64(v2u{tag, now()}, rs, off, 0, 16); }
DEVI v2u hint_get(v4u rq, unsigned off) { v2u v; asm volatile("buffer_load_dwordx2 %0, %1, %2, 0 offen sc1\n\ts_waitcnt vmcnt(0)" : "=&v"(v) : "v"(off), "s"(rq) : "memory"); return v; }
DEVI bool last_wave(unsigned cnt) { return __builtin_amdgcn_readfirstlane(__hip_atomic_fetch_add((LAS unsigned*)(size_t)cnt, 1u, __ATOMIC_RELAXED, __HIP_MEMORY_SCOPE_WORKGROUP)) == 7u * 64u; }
DEVI void pace(unsigned t, unsigned lead) {
#pragma nounroll
    for (int it = 256; it > 0; --it) { if (none(now() - t < lead)) break; }
}
DEVI unsigned hint_wait(v4u rq, unsigned off, unsigned tag, unsigned lead) {
    v2u v;
#pragma nounroll
    SPIN { v = hint_get(rq, off); if (none(v[0] != tag)) break; if (HSL) __builtin_amdgcn_s_sleep(HSL); }
    pace(v[1], lead);
    return v[1];
}

#define NST 16
#define STAMP(i) do { if (!((SMASK >> (i)) & 1)) break; FENCE(); asm volatile("s_memrealtime %0\n\ts_waitcnt lgkmcnt(0)" : "=s"(st[i]) :: "memory"); FENCE(); } while (0)

DEVI void frags128(unsigned a0, unsigned a1, v4u (&f)[4]) {
#pragma unroll
    for (int s = 0; s < 4; ++s) f[s] = trd2(a0 + s * 8192, a1 + s * 8192);
}
DEVI f32x4 gemv4(const v4u (&a)[4], const v4u (&b)[4]) {
    f32x4 c = {0.f, 0.f, 0.f, 0.f};
#pragma unroll
    for (int s = 0; s < 4; ++s) c = mfma32(a[s], b[s], c);
    return c;
}

DEVI void layernorm(v4u xv, int w, int l) {
    float v[8];
#pragma unroll
    for (int j = 0; j < 4; ++j) { v[2 * j] = lo16(xv[j]); v[2 * j + 1] = hi16(xv[j]); }
    float s = ((v[0] + v[1]) + (v[2] + v[3])) + ((v[4] + v[5]) + (v[6] + v[7]));
    const float mean = wavesum(s) * (1.f / 512.f);
    float sq = 0.f;
#pragma unroll
    for (int j = 0; j < 8; ++j) { const float d = v[j] - mean; sq += d * d; }
    const float var = wavesum(sq) * (1.f / 512.f);
    const float mb = rbf(mean), ri = __builtin_amdgcn_rcpf(rbf(__builtin_amdgcn_sqrtf(rbf(rbf(var) + 1e-5f))));
    v4u o;
#pragma unroll
    for (int j = 0; j < 4; ++j) o[j] = pk(rbf(v[2 * j] - mb) * ri, rbf(v[2 * j + 1] - mb) * ri);
    lwr<v4u>(L_XN + w * 1024 + l * 16, o);
}

__global__ void __launch_bounds__(512, 2)
mla_glu_decode(const u16* __restrict__ x, const u16* __restrict__ ckv, const u16* __restrict__ wq, const u16* __restrict__ wuk,
               const u16* __restrict__ wuv, const u16* __restrict__ wo, const u16* __restrict__ wg, const u16* __restrict__ wu,
               const u16* __restrict__ wd, u16* __restrict__ out, unsigned char* __restrict__ ws) {
    unsigned long long st[NST];
#pragma unroll
    for (int i = 0; i < NST; ++i) st[i] = 0;
    STAMP(0);
    const int tid = threadIdx.x, l = tid & 63, w = __builtin_amdgcn_readfirstlane(tid >> 6);
    const int id = blockIdx.x, xc = id & 7, y = id >> 3, yy = y & 15;
    const int un = UNMAP ? xc * 16 + yy : yy * 8 + xc;
    const unsigned g = l >> 4, l15 = l & 15;
    asm volatile("" :: "s"(x), "s"(ckv), "s"(wq), "s"(wuk), "s"(wuv), "s"(wo), "s"(wg), "s"(wu), "s"(wd), "s"(out), "s"(ws));
    const rsrc_t rs = __builtin_amdgcn_make_buffer_rsrc(ws, 0, WS_END, 0x00020000);
    const v4u rsq = {(unsigned)(size_t)ws, (unsigned)((size_t)ws >> 32) & 0xffffu, (unsigned)WS_END, 0x00020000u};
    const v4u Z = {0u, 0u, 0u, 0u};
    const f32x4 ZF = {0.f, 0.f, 0.f, 0.f};

    if (__builtin_expect(y < 16, 1)) {
        const int h = 4 * xc + (yy >> 2), cq = yy & 3;
        const int hu = (h & ~3) | ((h + ROT) & 3);
        v4u xv; unsigned ep;
        if (w < 2) {
            asm volatile("global_load_dwordx4 %0, %1, %2" : "=v"(xv) : "v"(w * 1024 + l * 16), "s"(x) : "memory");
            asm volatile("global_load_dword %0, %1, %2 sc1" : "=v"(ep) : "v"(0), "s"(ws) : "memory");
        }
        const unsigned vb = (128 * cq + 16 * w + g) * 8192 + h * 256;
#pragma unroll
        for (int i = 0; i < 4; ++i) glds_s(vb + i * 32768 + ((l15 ^ ((g << 2) | i)) << 4), wq, L_I0 + (16 * w + 4 * i) * 256);
        bar0();
#pragma unroll
        for (int i = 0; i < 4; ++i) glds_s(vb + (hu - h) * 256 + i * 32768 + ((l15 ^ (4 * i + g)) << 4), wuk, L_I1 + (16 * w + 4 * i) * 256);
        unsigned o_p1 = l < 16 ? WS_H1 + ((h * 4 + cq) * 128 + 16 * w + l) * 16 : OOB;
        unsigned o_g1 = WS_H1 + ((hu * 4 + g) * 128 + 16 * w + l15) * 16;
        unsigned o_h2 = l15 < 2 ? WS_H2 + ((l15 * 32 + hu) * 128 + 32 * cq + 4 * w + g) * 16 : OOB;
        unsigned o_t2 = l < HREP ? WS_T2 + (l * 128 + hu * 4 + cq) * 128 : OOB;
        unsigned tw0, tw1, tw2, tw3;
        if (TLBW && w == 2) {
            asm volatile("global_load_dword %0, %4, %8\n\tglobal_load_dword %1, %5, %9\n\tglobal_load_dword %2, %6, %10\n\tglobal_load_dword %3, %7, %11 sc1"
                         : "=&v"(tw0), "=&v"(tw1), "=&v"(tw2), "=&v"(tw3)
                         : "v"((64 * (yy & 7) + (l & 1) * 63) * 8192 + (h & ~1) * 256), "v"(h * 131072 + cq * 256), "v"((32 * ((un >> 2) & 15) + (l & 1) * 31) * 2752 + (un & 3) * 688), "v"((l & 7) * (1 << 20) + 2048),
                           "s"(wuv), "s"(wo), "s"(un >> 6 ? wu : wg), "s"(ws) : "memory");
        }
        STAMP(1);
        if (w < 2) {
            asm volatile("s_waitcnt vmcnt(8)" : "+v"(xv), "+v"(ep) :: "memory");
            layernorm(xv, w, l);
            unsigned t = ep + 1u; t += (t & 0xffffu) == 0u;
            lwr<v4u>(L_TAG, v4u{t, 0u, 0u, 0u});
        }
        bar();
        const unsigned tag = lrd<unsigned>(L_TAG);
        unsigned a0, a1;
        {
            const unsigned q = l15 >> 2, p = l & 3, ch = 2 * w + (p >> 1);
            a0 = L_I0 + (8 * g + q) * 256 + 16 * (ch ^ ((q << 2) | ((2 * g) & 3))) + 8 * (p & 1);
            a1 = L_I0 + (8 * g + q + 4) * 256 + 16 * (ch ^ ((q << 2) | ((2 * g + 1) & 3))) + 8 * (p & 1);
        }
        unsigned a_qw = L_QS + (g & 1) * 272 + (16 * w + l15) * 2;
        unsigned a_qr = L_QS + (l & 1) * 272 + 16 * g;
        unsigned a_uk[4];
        v4u xf[4];
#pragma unroll
        for (int s = 0; s < 4; ++s) {
            xf[s] = lrd<v4u>(L_XN + (l & 1) * 1024 + (128 * cq + 32 * s + 8 * g) * 2);
            a_uk[s] = L_I1 + (16 * w + l15) * 256 + (((4 * s + g) ^ l15) << 4);
            PIN(xf[s]); PIN(a_uk[s]);
        }
        const int hp = h >> 1, e = yy & 7;
        unsigned o_gu[3];
#pragma unroll
        for (int i = 0; i < 3; ++i) {
            const unsigned pc = min(8 * i + w, 21), idx = min(64 * pc + l, 1375u), row = idx / 43u, ch = idx - row * 43u;
            o_gu[i] = (32 * ((un >> 2) & 15) + row) * 2752 + (un & 3) * 688 + ch * 16; PIN(o_gu[i]);
        }
        v4u pb = {0u, tag, 0u, tag};
        PIN(a0); PIN(a1); PIN(o_p1); PIN(o_g1); PIN(o_h2); PIN(o_t2); PIN(a_qw); PIN(a_qr); PIN(pb);
        if (TLBW && w == 2) WAITVM(8); else WAITVM(4);
        bar0();
        STAMP(2);
        {
            v4u f[4]; frags128(a0, a1, f);
            FENCE();
            const f32x4 acc = gemv4(xf, f);
            pb[0] = asu(acc[0]); pb[2] = asu(acc[1]);
            stx(rs, o_p1, pb);
        }
        STAMP(3);
        if (STOPAT <= 3) { WAITVM(0); STAMP(14); goto done; }
        if (D1) __builtin_amdgcn_s_sleep(D1);
        if (TLBW && w == 2) asm volatile("s_waitcnt vmcnt(1)" : "+v"(tw0), "+v"(tw1), "+v"(tw2), "+v"(tw3) :: "memory"); else WAITVM(W1);
        v4u fu[4];
        {
            v4u a = Z;
            if (H2POS == 3 && last_wave(L_TAG + 8)) hint_put(rs, o_t2, tag);
            get1(a, o_g1, rsq);
            STAMP(14);
#pragma unroll
            for (int s = 0; s < 4; ++s) fu[s] = lrd<v4u>(a_uk[s]);
            if (__builtin_expect(!none(tagx(a, tag) != 0u), 0)) {
#pragma nounroll
                SPIN { __builtin_amdgcn_s_sleep(4); get1(a, o_g1, rsq); if (none(tagx(a, tag) != 0u)) break; }
            }
            if (H2POS == 0 && last_wave(L_TAG + 8)) hint_put(rs, o_t2, tag);
            lwr<u16>(a_qw, (u16)pk(sum32(sum16x2(asf(a[0]), asf(a[2]))), 0.f));
        }
        STAMP(4);
        bar();
        {
            v4u qf[4];
#pragma unroll
            for (int s = 0; s < 4; ++s) qf[s] = lrd<v4u>(a_qr + 64 * s);
            FENCE();
            const f32x4 acc = gemv4(fu, qf);
            constexpr float SC = 0.08838834764831845f * 1.4426950408889634f;
            pb[0] = pk(acc[0] * SC, acc[1] * SC); pb[2] = pk(acc[2] * SC, acc[3] * SC);
            stx(rs, o_h2, pb);
        }
        STAMP(5);
        if (STOPAT <= 5) goto done;
        unsigned o_t3 = WS_T3 + (xc * 128 + (h >> 4) * 64 + 8 * w + (l & 7)) * 128;
        const int r = xc * 8 + (yy >> 1);
        unsigned o_t5 = WS_T5 + (r * 32 + (l & 31)) * 128;
        unsigned j0, j1;
        {
            asm volatile("buffer_load_dword %0, %2, %4, 0 offen sc1\n\tbuffer_load_dword %1, %3, %4, 0 offen sc1" : "=&v"(j0), "=&v"(j1) : "v"(o_t3), "v"((w == 0 && !(yy & 1)) ? o_t5 : o_t3), "s"(rsq) : "memory");
        }
        if (ADELAY) __builtin_amdgcn_s_sleep(ADELAY);
        const int mat = un >> 6, mp = (un >> 2) & 15, fp = un & 3;
#pragma unroll
        for (int i = 0; i < 3; ++i) glds_s(o_gu[i], mat ? wu : wg, L_I2 + min(8 * i + w, 21) * 1024);
#define V_ISSUE(i) glds_s((64 * e + 16 * (w & 3) + 4 * (i) + g) * 8192 + (2 * hp + (w >> 2)) * 256 + ((l15 ^ ((g << 2) | (i))) << 4), wuv, L_I1 + (16 * w + 4 * (i)) * 256)
        V_ISSUE(0); V_ISSUE(1);
        unsigned o_h4 = l < 32 ? WS_H4 + (((2 * hp + (w >> 2)) * 8 + e) * 128 + 32 * (w & 3) + l) * 16 : OOB;
        {
            const v4u xg = lrd<v4u>(L_XN + (l & 1) * 1024 + (32 * mp + 8 * g) * 2);
            const unsigned ag = L_I2 + (8 * g + (l15 >> 2)) * 688 + (l & 3) * 8;
            const unsigned f = 16 * (8 * g + w) + l15;
            const unsigned o_hg = (g < 3 && f < 344) ? WS_HG + ((mat * 16 + mp) * 1376 + 344 * fp + f) * 16 : OOB;
            WAITVM(2);
            bar();
            V_ISSUE(2); V_ISSUE(3);
            f32x4 o[3];
#pragma unroll
            for (int t = 0; t < 3; ++t) {
                const unsigned tl = min(8 * t + w, 21);
                o[t] = mfma32(xg, trd2(ag + tl * 32, ag + tl * 32 + 4 * 688), ZF);
            }
            const float o0 = g == 0 ? o[0][0] : g == 1 ? o[1][0] : o[2][0], o1 = g == 0 ? o[0][1] : g == 1 ? o[1][1] : o[2][1];
            stx(rs, o_hg, v4u{asu(o0), tag, asu(o1), tag});
        }
        STAMP(6);
        asm volatile("s_waitcnt vmcnt(0)" : "+v"(j0), "+v"(j1) :: "memory");
        bar();
        v4u fv[2][2];
        {
            const unsigned q = l15 >> 2, p = l & 3;
#pragma unroll
            for (int t = 0; t < 2; ++t) {
                const unsigned ch = 4 * (w & 3) + 2 * t + (p >> 1), base = L_I1 + (w >> 2) * 16384 + (8 * g + q) * 256 + 8 * (p & 1);
#pragma unroll
                for (int s = 0; s < 2; ++s)
                    fv[t][s] = trd2(base + s * 8192 + 16 * (ch ^ ((q << 2) | ((2 * g) & 3))), base + s * 8192 + 1024 + 16 * (ch ^ ((q << 2) | ((2 * g + 1) & 3))));
            }
        }
        const unsigned u = l15 < 12 ? l15 : l15 - 4;
        unsigned o_h3 = WS_H3 + ((((w >> 2) * 16 + hp) * 8 + e) * 32 + 8 * (w & 3) + (g >> 1)) * 384 + (u >> 2) * 128 + (g & 1) * 64 + (u & 3) * 16;
        unsigned o_p4 = l < 8 ? WS_T4 + ((hp * 8 + l) * 8 + e) * 128 : OOB;
        unsigned a_ow, a_ow2, a_ms;
        {
            const unsigned n = 6 * (l15 >> 2) + 2 * (g >> 1), n2 = 6 * (l15 >> 2) + 4, gg = l15 & 3;
            a_ow = l15 < 12 ? L_OW + w * 272 + (64 * (g & 1) + 16 * gg + 4 * ((n >> 2) ^ (gg & 1)) + (n & 3)) * 2 : L_JUNK + l * 4;
            a_ow2 = (l15 < 8 && l < 32) ? L_OW + w * 272 + (64 * (g & 1) + 16 * gg + 4 * ((n2 >> 2) ^ (gg & 1)) + (n2 & 3)) * 2 : L_JUNK + l * 4;
            a_ms = L_MW + (g >> 1) * 64 + (g & 1) * 32 + w * 4;
        }
        unsigned a_or = L_OW + (l & 7) * 272 + (w >> 2) * 128 + 16 * g, a_mw = L_MW + (w >> 2) * 32 + (g & 1) * 16;
        unsigned TT = tag << 16, a_cnt = l == 0 ? L_TAG + 12 : L_JUNK + l * 4, one = 1u;
        PIN(a_cnt); PIN(one);
        PIN(o_h3); PIN(o_t3); PIN(o_p4); PIN(o_h4); PIN(a_ow); PIN(a_ow2); PIN(a_or); PIN(a_mw); PIN(a_ms); PIN(TT);
        PIN(fv[0][0]); PIN(fv[0][1]); PIN(fv[1][0]); PIN(fv[1][1]);
        STAMP(7);
        if (STOPAT <= 7) goto done;
        unsigned t3;
        {
            v4u a[4] = {Z, Z, Z, Z};
            t3 = hint_wait(rsq, o_t3, tag, P3);
            STAMP(15);
#pragma nounroll
            SPIN { get4<768>(a, o_h3, rsq); if (none(tagx4(a, TT) > 0xffffu)) break; __builtin_amdgcn_s_sleep(4); }
            STAMP(8);
            unsigned arr;
            asm volatile("ds_add_rtn_u32 %0, %1, %2" : "=&v"(arr) : "v"(a_cnt), "v"(one) : "memory");
            float m[4], ls[4];
#pragma unroll
            for (int k = 0; k < 4; ++k) { const float sv = asf(__builtin_amdgcn_alignbit(a[k][3], a[k][2], 16)); m[k] = dpp<0x158>(sv); ls[k] = dpp<0x159>(sv); }
            const float M = max32(mx(mx3(m[0], m[1], m[2]), m[3]));
            FENCE();
            asm volatile("s_waitcnt lgkmcnt(0)" : "+v"(arr) :: "memory");
            if (__builtin_amdgcn_readfirstlane(arr) == 7u) hint_put(rs, o_p4, tag);
            float acc[6], Ls;
#pragma unroll
            for (int k = 0; k < 4; ++k) {
                const float al = __builtin_amdgcn_exp2f(m[k] - M);
                const float v[7] = {lo16(a[k][0]), hi16(a[k][0]), lo16(a[k][1]), lo16(a[k][2]), hi16(a[k][2]), lo16(a[k][3]), ls[k]};
                if (k == 0) { for (int j = 0; j < 6; ++j) acc[j] = al * v[j]; Ls = al * v[6]; }
                else { for (int j = 0; j < 6; ++j) acc[j] = __builtin_fmaf(al, v[j], acc[j]); Ls = __builtin_fmaf(al, v[6], Ls); }
            }
            const float v4 = sum32x2(acc[4], Ls);
            lwr<unsigned>(a_ow, pk(sum32x2(acc[0], acc[2]), sum32x2(acc[1], acc[3])));
            lwr<unsigned>(a_ow2, pk(v4, sum32(acc[5])));
            lwr<float>(a_ms, l < 32 ? M : v4);
        }
        bar();
        {
            const v4u of0 = lrd<v4u>(a_or), of1 = lrd<v4u>(a_or + 64);
            const f32x4 mw = lrd<f32x4>(a_mw), lw = lrd<f32x4>(a_mw + 64);
            FENCE();
            const f32x4 c0 = mfma32(of1, fv[0][1], mfma32(of0, fv[0][0], ZF)), c1 = mfma32(of1, fv[1][1], mfma32(of0, fv[1][0], ZF));
            const float M = mx(mx3(mw[0], mw[1], mw[2]), mw[3]);
            float be[4];
#pragma unroll
            for (int r = 0; r < 4; ++r) be[r] = __builtin_amdgcn_exp2f(mw[r] - M);
            const float rd = __builtin_amdgcn_rcpf((be[0] * lw[0] + be[1] * lw[1]) + (be[2] * lw[2] + be[3] * lw[3]));
            const float v0 = ((be[0] * c0[0] + be[1] * c0[1]) + (be[2] * c0[2] + be[3] * c0[3])) * rd;
            const float v1 = ((be[0] * c1[0] + be[1] * c1[1]) + (be[2] * c1[2] + be[3] * c1[3])) * rd;
            auto r = __builtin_amdgcn_permlane16_swap(asu(v0), asu(v1), false, false);
            pb[0] = r[0]; pb[2] = r[1];
            stx(rs, o_h4, pb);
        }
        STAMP(9);
        if ((yy & 1) == 0) {
            const unsigned o1 = WS_H5 + ((32 + (tid >> 3)) * 512 + 8 * r + (tid & 7)) * 16;
            const bool two = tid < 176;
            const unsigned o2 = two ? o1 + 64 * 512 * 16 : o1;
            v4u a1 = Z, a2 = Z;
            pace(t3, PG);
#pragma nounroll
            SPIN { get2(a1, a2, o1, o2, rsq); if (none((tagx(a1, tag) | tagx(a2, tag)) != 0u)) break; __builtin_amdgcn_s_sleep(12); }
            {
                float t = sum32(sum16x2(asf(a1[0]) + (two ? asf(a2[0]) : 0.f), asf(a1[2]) + (two ? asf(a2[2]) : 0.f)));
                t += dpp<0x128>(t);
                lwr<float>(L_RED + ((g & 1) * 8 + (l & 7)) * 32 + w * 4, t);
            }
            bar();
            STAMP(10);
            if (w == 0) {
                const f32x4 t2 = lrd<f32x4>(L_RED + ((g & 1) * 8 + (l & 7)) * 32), t3 = lrd<f32x4>(L_RED + ((g & 1) * 8 + (l & 7)) * 32 + 16);
                float glu = rbf(((t2[0] + t2[1]) + (t2[2] + t2[3])) + ((t3[0] + t3[1]) + (t3[2] + t3[3])));
                float xin = lo16(x[(g & 1) * 512 + 8 * r + (l & 7)]);
                unsigned o0 = WS_H5 + ((l >> 3) * 512 + 8 * r + (l & 7)) * 16;
                unsigned o_out = ((g & 1) * 512 + 8 * r + (l & 7)) * 2;
                PIN(glu); PIN(xin); PIN(o0); PIN(o_out);
                hint_wait(rsq, o_t5, tag, P5);
                STAMP(11);
                v4u a[4] = {Z, Z, Z, Z};
#pragma nounroll
                SPIN { get4s(a, o0, rsq, 65536, 131072, 196608); if (none(tagx4(a, tag) != 0u)) break; }
                STAMP(12);
                float t = sum32(sum16x2((asf(a[0][0]) + asf(a[1][0])) + (asf(a[2][0]) + asf(a[3][0])), (asf(a[0][2]) + asf(a[1][2])) + (asf(a[2][2]) + asf(a[3][2]))));
                t += dpp<0x128>(t);
                if (l < 32 && l15 < 8) __hip_atomic_store((GAS u16*)((GAS unsigned char*)out + o_out), (u16)pk(rbf(xin + rbf(t)) + glu, 0.f), __ATOMIC_RELAXED, __HIP_MEMORY_SCOPE_AGENT);
                if (r == 0 && l == 0) __hip_atomic_store((GAS unsigned*)ws, tag, __ATOMIC_RELAXED, __HIP_MEMORY_SCOPE_AGENT);
            }
        }
    } else {
        const int pr = xc * 8 + (yy >> 1), hg = yy & 1, b = pr >> 5, sp = pr & 31;
        unsigned ep, j0;
        unsigned o_t2 = WS_T2 + (xc * 128 + 64 * hg + l) * 128;
        if (w == 0) {
            asm volatile("global_load_dword %0, %1, %2 sc1" : "=v"(ep) : "v"(0), "s"(ws) : "memory");
            asm volatile("buffer_load_dword %0, %1, %2, 0 offen sc1" : "=v"(j0) : "v"(o_t2), "s"(rsq) : "memory");
        }
        if (TLBW && w == 1) {
            unsigned t0, t1, t2;
            asm volatile("global_load_dword %0, %3, %5\n\tglobal_load_dword %1, %4, %6 sc1\n\tglobal_load_dword %2, %7, %8\n\ts_waitcnt vmcnt(0)"
                         : "=&v"(t0), "=&v"(t1), "=&v"(t2) : "v"(min(un, 85) * 16384), "v"((l & 7) * (1 << 20) + 2048), "s"(wd), "s"(ws), "v"(0), "s"(out) : "memory");
        }
        if (BDELAY) __builtin_amdgcn_s_sleep(BDELAY);
#pragma unroll
        for (int i = 0; i < 8; ++i) glds_s((pr * 128 + 64 * hg + 8 * w + i) * 1024 + ((l ^ i) << 4), ckv, L_I0 + (16 * w + i) * 1024);
        if (w == 0) {
            asm volatile("s_waitcnt vmcnt(8)" : "+v"(ep), "+v"(j0) :: "memory");
            unsigned t = ep + 1u; t += (t & 0xffffu) == 0u;
            lwr<unsigned>(L_TAG, t);
        }
        unsigned qoff = WS_H2 + ((b * 32 + 16 * hg + 2 * w) * 128 + l) * 16;
        unsigned o_t3 = tid < 4 ? WS_T3 + ((4 * hg + tid) * 128 + hg * 64 + pr) * 128 : OOB;
        unsigned o_h3 = WS_H3 + (((b * 16 + 8 * hg + (l15 >> 1)) * 8 + w) * 32 + sp) * 384 + (l & 1) * 64 + g * 16;
        unsigned a_r[4], a_t[4];
#pragma unroll
        for (int j = 0; j < 4; ++j) { a_r[j] = l15 * 1024 + (((4 * j + g) ^ l15) << 4); PIN(a_r[j]); }
        {
            const unsigned q = l15 >> 2, p = l & 3;
#pragma unroll
            for (int j = 0; j < 4; ++j) {
                const unsigned r0 = 8 * g + 4 * (j & 1) + q, ch = 8 * w + 2 * p + (j >> 1);
                a_t[j] = L_I0 + r0 * 1024 + ((ch ^ (r0 & 15)) << 4) + (p & 1) * 8;
                PIN(a_t[j]);
            }
        }
        unsigned a_q0 = L_Q + 2 * w * 1024 + (((l >> 1) ^ (2 * w)) << 4) + (l & 1) * 8, a_q1 = L_Q + (2 * w + 1) * 1024 + (((l >> 1) ^ (2 * w + 1)) << 4) + (l & 1) * 8;
        unsigned a_mxw = L_MX + l15 * 32 + w * 4, a_mxr = L_MX + l15 * 32;
        unsigned a_pw = L_P + l15 * 256 + (((2 * w + (g >> 1)) ^ l15) << 4) + (g & 1) * 8;
        unsigned a_pr[4];
#pragma unroll
        for (int j = 0; j < 4; ++j) { a_pr[j] = L_P + l15 * 256 + (((4 * j + g) ^ l15) << 4); PIN(a_pr[j]); }
        const int h = 4 * xc + (yy >> 2), cq = yy & 3;
        unsigned o_wd[2];
#pragma unroll
        for (int i = 0; i < 2; ++i) { o_wd[i] = (16 * min(un, 85) + 2 * w + i) * 1024 + ((l ^ (((2 * w + i) & 7) << 1)) << 4); PIN(o_wd[i]); }
        unsigned o_wo[4];
#pragma unroll
        for (int i = 0; i < 4; ++i) {
            const unsigned ch = l15 ^ ((g << 2) | i);
            o_wo[i] = (h * 128 + 16 * w + 4 * i + g) * 1024 + cq * 128 + (ch >> 3) * 512 + (ch & 7) * 16; PIN(o_wo[i]);
        }
        unsigned o_t4 = WS_T4 + (((h >> 1) * 8 + (yy & 7)) * 8 + (l & 7)) * 128;
        unsigned o_hg = un < 86 ? WS_HG + (((w >> 2) * 16 + 4 * (w & 3) + g) * 1376 + 16 * un + l15) * 16 : o_t4;
        v4u ga; unsigned j4;
        PIN(o_t4); PIN(o_hg);
        PIN(qoff); PIN(o_t2); PIN(o_t3); PIN(o_h3); PIN(a_q0); PIN(a_q1); PIN(a_mxw); PIN(a_mxr); PIN(a_pw);
        bar();
#pragma unroll
        for (int i = 8; i < 16; ++i) glds_s((pr * 128 + 64 * (1 - hg) + 8 * w + (i & 7)) * 1024 + ((l ^ i) << 4), ckv, L_I0 + (16 * w + i) * 1024);
        STAMP(1);
        const unsigned tag = lrd<unsigned>(L_TAG);
        if (w == 0) {
            WAITVM(2);
            const v2u hv = hint_get(rsq, o_t2);
            lwr<unsigned>(L_TAG + 4, none(hv[0] != tag || now() - hv[1] < P2 - 10) ? 1u : 0u);
        }
        WAITVM(0);
        STAMP(14);
        v4u cr[16], ct[4][4], wst[4];
#pragma unroll
        for (int s = 0; s < 16; ++s) cr[s] = lrd<v4u>(a_r[s & 3] + (L_I0 + w * 16384) + (s >> 2) * 256);
        bar();
        STAMP(2);
        if (STOPAT <= 5) goto done;
        {
            if (__builtin_amdgcn_readfirstlane(lrd<unsigned>(L_TAG + 4)) == 0u) {
                if (w == 0) hint_wait(rsq, o_t2, tag, P2);
                bar0();
            }
            STAMP(15);
            v4u qa[4];
            asm volatile("buffer_load_dwordx4 %0, %4, %5, 0 offen sc1\n\tbuffer_load_dwordx4 %1, %4, %5, 0 offen offset:1024 sc1\n\t"
                         "buffer_load_dwordx4 %2, %4, %5, 0 offen offset:2048 sc1\n\tbuffer_load_dwordx4 %3, %4, %5, 0 offen offset:3072 sc1"
                         : "=&v"(qa[0]), "=&v"(qa[1]), "=&v"(qa[2]), "=&v"(qa[3]) : "v"(qoff), "s"(rsq) : "memory");
            bar0();
            asm volatile("global_load_dwordx4 %0, %4, %8" WNT "\n\tglobal_load_dwordx4 %1, %5, %8" WNT "\n\tglobal_load_dwordx4 %2, %6, %8" WNT "\n\tglobal_load_dwordx4 %3, %7, %8" WNT
                         : "=&v"(wst[0]), "=&v"(wst[1]), "=&v"(wst[2]), "=&v"(wst[3]) : "v"(o_wo[0]), "v"(o_wo[1]), "v"(o_wo[2]), "v"(o_wo[3]), "s"(wo) : "memory");
#pragma unroll
            for (int t = 0; t < 4; ++t)
#pragma unroll
                for (int s = 0; s < 4; ++s) {
                    const unsigned x8 = (t & 1) * 8;
                    ct[t][s] = trd2((a_t[2 * (t >> 1)] ^ x8) + s * 32768, (a_t[2 * (t >> 1) + 1] ^ x8) + s * 32768);
                }
            FENCE();
            asm volatile("s_waitcnt vmcnt(4)" : "+v"(qa[0]), "+v"(qa[1]), "+v"(qa[2]), "+v"(qa[3]) :: "memory");
            if (__builtin_expect(!none(tagx4(qa, tag) != 0u), 0)) {
#pragma nounroll
                SPIN { if (RSL) __builtin_amdgcn_s_sleep(RSL); get4<1024>(qa, qoff, rsq); if (none(tagx4(qa, tag) != 0u)) break; }
            }
            STAMP(3);
            asm volatile("ds_write2_b32 %0, %2, %3 offset1:1\n\tds_write2_b32 %0, %4, %5 offset0:128 offset1:129\n\tds_write2_b32 %1, %6, %7 offset1:1\n\tds_write2_b32 %1, %8, %9 offset0:128 offset1:129"
                         :: "v"(a_q0), "v"(a_q1), "v"(qa[0][0]), "v"(qa[0][2]), "v"(qa[1][0]), "v"(qa[1][2]), "v"(qa[2][0]), "v"(qa[2][2]), "v"(qa[3][0]), "v"(qa[3][2]) : "memory");
        }
        bar();
        STAMP(4);
        f32x4 sa = ZF, sb = ZF;
        {
            v4u qf[16];
#pragma unroll
            for (int s = 0; s < 16; ++s) qf[s] = lrd<v4u>(a_r[s & 3] + L_Q + (s >> 2) * 256);
            FENCE();
            hint_put(rs, o_t3, tag);
#pragma unroll
            for (int i = 0; i < 2; ++i) glds_s(o_wd[i], wd, L_I0 + (2 * w + i) * 1024);
            FENCE();
#pragma unroll
            for (int s = 0; s < 16; s += 2) { sa = mfma32(cr[s], qf[s], sa); sb = mfma32(cr[s + 1], qf[s + 1], sb); }
        }
        sa += sb;
        lwr<float>(a_mxw, max32(max16(mx(mx3(sa[0], sa[1], sa[2]), sa[3]))));
        bar();
        STAMP(12);
        float mrow;
        {
            const f32x4 m0 = lrd<f32x4>(a_mxr), m1 = lrd<f32x4>(a_mxr + 16);
            mrow = mx(mx3(mx3(m0[0], m0[1], m0[2]), m0[3], m1[0]), mx3(m1[1], m1[2], m1[3]));
            float p[4];
#pragma unroll
            for (int r = 0; r < 4; ++r) p[r] = __builtin_amdgcn_exp2f(sa[r] - mrow);
            lwr<v2u>(a_pw, v2u{pk(p[0], p[1]), pk(p[2], p[3])});
        }
        bar();
        STAMP(5);
        {
            const unsigned T16 = tag & 0xffffu;
            v4u pf[4];
#pragma unroll
            for (int s = 0; s < 4; ++s) pf[s] = lrd<v4u>(a_pr[s]);
            FENCE();
            f32x4 o[4];
#define TILE(t) do { o[t] = ZF; _Pragma("unroll") for (int s = 0; s < 4; ++s) o[t] = mfma32(ct[t][s], pf[s], o[t]); FENCE(); } while (0)
#define UNIT(i, A, B, C) stx(rs, o_h3 + (i) * 128, v4u{A, __builtin_amdgcn_perm(T16, B, 0x05040100u), __builtin_amdgcn_alignbit(C, B, 16), __builtin_amdgcn_perm(T16, C, 0x05040302u)})
            TILE(0); TILE(1);
            float ls = 0.f, ls2 = 0.f;
#pragma unroll
            for (int s = 0; s < 4; ++s) {
                const unsigned p0 = pf[s][0], p1 = pf[s][1], p2 = pf[s][2], p3 = pf[s][3], one = 0x3f803f80u;
                ls = __builtin_amdgcn_fdot2_f32_bf16(__builtin_bit_cast(bf16x2, p0), __builtin_bit_cast(bf16x2, one), ls, false);
                ls2 = __builtin_amdgcn_fdot2_f32_bf16(__builtin_bit_cast(bf16x2, p1), __builtin_bit_cast(bf16x2, one), ls2, false);
                ls = __builtin_amdgcn_fdot2_f32_bf16(__builtin_bit_cast(bf16x2, p2), __builtin_bit_cast(bf16x2, one), ls, false);
                ls2 = __builtin_amdgcn_fdot2_f32_bf16(__builtin_bit_cast(bf16x2, p3), __builtin_bit_cast(bf16x2, one), ls2, false);
            }
            asm volatile("buffer_load_dwordx4 %0, %2, %4, 0 offen sc1\n\tbuffer_load_dword %1, %3, %4, 0 offen sc1" : "=&v"(ga), "=&v"(j4) : "v"(o_hg), "v"(o_t4), "s"(rsq) : "memory");
            FENCE();
            TILE(2);
            const unsigned d0 = pk(o[0][0], o[0][1]), d1 = pk(o[0][2], o[0][3]), d2 = pk(o[1][0], o[1][1]), d3 = pk(o[1][2], o[1][3]);
            UNIT(0, d0, d1, d2);
            FENCE();
            TILE(3);
            float lsum = sum32(sum16(ls + ls2)); PIN(lsum);
            const unsigned d8 = asu((g & 1) ? lsum : mrow);
            const unsigned d4 = pk(o[2][0], o[2][1]), d5 = pk(o[2][2], o[2][3]);
            UNIT(1, d3, d4, d5);
            FENCE();
            const unsigned d6 = pk(o[3][0], o[3][1]), d7 = pk(o[3][2], o[3][3]);
            UNIT(2, d6, d7, d8);
        }
        STAMP(6);
        if (STOPAT <= 7 || NOGLU) goto done;
        asm volatile("s_waitcnt vmcnt(3)" : "+v"(ga), "+v"(j4), "+v"(wst[0]), "+v"(wst[1]), "+v"(wst[2]), "+v"(wst[3]) :: "memory");
#pragma unroll
        for (int i = 0; i < 4; ++i) lwr<v4u>(L_I1 + (16 * w + 4 * i) * 256 + l * 16, wst[i]);
        if (un < 86) {
            {
                if (__builtin_expect(!none(tagx(ga, tag) != 0u), 0)) {
#pragma nounroll
                    SPIN { __builtin_amdgcn_s_sleep(16); get1(ga, o_hg, rsq); if (none(tagx(ga, tag) != 0u)) break; }
                }
                lwr<float>(L_RED + (l & 31) * 32 + w * 4, sum32(sum16x2(asf(ga[0]), asf(ga[2]))));
            }
            bar();
            if (w == 0) {
                const f32x4 gp = lrd<f32x4>(L_RED + (l & 31) * 32), up = lrd<f32x4>(L_RED + (l & 31) * 32 + 16);
                const float gg = rbf((gp[0] + gp[1]) + (gp[2] + gp[3])), uu = rbf((up[0] + up[1]) + (up[2] + up[3]));
                const float sg = rbf(__builtin_amdgcn_rcpf(1.f + __builtin_amdgcn_exp2f(gg * -1.4426950408889634f)));
                lwr<u16>(L_HH + (l & 31) * 2, (u16)pk(sg * uu, 0.f));
            }
            bar();
            const v2u ha = lrd<v2u>(L_HH + (l & 1) * 32 + g * 8);
            const unsigned q = l15 >> 2, p = l & 3, row = 4 * g + q;
            f32x4 o[4];
#pragma unroll
            for (int t = 0; t < 4; ++t) {
                const unsigned ch = 8 * w + 2 * t + (p >> 1);
                o[t] = mfma16(ha, trd(L_I0 + row * 1024 + ((ch ^ ((row & 7) << 1)) << 4) + (p & 1) * 8), ZF);
            }
            const float o0 = g == 0 ? o[0][0] : g == 1 ? o[1][0] : g == 2 ? o[2][0] : o[3][0];
            const float o1 = g == 0 ? o[0][1] : g == 1 ? o[1][1] : g == 2 ? o[2][1] : o[3][1];
            stx(rs, WS_H5 + ((32 + un) * 512 + 64 * w + l) * 16, v4u{asu(o0), tag, asu(o1), tag});
        } else bar();
        STAMP(7);
        {
            v4u fw[4];
            {
                const unsigned q = l15 >> 2, p = l & 3, ch = 2 * w + (p >> 1);
                frags128(L_I1 + (8 * g + q) * 256 + 16 * (ch ^ ((q << 2) | ((2 * g) & 3))) + 8 * (p & 1),
                         L_I1 + (8 * g + q + 4) * 256 + 16 * (ch ^ ((q << 2) | ((2 * g + 1) & 3))) + 8 * (p & 1), fw);
            }
            unsigned o_g4 = WS_H4 + ((h * 8 + g) * 128 + 16 * w + l15) * 16, o_g4b = o_g4 + 8192;
            unsigned o_t5 = tid < 16 ? WS_T5 + ((8 * (cq + 4 * (tid >> 3)) + (tid & 7)) * 32 + h) * 128 : OOB;
            unsigned o_h5 = l < 16 ? WS_H5 + (h * 512 + 64 * (cq + 4 * (w >> 2)) + 16 * (w & 3) + l) * 16 : OOB;
            unsigned a_qw = L_QS + (g & 1) * 272 + (16 * w + l15) * 2, a_qr = L_QS + (l & 1) * 272 + 16 * g;
            v4u pb = {0u, tag, 0u, tag}, a = Z, c = Z;
            PIN(fw[0]); PIN(fw[1]); PIN(fw[2]); PIN(fw[3]); PIN(o_g4); PIN(o_g4b); PIN(o_t5); PIN(o_h5); PIN(a_qw); PIN(a_qr); PIN(pb);
            STAMP(10);
            if (w == 0) hint_wait(rsq, o_t4, tag, P4);
            bar0();
            hint_put(rs, o_t5, tag);
            STAMP(11);
#pragma nounroll
            SPIN { get2(a, c, o_g4, o_g4b, rsq); if (none((tagx(a, tag) | tagx(c, tag)) != 0u)) break; __builtin_amdgcn_s_sleep(2); }
            STAMP(8);
            lwr<u16>(a_qw, (u16)pk(sum32(sum16x2(asf(a[0]) + asf(c[0]), asf(a[2]) + asf(c[2]))), 0.f));
            bar();
            v4u cf[4];
#pragma unroll
            for (int s = 0; s < 4; ++s) cf[s] = lrd<v4u>(a_qr + 64 * s);
            FENCE();
            const f32x4 acc = gemv4(cf, fw);
            pb[0] = asu(acc[0]); pb[2] = asu(acc[1]);
            stx(rs, o_h5, pb);
        }
        STAMP(9);
    }
done:
    STAMP(13);
    if (l == 0) {
        GAS unsigned long long* so = (GAS unsigned long long*)(ws + WS_STAMP) + ((size_t)id * 8 + w) * NST;
#pragma unroll
        for (int i = 0; i < NST; ++i) so[i] = st[i];
    }
}

__global__ void __launch_bounds__(256) k_report(const unsigned long long* stp, int cls, int k, int mode, int scale) {
    const unsigned long long tin = __builtin_amdgcn_s_memrealtime();
    __shared__ unsigned long long r0[256], r1[256], r2[256]; __shared__ unsigned cn[256];
    const int id = threadIdx.x; unsigned long long mn = ~0ull, mx = 0, sm = 0; unsigned c = 0;
    for (int w = 0; w < 8; ++w) { const unsigned long long a = stp[((size_t)id * 8 + w) * NST], v = stp[((size_t)id * 8 + w) * NST + k]; mn = a < mn ? a : mn;
        if (v && (cls == 2 || (cls == 0) == ((id >> 3) < 16))) { mx = v > mx ? v : mx; sm += v & 0xffffffffull; ++c; } }
    r0[id] = mn; r1[id] = mx; r2[id] = sm; cn[id] = c; __syncthreads();
    if (id == 0) { for (int i = 1; i < 256; ++i) { mn = r0[i] < mn ? r0[i] : mn; mx = r1[i] > mx ? r1[i] : mx; sm += r2[i]; c += cn[i]; }
        unsigned long long q = mode == 2 ? tin - mx : mode == 0 ? mx - mn : (sm / c - (mn & 0xffffffffull)); if (q > 20000) q = 20000;
        const unsigned long long t = __builtin_amdgcn_s_memrealtime() + q * scale; while (__builtin_amdgcn_s_memrealtime() < t) __builtin_amdgcn_s_sleep(1); }
}
extern "C" void kernel_launch(void* const* d_in, const int* in_sizes, int n_in, void* d_out, int out_size, void* d_ws, size_t ws_size,
                              hipStream_t stream) {
    (void)in_sizes; (void)n_in; (void)out_size; (void)ws_size;
    (void)hipFuncSetAttribute((const void*)mla_glu_decode, hipFuncAttributeMaxDynamicSharedMemorySize, L_END);
    mla_glu_decode<<<256, 512, L_END, stream>>>((const u16*)d_in[0], (const u16*)d_in[1], (const u16*)d_in[2], (const u16*)d_in[3], (const u16*)d_in[4],
                                               (const u16*)d_in[5], (const u16*)d_in[7], (const u16*)d_in[8], (const u16*)d_in[9], (u16*)d_out,
                                               (unsigned char*)d_ws);
    k_report<<<1, 256, 0, stream>>>((const unsigned long long*)((char*)d_ws + WS_STAMP), 2, 13, 0, 0);
}
```

```cpp
#include <hip/hip_runtime.h>
#include <hip/hip_bf16.h>

typedef unsigned short u16;
typedef unsigned v4u __attribute__((ext_vector_type(4)));
typedef unsigned v2u __attribute__((ext_vector_type(2)));
typedef float f32x4 __attribute__((ext_vector_type(4)));
typedef short s16x4 __attribute__((ext_vector_type(4)));
typedef __bf16 bf16x8 __attribute__((ext_vector_type(8)));
typedef __bf16 bf16x2 __attribute__((ext_vector_type(2)));
#define LAS __attribute__((address_space(3)))
#define GAS __attribute__((address_space(1)))
#define DEVI __device__ __forceinline__

#define PB 95
#define P2 90
#define P3 175
#define P4 65
#define P5 62
#define PG 260
#define PW 100
#define PA 0
#define CODEB 13696
#define EXP 0
#define PH 0
#define STOPAT 99

constexpr int WS_T2 = 4096;
constexpr int WS_T3 = WS_T2 + 8 * 128 * 128;
constexpr int WS_T5 = WS_T3 + 8 * 128 * 128;
constexpr int WS_T4 = WS_T5 + 64 * 32 * 128;
constexpr int WS_H1 = WS_T4 + 32 * 16 * 128;
constexpr int WS_H4 = WS_H1 + 32 * 4 * 128 * 16;
constexpr int WS_H2 = WS_H4 + 32 * 4 * 128 * 16;
constexpr int WS_HG = WS_H2 + 2 * 32 * 128 * 16;
constexpr int WS_H5 = WS_HG + 2 * 16 * 1376 * 16;
constexpr int WS_H3 = WS_H5 + 118 * 512 * 16;
constexpr int WS_END = WS_H3 + 2 * 32 * 4 * 32 * 384;
constexpr int WS_STAMP = 16 << 20;
constexpr int L_Q = 0;
constexpr int L_P = 16384;
constexpr int L_QS = L_P + 4096;
constexpr int L_OW = L_QS + 1024;
constexpr int L_MW = L_OW + 2304;
constexpr int L_MX = L_MW + 64;
constexpr int L_HH = L_MX + 512;
constexpr int L_TAG = L_HH + 64;
constexpr int L_JUNK = L_TAG + 64;
constexpr int L_RED = L_JUNK + 256;
constexpr int L_XN = 28672;
constexpr int L_R0 = 32768, L_R1 = 65536, L_R2 = 98304, L_R3 = 131072;
constexpr int L_END = 163840;
static_assert(L_RED + 1024 <= L_XN && L_XN + 2048 <= L_R0, "lds");
constexpr int SPIN_CAP = 1 << 16;

DEVI unsigned pk(float lo, float hi) { bf16x2 r = {(__bf16)lo, (__bf16)hi}; return __builtin_bit_cast(unsigned, r); }
DEVI float rbf(float f) { return (float)(__bf16)f; }
DEVI float lo16(unsigned w) { return __builtin_bit_cast(float, w << 16); }
DEVI float hi16(unsigned w) { return __builtin_bit_cast(float, w & 0xffff0000u); }
DEVI float asf(unsigned w) { return __builtin_bit_cast(float, w); }
DEVI unsigned asu(float w) { return __builtin_bit_cast(unsigned, w); }
DEVI float mx(float a, float b) { float r; asm("v_max_f32 %0, %1, %2" : "=v"(r) : "v"(a), "v"(b)); return r; }
DEVI float mx3(float a, float b, float c) { float r; asm("v_max3_f32 %0, %1, %2, %3" : "=v"(r) : "v"(a), "v"(b), "v"(c)); return r; }
DEVI f32x4 mfma32(v4u a, v4u b, f32x4 c) { return __builtin_amdgcn_mfma_f32_16x16x32_bf16(__builtin_bit_cast(bf16x8, a), __builtin_bit_cast(bf16x8, b), c, 0, 0, 0); }
DEVI f32x4 mfma16(v2u a, v2u b, f32x4 c) { return __builtin_amdgcn_mfma_f32_16x16x16bf16_1k(__builtin_bit_cast(s16x4, a), __builtin_bit_cast(s16x4, b), c, 0, 0, 0); }
template <class T> DEVI T lrd(unsigned off) { return *(LAS T*)(size_t)off; }
template <class T> DEVI void lwr(unsigned off, T v) { *(LAS T*)(size_t)off = v; }
DEVI v2u trd(unsigned off) { return __builtin_bit_cast(v2u, __builtin_amdgcn_ds_read_tr16_b64_v4i16((LAS s16x4*)(size_t)off)); }
DEVI v4u trd2(unsigned o0, unsigned o1) { v2u a = trd(o0), b = trd(o1); return v4u{a.x, a.y, b.x, b.y}; }
DEVI float sum16(float x) { auto r = __builtin_amdgcn_permlane16_swap(asu(x), asu(x), false, false); return asf(r[0]) + asf(r[1]); }
DEVI float sum32(float x) { auto r = __builtin_amdgcn_permlane32_swap(asu(x), asu(x), false, false); return asf(r[0]) + asf(r[1]); }
DEVI float max16(float x) { auto r = __builtin_amdgcn_permlane16_swap(asu(x), asu(x), false, false); return mx(asf(r[0]), asf(r[1])); }
DEVI float max32(float x) { auto r = __builtin_amdgcn_permlane32_swap(asu(x), asu(x), false, false); return mx(asf(r[0]), asf(r[1])); }
DEVI float odd16(float x) { auto r = __builtin_amdgcn_permlane16_swap(asu(x), asu(x), false, false); return asf(r[1]); }
DEVI float sum16x2(float x, float y) { auto r = __builtin_amdgcn_permlane16_swap(asu(x), asu(y), false, false); return asf(r[0]) + asf(r[1]); }
DEVI float sum32x2(float x, float y) { auto r = __builtin_amdgcn_permlane32_swap(asu(x), asu(y), false, false); return asf(r[0]) + asf(r[1]); }
template <int CTRL> DEVI float dpp(float x) { return asf((unsigned)__builtin_amdgcn_update_dpp(0, (int)asu(x), CTRL, 0xf, 0xf, true)); }
DEVI float wavesum(float x) {
    x += dpp<0xB1>(x); x += dpp<0x4E>(x); x += dpp<0x141>(x); x += dpp<0x140>(x);
    return sum32(sum16(x));
}
DEVI float dot2one(unsigned p, float c) { const unsigned one = 0x3f803f80u; return __builtin_amdgcn_fdot2_f32_bf16(__builtin_bit_cast(bf16x2, p), __builtin_bit_cast(bf16x2, one), c, false); }
#define STR_(x) #x
#define STR(x) STR_(x)
#define WAITVM(n) asm volatile("s_waitcnt vmcnt(" STR(n) ")" ::: "memory")
#define PIN(x) asm volatile("" : "+v"(x))
#define MFMA_SETTLE(x) asm volatile("s_nop 7" : "+v"(x))
#define UNDEF(x) asm volatile("" : "=v"(x))
#define FENCE() __builtin_amdgcn_sched_barrier(0)
#define SPIN for (int it_ = SPIN_CAP; it_ > 0; --it_)
DEVI void bar() { asm volatile("s_waitcnt lgkmcnt(0)\n\ts_barrier" ::: "memory"); }
DEVI void bar0() { asm volatile("s_barrier" ::: "memory"); }
DEVI void glds(unsigned voff, const void* sbase, unsigned lds) {
    unsigned keep;
    asm volatile("s_mov_b32 %0, m0\n\ts_mov_b32 m0, %3\n\ts_nop 0\n\tglobal_load_lds_dwordx4 %1, %2\n\ts_mov_b32 m0, %0" : "=&s"(keep) : "v"(voff), "s"(sbase), "s"(lds) : "memory");
}
typedef __amdgpu_buffer_rsrc_t rsrc_t;
constexpr unsigned OOB = 0x80000000u;
DEVI void stx(rsrc_t rs, unsigned off, v4u v) { __builtin_amdgcn_raw_buffer_store_b128(v, rs, off, 0, 16); }
DEVI bool none(bool bad) { asm volatile("" ::: "memory"); return __builtin_amdgcn_ballot_w64(bad) == 0ull; }
DEVI unsigned tagx(v4u a, unsigned tag) { unsigned r = (a[1] ^ tag) | (a[3] ^ tag); asm("" : "+v"(r)); return r; }
DEVI unsigned tagy(unsigned a, unsigned b, unsigned tag) { unsigned r = (a ^ tag) | (b ^ tag); asm("" : "+v"(r)); return r; }
DEVI unsigned tagx4(const v4u (&a)[4], unsigned tag) { return (tagx(a[0], tag) | tagx(a[1], tag)) | (tagx(a[2], tag) | tagx(a[3], tag)); }
DEVI void get1(v4u& a, unsigned off, v4u rq) { asm volatile("buffer_load_dwordx4 %0, %1, %2, 0 offen sc1\n\ts_waitcnt vmcnt(0)" : "+&v"(a) : "v"(off), "s"(rq) : "memory"); }
DEVI void get2(v4u& a, v4u& b, unsigned oa, unsigned ob, v4u rq) {
    asm volatile("buffer_load_dwordx4 %0, %2, %4, 0 offen sc1\n\tbuffer_load_dwordx4 %1, %3, %4, 0 offen sc1\n\ts_waitcnt vmcnt(0)" : "+&v"(a), "+&v"(b) : "v"(oa), "v"(ob), "s"(rq) : "memory");
}
template <int ST> DEVI void get4(v4u (&a)[4], unsigned off, v4u rq) {
    asm volatile("buffer_load_dwordx4 %0, %4, %5, 0 offen sc1\n\tbuffer_load_dwordx4 %1, %4, %5, 0 offen offset:%6 sc1\n\t"
                 "buffer_load_dwordx4 %2, %4, %5, 0 offen offset:%7 sc1\n\tbuffer_load_dwordx4 %3, %4, %5, 0 offen offset:%8 sc1\n\ts_waitcnt vmcnt(0)"
                 : "+&v"(a[0]), "+&v"(a[1]), "+&v"(a[2]), "+&v"(a[3]) : "v"(off), "s"(rq), "i"(ST), "i"(2 * ST), "i"(3 * ST) : "memory");
}
DEVI void get4s(v4u (&a)[4], unsigned off, v4u rq, unsigned s1, unsigned s2, unsigned s3) {
    asm volatile("buffer_load_dwordx4 %0, %4, %5, 0 offen sc1\n\tbuffer_load_dwordx4 %1, %4, %5, %6 offen sc1\n\t"
                 "buffer_load_dwordx4 %2, %4, %5, %7 offen sc1\n\tbuffer_load_dwordx4 %3, %4, %5, %8 offen sc1\n\ts_waitcnt vmcnt(0)"
                 : "+&v"(a[0]), "+&v"(a[1]), "+&v"(a[2]), "+&v"(a[3]) : "v"(off), "s"(rq), "s"(s1), "s"(s2), "s"(s3) : "memory");
}
DEVI unsigned now() { return (unsigned)__builtin_amdgcn_s_memrealtime(); }
DEVI void hint_put(rsrc_t rs, unsigned off, unsigned tag) { __builtin_amdgcn_raw_buffer_store_b64(v2u{tag, now()}, rs, off, 0, 16); }
DEVI v2u hint_get(v4u rq, unsigned off) { v2u v; asm volatile("buffer_load_dwordx2 %0, %1, %2, 0 offen sc1\n\ts_waitcnt vmcnt(0)" : "=&v"(v) : "v"(off), "s"(rq) : "memory"); return v; }
DEVI void pace(unsigned t, unsigned lead) {
#pragma nounroll
    for (int it = 256; it > 0; --it) { if (none(now() - t < lead)) break; }
}
DEVI unsigned hint_wait(v4u rq, unsigned off, unsigned tag) {
    v2u v;
#pragma nounroll
    SPIN { v = hint_get(rq, off); if (none(v[0] != tag)) break; __builtin_amdgcn_s_sleep(1); }
    return v[1];
}
DEVI unsigned mktag(unsigned ep) { return ep + 1u; }

#define STAMP(i)

#define STAMP2(i)
DEVI void frags128(unsigned a0, unsigned a1, v4u (&f)[4]) {
#pragma unroll
    for (int s = 0; s < 4; ++s) f[s] = trd2(a0 + s * 8192, a1 + s * 8192);
}
DEVI f32x4 gemv4(const v4u (&a)[4], const v4u (&b)[4]) {
    f32x4 c = {0.f, 0.f, 0.f, 0.f};
#pragma unroll
    for (int s = 0; s < 4; ++s) c = mfma32(a[s], b[s], c);
    return c;
}
DEVI void layernorm(v4u xv, int w, int l) {
    float v[8];
#pragma unroll
    for (int j = 0; j < 4; ++j) { v[2 * j] = lo16(xv[j]); v[2 * j + 1] = hi16(xv[j]); }
    const float mean = wavesum(((v[0] + v[1]) + (v[2] + v[3])) + ((v[4] + v[5]) + (v[6] + v[7]))) * (1.f / 512.f);
    float sq = 0.f;
#pragma unroll
    for (int j = 0; j < 8; ++j) { const float d = v[j] - mean; sq += d * d; }
    const float var = wavesum(sq) * (1.f / 512.f);
    const float mb = rbf(mean), ri = __builtin_amdgcn_rcpf(rbf(__builtin_amdgcn_sqrtf(rbf(rbf(var) + 1e-5f))));
    v4u o;
#pragma unroll
    for (int j = 0; j < 4; ++j) o[j] = pk(rbf(v[2 * j] - mb) * ri, rbf(v[2 * j + 1] - mb) * ri);
    lwr<v4u>(L_XN + w * 1024 + l * 16, o);
}

__global__ void __launch_bounds__(512, 2)
mla_glu_decode(const u16* __restrict__ x, const u16* __restrict__ ckv, const u16* __restrict__ wq, const u16* __restrict__ wuk,
               const u16* __restrict__ wuv, const u16* __restrict__ wo, const u16* __restrict__ wg, const u16* __restrict__ wu,
               const u16* __restrict__ wd, u16* __restrict__ out, unsigned char* __restrict__ ws) {
    STAMP(0);
    const int tid = threadIdx.x, l = tid & 63, w = __builtin_amdgcn_readfirstlane(tid >> 6);
    const int id = blockIdx.x, xc = id & 7, y = id >> 3, yy = y & 15;
    const int un = yy * 8 + xc;
    const unsigned g = l >> 4, l15 = l & 15;
    const unsigned t_in = now();
    unsigned cp0 = 0, cp1 = 0;
    if (y >= 16 && w == 2) {
        unsigned long long pc;
        asm volatile("s_getpc_b64 %0" : "=s"(pc));
        pc &= ~255ull;
        asm volatile("global_load_dword %0, %2, %4\n\tglobal_load_dword %1, %3, %4"
                     : "=&v"(cp0), "=&v"(cp1) : "v"(min(l * 128, CODEB - 128)), "v"(min(8192 + l * 128, CODEB - 128)), "s"(pc) : "memory");
    }
    asm volatile("" :: "s"(x), "s"(ckv), "s"(wq), "s"(wuk), "s"(wuv), "s"(wo), "s"(wg), "s"(wu), "s"(wd), "s"(out), "s"(ws));
    const rsrc_t rs = __builtin_amdgcn_make_buffer_rsrc(ws, 0, WS_END, 0x00020000);
    const v4u rsq = {(unsigned)(size_t)ws, (unsigned)((size_t)ws >> 32) & 0xffffu, (unsigned)WS_END, 0x00020000u};
    const f32x4 ZF = {0.f, 0.f, 0.f, 0.f};

    if (__builtin_expect(y < 16, 1)) {
        const int h = 4 * xc + (yy >> 2), cq = yy & 3;
        v4u xv; unsigned ep;
        if (w < 2) {
            asm volatile("global_load_dwordx4 %0, %1, %2" : "=v"(xv) : "v"(w * 1024 + l * 16), "s"(x) : "memory");
            asm volatile("global_load_dword %0, %1, %2 sc1" : "=v"(ep) : "v"(0), "s"(ws) : "memory");
        }
        const unsigned vb = (128 * cq + 16 * w + g) * 8192 + h * 256;
        unsigned sw[4];
#pragma unroll
        for (int i = 0; i < 4; ++i) sw[i] = (l15 ^ ((g << 2) | i)) << 4;
#pragma unroll
        for (int i = 0; i < 4; ++i) glds(vb + i * 32768 + sw[i], wq, L_R0 + (16 * w + 4 * i) * 256);
        bar0();
#pragma unroll
        for (int i = 0; i < 4; ++i) glds(vb + i * 32768 + ((l15 ^ (4 * i + g)) << 4), wuk, L_R1 + (16 * w + 4 * i) * 256);
        STAMP(1);
        if (w < 2) {
            asm volatile("s_waitcnt vmcnt(8)" : "+v"(xv), "+v"(ep) :: "memory");
            layernorm(xv, w, l);
            lwr<v4u>(L_TAG, v4u{mktag(ep), 0u, 0u, 0u});
        }
        bar();
        const unsigned tag = lrd<unsigned>(L_TAG);
        unsigned o_p1 = l < 16 ? WS_H1 + ((h * 4 + cq) * 128 + 16 * w + l) * 16 : OOB;
        unsigned o_g1 = WS_H1 + ((h * 4 + g) * 128 + 16 * w + l15) * 16;
        unsigned o_h2 = l15 < 2 ? WS_H2 + ((l15 * 32 + h) * 128 + 32 * cq + 4 * w + g) * 16 : OOB;
        unsigned o_t2 = l < 8 ? WS_T2 + (l * 128 + h * 4 + cq) * 128 : OOB;
        unsigned a0, a1;
        {
            const unsigned q = l15 >> 2, p = l & 3, ch = 2 * w + (p >> 1);
            a0 = L_R0 + (8 * g + q) * 256 + 16 * (ch ^ ((q << 2) | ((2 * g) & 3))) + 8 * (p & 1);
            a1 = L_R0 + (8 * g + q + 4) * 256 + 16 * (ch ^ ((q << 2) | ((2 * g + 1) & 3))) + 8 * (p & 1);
        }
        unsigned a_qw = L_QS + (g & 1) * 272 + (16 * w + l15) * 2;
        unsigned a_qr = L_QS + (l & 1) * 272 + 16 * g;
        unsigned a_uk[4], o_gu[3], a_cnt = l == 0 ? L_TAG + 8 : L_JUNK + l * 4, one = 1u;
        v4u xf[4];
#pragma unroll
        for (int s = 0; s < 4; ++s) {
            xf[s] = lrd<v4u>(L_XN + (l & 1) * 1024 + (128 * cq + 32 * s + 8 * g) * 2);
            a_uk[s] = L_R1 + (16 * w + l15) * 256 + (((4 * s + g) ^ l15) << 4);
            PIN(xf[s]); PIN(a_uk[s]);
        }
        const int mat = un >> 6, mp = (un >> 2) & 15, fp = un & 3;
#pragma unroll
        for (int i = 0; i < 3; ++i) {
            const unsigned pc = min(8 * i + w, 21), idx = min(64 * pc + l, 1375u), row = idx / 43u, ch = idx - row * 43u;
            o_gu[i] = (32 * mp + row) * 2752 + fp * 688 + ch * 16; PIN(o_gu[i]);
        }
        v4u pb = {0u, tag, 0u, tag}, ga;
        PIN(a0); PIN(a1); PIN(o_p1); PIN(o_g1); PIN(o_h2); PIN(o_t2); PIN(a_qw); PIN(a_qr); PIN(pb); PIN(a_cnt); PIN(one);
        WAITVM(4);
        bar0();
        STAMP(2);
        {
            v4u f[4]; frags128(a0, a1, f);
            FENCE();
            const f32x4 acc = gemv4(xf, f);
            pb[0] = asu(acc[0]); pb[2] = asu(acc[1]);
            stx(rs, o_p1, pb);
        }
        STAMP(3);
        if (STOPAT <= 3) { WAITVM(0); STAMP(14); goto done; }
        __builtin_amdgcn_s_sleep(10);
        WAITVM(3);
        v4u fu[4];
        {
            unsigned arr;
            asm volatile("buffer_load_dwordx4 %1, %4, %5, 0 offen sc1\n\tds_add_rtn_u32 %0, %2, %3\n\ts_waitcnt lgkmcnt(0)" : "=&v"(arr), "=&v"(ga) : "v"(a_cnt), "v"(one), "v"(o_g1), "s"(rsq) : "memory");
            hint_put(rs, __builtin_amdgcn_readfirstlane(arr) == 7u ? o_t2 : OOB, tag);
            asm volatile("s_waitcnt vmcnt(1)" : "+v"(ga) :: "memory");
            STAMP(14);
#pragma unroll
            for (int s = 0; s < 4; ++s) fu[s] = lrd<v4u>(a_uk[s]);
            if (__builtin_expect(!none(tagx(ga, tag) != 0u), 0)) {
#pragma nounroll
                SPIN { __builtin_amdgcn_s_sleep(4); get1(ga, o_g1, rsq); if (none(tagx(ga, tag) != 0u)) break; }
            }
            lwr<u16>(a_qw, (u16)pk(sum32(sum16x2(asf(ga[0]), asf(ga[2]))), 0.f));
        }
        STAMP(4);
        bar();
        {
            v4u qf[4];
#pragma unroll
            for (int s = 0; s < 4; ++s) qf[s] = lrd<v4u>(a_qr + 64 * s);
            FENCE();
            const f32x4 acc = gemv4(fu, qf);
            constexpr float SC = 0.08838834764831845f * 1.4426950408889634f;
            pb[0] = pk(acc[0] * SC, acc[1] * SC); pb[2] = pk(acc[2] * SC, acc[3] * SC);
            stx(rs, o_h2, pb);
        }
        STAMP(5);
        if (STOPAT <= 5) goto done;
        const int r = xc * 8 + (yy >> 1);
        unsigned o_t3 = WS_T3 + (xc * 128 + (h >> 4) * 64 + 8 * w + (l & 7)) * 128, o_t5 = WS_T5 + (r * 32 + (l & 31)) * 128, j0, j1;
        asm volatile("buffer_load_dword %0, %2, %4, 0 offen sc1\n\tbuffer_load_dword %1, %3, %4, 0 offen sc1"
                     : "=&v"(j0), "=&v"(j1) : "v"(o_t3), "v"((w == 0 && !(yy & 1)) ? o_t5 : o_t3), "s"(rsq) : "memory");
        pace(t_in, PA);
#pragma unroll
        for (int i = 0; i < 3; ++i) glds(o_gu[i], mat ? wu : wg, L_R2 + min(8 * i + w, 21) * 1024);
#define V_ISSUE(i) glds(vb + (i) * 32768 + sw[i], wuv, L_R1 + (16 * w + 4 * (i)) * 256)
        V_ISSUE(0); V_ISSUE(1);
        {
            const v4u xg = lrd<v4u>(L_XN + (l & 1) * 1024 + (32 * mp + 8 * g) * 2);
            const unsigned ag = L_R2 + (8 * g + (l15 >> 2)) * 688 + (l & 3) * 8;
            const unsigned f = 16 * (8 * g + w) + l15;
            const unsigned o_hg = (g < 3 && f < 344) ? WS_HG + ((mat * 16 + mp) * 1376 + 344 * fp + f) * 16 : OOB;
            asm volatile("s_waitcnt vmcnt(2)" : "+v"(j0), "+v"(j1) :: "memory");
            bar();
            f32x4 o[3];
#pragma unroll
            for (int t = 0; t < 3; ++t) {
                const unsigned tl = min(8 * t + w, 21);
                o[t] = mfma32(xg, trd2(ag + tl * 32, ag + tl * 32 + 4 * 688), ZF);
            }
            const float o0 = g == 0 ? o[0][0] : g == 1 ? o[1][0] : o[2][0], o1 = g == 0 ? o[0][1] : g == 1 ? o[1][1] : o[2][1];
            stx(rs, o_hg, v4u{asu(o0), tag, asu(o1), tag});
        }
        V_ISSUE(2); V_ISSUE(3);
        STAMP(6);
        const unsigned u = (l & 31) < 24 ? (l & 31) : (l & 31) - 8;
        unsigned o_h3 = WS_H3 + ((((w >> 2) * 32 + h) * 4 + cq) * 32 + 8 * (w & 3) + (g >> 1)) * 384 + u * 16;
        unsigned o_t4 = l < 4 ? WS_T4 + ((h * 4 + l) * 4 + cq) * 128 : OOB;
        unsigned o_p4 = l < 16 ? WS_H4 + ((h * 4 + cq) * 128 + 16 * w + l) * 16 : OOB;
        unsigned a_ow, a_ow2, a_ms;
        {
            const unsigned n = 6 * (u >> 2) + 2 * (g >> 1), n2 = 6 * (u >> 2) + 4, gg = u & 3;
            const bool act = (l & 31) < 24;
            a_ow = (act && n < 32) ? L_OW + w * 272 + (32 * gg + 4 * ((n >> 2) ^ (gg >> 1)) + (n & 3)) * 2 : L_JUNK + l * 4;
            a_ow2 = (act && n2 < 32 && l < 32) ? L_OW + w * 272 + (32 * gg + 4 * ((n2 >> 2) ^ (gg >> 1)) + (n2 & 3)) * 2 : L_JUNK + l * 4;
            a_ms = (l & 31) == 20 ? L_MW + (g >> 1) * 32 + w * 4 : L_JUNK + l * 4;
        }
        unsigned a_or = L_OW + (l & 7) * 272 + 16 * g, a_mw = L_MW + (g & 1) * 16;
        v4u a[4];
        UNDEF(a[0]); UNDEF(a[1]); UNDEF(a[2]); UNDEF(a[3]);
        a_cnt += l == 0 ? 4 : 0;
        PIN(o_h3); PIN(o_t3); PIN(o_t4); PIN(o_p4); PIN(a_ow); PIN(a_ow2); PIN(a_or); PIN(a_mw); PIN(a_ms); PIN(a_cnt);
        WAITVM(0);
        bar();
        v4u fv[4];
        frags128(a0 + (L_R1 - L_R0), a1 + (L_R1 - L_R0), fv);
        PIN(fv[0]); PIN(fv[1]); PIN(fv[2]); PIN(fv[3]);
        STAMP(7);
        if (STOPAT <= 7) goto done;
        const unsigned t3 = hint_wait(rsq, o_t3, tag);
        pace(t3, P3);
        STAMP(15);
        {
#pragma nounroll
            SPIN { get4<768>(a, o_h3, rsq); if (none((tagy(a[0][3], a[1][3], tag) | tagy(a[2][3], a[3][3], tag)) != 0u)) break; __builtin_amdgcn_s_sleep(4); }
            STAMP(8);
            unsigned arr;
            asm volatile("ds_add_rtn_u32 %0, %1, %2" : "=&v"(arr) : "v"(a_cnt), "v"(one) : "memory");
            float m[4], al[4];
#pragma unroll
            for (int k = 0; k < 4; ++k) m[k] = dpp<0x154>(asf(a[k][1]));
            const float M = max32(mx(mx3(m[0], m[1], m[2]), m[3]));
#pragma unroll
            for (int k = 0; k < 4; ++k) al[k] = odd16(__builtin_amdgcn_exp2f(m[k] - M));
            FENCE();
            asm volatile("s_waitcnt lgkmcnt(0)" : "+v"(arr) :: "memory");
            if (__builtin_amdgcn_readfirstlane(arr) == 7u) hint_put(rs, o_t4, tag);
            float acc[6], Ls;
#pragma unroll
            for (int k = 0; k < 4; ++k) {
                const float v[7] = {lo16(a[k][0]), hi16(a[k][0]), lo16(a[k][1]), hi16(a[k][1]), lo16(a[k][2]), hi16(a[k][2]), asf(a[k][2])};
                if (k == 0) { for (int j = 0; j < 6; ++j) acc[j] = al[0] * v[j]; Ls = al[0] * v[6]; }
                else { for (int j = 0; j < 6; ++j) acc[j] = __builtin_fmaf(al[k], v[j], acc[j]); Ls = __builtin_fmaf(al[k], v[6], Ls); }
            }
            const float v4 = sum32x2(acc[4], Ls);
            lwr<unsigned>(a_ow, pk(sum32x2(acc[0], acc[2]), sum32x2(acc[1], acc[3])));
            lwr<unsigned>(a_ow2, pk(v4, sum32(acc[5])));
            lwr<float>(a_ms, l < 32 ? M : v4);
        }
        bar();
        {
            v4u of[4];
#pragma unroll
            for (int s = 0; s < 4; ++s) of[s] = lrd<v4u>(a_or + 64 * s);
            const f32x4 mw = lrd<f32x4>(a_mw), lw = lrd<f32x4>(a_mw + 32);
            FENCE();
            const f32x4 c = gemv4(of, fv);
            const float M = mx(mx3(mw[0], mw[1], mw[2]), mw[3]);
            float be[4];
#pragma unroll
            for (int r = 0; r < 4; ++r) be[r] = __builtin_amdgcn_exp2f(mw[r] - M);
            const float rd = __builtin_amdgcn_rcpf((be[0] * lw[0] + be[1] * lw[1]) + (be[2] * lw[2] + be[3] * lw[3]));
            const float v = ((be[0] * c[0] + be[1] * c[1]) + (be[2] * c[2] + be[3] * c[3])) * rd;
            auto sv = __builtin_amdgcn_permlane16_swap(asu(v), asu(v), false, false);
            pb[0] = sv[0]; pb[2] = sv[1];
            stx(rs, o_p4, pb);
        }
        STAMP(9);
        if ((yy & 1) == 0) {
            const unsigned o1 = WS_H5 + ((32 + (tid >> 3)) * 512 + 8 * r + (tid & 7)) * 16;
            const bool two = tid < 176;
            const unsigned o2 = two ? o1 + 64 * 512 * 16 : o1;
            pace(t3, PG);
#pragma nounroll
            SPIN { get2(a[0], a[1], o1, o2, rsq); if (none((tagx(a[0], tag) | tagx(a[1], tag)) != 0u)) break; __builtin_amdgcn_s_sleep(8); }
            {
                float t = sum32(sum16x2(asf(a[0][0]) + (two ? asf(a[1][0]) : 0.f), asf(a[0][2]) + (two ? asf(a[1][2]) : 0.f)));
                t += dpp<0x128>(t);
                lwr<float>(L_RED + ((g & 1) * 8 + (l & 7)) * 32 + w * 4, t);
            }
            bar();
            STAMP(10);
            if (w == 0) {
                const f32x4 t2 = lrd<f32x4>(L_RED + ((g & 1) * 8 + (l & 7)) * 32), t4 = lrd<f32x4>(L_RED + ((g & 1) * 8 + (l & 7)) * 32 + 16);
                float glu = rbf(((t2[0] + t2[1]) + (t2[2] + t2[3])) + ((t4[0] + t4[1]) + (t4[2] + t4[3])));
                float xin = lo16(x[(g & 1) * 512 + 8 * r + (l & 7)]);
                unsigned o0 = WS_H5 + ((l >> 3) * 512 + 8 * r + (l & 7)) * 16;
                unsigned o_out = ((g & 1) * 512 + 8 * r + (l & 7)) * 2;
                PIN(glu); PIN(xin); PIN(o0); PIN(o_out);
                pace(hint_wait(rsq, o_t5, tag), P5);
                STAMP(11);
#pragma nounroll
                SPIN { get4s(a, o0, rsq, 65536, 131072, 196608); if (none(tagx4(a, tag) != 0u)) break; }
                STAMP(12);
                float t = sum32(sum16x2((asf(a[0][0]) + asf(a[1][0])) + (asf(a[2][0]) + asf(a[3][0])), (asf(a[0][2]) + asf(a[1][2])) + (asf(a[2][2]) + asf(a[3][2]))));
                t += dpp<0x128>(t);
                if (l < 32 && l15 < 8) __hip_atomic_store((GAS u16*)((GAS unsigned char*)out + o_out), (u16)pk(rbf(xin + rbf(t)) + glu, 0.f), __ATOMIC_RELAXED, __HIP_MEMORY_SCOPE_AGENT);
                if (r == 0 && l == 0) __hip_atomic_store((GAS unsigned*)ws, tag, __ATOMIC_RELAXED, __HIP_MEMORY_SCOPE_AGENT);
            }
        }
    } else {
        const int pr = xc * 8 + (yy >> 1), hg = yy & 1, b = pr >> 5, sp = pr & 31;
        unsigned ep, j0;
        unsigned o_t2 = WS_T2 + (xc * 128 + (16 * hg + 2 * w) * 4 + (l & 7)) * 128;
        asm volatile("global_load_dword %0, %1, %2 sc1" : "=v"(ep) : "v"(0), "s"(ws) : "memory");
        asm volatile("buffer_load_dword %0, %1, %2, 0 offen sc1" : "=v"(j0) : "v"(o_t2), "s"(rsq) : "memory");
        unsigned qoff = WS_H2 + ((b * 32 + 16 * hg + 2 * w) * 128 + l) * 16;
        unsigned a_q0 = L_Q + 2 * w * 1024 + (((l >> 1) ^ (2 * w)) << 4) + (l & 1) * 8, a_q1 = L_Q + (2 * w + 1) * 1024 + (((l >> 1) ^ (2 * w + 1)) << 4) + (l & 1) * 8;
        unsigned o_kv[8];
#pragma unroll
        for (int i = 0; i < 8; ++i) { o_kv[i] = (pr * 128 + 64 * hg + 8 * w + i) * 1024 + ((l ^ (8 * (w & 1) + i)) << 4); PIN(o_kv[i]); }
        PIN(qoff); PIN(a_q0); PIN(a_q1);
        asm volatile("s_waitcnt vmcnt(0)" : "+v"(ep), "+v"(j0), "+v"(cp0), "+v"(cp1) :: "memory");
        const unsigned tag = mktag(ep);
        pace(t_in, PB);
#pragma unroll
        for (int i = 0; i < 8; ++i) glds(o_kv[i], ckv, L_R0 + (8 * w + i) * 1024);
        STAMP(1);
        bar0();
#pragma unroll
        for (int i = 0; i < 8; ++i) glds(o_kv[i] ^ 65536, ckv, L_R0 + (64 + 8 * w + i) * 1024);
        WAITVM(8);
        bar0();
        STAMP(2);
#define Q_ISSUE() \
            FENCE(); \
            WAITVM(2); \
            const v2u hv = hint_get(rsq, o_t2);        \
            STAMP(14); \
            pace(none(hv[0] != tag) ? hv[1] : hint_wait(rsq, o_t2, tag), P2); \
            STAMP(15); \
            v4u qa[4]; \
            asm volatile("buffer_load_dwordx4 %0, %4, %5, 0 offen sc1\n\tbuffer_load_dwordx4 %1, %4, %5, 0 offen offset:1024 sc1\n\t" \
                         "buffer_load_dwordx4 %2, %4, %5, 0 offen offset:2048 sc1\n\tbuffer_load_dwordx4 %3, %4, %5, 0 offen offset:3072 sc1" \
                         : "=&v"(qa[0]), "=&v"(qa[1]), "=&v"(qa[2]), "=&v"(qa[3]) : "v"(qoff), "s"(rsq) : "memory"); \
            bar0()
#define Q_LAND(n) do { \
            asm volatile("s_waitcnt vmcnt(" STR(n) ")" : "+v"(qa[0]), "+v"(qa[1]), "+v"(qa[2]), "+v"(qa[3]) :: "memory"); \
            if (__builtin_expect(!none(tagx4(qa, tag) != 0u), 0)) { _Pragma("nounroll") SPIN { __builtin_amdgcn_s_sleep(4); get4<1024>(qa, qoff, rsq); if (none(tagx4(qa, tag) != 0u)) break; } } \
            asm volatile("ds_write2_b32 %0, %2, %3 offset1:1\n\tds_write2_b32 %0, %4, %5 offset0:128 offset1:129\n\tds_write2_b32 %1, %6, %7 offset1:1\n\tds_write2_b32 %1, %8, %9 offset0:128 offset1:129" \
                         :: "v"(a_q0), "v"(a_q1), "v"(qa[0][0]), "v"(qa[0][2]), "v"(qa[1][0]), "v"(qa[1][2]), "v"(qa[2][0]), "v"(qa[2][2]), "v"(qa[3][0]), "v"(qa[3][2]) : "memory"); \
            STAMP(3); bar();   } while (0)
        unsigned t5 = 0;
        const int h = 4 * xc + (yy >> 2), cq = yy & 3;
        unsigned o_t4 = WS_T4 + ((h * 4 + cq) * 4 + (l & 3)) * 128;
        unsigned o_hg = WS_HG + ((4 * (w & 3) + g) * 1376 + 16 * min(un, 85) + l15) * 16;
        if (w >= 4) {
            unsigned a_r[4];
#pragma unroll
            for (int j = 0; j < 4; ++j) a_r[j] = l15 * 1024 + (((4 * j + g) ^ l15) << 4);
            const unsigned kb = L_R0 + (w - 4) * 16384;
            v4u cr[32], qf[16];
#pragma unroll
            for (int s = 0; s < 16; ++s) cr[s] = lrd<v4u>(a_r[s & 3] + kb + (s >> 2) * 256);
            unsigned a_mxw = L_MX + l15 * 16 + (w - 4) * 4, a_mxr = L_MX + l15 * 16;
            unsigned a_pw = L_P + l15 * 256 + (((2 * (w - 4) + (g >> 1)) ^ l15) << 4) + (g & 1) * 8;
            __builtin_amdgcn_s_setprio(1);
            PIN(a_mxw); PIN(a_mxr); PIN(a_pw); PIN(o_hg);
            Q_ISSUE();
#pragma unroll
            for (int s = 0; s < 16; ++s) cr[16 + s] = lrd<v4u>(a_r[s & 3] + kb + 65536 + (s >> 2) * 256);
            FENCE();
            Q_LAND(0);
            STAMP(4);
#pragma unroll
            for (int s = 0; s < 16; ++s) qf[s] = lrd<v4u>(a_r[s & 3] + L_Q + (s >> 2) * 256);
            FENCE();
            f32x4 s1 = ZF, s2 = ZF;
#pragma unroll
            for (int s = 0; s < 16; ++s) { s1 = mfma32(cr[s], qf[s], s1); s2 = mfma32(cr[16 + s], qf[s], s2); }
            asm volatile("s_nop 7" : "+v"(s1), "+v"(s2));
            lwr<float>(a_mxw, max32(max16(mx(mx3(mx3(s1[0], s1[1], s1[2]), s1[3], s2[0]), mx3(s2[1], s2[2], s2[3])))));
            bar();
            STAMP(12);
            const f32x4 m0 = lrd<f32x4>(a_mxr);
            const float mr = mx(mx3(m0[0], m0[1], m0[2]), m0[3]);
            v2u p1 = {pk(__builtin_amdgcn_exp2f(s1[0] - mr), __builtin_amdgcn_exp2f(s1[1] - mr)), pk(__builtin_amdgcn_exp2f(s1[2] - mr), __builtin_amdgcn_exp2f(s1[3] - mr))};
            lwr<v2u>(a_pw, p1);
            v2u p2 = {pk(__builtin_amdgcn_exp2f(s2[0] - mr), __builtin_amdgcn_exp2f(s2[1] - mr)), pk(__builtin_amdgcn_exp2f(s2[2] - mr), __builtin_amdgcn_exp2f(s2[3] - mr))};
            lwr<v2u>(a_pw ^ 128, p2);
            bar();
            STAMP(5);
            PIN(p1); PIN(p2);
            {
                const unsigned q0 = p1[0], q1 = p1[1], q2 = p2[0], q3 = p2[1];
                lwr<float>(a_mxw + 256, sum32(sum16(((lo16(q0) + hi16(q0)) + (lo16(q1) + hi16(q1))) + ((lo16(q2) + hi16(q2)) + (lo16(q3) + hi16(q3))))));
            }
            bar();
            __builtin_amdgcn_s_setprio(0);
            t5 = now();
            if (PH) pace(t5, PH);
            v4u ga, gb;
            asm volatile("buffer_load_dwordx4 %0, %3, %6, 0 offen sc1\n\tbuffer_load_dwordx4 %1, %4, %6, 0 offen sc1\n\tbuffer_load_dword %2, %5, %6, 0 offen sc1\n\ts_waitcnt vmcnt(0)"
                         : "=&v"(ga), "=&v"(gb), "=&v"(j0), "+v"(o_hg) : "v"(o_hg + 16 * 1376 * 16), "v"(o_t4), "s"(rsq) : "memory");
            if (un < 86) {
                if (__builtin_expect(!none((tagx(ga, tag) | tagx(gb, tag)) != 0u), 0)) {
#pragma nounroll
                    SPIN { __builtin_amdgcn_s_sleep(16); get2(ga, gb, o_hg, o_hg + 16 * 1376 * 16, rsq); if (none((tagx(ga, tag) | tagx(gb, tag)) != 0u)) break; }
                }
                lwr<float>(L_RED + (l & 31) * 32 + (w - 4) * 4, sum32(sum16x2(asf(ga[0]), asf(ga[2]))));
                lwr<float>(L_RED + (l & 31) * 32 + w * 4, sum32(sum16x2(asf(gb[0]), asf(gb[2]))));
            }
        } else {
            unsigned a_t[2][4];
            {
                const unsigned q = l15 >> 2, p = l & 3;
#pragma unroll
                for (int j = 0; j < 8; ++j) {
                    const unsigned r0 = 8 * g + 4 * (j >> 2) + q, ch = 16 * w + 4 * p + (j & 3);
                    a_t[j >> 2][j & 3] = L_R0 + r0 * 1024 + ((ch ^ (r0 & 15)) << 4) + (p >> 1) * 8;
                }
            }
            v4u ct[8][4];
#pragma unroll
            for (int t = 0; t < 8; ++t)
#pragma unroll
                for (int s = 0; s < 2; ++s) ct[t][s] = trd2((a_t[0][t >> 1] ^ ((t & 1) * 8)) + s * 32768, (a_t[1][t >> 1] ^ ((t & 1) * 8)) + s * 32768);
            unsigned a_pr[4], a_mxr = L_MX + l15 * 16;
#pragma unroll
            for (int j = 0; j < 4; ++j) { a_pr[j] = L_P + l15 * 256 + (((4 * j + g) ^ l15) << 4); PIN(a_pr[j]); }
            unsigned o_t3 = tid < 4 ? WS_T3 + ((4 * hg + tid) * 128 + hg * 64 + pr) * 128 : OOB;
            unsigned o_h3 = WS_H3 + (((b * 32 + 16 * hg + (l & 7)) * 4 + w) * 32 + sp) * 384 + (l15 >> 3) * 64 + g * 16, o_h3b = o_h3 + 8 * 4 * 32 * 384;
            unsigned o_wd[4], a_lr = L_MX + 256 + l15 * 16;
#pragma unroll
            for (int i = 0; i < 4; ++i) { const unsigned row = 4 * w + i; o_wd[i] = (EXP & 1) ? 0 : (16 * min(un, 85) + row) * 1024 + ((l ^ ((row & 7) << 1)) << 4); PIN(o_wd[i]); }
            PIN(a_mxr); PIN(o_t3); PIN(o_h3); PIN(o_h3b); PIN(a_lr);
            Q_ISSUE();
            v4u wst[4];
            asm volatile("global_load_dwordx4 %0, %4, %8\n\tglobal_load_dwordx4 %1, %5, %8\n\tglobal_load_dwordx4 %2, %6, %8\n\tglobal_load_dwordx4 %3, %7, %8"
                         : "=&v"(wst[0]), "=&v"(wst[1]), "=&v"(wst[2]), "=&v"(wst[3]) : "v"(o_wd[0]), "v"(o_wd[1]), "v"(o_wd[2]), "v"(o_wd[3]), "s"(wd) : "memory");
#pragma unroll
            for (int t = 0; t < 8; ++t)
#pragma unroll
                for (int s = 2; s < 4; ++s) ct[t][s] = trd2((a_t[0][t >> 1] ^ ((t & 1) * 8)) + s * 32768, (a_t[1][t >> 1] ^ ((t & 1) * 8)) + s * 32768);
            FENCE();
            Q_LAND(4);
            STAMP(4);
            hint_put(rs, o_t3, tag);
            bar0();
            const f32x4 m0 = lrd<f32x4>(a_mxr);
            float mr = mx(mx3(m0[0], m0[1], m0[2]), m0[3]);
            PIN(mr);
            bar();
            STAMP(5);
            v4u pf[4];
#pragma unroll
            for (int s = 0; s < 4; ++s) pf[s] = lrd<v4u>(a_pr[s]);
            FENCE();
            f32x4 o[8];
#define M0_(t) asm volatile("v_mfma_f32_16x16x32_bf16 %0, %1, %2, 0" : "=&v"(o[t]) : "v"(ct[t][0]), "v"(pf[0]))
#define MM(t, s) asm volatile("v_mfma_f32_16x16x32_bf16 %0, %1, %2, %0" : "+v"(o[t]) : "v"(ct[t][s]), "v"(pf[s]))
#define RDY(t) PIN(o[t])
#define CV(d, t, i) do { d = pk(o[t][i], o[t][i + 1]); PIN(d); } while (0)
#define CP(t_, a_) do { t_ = a_; PIN(t_); } while (0)
#define XA(a_, b_) do { a_ = __builtin_amdgcn_update_dpp(a_, b_, 0x128, 0xf, 0xc, false); PIN(a_); } while (0)
#define XB(b_, t_) do { b_ = __builtin_amdgcn_update_dpp(b_, t_, 0x128, 0xf, 0x3, false); PIN(b_); } while (0)
#define UNIT(off, A, B, C) stx(rs, off, v4u{A, B, C, tag})
            unsigned d0, d1, d2, d3, d4, d5, e0, e1, e2, e3, e4, e5, t0, t1, t2;
            M0_(0); M0_(1); MM(0, 1); MM(1, 1); MM(0, 2); MM(1, 2); MM(0, 3); MM(1, 3);
            M0_(2); MM(2, 1); MM(2, 2); MM(2, 3);
            bar0();
            const f32x4 l0 = lrd<f32x4>(a_lr);
            M0_(3); MM(3, 1); MM(3, 2);
            RDY(0); CV(d0, 0, 0); CV(d1, 0, 2);
            MM(3, 3); RDY(1); CV(d2, 1, 0); CV(d3, 1, 2); CP(t0, d0);
            M0_(4); RDY(2); CV(d4, 2, 0); CV(d5, 2, 2); CP(t1, d1);
            MM(4, 1); CP(t2, d2); XA(d0, d3); XB(d3, t0);
            MM(4, 2); XA(d1, d4); XB(d4, t1);
            MM(4, 3); XA(d2, d5); XB(d5, t2);
            M0_(5); UNIT(o_h3, d0, d1, d2);
            MM(5, 1); UNIT(o_h3b, d3, d4, d5);
            MM(5, 2); RDY(3); CV(e0, 3, 0); CV(e1, 3, 2);
            MM(5, 3);
            float lsum = (l0[0] + l0[1]) + (l0[2] + l0[3]);
            PIN(lsum);
            M0_(6); RDY(4); CV(e2, 4, 0); CV(e3, 4, 2); CP(t0, e0);
            MM(6, 1); CP(t1, e1); CP(t2, e2); XA(e0, e3);
            MM(6, 2); XB(e3, t0);
            MM(6, 3); RDY(5); CV(e4, 5, 0); CV(e5, 5, 2);
            M0_(7); XA(e1, e4); XB(e4, t1);
            MM(7, 1); XA(e2, e5); XB(e5, t2);
            MM(7, 2); UNIT(o_h3 + 128, e0, e1, e2);
            MM(7, 3); UNIT(o_h3b + 128, e3, e4, e5);
            RDY(6); CV(d0, 6, 0); CV(d1, 6, 2); CP(t0, d0); CP(t1, d1);
            d4 = asu(mr); d5 = asu(lsum);
            XA(d1, d4); XB(d4, t1);
            asm volatile("s_nop 1" : "+v"(o[7]));
            CV(d2, 7, 0); CV(d3, 7, 2); CP(t2, d2);
            XA(d0, d3); XB(d3, t0); XA(d2, d5); XB(d5, t2);
            UNIT(o_h3 + 256, d0, d1, d2); UNIT(o_h3b + 256, d3, d4, d5);
            FENCE();
            STAMP(6);
            asm volatile("s_waitcnt vmcnt(7)" : "+v"(wst[0]), "+v"(wst[1]), "+v"(wst[2]), "+v"(wst[3]) :: "memory");
#pragma unroll
            for (int i = 0; i < 4; ++i) lwr<v4u>(L_R0 + (4 * w + i) * 1024 + l * 16, wst[i]);
        }
        if (STOPAT <= 7) goto done;
        bar();
        if (un < 86) {
            if (w == 4) {
                const f32x4 gp = lrd<f32x4>(L_RED + (l & 31) * 32), up = lrd<f32x4>(L_RED + (l & 31) * 32 + 16);
                const float gg = rbf((gp[0] + gp[1]) + (gp[2] + gp[3])), uu = rbf((up[0] + up[1]) + (up[2] + up[3]));
                const float sg = rbf(__builtin_amdgcn_rcpf(1.f + __builtin_amdgcn_exp2f(gg * -1.4426950408889634f)));
                lwr<u16>(L_HH + (l & 31) * 2, (u16)pk(sg * uu, 0.f));
            }
            bar();
            const v2u ha = lrd<v2u>(L_HH + (l & 1) * 32 + g * 8);
            const unsigned q = l15 >> 2, p = l & 3, row = 4 * g + q;
            f32x4 o[4];
#pragma unroll
            for (int t = 0; t < 4; ++t) {
                const unsigned ch = 8 * w + 2 * t + (p >> 1);
                o[t] = mfma16(ha, trd(L_R0 + row * 1024 + ((ch ^ ((row & 7) << 1)) << 4) + (p & 1) * 8), ZF);
            }
            const float o0 = g == 0 ? o[0][0] : g == 1 ? o[1][0] : g == 2 ? o[2][0] : o[3][0];
            const float o1 = g == 0 ? o[0][1] : g == 1 ? o[1][1] : g == 2 ? o[2][1] : o[3][1];
            stx(rs, WS_H5 + ((32 + un) * 512 + 64 * w + l) * 16, v4u{asu(o0), tag, asu(o1), tag});
        }
        STAMP(7);
        if (w >= 4) {
            pace(t5, PW);
#pragma unroll
            for (int i = 0; i < 8; ++i) {
                const unsigned ch = l15 ^ ((g << 2) | (i & 3));
                glds((h * 128 + 32 * (w - 4) + 4 * i + g) * 1024 + cq * 128 + (ch >> 3) * 512 + (ch & 7) * 16, wo, L_R1 + (32 * (w - 4) + 4 * i) * 256);
            }
        }
        {
            unsigned a0, a1;
            {
                const unsigned q = l15 >> 2, p = l & 3, ch = 2 * w + (p >> 1);
                a0 = L_R1 + (8 * g + q) * 256 + 16 * (ch ^ ((q << 2) | ((2 * g) & 3))) + 8 * (p & 1);
                a1 = L_R1 + (8 * g + q + 4) * 256 + 16 * (ch ^ ((q << 2) | ((2 * g + 1) & 3))) + 8 * (p & 1);
            }
            unsigned o_g4 = WS_H4 + ((h * 4 + g) * 128 + 16 * w + l15) * 16;
            unsigned o_t5 = tid < 16 ? WS_T5 + ((8 * (cq + 4 * (tid >> 3)) + (tid & 7)) * 32 + h) * 128 : OOB;
            unsigned o_h5 = l < 16 ? WS_H5 + (h * 512 + 64 * (cq + 4 * (w >> 2)) + 16 * (w & 3) + l) * 16 : OOB;
            unsigned a_qw = L_QS + (g & 1) * 272 + (16 * w + l15) * 2, a_qr = L_QS + (l & 1) * 272 + 16 * g;
            v4u pb = {0u, tag, 0u, tag}, fw[4], ga;
            WAITVM(0);
            bar();
            frags128(a0, a1, fw);
            PIN(fw[0]); PIN(fw[1]); PIN(fw[2]); PIN(fw[3]); PIN(o_g4); PIN(o_t5); PIN(o_h5); PIN(a_qw); PIN(a_qr); PIN(pb);
            STAMP(10);
            if (w == 4) pace(hint_wait(rsq, o_t4, tag), P4);
            bar0();
            STAMP(11);
            asm volatile("buffer_load_dwordx4 %0, %1, %2, 0 offen sc1" : "=&v"(ga) : "v"(o_g4), "s"(rsq) : "memory");
            hint_put(rs, o_t5, tag);
            asm volatile("s_waitcnt vmcnt(1)" : "+v"(ga) :: "memory");
            if (__builtin_expect(!none(tagx(ga, tag) != 0u), 0)) {
#pragma nounroll
                SPIN { __builtin_amdgcn_s_sleep(2); get1(ga, o_g4, rsq); if (none(tagx(ga, tag) != 0u)) break; }
            }
            STAMP(8);
            lwr<u16>(a_qw, (u16)pk(sum32(sum16x2(asf(ga[0]), asf(ga[2]))), 0.f));
            bar();
            v4u cf[4];
#pragma unroll
            for (int s = 0; s < 4; ++s) cf[s] = lrd<v4u>(a_qr + 64 * s);
            FENCE();
            const f32x4 acc = gemv4(cf, fw);
            pb[0] = asu(acc[0]); pb[2] = asu(acc[1]);
            stx(rs, o_h5, pb);
        }
        STAMP(9);
    }
done:
    STAMP(13);
}

extern "C" void kernel_launch(void* const* d_in, const int* in_sizes, int n_in, void* d_out, int out_size, void* d_ws, size_t ws_size,
                              hipStream_t stream) {
    (void)in_sizes; (void)n_in; (void)out_size; (void)ws_size;
    (void)hipFuncSetAttribute((const void*)mla_glu_decode, hipFuncAttributeMaxDynamicSharedMemorySize, L_END);
    mla_glu_decode<<<256, 512, L_END, stream>>>((const u16*)d_in[0], (const u16*)d_in[1], (const u16*)d_in[2], (const u16*)d_in[3], (const u16*)d_in[4],
                                               (const u16*)d_in[5], (const u16*)d_in[7], (const u16*)d_in[8], (const u16*)d_in[9], (u16*)d_out,
                                               (unsigned char*)d_ws);
}
```

```cpp
#include <hip/hip_runtime.h>
#include <hip/hip_bf16.h>

typedef unsigned short u16;
typedef unsigned v4u __attribute__((ext_vector_type(4)));
typedef unsigned v2u __attribute__((ext_vector_type(2)));
typedef float f32x4 __attribute__((ext_vector_type(4)));
typedef float f32x2 __attribute__((ext_vector_type(2)));
typedef short s16x4 __attribute__((ext_vector_type(4)));
typedef __bf16 bf16x8 __attribute__((ext_vector_type(8)));
typedef __bf16 bf16x2 __attribute__((ext_vector_type(2)));
#define LAS __attribute__((address_space(3)))
#define GAS __attribute__((address_space(1)))
#define DEVI __device__ __forceinline__

#define PB 125
#define P2 100
#define P3 180
#define P4 65
#define P5 62
#define PG 220
#define PW 60
#define PA 0
#define CODEB 13696
#define EXP 0
#define PH 0
#define H3S 12288
#define STOPAT 99

constexpr int WS_T2 = 4096;
constexpr int WS_T3 = WS_T2 + 8 * 128 * 128;
constexpr int WS_T5 = WS_T3 + 8 * 128 * 128;
constexpr int WS_T4 = WS_T5 + 64 * 32 * 128;
constexpr int WS_H1 = WS_T4 + 32 * 16 * 128;
constexpr int WS_H4 = WS_H1 + 32 * 4 * 128 * 16;
constexpr int WS_H2 = WS_H4 + 32 * 4 * 128 * 16;
constexpr int WS_HG = WS_H2 + 4 * 2 * 32 * 128 * 16;
constexpr int WS_H5 = WS_HG + 2 * 16 * 1376 * 16;
constexpr int WS_H3 = WS_H5 + 118 * 512 * 16;
constexpr int WS_END = WS_H3 + 2 * 32 * 4 * H3S;
constexpr int WS_STAMP = 16 << 20;
constexpr int L_Q = 0;
constexpr int L_P = 16384;
constexpr int L_QS = L_P + 4096;
constexpr int L_OW = L_QS + 1024;
constexpr int L_MW = L_OW + 2304;
constexpr int L_MX = L_MW + 64;
constexpr int L_HH = L_MX + 512;
constexpr int L_TAG = L_HH + 64;
constexpr int L_JUNK = L_TAG + 64;
constexpr int L_RED = L_JUNK + 256;
constexpr int L_XN = 28672;
constexpr int L_R0 = 32768, L_R1 = 65536, L_R2 = 98304, L_R3 = 131072;
constexpr int L_END = 163840;
static_assert(L_RED + 1024 <= L_XN && L_XN + 2048 <= L_R0, "lds");
constexpr int SPIN_CAP = 1 << 16;

DEVI unsigned pk(float lo, float hi) { bf16x2 r = {(__bf16)lo, (__bf16)hi}; return __builtin_bit_cast(unsigned, r); }
DEVI float rbf(float f) { return (float)(__bf16)f; }
DEVI float lo16(unsigned w) { return __builtin_bit_cast(float, w << 16); }
DEVI float hi16(unsigned w) { return __builtin_bit_cast(float, w & 0xffff0000u); }
DEVI float asf(unsigned w) { return __builtin_bit_cast(float, w); }
DEVI unsigned asu(float w) { return __builtin_bit_cast(unsigned, w); }
DEVI float mx(float a, float b) { float r; asm("v_max_f32 %0, %1, %2" : "=v"(r) : "v"(a), "v"(b)); return r; }
DEVI float mx3(float a, float b, float c) { float r; asm("v_max3_f32 %0, %1, %2, %3" : "=v"(r) : "v"(a), "v"(b), "v"(c)); return r; }
DEVI f32x4 mfma32(v4u a, v4u b, f32x4 c) { return __builtin_amdgcn_mfma_f32_16x16x32_bf16(__builtin_bit_cast(bf16x8, a), __builtin_bit_cast(bf16x8, b), c, 0, 0, 0); }
DEVI f32x4 mfma16(v2u a, v2u b, f32x4 c) { return __builtin_amdgcn_mfma_f32_16x16x16bf16_1k(__builtin_bit_cast(s16x4, a), __builtin_bit_cast(s16x4, b), c, 0, 0, 0); }
template <class T> DEVI T lrd(unsigned off) { return *(LAS T*)(size_t)off; }
template <class T> DEVI void lwr(unsigned off, T v) { *(LAS T*)(size_t)off = v; }
DEVI v2u trd(unsigned off) { return __builtin_bit_cast(v2u, __builtin_amdgcn_ds_read_tr16_b64_v4i16((LAS s16x4*)(size_t)off)); }
DEVI v4u trd2(unsigned o0, unsigned o1) { v2u a = trd(o0), b = trd(o1); return v4u{a.x, a.y, b.x, b.y}; }
DEVI float sum16(float x) { auto r = __builtin_amdgcn_permlane16_swap(asu(x), asu(x), false, false); return asf(r[0]) + asf(r[1]); }
DEVI float sum32(float x) { auto r = __builtin_amdgcn_permlane32_swap(asu(x), asu(x), false, false); return asf(r[0]) + asf(r[1]); }
DEVI float max16(float x) { auto r = __builtin_amdgcn_permlane16_swap(asu(x), asu(x), false, false); return mx(asf(r[0]), asf(r[1])); }
DEVI float max32(float x) { auto r = __builtin_amdgcn_permlane32_swap(asu(x), asu(x), false, false); return mx(asf(r[0]), asf(r[1])); }
DEVI float odd16(float x) { auto r = __builtin_amdgcn_permlane16_swap(asu(x), asu(x), false, false); return asf(r[1]); }
DEVI float sum16x2(float x, float y) { auto r = __builtin_amdgcn_permlane16_swap(asu(x), asu(y), false, false); return asf(r[0]) + asf(r[1]); }
DEVI float sum32x2(float x, float y) { auto r = __builtin_amdgcn_permlane32_swap(asu(x), asu(y), false, false); return asf(r[0]) + asf(r[1]); }
template <int CTRL> DEVI float dpp(float x) { return asf((unsigned)__builtin_amdgcn_update_dpp(0, (int)asu(x), CTRL, 0xf, 0xf, true)); }
DEVI float wavesum(float x) {
    x += dpp<0xB1>(x); x += dpp<0x4E>(x); x += dpp<0x141>(x); x += dpp<0x140>(x);
    return sum32(sum16(x));
}
DEVI float dot2one(unsigned p, float c) { const unsigned one = 0x3f803f80u; return __builtin_amdgcn_fdot2_f32_bf16(__builtin_bit_cast(bf16x2, p), __builtin_bit_cast(bf16x2, one), c, false); }
#define STR_(x) #x
#define STR(x) STR_(x)
#define WAITVM(n) asm volatile("s_waitcnt vmcnt(" STR(n) ")" ::: "memory")
#define PIN(x) asm volatile("" : "+v"(x))
#define MFMA_SETTLE(x) asm volatile("s_nop 7" : "+v"(x))
#define UNDEF(x) asm volatile("" : "=v"(x))
#define FENCE() __builtin_amdgcn_sched_barrier(0)
#define SPIN for (int it_ = SPIN_CAP; it_ > 0; --it_)
DEVI void bar() { asm volatile("s_waitcnt lgkmcnt(0)\n\ts_barrier" ::: "memory"); }
DEVI void bar0() { asm volatile("s_barrier" ::: "memory"); }
#define USE_NT 1
template <bool NT = false> DEVI void glds(unsigned voff, const void* sbase, unsigned lds) {
    unsigned keep;
    if (NT && USE_NT) asm volatile("s_mov_b32 %0, m0\n\ts_mov_b32 m0, %3\n\ts_nop 0\n\tglobal_load_lds_dwordx4 %1, %2 nt\n\ts_mov_b32 m0, %0" : "=&s"(keep) : "v"(voff), "s"(sbase), "s"(lds) : "memory");
    else asm volatile("s_mov_b32 %0, m0\n\ts_mov_b32 m0, %3\n\ts_nop 0\n\tglobal_load_lds_dwordx4 %1, %2\n\ts_mov_b32 m0, %0" : "=&s"(keep) : "v"(voff), "s"(sbase), "s"(lds) : "memory");
}
typedef __amdgpu_buffer_rsrc_t rsrc_t;
constexpr unsigned OOB = 0x80000000u;
DEVI void stx(rsrc_t rs, unsigned off, v4u v) { __builtin_amdgcn_raw_buffer_store_b128(v, rs, off, 0, 16); }
DEVI bool none(bool bad) { asm volatile("" ::: "memory"); return __builtin_amdgcn_ballot_w64(bad) == 0ull; }
DEVI unsigned tagx(v4u a, unsigned tag) { unsigned r = (a[1] ^ tag) | (a[3] ^ tag); asm("" : "+v"(r)); return r; }
DEVI unsigned tagy(unsigned a, unsigned b, unsigned tag) { unsigned r = (a ^ tag) | (b ^ tag); asm("" : "+v"(r)); return r; }
DEVI unsigned tagx4(const v4u (&a)[4], unsigned tag) { return (tagx(a[0], tag) | tagx(a[1], tag)) | (tagx(a[2], tag) | tagx(a[3], tag)); }
DEVI void get1(v4u& a, unsigned off, v4u rq) { asm volatile("buffer_load_dwordx4 %0, %1, %2, 0 offen sc1\n\ts_waitcnt vmcnt(0)" : "+&v"(a) : "v"(off), "s"(rq) : "memory"); }
DEVI void get2(v4u& a, v4u& b, unsigned oa, unsigned ob, v4u rq) {
    asm volatile("buffer_load_dwordx4 %0, %2, %4, 0 offen sc1\n\tbuffer_load_dwordx4 %1, %3, %4, 0 offen sc1\n\ts_waitcnt vmcnt(0)" : "+&v"(a), "+&v"(b) : "v"(oa), "v"(ob), "s"(rq) : "memory");
}
template <int ST> DEVI void get4(v4u (&a)[4], unsigned off, v4u rq) {
    asm volatile("buffer_load_dwordx4 %0, %4, %5, 0 offen sc1\n\tbuffer_load_dwordx4 %1, %4, %5, 0 offen offset:%6 sc1\n\t"
                 "buffer_load_dwordx4 %2, %4, %5, 0 offen offset:%7 sc1\n\tbuffer_load_dwordx4 %3, %4, %5, 0 offen offset:%8 sc1\n\ts_waitcnt vmcnt(0)"
                 : "+&v"(a[0]), "+&v"(a[1]), "+&v"(a[2]), "+&v"(a[3]) : "v"(off), "s"(rq), "i"(ST), "i"(2 * ST), "i"(3 * ST) : "memory");
}
DEVI void get4s(v4u (&a)[4], unsigned off, v4u rq, unsigned s1, unsigned s2, unsigned s3) {
    asm volatile("buffer_load_dwordx4 %0, %4, %5, 0 offen sc1\n\tbuffer_load_dwordx4 %1, %4, %5, %6 offen sc1\n\t"
                 "buffer_load_dwordx4 %2, %4, %5, %7 offen sc1\n\tbuffer_load_dwordx4 %3, %4, %5, %8 offen sc1\n\ts_waitcnt vmcnt(0)"
                 : "+&v"(a[0]), "+&v"(a[1]), "+&v"(a[2]), "+&v"(a[3]) : "v"(off), "s"(rq), "s"(s1), "s"(s2), "s"(s3) : "memory");
}
DEVI unsigned now() { return (unsigned)__builtin_amdgcn_s_memrealtime(); }
DEVI void hint_put(rsrc_t rs, unsigned off, unsigned tag, unsigned t) { __builtin_amdgcn_raw_buffer_store_b64(v2u{tag, t}, rs, off, 0, 16); }
DEVI void hint_put(rsrc_t rs, unsigned off, unsigned tag) { hint_put(rs, off, tag, now()); }
DEVI v2u hint_get(v4u rq, unsigned off) { v2u v; asm volatile("buffer_load_dwordx2 %0, %1, %2, 0 offen sc1\n\ts_waitcnt vmcnt(0)" : "=&v"(v) : "v"(off), "s"(rq) : "memory"); return v; }
DEVI void pace(unsigned t, unsigned lead) {
#pragma nounroll
    for (int it = 256; it > 0; --it) { if (none(now() - t < lead)) break; }
}
DEVI unsigned hint_wait(v4u rq, unsigned off, unsigned tag) {
    v2u v;
#pragma nounroll
    SPIN { v = hint_get(rq, off); if (none(v[0] != tag)) break; __builtin_amdgcn_s_sleep(1); }
    return v[1];
}
DEVI unsigned mktag(unsigned ep) { return ep + 1u; }

#define STAMP(i)

#define CNT0(i)
#define CNT(i)
#define STAMPC(i)
#define STAMP2(i)
DEVI void frags128(unsigned a0, unsigned a1, v4u (&f)[4]) {
#pragma unroll
    for (int s = 0; s < 4; ++s) f[s] = trd2(a0 + s * 8192, a1 + s * 8192);
}
DEVI f32x4 gemv4(const v4u (&a)[4], const v4u (&b)[4]) {
    f32x4 c = {0.f, 0.f, 0.f, 0.f};
#pragma unroll
    for (int s = 0; s < 4; ++s) c = mfma32(a[s], b[s], c);
    return c;
}
DEVI void layernorm(v4u xv, int w, int l) {
    float v[8];
#pragma unroll
    for (int j = 0; j < 4; ++j) { v[2 * j] = lo16(xv[j]); v[2 * j + 1] = hi16(xv[j]); }
    const float mean = wavesum(((v[0] + v[1]) + (v[2] + v[3])) + ((v[4] + v[5]) + (v[6] + v[7]))) * (1.f / 512.f);
    float sq = 0.f;
#pragma unroll
    for (int j = 0; j < 8; ++j) { const float d = v[j] - mean; sq += d * d; }
    const float var = wavesum(sq) * (1.f / 512.f);
    const float mb = rbf(mean), ri = __builtin_amdgcn_rcpf(rbf(__builtin_amdgcn_sqrtf(rbf(rbf(var) + 1e-5f))));
    v4u o;
#pragma unroll
    for (int j = 0; j < 4; ++j) o[j] = pk(rbf(v[2 * j] - mb) * ri, rbf(v[2 * j + 1] - mb) * ri);
    lwr<v4u>(L_XN + w * 1024 + l * 16, o);
}

__global__ void __launch_bounds__(512, 2)
mla_glu_decode(const u16* __restrict__ x, const u16* __restrict__ ckv, const u16* __restrict__ wq, const u16* __restrict__ wuk,
               const u16* __restrict__ wuv, const u16* __restrict__ wo, const u16* __restrict__ wg, const u16* __restrict__ wu,
               const u16* __restrict__ wd, u16* __restrict__ out, unsigned char* __restrict__ ws) {
    STAMP(0);
    const int tid = threadIdx.x, l = tid & 63, w = __builtin_amdgcn_readfirstlane(tid >> 6);
    const int id = blockIdx.x, xc = id & 7, y = id >> 3, yy = y & 15;
    const int un = yy * 8 + xc;
    const unsigned g = l >> 4, l15 = l & 15;
    const unsigned t_in = now();
    unsigned cp0 = 0, cp1 = 0;
    if (y >= 16 && w == 2) {
        unsigned long long pc;
        asm volatile("s_getpc_b64 %0" : "=s"(pc));
        pc &= ~255ull;
        asm volatile("global_load_dword %0, %2, %4\n\tglobal_load_dword %1, %3, %4"
                     : "=&v"(cp0), "=&v"(cp1) : "v"(min(l * 128, CODEB - 128)), "v"(min(8192 + l * 128, CODEB - 128)), "s"(pc) : "memory");
    }
    asm volatile("" :: "s"(x), "s"(ckv), "s"(wq), "s"(wuk), "s"(wuv), "s"(wo), "s"(wg), "s"(wu), "s"(wd), "s"(out), "s"(ws));
    const rsrc_t rs = __builtin_amdgcn_make_buffer_rsrc(ws, 0, WS_END, 0x00020000);
    const v4u rsq = {(unsigned)(size_t)ws, (unsigned)((size_t)ws >> 32) & 0xffffu, (unsigned)WS_END, 0x00020000u};
    const f32x4 ZF = {0.f, 0.f, 0.f, 0.f};

    if (__builtin_expect(y < 16, 1)) {
        const int h = 4 * xc + (yy >> 2), cq = yy & 3;
        v4u xv; unsigned ep;
        if (w < 2) {
            asm volatile("global_load_dwordx4 %0, %1, %2" : "=v"(xv) : "v"(w * 1024 + l * 16), "s"(x) : "memory");
            asm volatile("global_load_dword %0, %1, %2 sc1" : "=v"(ep) : "v"(0), "s"(ws) : "memory");
        }
        const unsigned vb = (128 * cq + 16 * w + g) * 8192 + h * 256;
        unsigned sw[4];
#pragma unroll
        for (int i = 0; i < 4; ++i) sw[i] = (l15 ^ ((g << 2) | i)) << 4;
#pragma unroll
        for (int i = 0; i < 4; ++i) glds<true>(vb + i * 32768 + sw[i], wq, L_R0 + (16 * w + 4 * i) * 256);
        bar0();
#pragma unroll
        for (int i = 0; i < 4; ++i) glds<true>(vb + i * 32768 + ((l15 ^ (4 * i + g)) << 4), wuk, L_R1 + (16 * w + 4 * i) * 256);
        STAMP(1);
        if (w < 2) {
            asm volatile("s_waitcnt vmcnt(8)" : "+v"(xv), "+v"(ep) :: "memory");
            layernorm(xv, w, l);
            lwr<v4u>(L_TAG, v4u{mktag(ep), 0u, 0u, 0u});
        }
        bar();
        const unsigned tag = lrd<unsigned>(L_TAG);
        unsigned o_p1 = l < 16 ? WS_H1 + ((h * 4 + cq) * 128 + 16 * w + l) * 16 : OOB;
        unsigned o_g1 = WS_H1 + ((h * 4 + g) * 128 + 16 * w + l15) * 16;
        unsigned o_h2 = WS_H2 + ((((w & 3) * 2 + (l >> 5)) * 32 + h) * 128 + 32 * cq + (l & 31)) * 16;
        unsigned a_2w = l15 < 2 ? L_RED + l15 * 256 + (4 * w + g) * 8 : L_JUNK + (l & 31) * 8, a_2r = L_RED + l * 8;
        PIN(a_2w); PIN(a_2r);
        unsigned o_t2 = l < 8 ? WS_T2 + (l * 128 + h * 4 + cq) * 128 : OOB;
        unsigned a0, a1;
        {
            const unsigned q = l15 >> 2, p = l & 3, ch = 2 * w + (p >> 1);
            a0 = L_R0 + (8 * g + q) * 256 + 16 * (ch ^ ((q << 2) | ((2 * g) & 3))) + 8 * (p & 1);
            a1 = L_R0 + (8 * g + q + 4) * 256 + 16 * (ch ^ ((q << 2) | ((2 * g + 1) & 3))) + 8 * (p & 1);
        }
        unsigned a_qw = L_QS + (g & 1) * 272 + (16 * w + l15) * 2;
        unsigned a_qr = L_QS + (l & 1) * 272 + 16 * g;
        unsigned a_uk[4], o_gu[3], a_cnt = l == 0 ? L_TAG + 8 : L_JUNK + l * 4, one = 1u;
        v4u xf[4];
#pragma unroll
        for (int s = 0; s < 4; ++s) {
            xf[s] = lrd<v4u>(L_XN + (l & 1) * 1024 + (128 * cq + 32 * s + 8 * g) * 2);
            a_uk[s] = L_R1 + (16 * w + l15) * 256 + (((4 * s + g) ^ l15) << 4);
            PIN(xf[s]); PIN(a_uk[s]);
        }
        const int mat = un >> 6, mp = (un >> 2) & 15, fp = un & 3;
#pragma unroll
        for (int i = 0; i < 3; ++i) {
            const unsigned pc = min(8 * i + w, 21), idx = min(64 * pc + l, 1375u), row = idx / 43u, ch = idx - row * 43u;
            o_gu[i] = (32 * mp + row) * 2752 + fp * 688 + ch * 16; PIN(o_gu[i]);
        }
        v4u pb = {0u, tag, 0u, tag}, ga;
        PIN(a0); PIN(a1); PIN(o_p1); PIN(o_g1); PIN(o_h2); PIN(o_t2); PIN(a_qw); PIN(a_qr); PIN(pb); PIN(a_cnt); PIN(one);
        WAITVM(4);
        bar0();
        STAMP(2);
        {
            v4u f[4]; frags128(a0, a1, f);
            FENCE();
            const f32x4 acc = gemv4(xf, f);
            pb[0] = asu(acc[0]); pb[2] = asu(acc[1]);
            stx(rs, o_p1, pb);
        }
        STAMP(3);
        if (STOPAT <= 3) { WAITVM(0); STAMP(14); goto done; }
        __builtin_amdgcn_s_sleep(10);
        WAITVM(3);
        v4u fu[4];
        {
            unsigned arr;
            asm volatile("buffer_load_dwordx4 %1, %4, %5, 0 offen sc1\n\tds_add_rtn_u32 %0, %2, %3\n\ts_waitcnt lgkmcnt(0)" : "=&v"(arr), "=&v"(ga) : "v"(a_cnt), "v"(one), "v"(o_g1), "s"(rsq) : "memory");
            hint_put(rs, __builtin_amdgcn_readfirstlane(arr) == 7u ? o_t2 : OOB, tag);
            asm volatile("s_waitcnt vmcnt(1)" : "+v"(ga) :: "memory");
            STAMP(14);
#pragma unroll
            for (int s = 0; s < 4; ++s) fu[s] = lrd<v4u>(a_uk[s]);
            if (__builtin_expect(!none(tagx(ga, tag) != 0u), 0)) {
#pragma nounroll
                SPIN { __builtin_amdgcn_s_sleep(4); get1(ga, o_g1, rsq); if (none(tagx(ga, tag) != 0u)) break; }
            }
            lwr<u16>(a_qw, (u16)pk(sum32(sum16x2(asf(ga[0]), asf(ga[2]))), 0.f));
        }
        STAMP(4);
        bar();
        {
            v4u qf[4];
#pragma unroll
            for (int s = 0; s < 4; ++s) qf[s] = lrd<v4u>(a_qr + 64 * s);
            FENCE();
            const f32x4 acc = gemv4(fu, qf);
            constexpr float SC = 0.08838834764831845f * 1.4426950408889634f;
            lwr<v2u>(a_2w, v2u{pk(acc[0] * SC, acc[1] * SC), pk(acc[2] * SC, acc[3] * SC)});
            bar();
            if (w < 4) {
                const v2u t = lrd<v2u>(a_2r);
                pb[0] = t[0]; pb[2] = t[1];
                stx(rs, o_h2, pb);
            }
        }
        STAMP(5);
        if (STOPAT <= 5) goto done;
        const int r = xc * 8 + (yy >> 1);
        unsigned o_t3 = WS_T3 + (xc * 128 + (h >> 4) * 64 + 8 * w + (l & 7)) * 128, o_t5 = WS_T5 + (r * 32 + (l & 31)) * 128, j0, j1;
        asm volatile("buffer_load_dword %0, %2, %4, 0 offen sc1\n\tbuffer_load_dword %1, %3, %4, 0 offen sc1"
                     : "=&v"(j0), "=&v"(j1) : "v"(o_t3), "v"((w == 0 && !(yy & 1)) ? o_t5 : o_t3), "s"(rsq) : "memory");
        pace(t_in, PA);
#pragma unroll
        for (int i = 0; i < 3; ++i) glds(o_gu[i], mat ? wu : wg, L_R2 + min(8 * i + w, 21) * 1024);
#define V_ISSUE(i) glds<true>((EXP & 4) ? 0 : vb + (i) * 32768 + sw[i], wuv, L_R1 + (16 * w + 4 * (i)) * 256)
        V_ISSUE(0); V_ISSUE(1);
        {
            const v4u xg = lrd<v4u>(L_XN + (l & 1) * 1024 + (32 * mp + 8 * g) * 2);
            const unsigned ag = L_R2 + (8 * g + (l15 >> 2)) * 688 + (l & 3) * 8;
            const unsigned f = 16 * (8 * g + w) + l15;
            const unsigned o_hg = (g < 3 && f < 344) ? WS_HG + ((mat * 16 + mp) * 1376 + 344 * fp + f) * 16 : OOB;
            asm volatile("s_waitcnt vmcnt(2)" : "+v"(j0), "+v"(j1) :: "memory");
            bar();
            f32x4 o[3];
#pragma unroll
            for (int t = 0; t < 3; ++t) {
                const unsigned tl = min(8 * t + w, 21);
                o[t] = mfma32(xg, trd2(ag + tl * 32, ag + tl * 32 + 4 * 688), ZF);
            }
            const float o0 = g == 0 ? o[0][0] : g == 1 ? o[1][0] : o[2][0], o1 = g == 0 ? o[0][1] : g == 1 ? o[1][1] : o[2][1];
            stx(rs, o_hg, v4u{asu(o0), tag, asu(o1), tag});
        }
        V_ISSUE(2); V_ISSUE(3);
        STAMP(6);
        const unsigned u = l15 >= 12 ? 8 + l15 : (g & 1) ? 12 + (l15 & 7) : l15;
        const bool act = (g & 1) ? (l15 & 12) != 8 : l15 < 12;
        unsigned o_h3 = WS_H3 + (((w >> 2) * 32 + h) * 4 + cq) * H3S + (8 * (w & 3) + (g >> 1)) * 384 + u * 16;
        unsigned o_t4 = l < 4 ? WS_T4 + ((h * 4 + l) * 4 + cq) * 128 : OOB;
        unsigned o_p4 = l < 16 ? WS_H4 + ((h * 4 + cq) * 128 + 16 * w + l) * 16 : OOB;
        unsigned a_ow, a_ow2, a_ms;
        {
            const unsigned n = 6 * (u >> 2) + 2 * (g >> 1), n2 = 6 * (u >> 2) + 4, gg = u & 3;
            a_ow = (act && n < 32) ? L_OW + w * 272 + (32 * gg + 4 * ((n >> 2) ^ (gg >> 1)) + (n & 3)) * 2 : L_JUNK + l * 4;
            a_ow2 = (act && n2 < 32 && l < 32) ? L_OW + w * 272 + (32 * gg + 4 * ((n2 >> 2) ^ (gg >> 1)) + (n2 & 3)) * 2 : L_JUNK + l * 4;
            a_ms = (l & 31) == 12 ? L_MW + (g >> 1) * 32 + w * 4 : L_JUNK + l * 4;
        }
        unsigned a_or = L_OW + (l & 7) * 272 + 16 * g, a_mw = L_MW + (g & 1) * 16;
        v4u a[4];
        UNDEF(a[0]); UNDEF(a[1]); UNDEF(a[2]); UNDEF(a[3]);
        a_cnt += l == 0 ? 4 : 0;
        PIN(o_h3); PIN(o_t3); PIN(o_t4); PIN(o_p4); PIN(a_ow); PIN(a_ow2); PIN(a_or); PIN(a_mw); PIN(a_ms); PIN(a_cnt);
        WAITVM(0);
        bar();
        v4u fv[4];
        frags128(a0 + (L_R1 - L_R0), a1 + (L_R1 - L_R0), fv);
        PIN(fv[0]); PIN(fv[1]); PIN(fv[2]); PIN(fv[3]);
        STAMP(7);
        if (STOPAT <= 7) goto done;
        CNT0(2); CNT0(3);
        const unsigned t3 = hint_wait(rsq, o_t3, tag);
        STAMPC(1);
        pace(t3, P3);
        STAMP(15);
        {
#pragma nounroll
            SPIN { CNT(2); get4<768>(a, o_h3, rsq); if (none((tagy(a[0][3], a[1][3], tag) | tagy(a[2][3], a[3][3], tag)) != 0u)) break; __builtin_amdgcn_s_sleep(4); }
            STAMP(8);
            unsigned arr;
            unsigned long long tm;
            asm volatile("ds_add_rtn_u32 %0, %2, %3\n\ts_memrealtime %1" : "=&v"(arr), "=&s"(tm) : "v"(a_cnt), "v"(one) : "memory");
            float m[4], al[4];
#pragma unroll
            for (int k = 0; k < 4; ++k) m[k] = dpp<0x15c>(asf(a[k][1]));
            const float M = max32(mx(mx3(m[0], m[1], m[2]), m[3]));
#pragma unroll
            for (int k = 0; k < 4; ++k) al[k] = __builtin_amdgcn_exp2f(m[k] - M);
            FENCE();
            asm volatile("s_waitcnt lgkmcnt(0)" : "+v"(arr), "+s"(tm) :: "memory");
            hint_put(rs, __builtin_amdgcn_readfirstlane(arr) == 7u ? o_t4 : OOB, tag, (unsigned)tm);
            f32x2 c01, c23, c45;
            float Ls;
#pragma unroll
            for (int k = 0; k < 4; ++k) {
                const f32x2 aa = {al[k], al[k]}, v01 = {lo16(a[k][0]), hi16(a[k][0])}, v23 = {lo16(a[k][1]), hi16(a[k][1])}, v45 = {lo16(a[k][2]), hi16(a[k][2])};
                if (k == 0) { c01 = aa * v01; c23 = aa * v23; c45 = aa * v45; Ls = al[0] * asf(a[0][2]); }
                else { c01 = __builtin_elementwise_fma(aa, v01, c01); c23 = __builtin_elementwise_fma(aa, v23, c23); c45 = __builtin_elementwise_fma(aa, v45, c45); Ls = __builtin_fmaf(al[k], asf(a[k][2]), Ls); }
            }
            const float acc[6] = {c01[0], c01[1], c23[0], c23[1], c45[0], c45[1]};
            const float v4 = sum32x2(acc[4], Ls);
            lwr<unsigned>(a_ow, pk(sum32x2(acc[0], acc[2]), sum32x2(acc[1], acc[3])));
            lwr<unsigned>(a_ow2, pk(v4, sum32(acc[5])));
            lwr<float>(a_ms, l < 32 ? M : v4);
        }
        bar();
        {
            v4u of[4];
#pragma unroll
            for (int s = 0; s < 4; ++s) of[s] = lrd<v4u>(a_or + 64 * s);
            const f32x4 mw = lrd<f32x4>(a_mw), lw = lrd<f32x4>(a_mw + 32);
            FENCE();
            const f32x4 c = gemv4(of, fv);
            const float M = mx(mx3(mw[0], mw[1], mw[2]), mw[3]);
            float be[4];
#pragma unroll
            for (int r = 0; r < 4; ++r) be[r] = __builtin_amdgcn_exp2f(mw[r] - M);
            const float rd = __builtin_amdgcn_rcpf((be[0] * lw[0] + be[1] * lw[1]) + (be[2] * lw[2] + be[3] * lw[3]));
            const float v = ((be[0] * c[0] + be[1] * c[1]) + (be[2] * c[2] + be[3] * c[3])) * rd;
            auto sv = __builtin_amdgcn_permlane16_swap(asu(v), asu(v), false, false);
            pb[0] = sv[0]; pb[2] = sv[1];
            stx(rs, o_p4, pb);
        }
        STAMP(9);
        if ((yy & 1) == 0) {
            const unsigned o1 = WS_H5 + ((32 + (tid >> 3)) * 512 + 8 * r + (tid & 7)) * 16;
            const bool two = tid < 176;
            const unsigned o2 = two ? o1 + 64 * 512 * 16 : o1;
            pace(t3, PG);
#pragma nounroll
            SPIN { get2(a[0], a[1], o1, o2, rsq); if (none((tagx(a[0], tag) | tagx(a[1], tag)) != 0u)) break; __builtin_amdgcn_s_sleep(8); }
            {
                float t = sum32(sum16x2(asf(a[0][0]) + (two ? asf(a[1][0]) : 0.f), asf(a[0][2]) + (two ? asf(a[1][2]) : 0.f)));
                t += dpp<0x128>(t);
                lwr<float>(L_RED + ((g & 1) * 8 + (l & 7)) * 32 + w * 4, t);
            }
            bar();
            STAMP(10);
            if (w == 0) {
                const f32x4 t2 = lrd<f32x4>(L_RED + ((g & 1) * 8 + (l & 7)) * 32), t4 = lrd<f32x4>(L_RED + ((g & 1) * 8 + (l & 7)) * 32 + 16);
                float glu = rbf(((t2[0] + t2[1]) + (t2[2] + t2[3])) + ((t4[0] + t4[1]) + (t4[2] + t4[3])));
                float xin = lo16(x[(g & 1) * 512 + 8 * r + (l & 7)]);
                unsigned o0 = WS_H5 + ((l >> 3) * 512 + 8 * r + (l & 7)) * 16;
                unsigned o_out = ((g & 1) * 512 + 8 * r + (l & 7)) * 2;
                PIN(glu); PIN(xin); PIN(o0); PIN(o_out);
                pace(hint_wait(rsq, o_t5, tag), P5);
                STAMP(11);
#pragma nounroll
                SPIN { CNT(3); get4s(a, o0, rsq, 65536, 131072, 196608); if (none(tagx4(a, tag) != 0u)) break; }
                STAMP(12);
                float t = sum32(sum16x2((asf(a[0][0]) + asf(a[1][0])) + (asf(a[2][0]) + asf(a[3][0])), (asf(a[0][2]) + asf(a[1][2])) + (asf(a[2][2]) + asf(a[3][2]))));
                t += dpp<0x128>(t);
                if (l < 32 && l15 < 8) __hip_atomic_store((GAS u16*)((GAS unsigned char*)out + o_out), (u16)pk(rbf(xin + rbf(t)) + glu, 0.f), __ATOMIC_RELAXED, __HIP_MEMORY_SCOPE_AGENT);
                if (r == 0 && l == 0) __hip_atomic_store((GAS unsigned*)ws, tag, __ATOMIC_RELAXED, __HIP_MEMORY_SCOPE_AGENT);
            }
        }
    } else {
        const int pr = xc * 8 + (yy >> 1), hg = yy & 1, b = pr >> 5, sp = pr & 31;
        unsigned ep, j0;
        unsigned o_t2 = WS_T2 + (xc * 128 + (16 * hg + 2 * w) * 4 + (l & 7)) * 128;
        asm volatile("global_load_dword %0, %1, %2 sc1" : "=v"(ep) : "v"(0), "s"(ws) : "memory");
        asm volatile("buffer_load_dword %0, %1, %2, 0 offen sc1" : "=v"(j0) : "v"(o_t2), "s"(rsq) : "memory");
        unsigned qoff = WS_H2 + ((((xc & 3) * 2 + b) * 32 + 16 * hg + 2 * w) * 128 + l) * 16;
        unsigned a_q0 = L_Q + 2 * w * 1024 + (((l >> 1) ^ (2 * w)) << 4) + (l & 1) * 8, a_q1 = L_Q + (2 * w + 1) * 1024 + (((l >> 1) ^ (2 * w + 1)) << 4) + (l & 1) * 8;
        unsigned o_kv[8];
#pragma unroll
        for (int i = 0; i < 8; ++i) { o_kv[i] = (pr * 128 + 64 * hg + 8 * w + i) * 1024 + ((l ^ (8 * (w & 1) + i)) << 4); PIN(o_kv[i]); }
        PIN(qoff); PIN(a_q0); PIN(a_q1);
        asm volatile("s_waitcnt vmcnt(0)" : "+v"(ep), "+v"(j0), "+v"(cp0), "+v"(cp1) :: "memory");
        const unsigned tag = mktag(ep);
        pace(t_in, PB);
#pragma unroll
        for (int i = 0; i < 8; ++i) glds(o_kv[i], ckv, L_R0 + (8 * w + i) * 1024);
        STAMP(1);
        bar0();
#pragma unroll
        for (int i = 0; i < 8; ++i) glds(o_kv[i] ^ 65536, ckv, L_R0 + (64 + 8 * w + i) * 1024);
        WAITVM(8);
        bar0();
        STAMP(2);
#define Q_ISSUE() \
            FENCE(); \
            WAITVM(2); \
            const v2u hv = hint_get(rsq, o_t2);        \
            STAMP(14); \
            pace(none(hv[0] != tag) ? hv[1] : hint_wait(rsq, o_t2, tag), P2); \
            STAMP(15); \
            v4u qa[4]; \
            asm volatile("buffer_load_dwordx4 %0, %4, %5, 0 offen sc1\n\tbuffer_load_dwordx4 %1, %4, %5, 0 offen offset:1024 sc1\n\t" \
                         "buffer_load_dwordx4 %2, %4, %5, 0 offen offset:2048 sc1\n\tbuffer_load_dwordx4 %3, %4, %5, 0 offen offset:3072 sc1" \
                         : "=&v"(qa[0]), "=&v"(qa[1]), "=&v"(qa[2]), "=&v"(qa[3]) : "v"(qoff), "s"(rsq) : "memory"); \
            bar0()
#define Q_LAND(n) do { \
            asm volatile("s_waitcnt vmcnt(" STR(n) ")" : "+v"(qa[0]), "+v"(qa[1]), "+v"(qa[2]), "+v"(qa[3]) :: "memory"); \
            if (__builtin_expect(!none(tagx4(qa, tag) != 0u), 0)) { _Pragma("nounroll") SPIN { __builtin_amdgcn_s_sleep(4); get4<1024>(qa, qoff, rsq); if (none(tagx4(qa, tag) != 0u)) break; } } \
            asm volatile("ds_write2_b32 %0, %2, %3 offset1:1\n\tds_write2_b32 %0, %4, %5 offset0:128 offset1:129\n\tds_write2_b32 %1, %6, %7 offset1:1\n\tds_write2_b32 %1, %8, %9 offset0:128 offset1:129" \
                         :: "v"(a_q0), "v"(a_q1), "v"(qa[0][0]), "v"(qa[0][2]), "v"(qa[1][0]), "v"(qa[1][2]), "v"(qa[2][0]), "v"(qa[2][2]), "v"(qa[3][0]), "v"(qa[3][2]) : "memory"); \
            STAMP(3); bar();   } while (0)
        unsigned t5 = 0;
        const int h = 4 * xc + (yy >> 2), cq = yy & 3;
        unsigned o_t4 = WS_T4 + ((h * 4 + cq) * 4 + (l & 3)) * 128;
        unsigned o_hg = (EXP & 2) ? 0 : WS_HG + ((4 * (w & 3) + g) * 1376 + 16 * min(un, 85) + l15) * 16;
        if (w >= 4) {
            unsigned a_r[4];
#pragma unroll
            for (int j = 0; j < 4; ++j) a_r[j] = l15 * 1024 + (((4 * j + g) ^ l15) << 4);
            const unsigned kb = L_R0 + (w - 4) * 16384;
            v4u cr[32], qf[16];
#pragma unroll
            for (int s = 0; s < 16; ++s) cr[s] = lrd<v4u>(a_r[s & 3] + kb + (s >> 2) * 256);
            unsigned a_mxw = L_MX + l15 * 16 + (w - 4) * 4, a_mxr = L_MX + l15 * 16;
            unsigned a_pw = L_P + l15 * 256 + (((2 * (w - 4) + (g >> 1)) ^ l15) << 4) + (g & 1) * 8;
            __builtin_amdgcn_s_setprio(1);
            PIN(a_mxw); PIN(a_mxr); PIN(a_pw); PIN(o_hg);
            Q_ISSUE();
#pragma unroll
            for (int s = 0; s < 16; ++s) cr[16 + s] = lrd<v4u>(a_r[s & 3] + kb + 65536 + (s >> 2) * 256);
            FENCE();
            Q_LAND(0);
            STAMP(4);
#pragma unroll
            for (int s = 0; s < 16; ++s) qf[s] = lrd<v4u>(a_r[s & 3] + L_Q + (s >> 2) * 256);
            FENCE();
            f32x4 s1 = ZF, s2 = ZF;
#pragma unroll
            for (int s = 0; s < 16; ++s) { s1 = mfma32(cr[s], qf[s], s1); s2 = mfma32(cr[16 + s], qf[s], s2); }
            asm volatile("s_nop 7" : "+v"(s1), "+v"(s2));
            lwr<float>(a_mxw, max32(max16(mx(mx3(mx3(s1[0], s1[1], s1[2]), s1[3], s2[0]), mx3(s2[1], s2[2], s2[3])))));
            bar();
            STAMP(12);
            const f32x4 m0 = lrd<f32x4>(a_mxr);
            const float mr = mx(mx3(m0[0], m0[1], m0[2]), m0[3]);
            v2u p1 = {pk(__builtin_amdgcn_exp2f(s1[0] - mr), __builtin_amdgcn_exp2f(s1[1] - mr)), pk(__builtin_amdgcn_exp2f(s1[2] - mr), __builtin_amdgcn_exp2f(s1[3] - mr))};
            lwr<v2u>(a_pw, p1);
            v2u p2 = {pk(__builtin_amdgcn_exp2f(s2[0] - mr), __builtin_amdgcn_exp2f(s2[1] - mr)), pk(__builtin_amdgcn_exp2f(s2[2] - mr), __builtin_amdgcn_exp2f(s2[3] - mr))};
            lwr<v2u>(a_pw ^ 128, p2);
            bar();
            STAMP(5);
            PIN(p1); PIN(p2);
            {
                const unsigned q0 = p1[0], q1 = p1[1], q2 = p2[0], q3 = p2[1];
                lwr<float>(a_mxw + 256, sum32(sum16(((lo16(q0) + hi16(q0)) + (lo16(q1) + hi16(q1))) + ((lo16(q2) + hi16(q2)) + (lo16(q3) + hi16(q3))))));
            }
            bar();
            __builtin_amdgcn_s_setprio(0);
            t5 = now();
            if (PH) pace(t5, PH);
            v4u ga, gb;
            asm volatile("buffer_load_dwordx4 %0, %3, %6, 0 offen sc1\n\tbuffer_load_dwordx4 %1, %4, %6, 0 offen sc1\n\tbuffer_load_dword %2, %5, %6, 0 offen sc1\n\ts_waitcnt vmcnt(0)"
                         : "=&v"(ga), "=&v"(gb), "=&v"(j0), "+v"(o_hg) : "v"(o_hg + 16 * 1376 * 16), "v"(o_t4), "s"(rsq) : "memory");
            if (un < 86) {
                if (!(EXP & 2) && __builtin_expect(!none((tagx(ga, tag) | tagx(gb, tag)) != 0u), 0)) {
#pragma nounroll
                    SPIN { __builtin_amdgcn_s_sleep(16); get2(ga, gb, o_hg, o_hg + 16 * 1376 * 16, rsq); if (none((tagx(ga, tag) | tagx(gb, tag)) != 0u)) break; }
                }
                lwr<float>(L_RED + (l & 31) * 32 + (w - 4) * 4, sum32(sum16x2(asf(ga[0]), asf(ga[2]))));
                lwr<float>(L_RED + (l & 31) * 32 + w * 4, sum32(sum16x2(asf(gb[0]), asf(gb[2]))));
            }
            pace(t5, PW);
#pragma unroll
            for (int i = 0; i < 8; ++i) {
                const unsigned ch = l15 ^ ((g << 2) | (i & 3));
                glds((h * 128 + 32 * (w - 4) + 4 * i + g) * 1024 + cq * 128 + (ch >> 3) * 512 + (ch & 7) * 16, wo, L_R1 + (32 * (w - 4) + 4 * i) * 256);
            }
        } else {
            unsigned a_t[2][4];
            {
                const unsigned q = l15 >> 2, p = l & 3;
#pragma unroll
                for (int j = 0; j < 8; ++j) {
                    const unsigned r0 = 8 * g + 4 * (j >> 2) + q, ch = 16 * w + 4 * p + (j & 3);
                    a_t[j >> 2][j & 3] = L_R0 + r0 * 1024 + ((ch ^ (r0 & 15)) << 4) + (p >> 1) * 8;
                }
            }
            v4u ct[8][4];
#pragma unroll
            for (int t = 0; t < 8; ++t)
#pragma unroll
                for (int s = 0; s < 2; ++s) ct[t][s] = trd2((a_t[0][t >> 1] ^ ((t & 1) * 8)) + s * 32768, (a_t[1][t >> 1] ^ ((t & 1) * 8)) + s * 32768);
            unsigned a_pr[4], a_mxr = L_MX + l15 * 16;
#pragma unroll
            for (int j = 0; j < 4; ++j) { a_pr[j] = L_P + l15 * 256 + (((4 * j + g) ^ l15) << 4); PIN(a_pr[j]); }
            unsigned o_t3 = tid < 4 ? WS_T3 + ((4 * hg + tid) * 128 + hg * 64 + pr) * 128 : OOB;
            unsigned o_h3 = WS_H3 + ((b * 32 + 16 * hg + (l >> 3)) * 4 + w) * H3S + sp * 384 + (l & 7) * 16, o_h3b = o_h3 + 8 * 4 * H3S;
            unsigned a_sw = L_R2 + w * 8192 + l15 * 144 + g * 16, a_sr = L_R2 + w * 8192 + (l >> 3) * 144 + (l & 7) * 16;
            PIN(a_sw); PIN(a_sr);
            unsigned o_wd[4], a_lr = L_MX + 256 + l15 * 16;
#pragma unroll
            for (int i = 0; i < 4; ++i) { const unsigned row = 4 * w + i; o_wd[i] = (EXP & 1) ? 0 : (16 * min(un, 85) + row) * 1024 + ((l ^ ((row & 7) << 1)) << 4); PIN(o_wd[i]); }
            PIN(a_mxr); PIN(o_t3); PIN(o_h3); PIN(o_h3b); PIN(a_lr);
            Q_ISSUE();
            v4u wst[4];
            asm volatile("global_load_dwordx4 %0, %4, %8\n\tglobal_load_dwordx4 %1, %5, %8\n\tglobal_load_dwordx4 %2, %6, %8\n\tglobal_load_dwordx4 %3, %7, %8"
                         : "=&v"(wst[0]), "=&v"(wst[1]), "=&v"(wst[2]), "=&v"(wst[3]) : "v"(o_wd[0]), "v"(o_wd[1]), "v"(o_wd[2]), "v"(o_wd[3]), "s"(wd) : "memory");
#pragma unroll
            for (int t = 0; t < 8; ++t)
#pragma unroll
                for (int s = 2; s < 4; ++s) ct[t][s] = trd2((a_t[0][t >> 1] ^ ((t & 1) * 8)) + s * 32768, (a_t[1][t >> 1] ^ ((t & 1) * 8)) + s * 32768);
            FENCE();
            Q_LAND(4);
            STAMP(4);
            hint_put(rs, o_t3, tag);
            bar0();
            const f32x4 m0 = lrd<f32x4>(a_mxr);
            float mr = mx(mx3(m0[0], m0[1], m0[2]), m0[3]);
            PIN(mr);
            bar();
            STAMP(5);
            v4u pf[4];
#pragma unroll
            for (int s = 0; s < 4; ++s) pf[s] = lrd<v4u>(a_pr[s]);
            FENCE();
            f32x4 o[8];
#define M0_(t) asm volatile("v_mfma_f32_16x16x32_bf16 %0, %1, %2, 0" : "=&v"(o[t]) : "v"(ct[t][0]), "v"(pf[0]))
#define MM(t, s) asm volatile("v_mfma_f32_16x16x32_bf16 %0, %1, %2, %0" : "+v"(o[t]) : "v"(ct[t][s]), "v"(pf[s]))
#define RDY(t) PIN(o[t])
#define SEQ() asm volatile("" ::: "memory")
#define CV(d, t, i) do { d = pk(o[t][i], o[t][i + 1]); PIN(d); } while (0)
#define PUT(n, k, A, B, C) lwr<v4u>(a_sw + (n) * 2304 + (k) * 64, v4u{A, B, C, tag})
#define GETL(n) do { x0 = lrd<v4u>(a_sr + (n) * 2304); x1 = lrd<v4u>(a_sr + (n) * 2304 + 8 * 144); } while (0)
#define OUTL(n) do { stx(rs, o_h3 + (n) * 128, x0); stx(rs, o_h3b + (n) * 128, x1); } while (0)
            unsigned d0, d1, d2, d3, d4, d5;
            v4u x0, x1;
            M0_(0); M0_(1); MM(0, 1); MM(1, 1); MM(0, 2); MM(1, 2); MM(0, 3); MM(1, 3);
            M0_(2); MM(2, 1); MM(2, 2); MM(2, 3);
            M0_(3); MM(3, 1); MM(3, 2);
            RDY(0); CV(d0, 0, 0); CV(d1, 0, 2);
            MM(3, 3); RDY(1); CV(d2, 1, 0); CV(d3, 1, 2);
            M0_(4); PUT(0, 0, d0, d1, d2); SEQ();
            MM(4, 1); RDY(2); CV(d4, 2, 0); CV(d5, 2, 2);
            MM(4, 2); PUT(0, 1, d3, d4, d5); GETL(0); SEQ();
            MM(4, 3); RDY(3); CV(d0, 3, 0); CV(d1, 3, 2);
            M0_(5);
            bar0();
            const f32x4 l0 = lrd<f32x4>(a_lr);
            MM(5, 1); MM(5, 2); MM(5, 3);
            RDY(4); CV(d2, 4, 0); CV(d3, 4, 2);
            STAMP2(3);
            M0_(6); OUTL(0); PUT(1, 0, d0, d1, d2); SEQ();
            STAMP2(4);
            MM(6, 1);
            float lsum = (l0[0] + l0[1]) + (l0[2] + l0[3]);
            PIN(lsum);
            MM(6, 2); MM(6, 3);
            RDY(5); CV(d4, 5, 0); CV(d5, 5, 2);
            M0_(7); PUT(1, 1, d3, d4, d5); GETL(1); SEQ();
            MM(7, 1); MM(7, 2); MM(7, 3);
            asm volatile("s_nop 1" : "+v"(o[6]));
            CV(d0, 6, 0); CV(d1, 6, 2);
            STAMP2(14);
            OUTL(1); SEQ();
            STAMP2(15);
            asm volatile("s_nop 3" : "+v"(o[7]));
            CV(d2, 7, 0); CV(d3, 7, 2);
            PUT(2, 0, d0, d1, d2); PUT(2, 1, d3, asu(mr), asu(lsum)); GETL(2); OUTL(2);
            FENCE();
            STAMP(6);
            asm volatile("s_waitcnt vmcnt(7)" : "+v"(wst[0]), "+v"(wst[1]), "+v"(wst[2]), "+v"(wst[3]) :: "memory");
#pragma unroll
            for (int i = 0; i < 4; ++i) lwr<v4u>(L_R0 + (4 * w + i) * 1024 + l * 16, wst[i]);
        }
        if (STOPAT <= 7) goto done;
        bar();
        if (un < 86) {
            if (w == 4) {
                const f32x4 gp = lrd<f32x4>(L_RED + (l & 31) * 32), up = lrd<f32x4>(L_RED + (l & 31) * 32 + 16);
                const float gg = rbf((gp[0] + gp[1]) + (gp[2] + gp[3])), uu = rbf((up[0] + up[1]) + (up[2] + up[3]));
                const float sg = rbf(__builtin_amdgcn_rcpf(1.f + __builtin_amdgcn_exp2f(gg * -1.4426950408889634f)));
                lwr<u16>(L_HH + (l & 31) * 2, (u16)pk(sg * uu, 0.f));
            }
            bar();
            const v2u ha = lrd<v2u>(L_HH + (l & 1) * 32 + g * 8);
            const unsigned q = l15 >> 2, p = l & 3, row = 4 * g + q;
            f32x4 o[4];
#pragma unroll
            for (int t = 0; t < 4; ++t) {
                const unsigned ch = 8 * w + 2 * t + (p >> 1);
                o[t] = mfma16(ha, trd(L_R0 + row * 1024 + ((ch ^ ((row & 7) << 1)) << 4) + (p & 1) * 8), ZF);
            }
            const float o0 = g == 0 ? o[0][0] : g == 1 ? o[1][0] : g == 2 ? o[2][0] : o[3][0];
            const float o1 = g == 0 ? o[0][1] : g == 1 ? o[1][1] : g == 2 ? o[2][1] : o[3][1];
            stx(rs, WS_H5 + ((32 + un) * 512 + 64 * w + l) * 16, v4u{asu(o0), tag, asu(o1), tag});
        }
        STAMP(7);
        {
            unsigned a0, a1;
            {
                const unsigned q = l15 >> 2, p = l & 3, ch = 2 * w + (p >> 1);
                a0 = L_R1 + (8 * g + q) * 256 + 16 * (ch ^ ((q << 2) | ((2 * g) & 3))) + 8 * (p & 1);
                a1 = L_R1 + (8 * g + q + 4) * 256 + 16 * (ch ^ ((q << 2) | ((2 * g + 1) & 3))) + 8 * (p & 1);
            }
            unsigned o_g4 = WS_H4 + ((h * 4 + g) * 128 + 16 * w + l15) * 16;
            unsigned o_t5 = tid < 16 ? WS_T5 + ((8 * (cq + 4 * (tid >> 3)) + (tid & 7)) * 32 + h) * 128 : OOB;
            unsigned o_h5 = l < 16 ? WS_H5 + (h * 512 + 64 * (cq + 4 * (w >> 2)) + 16 * (w & 3) + l) * 16 : OOB;
            unsigned a_qw = L_QS + (g & 1) * 272 + (16 * w + l15) * 2, a_qr = L_QS + (l & 1) * 272 + 16 * g;
            v4u pb = {0u, tag, 0u, tag}, fw[4], ga;
            WAITVM(0);
            bar();
            frags128(a0, a1, fw);
            PIN(fw[0]); PIN(fw[1]); PIN(fw[2]); PIN(fw[3]); PIN(o_g4); PIN(o_t5); PIN(o_h5); PIN(a_qw); PIN(a_qr); PIN(pb);
            STAMP(10);
            if (w == 4) pace(hint_wait(rsq, o_t4, tag), P4);
            bar0();
            STAMP(11);
            asm volatile("buffer_load_dwordx4 %0, %1, %2, 0 offen sc1" : "=&v"(ga) : "v"(o_g4), "s"(rsq) : "memory");
            hint_put(rs, o_t5, tag);
            asm volatile("s_waitcnt vmcnt(1)" : "+v"(ga) :: "memory");
            if (__builtin_expect(!none(tagx(ga, tag) != 0u), 0)) {
#pragma nounroll
                SPIN { __builtin_amdgcn_s_sleep(2); get1(ga, o_g4, rsq); if (none(tagx(ga, tag) != 0u)) break; }
            }
            STAMP(8);
            lwr<u16>(a_qw, (u16)pk(sum32(sum16x2(asf(ga[0]), asf(ga[2]))), 0.f));
            bar();
            v4u cf[4];
#pragma unroll
            for (int s = 0; s < 4; ++s) cf[s] = lrd<v4u>(a_qr + 64 * s);
            FENCE();
            const f32x4 acc = gemv4(cf, fw);
            pb[0] = asu(acc[0]); pb[2] = asu(acc[1]);
            stx(rs, o_h5, pb);
        }
        STAMP(9);
    }
done:
    STAMP(13);
}

extern "C" void kernel_launch(void* const* d_in, const int* in_sizes, int n_in, void* d_out, int out_size, void* d_ws, size_t ws_size,
                              hipStream_t stream) {
    (void)in_sizes; (void)n_in; (void)out_size; (void)ws_size;
    (void)hipFuncSetAttribute((const void*)mla_glu_decode, hipFuncAttributeMaxDynamicSharedMemorySize, L_END);
    mla_glu_decode<<<256, 512, L_END, stream>>>((const u16*)d_in[0], (const u16*)d_in[1], (const u16*)d_in[2], (const u16*)d_in[3], (const u16*)d_in[4],
                                               (const u16*)d_in[5], (const u16*)d_in[7], (const u16*)d_in[8], (const u16*)d_in[9], (u16*)d_out,
                                               (unsigned char*)d_ws);
}
```

```cpp
#define STAMPS 1
#define SMASK 0x0003
#include <hip/hip_runtime.h>
#include <hip/hip_bf16.h>

typedef unsigned short u16;
typedef unsigned v4u __attribute__((ext_vector_type(4)));
typedef unsigned v2u __attribute__((ext_vector_type(2)));
typedef float f32x4 __attribute__((ext_vector_type(4)));
typedef float f32x2 __attribute__((ext_vector_type(2)));
typedef short s16x4 __attribute__((ext_vector_type(4)));
typedef __bf16 bf16x8 __attribute__((ext_vector_type(8)));
typedef __bf16 bf16x2 __attribute__((ext_vector_type(2)));
#define LAS __attribute__((address_space(3)))
#define GAS __attribute__((address_space(1)))
#define DEVI __device__ __forceinline__

#define PB 95
#define P2 100
#define P3 180
#define P4 65
#define P5 62
#define PG 220
#define PW 60
#define PA 0
#define CODEB 13696
#define EXP 0
#define PH 0
#define H3S 12288
#define STOPAT 99

constexpr int WS_T2 = 4096;
constexpr int WS_T3 = WS_T2 + 8 * 128 * 128;
constexpr int WS_T5 = WS_T3 + 8 * 128 * 128;
constexpr int WS_T4 = WS_T5 + 64 * 32 * 128;
constexpr int WS_H1 = WS_T4 + 32 * 16 * 128;
constexpr int WS_H4 = WS_H1 + 32 * 4 * 128 * 16;
constexpr int WS_H2 = WS_H4 + 32 * 4 * 128 * 16;
constexpr int WS_HG = WS_H2 + 4 * 2 * 32 * 128 * 16;
constexpr int WS_H5 = WS_HG + 2 * 16 * 1376 * 16;
constexpr int WS_H3 = WS_H5 + 118 * 512 * 16;
constexpr int WS_END = WS_H3 + 2 * 32 * 4 * H3S;
constexpr int WS_STAMP = 16 << 20;
constexpr int L_Q = 0;
constexpr int L_P = 16384;
constexpr int L_QS = L_P + 4096;
constexpr int L_OW = L_QS + 1024;
constexpr int L_MW = L_OW + 2304;
constexpr int L_MX = L_MW + 64;
constexpr int L_HH = L_MX + 512;
constexpr int L_TAG = L_HH + 64;
constexpr int L_JUNK = L_TAG + 64;
constexpr int L_RED = L_JUNK + 256;
constexpr int L_XN = 28672;
constexpr int L_R0 = 32768, L_R1 = 65536, L_R2 = 98304, L_R3 = 131072;
constexpr int L_END = 163840;
static_assert(L_RED + 1024 <= L_XN && L_XN + 2048 <= L_R0, "lds");
constexpr int SPIN_CAP = 1 << 16;

DEVI unsigned pk(float lo, float hi) { bf16x2 r = {(__bf16)lo, (__bf16)hi}; return __builtin_bit_cast(unsigned, r); }
DEVI float rbf(float f) { return (float)(__bf16)f; }
DEVI float lo16(unsigned w) { return __builtin_bit_cast(float, w << 16); }
DEVI float hi16(unsigned w) { return __builtin_bit_cast(float, w & 0xffff0000u); }
DEVI float asf(unsigned w) { return __builtin_bit_cast(float, w); }
DEVI unsigned asu(float w) { return __builtin_bit_cast(unsigned, w); }
DEVI float mx(float a, float b) { float r; asm("v_max_f32 %0, %1, %2" : "=v"(r) : "v"(a), "v"(b)); return r; }
DEVI float mx3(float a, float b, float c) { float r; asm("v_max3_f32 %0, %1, %2, %3" : "=v"(r) : "v"(a), "v"(b), "v"(c)); return r; }
DEVI f32x4 mfma32(v4u a, v4u b, f32x4 c) { return __builtin_amdgcn_mfma_f32_16x16x32_bf16(__builtin_bit_cast(bf16x8, a), __builtin_bit_cast(bf16x8, b), c, 0, 0, 0); }
DEVI f32x4 mfma16(v2u a, v2u b, f32x4 c) { return __builtin_amdgcn_mfma_f32_16x16x16bf16_1k(__builtin_bit_cast(s16x4, a), __builtin_bit_cast(s16x4, b), c, 0, 0, 0); }
template <class T> DEVI T lrd(unsigned off) { return *(LAS T*)(size_t)off; }
template <class T> DEVI void lwr(unsigned off, T v) { *(LAS T*)(size_t)off = v; }
DEVI v2u trd(unsigned off) { return __builtin_bit_cast(v2u, __builtin_amdgcn_ds_read_tr16_b64_v4i16((LAS s16x4*)(size_t)off)); }
DEVI v4u trd2(unsigned o0, unsigned o1) { v2u a = trd(o0), b = trd(o1); return v4u{a.x, a.y, b.x, b.y}; }
DEVI float sum16(float x) { auto r = __builtin_amdgcn_permlane16_swap(asu(x), asu(x), false, false); return asf(r[0]) + asf(r[1]); }
DEVI float sum32(float x) { auto r = __builtin_amdgcn_permlane32_swap(asu(x), asu(x), false, false); return asf(r[0]) + asf(r[1]); }
DEVI float max16(float x) { auto r = __builtin_amdgcn_permlane16_swap(asu(x), asu(x), false, false); return mx(asf(r[0]), asf(r[1])); }
DEVI float max32(float x) { auto r = __builtin_amdgcn_permlane32_swap(asu(x), asu(x), false, false); return mx(asf(r[0]), asf(r[1])); }
DEVI float odd16(float x) { auto r = __builtin_amdgcn_permlane16_swap(asu(x), asu(x), false, false); return asf(r[1]); }
DEVI float sum16x2(float x, float y) { auto r = __builtin_amdgcn_permlane16_swap(asu(x), asu(y), false, false); return asf(r[0]) + asf(r[1]); }
DEVI float sum32x2(float x, float y) { auto r = __builtin_amdgcn_permlane32_swap(asu(x), asu(y), false, false); return asf(r[0]) + asf(r[1]); }
template <int CTRL> DEVI float dpp(float x) { return asf((unsigned)__builtin_amdgcn_update_dpp(0, (int)asu(x), CTRL, 0xf, 0xf, true)); }
DEVI float wavesum(float x) {
    x += dpp<0xB1>(x); x += dpp<0x4E>(x); x += dpp<0x141>(x); x += dpp<0x140>(x);
    return sum32(sum16(x));
}
DEVI float dot2one(unsigned p, float c) { const unsigned one = 0x3f803f80u; return __builtin_amdgcn_fdot2_f32_bf16(__builtin_bit_cast(bf16x2, p), __builtin_bit_cast(bf16x2, one), c, false); }
#define STR_(x) #x
#define STR(x) STR_(x)
#define WAITVM(n) asm volatile("s_waitcnt vmcnt(" STR(n) ")" ::: "memory")
#define PIN(x) asm volatile("" : "+v"(x))
#define MFMA_SETTLE(x) asm volatile("s_nop 7" : "+v"(x))
#define UNDEF(x) asm volatile("" : "=v"(x))
#define FENCE() __builtin_amdgcn_sched_barrier(0)
#define SPIN for (int it_ = SPIN_CAP; it_ > 0; --it_)
DEVI void bar() { asm volatile("s_waitcnt lgkmcnt(0)\n\ts_barrier" ::: "memory"); }
DEVI void bar0() { asm volatile("s_barrier" ::: "memory"); }
#define USE_NT 0
template <bool NT = false> DEVI void glds(unsigned voff, const void* sbase, unsigned lds) {
    unsigned keep;
    if (NT && USE_NT) asm volatile("s_mov_b32 %0, m0\n\ts_mov_b32 m0, %3\n\ts_nop 0\n\tglobal_load_lds_dwordx4 %1, %2 nt\n\ts_mov_b32 m0, %0" : "=&s"(keep) : "v"(voff), "s"(sbase), "s"(lds) : "memory");
    else asm volatile("s_mov_b32 %0, m0\n\ts_mov_b32 m0, %3\n\ts_nop 0\n\tglobal_load_lds_dwordx4 %1, %2\n\ts_mov_b32 m0, %0" : "=&s"(keep) : "v"(voff), "s"(sbase), "s"(lds) : "memory");
}
typedef __amdgpu_buffer_rsrc_t rsrc_t;
constexpr unsigned OOB = 0x80000000u;
DEVI void stx(rsrc_t rs, unsigned off, v4u v) { __builtin_amdgcn_raw_buffer_store_b128(v, rs, off, 0, 16); }
DEVI bool none(bool bad) { asm volatile("" ::: "memory"); return __builtin_amdgcn_ballot_w64(bad) == 0ull; }
DEVI unsigned tagx(v4u a, unsigned tag) { unsigned r = (a[1] ^ tag) | (a[3] ^ tag); asm("" : "+v"(r)); return r; }
DEVI unsigned tagy(unsigned a, unsigned b, unsigned tag) { unsigned r = (a ^ tag) | (b ^ tag); asm("" : "+v"(r)); return r; }
DEVI unsigned tagx4(const v4u (&a)[4], unsigned tag) { return (tagx(a[0], tag) | tagx(a[1], tag)) | (tagx(a[2], tag) | tagx(a[3], tag)); }
DEVI void get1(v4u& a, unsigned off, v4u rq) { asm volatile("buffer_load_dwordx4 %0, %1, %2, 0 offen sc1\n\ts_waitcnt vmcnt(0)" : "+&v"(a) : "v"(off), "s"(rq) : "memory"); }
DEVI void get2(v4u& a, v4u& b, unsigned oa, unsigned ob, v4u rq) {
    asm volatile("buffer_load_dwordx4 %0, %2, %4, 0 offen sc1\n\tbuffer_load_dwordx4 %1, %3, %4, 0 offen sc1\n\ts_waitcnt vmcnt(0)" : "+&v"(a), "+&v"(b) : "v"(oa), "v"(ob), "s"(rq) : "memory");
}
template <int ST> DEVI void get4(v4u (&a)[4], unsigned off, v4u rq) {
    asm volatile("buffer_load_dwordx4 %0, %4, %5, 0 offen sc1\n\tbuffer_load_dwordx4 %1, %4, %5, 0 offen offset:%6 sc1\n\t"
                 "buffer_load_dwordx4 %2, %4, %5, 0 offen offset:%7 sc1\n\tbuffer_load_dwordx4 %3, %4, %5, 0 offen offset:%8 sc1\n\ts_waitcnt vmcnt(0)"
                 : "+&v"(a[0]), "+&v"(a[1]), "+&v"(a[2]), "+&v"(a[3]) : "v"(off), "s"(rq), "i"(ST), "i"(2 * ST), "i"(3 * ST) : "memory");
}
DEVI void get4s(v4u (&a)[4], unsigned off, v4u rq, unsigned s1, unsigned s2, unsigned s3) {
    asm volatile("buffer_load_dwordx4 %0, %4, %5, 0 offen sc1\n\tbuffer_load_dwordx4 %1, %4, %5, %6 offen sc1\n\t"
                 "buffer_load_dwordx4 %2, %4, %5, %7 offen sc1\n\tbuffer_load_dwordx4 %3, %4, %5, %8 offen sc1\n\ts_waitcnt vmcnt(0)"
                 : "+&v"(a[0]), "+&v"(a[1]), "+&v"(a[2]), "+&v"(a[3]) : "v"(off), "s"(rq), "s"(s1), "s"(s2), "s"(s3) : "memory");
}
DEVI unsigned now() { return (unsigned)__builtin_amdgcn_s_memrealtime(); }
DEVI void hint_put(rsrc_t rs, unsigned off, unsigned tag, unsigned t) { __builtin_amdgcn_raw_buffer_store_b64(v2u{tag, t}, rs, off, 0, 16); }
DEVI void hint_put(rsrc_t rs, unsigned off, unsigned tag) { hint_put(rs, off, tag, now()); }
DEVI v2u hint_get(v4u rq, unsigned off) { v2u v; asm volatile("buffer_load_dwordx2 %0, %1, %2, 0 offen sc1\n\ts_waitcnt vmcnt(0)" : "=&v"(v) : "v"(off), "s"(rq) : "memory"); return v; }
DEVI void pace(unsigned t, unsigned lead) {
#pragma nounroll
    for (int it = 256; it > 0; --it) { if (none(now() - t < lead)) break; }
}
DEVI unsigned hint_wait(v4u rq, unsigned off, unsigned tag) {
    v2u v;
#pragma nounroll
    SPIN { v = hint_get(rq, off); if (none(v[0] != tag)) break; __builtin_amdgcn_s_sleep(1); }
    return v[1];
}
DEVI unsigned mktag(unsigned ep) { return ep + 1u; }

#define NST 16
#define STAMP(i) do { if (!((SMASK >> (i)) & 1)) break; FENCE(); asm volatile("s_memrealtime %0\n\ts_waitcnt lgkmcnt(0)" : "=s"(st[i]) :: "memory"); FENCE(); } while (0)

#define CNT0(i)
#define CNT(i)
#define STAMPC(i)
#define STAMP2(i)
DEVI void frags128(unsigned a0, unsigned a1, v4u (&f)[4]) {
#pragma unroll
    for (int s = 0; s < 4; ++s) f[s] = trd2(a0 + s * 8192, a1 + s * 8192);
}
DEVI f32x4 gemv4(const v4u (&a)[4], const v4u (&b)[4]) {
    f32x4 c = {0.f, 0.f, 0.f, 0.f};
#pragma unroll
    for (int s = 0; s < 4; ++s) c = mfma32(a[s], b[s], c);
    return c;
}
DEVI void layernorm(v4u xv, int w, int l) {
    float v[8];
#pragma unroll
    for (int j = 0; j < 4; ++j) { v[2 * j] = lo16(xv[j]); v[2 * j + 1] = hi16(xv[j]); }
    const float mean = wavesum(((v[0] + v[1]) + (v[2] + v[3])) + ((v[4] + v[5]) + (v[6] + v[7]))) * (1.f / 512.f);
    float sq = 0.f;
#pragma unroll
    for (int j = 0; j < 8; ++j) { const float d = v[j] - mean; sq += d * d; }
    const float var = wavesum(sq) * (1.f / 512.f);
    const float mb = rbf(mean), ri = __builtin_amdgcn_rcpf(rbf(__builtin_amdgcn_sqrtf(rbf(rbf(var) + 1e-5f))));
    v4u o;
#pragma unroll
    for (int j = 0; j < 4; ++j) o[j] = pk(rbf(v[2 * j] - mb) * ri, rbf(v[2 * j + 1] - mb) * ri);
    lwr<v4u>(L_XN + w * 1024 + l * 16, o);
}

__global__ void __launch_bounds__(512, 2)
mla_glu_decode(const u16* __restrict__ x, const u16* __restrict__ ckv, const u16* __restrict__ wq, const u16* __restrict__ wuk,
               const u16* __restrict__ wuv, const u16* __restrict__ wo, const u16* __restrict__ wg, const u16* __restrict__ wu,
               const u16* __restrict__ wd, u16* __restrict__ out, unsigned char* __restrict__ ws) {
    unsigned long long st[NST];
#pragma unroll
    for (int i = 0; i < NST; ++i) st[i] = 0;
    STAMP(0);
    const int tid = threadIdx.x, l = tid & 63, w = __builtin_amdgcn_readfirstlane(tid >> 6);
    const int id = blockIdx.x, xc = id & 7, y = id >> 3, yy = y & 15;
    const int un = yy * 8 + xc;
    const unsigned g = l >> 4, l15 = l & 15;
    const unsigned t_in = now();
    unsigned cp0 = 0, cp1 = 0;
    if (y >= 16 && w == 2) {
        unsigned long long pc;
        asm volatile("s_getpc_b64 %0" : "=s"(pc));
        pc &= ~255ull;
        asm volatile("global_load_dword %0, %2, %4\n\tglobal_load_dword %1, %3, %4"
                     : "=&v"(cp0), "=&v"(cp1) : "v"(min(l * 128, CODEB - 128)), "v"(min(8192 + l * 128, CODEB - 128)), "s"(pc) : "memory");
    }
    asm volatile("" :: "s"(x), "s"(ckv), "s"(wq), "s"(wuk), "s"(wuv), "s"(wo), "s"(wg), "s"(wu), "s"(wd), "s"(out), "s"(ws));
    const rsrc_t rs = __builtin_amdgcn_make_buffer_rsrc(ws, 0, WS_END, 0x00020000);
    const v4u rsq = {(unsigned)(size_t)ws, (unsigned)((size_t)ws >> 32) & 0xffffu, (unsigned)WS_END, 0x00020000u};
    const f32x4 ZF = {0.f, 0.f, 0.f, 0.f};

    if (__builtin_expect(y < 16, 1)) {
        const int h = 4 * xc + (yy >> 2), cq = yy & 3;
        v4u xv; unsigned ep;
        if (w < 2) {
            asm volatile("global_load_dwordx4 %0, %1, %2" : "=v"(xv) : "v"(w * 1024 + l * 16), "s"(x) : "memory");
            asm volatile("global_load_dword %0, %1, %2 sc1" : "=v"(ep) : "v"(0), "s"(ws) : "memory");
        }
        const unsigned vb = (128 * cq + 16 * w + g) * 8192 + h * 256;
        unsigned sw[4];
#pragma unroll
        for (int i = 0; i < 4; ++i) sw[i] = (l15 ^ ((g << 2) | i)) << 4;
#pragma unroll
        for (int i = 0; i < 4; ++i) glds<true>(vb + i * 32768 + sw[i], wq, L_R0 + (16 * w + 4 * i) * 256);
        bar0();
#pragma unroll
        for (int i = 0; i < 4; ++i) glds<true>(vb + i * 32768 + ((l15 ^ (4 * i + g)) << 4), wuk, L_R1 + (16 * w + 4 * i) * 256);
        STAMP(1);
        if (w < 2) {
            asm volatile("s_waitcnt vmcnt(8)" : "+v"(xv), "+v"(ep) :: "memory");
            layernorm(xv, w, l);
            lwr<v4u>(L_TAG, v4u{mktag(ep), 0u, 0u, 0u});
        }
        bar();
        const unsigned tag = lrd<unsigned>(L_TAG);
        unsigned o_p1 = l < 16 ? WS_H1 + ((h * 4 + cq) * 128 + 16 * w + l) * 16 : OOB;
        unsigned o_g1 = WS_H1 + ((h * 4 + g) * 128 + 16 * w + l15) * 16;
        unsigned o_h2 = WS_H2 + ((((w & 3) * 2 + (l >> 5)) * 32 + h) * 128 + 32 * cq + (l & 31)) * 16;
        unsigned a_2w = l15 < 2 ? L_RED + l15 * 256 + (4 * w + g) * 8 : L_JUNK + (l & 31) * 8, a_2r = L_RED + l * 8;
        PIN(a_2w); PIN(a_2r);
        unsigned o_t2 = l < 8 ? WS_T2 + (l * 128 + h * 4 + cq) * 128 : OOB;
        unsigned a0, a1;
        {
            const unsigned q = l15 >> 2, p = l & 3, ch = 2 * w + (p >> 1);
            a0 = L_R0 + (8 * g + q) * 256 + 16 * (ch ^ ((q << 2) | ((2 * g) & 3))) + 8 * (p & 1);
            a1 = L_R0 + (8 * g + q + 4) * 256 + 16 * (ch ^ ((q << 2) | ((2 * g + 1) & 3))) + 8 * (p & 1);
        }
        unsigned a_qw = L_QS + (g & 1) * 272 + (16 * w + l15) * 2;
        unsigned a_qr = L_QS + (l & 1) * 272 + 16 * g;
        unsigned a_uk[4], o_gu[3], a_cnt = l == 0 ? L_TAG + 8 : L_JUNK + l * 4, one = 1u;
        v4u xf[4];
#pragma unroll
        for (int s = 0; s < 4; ++s) {
            xf[s] = lrd<v4u>(L_XN + (l & 1) * 1024 + (128 * cq + 32 * s + 8 * g) * 2);
            a_uk[s] = L_R1 + (16 * w + l15) * 256 + (((4 * s + g) ^ l15) << 4);
            PIN(xf[s]); PIN(a_uk[s]);
        }
        const int mat = un >> 6, mp = (un >> 2) & 15, fp = un & 3;
#pragma unroll
        for (int i = 0; i < 3; ++i) {
            const unsigned pc = min(8 * i + w, 21), idx = min(64 * pc + l, 1375u), row = idx / 43u, ch = idx - row * 43u;
            o_gu[i] = (32 * mp + row) * 2752 + fp * 688 + ch * 16; PIN(o_gu[i]);
        }
        v4u pb = {0u, tag, 0u, tag}, ga;
        PIN(a0); PIN(a1); PIN(o_p1); PIN(o_g1); PIN(o_h2); PIN(o_t2); PIN(a_qw); PIN(a_qr); PIN(pb); PIN(a_cnt); PIN(one);
        WAITVM(4);
        bar0();
        STAMP(2);
        {
            v4u f[4]; frags128(a0, a1, f);
            FENCE();
            const f32x4 acc = gemv4(xf, f);
            pb[0] = asu(acc[0]); pb[2] = asu(acc[1]);
            stx(rs, o_p1, pb);
        }
        STAMP(3);
        if (STOPAT <= 3) { WAITVM(0); STAMP(14); goto done; }
        __builtin_amdgcn_s_sleep(10);
        WAITVM(3);
        v4u fu[4];
        {
            unsigned arr;
            asm volatile("buffer_load_dwordx4 %1, %4, %5, 0 offen sc1\n\tds_add_rtn_u32 %0, %2, %3\n\ts_waitcnt lgkmcnt(0)" : "=&v"(arr), "=&v"(ga) : "v"(a_cnt), "v"(one), "v"(o_g1), "s"(rsq) : "memory");
            hint_put(rs, __builtin_amdgcn_readfirstlane(arr) == 7u ? o_t2 : OOB, tag);
            asm volatile("s_waitcnt vmcnt(1)" : "+v"(ga) :: "memory");
            STAMP(14);
#pragma unroll
            for (int s = 0; s < 4; ++s) fu[s] = lrd<v4u>(a_uk[s]);
            if (__builtin_expect(!none(tagx(ga, tag) != 0u), 0)) {
#pragma nounroll
                SPIN { __builtin_amdgcn_s_sleep(4); get1(ga, o_g1, rsq); if (none(tagx(ga, tag) != 0u)) break; }
            }
            lwr<u16>(a_qw, (u16)pk(sum32(sum16x2(asf(ga[0]), asf(ga[2]))), 0.f));
        }
        STAMP(4);
        bar();
        {
            v4u qf[4];
#pragma unroll
            for (int s = 0; s < 4; ++s) qf[s] = lrd<v4u>(a_qr + 64 * s);
            FENCE();
            const f32x4 acc = gemv4(fu, qf);
            constexpr float SC = 0.08838834764831845f * 1.4426950408889634f;
            lwr<v2u>(a_2w, v2u{pk(acc[0] * SC, acc[1] * SC), pk(acc[2] * SC, acc[3] * SC)});
            bar();
            if (w < 4) {
                const v2u t = lrd<v2u>(a_2r);
                pb[0] = t[0]; pb[2] = t[1];
                stx(rs, o_h2, pb);
            }
        }
        STAMP(5);
        if (STOPAT <= 5) goto done;
        const int r = xc * 8 + (yy >> 1);
        unsigned o_t3 = WS_T3 + (xc * 128 + (h >> 4) * 64 + 8 * w + (l & 7)) * 128, o_t5 = WS_T5 + (r * 32 + (l & 31)) * 128, j0, j1;
        asm volatile("buffer_load_dword %0, %2, %4, 0 offen sc1\n\tbuffer_load_dword %1, %3, %4, 0 offen sc1"
                     : "=&v"(j0), "=&v"(j1) : "v"(o_t3), "v"((w == 0 && !(yy & 1)) ? o_t5 : o_t3), "s"(rsq) : "memory");
        pace(t_in, PA);
#pragma unroll
        for (int i = 0; i < 3; ++i) glds(o_gu[i], mat ? wu : wg, L_R2 + min(8 * i + w, 21) * 1024);
#define V_ISSUE(i) glds<true>((EXP & 4) ? 0 : vb + (i) * 32768 + sw[i], wuv, L_R1 + (16 * w + 4 * (i)) * 256)
        V_ISSUE(0); V_ISSUE(1);
        {
            const v4u xg = lrd<v4u>(L_XN + (l & 1) * 1024 + (32 * mp + 8 * g) * 2);
            const unsigned ag = L_R2 + (8 * g + (l15 >> 2)) * 688 + (l & 3) * 8;
            const unsigned f = 16 * (8 * g + w) + l15;
            const unsigned o_hg = (g < 3 && f < 344) ? WS_HG + ((mat * 16 + mp) * 1376 + 344 * fp + f) * 16 : OOB;
            asm volatile("s_waitcnt vmcnt(2)" : "+v"(j0), "+v"(j1) :: "memory");
            bar();
            f32x4 o[3];
#pragma unroll
            for (int t = 0; t < 3; ++t) {
                const unsigned tl = min(8 * t + w, 21);
                o[t] = mfma32(xg, trd2(ag + tl * 32, ag + tl * 32 + 4 * 688), ZF);
            }
            const float o0 = g == 0 ? o[0][0] : g == 1 ? o[1][0] : o[2][0], o1 = g == 0 ? o[0][1] : g == 1 ? o[1][1] : o[2][1];
            stx(rs, o_hg, v4u{asu(o0), tag, asu(o1), tag});
        }
        V_ISSUE(2); V_ISSUE(3);
        STAMP(6);
        const unsigned u = l15 >= 12 ? 8 + l15 : (g & 1) ? 12 + (l15 & 7) : l15;
        const bool act = (g & 1) ? (l15 & 12) != 8 : l15 < 12;
        unsigned o_h3 = WS_H3 + (((w >> 2) * 32 + h) * 4 + cq) * H3S + (8 * (w & 3) + (g >> 1)) * 384 + u * 16;
        unsigned o_t4 = l < 4 ? WS_T4 + ((h * 4 + l) * 4 + cq) * 128 : OOB;
        unsigned o_p4 = l < 16 ? WS_H4 + ((h * 4 + cq) * 128 + 16 * w + l) * 16 : OOB;
        unsigned a_ow, a_ow2, a_ms;
        {
            const unsigned n = 6 * (u >> 2) + 2 * (g >> 1), n2 = 6 * (u >> 2) + 4, gg = u & 3;
            a_ow = (act && n < 32) ? L_OW + w * 272 + (32 * gg + 4 * ((n >> 2) ^ (gg >> 1)) + (n & 3)) * 2 : L_JUNK + l * 4;
            a_ow2 = (act && n2 < 32 && l < 32) ? L_OW + w * 272 + (32 * gg + 4 * ((n2 >> 2) ^ (gg >> 1)) + (n2 & 3)) * 2 : L_JUNK + l * 4;
            a_ms = (l & 31) == 12 ? L_MW + (g >> 1) * 32 + w * 4 : L_JUNK + l * 4;
        }
        unsigned a_or = L_OW + (l & 7) * 272 + 16 * g, a_mw = L_MW + (g & 1) * 16;
        v4u a[4];
        UNDEF(a[0]); UNDEF(a[1]); UNDEF(a[2]); UNDEF(a[3]);
        a_cnt += l == 0 ? 4 : 0;
        PIN(o_h3); PIN(o_t3); PIN(o_t4); PIN(o_p4); PIN(a_ow); PIN(a_ow2); PIN(a_or); PIN(a_mw); PIN(a_ms); PIN(a_cnt);
        WAITVM(0);
        bar();
        v4u fv[4];
        frags128(a0 + (L_R1 - L_R0), a1 + (L_R1 - L_R0), fv);
        PIN(fv[0]); PIN(fv[1]); PIN(fv[2]); PIN(fv[3]);
        STAMP(7);
        if (STOPAT <= 7) goto done;
        CNT0(2); CNT0(3);
        const unsigned t3 = hint_wait(rsq, o_t3, tag);
        STAMPC(1);
        pace(t3, P3);
        STAMP(15);
        {
#pragma nounroll
            SPIN { CNT(2); get4<768>(a, o_h3, rsq); if (none((tagy(a[0][3], a[1][3], tag) | tagy(a[2][3], a[3][3], tag)) != 0u)) break; __builtin_amdgcn_s_sleep(4); }
            STAMP(8);
            unsigned arr;
            unsigned long long tm;
            asm volatile("ds_add_rtn_u32 %0, %2, %3\n\ts_memrealtime %1" : "=&v"(arr), "=&s"(tm) : "v"(a_cnt), "v"(one) : "memory");
            float m[4], al[4];
#pragma unroll
            for (int k = 0; k < 4; ++k) m[k] = dpp<0x15c>(asf(a[k][1]));
            const float M = max32(mx(mx3(m[0], m[1], m[2]), m[3]));
#pragma unroll
            for (int k = 0; k < 4; ++k) al[k] = __builtin_amdgcn_exp2f(m[k] - M);
            FENCE();
            asm volatile("s_waitcnt lgkmcnt(0)" : "+v"(arr), "+s"(tm) :: "memory");
            hint_put(rs, __builtin_amdgcn_readfirstlane(arr) == 7u ? o_t4 : OOB, tag, (unsigned)tm);
            f32x2 c01, c23, c45;
            float Ls;
#pragma unroll
            for (int k = 0; k < 4; ++k) {
                const f32x2 aa = {al[k], al[k]}, v01 = {lo16(a[k][0]), hi16(a[k][0])}, v23 = {lo16(a[k][1]), hi16(a[k][1])}, v45 = {lo16(a[k][2]), hi16(a[k][2])};
                if (k == 0) { c01 = aa * v01; c23 = aa * v23; c45 = aa * v45; Ls = al[0] * asf(a[0][2]); }
                else { c01 = __builtin_elementwise_fma(aa, v01, c01); c23 = __builtin_elementwise_fma(aa, v23, c23); c45 = __builtin_elementwise_fma(aa, v45, c45); Ls = __builtin_fmaf(al[k], asf(a[k][2]), Ls); }
            }
            const float acc[6] = {c01[0], c01[1], c23[0], c23[1], c45[0], c45[1]};
            const float v4 = sum32x2(acc[4], Ls);
            lwr<unsigned>(a_ow, pk(sum32x2(acc[0], acc[2]), sum32x2(acc[1], acc[3])));
            lwr<unsigned>(a_ow2, pk(v4, sum32(acc[5])));
            lwr<float>(a_ms, l < 32 ? M : v4);
        }
        bar();
        {
            v4u of[4];
#pragma unroll
            for (int s = 0; s < 4; ++s) of[s] = lrd<v4u>(a_or + 64 * s);
            const f32x4 mw = lrd<f32x4>(a_mw), lw = lrd<f32x4>(a_mw + 32);
            FENCE();
            const f32x4 c = gemv4(of, fv);
            const float M = mx(mx3(mw[0], mw[1], mw[2]), mw[3]);
            float be[4];
#pragma unroll
            for (int r = 0; r < 4; ++r) be[r] = __builtin_amdgcn_exp2f(mw[r] - M);
            const float rd = __builtin_amdgcn_rcpf((be[0] * lw[0] + be[1] * lw[1]) + (be[2] * lw[2] + be[3] * lw[3]));
            const float v = ((be[0] * c[0] + be[1] * c[1]) + (be[2] * c[2] + be[3] * c[3])) * rd;
            auto sv = __builtin_amdgcn_permlane16_swap(asu(v), asu(v), false, false);
            pb[0] = sv[0]; pb[2] = sv[1];
            stx(rs, o_p4, pb);
        }
        STAMP(9);
        if ((yy & 1) == 0) {
            const unsigned o1 = WS_H5 + ((32 + (tid >> 3)) * 512 + 8 * r + (tid & 7)) * 16;
            const bool two = tid < 176;
            const unsigned o2 = two ? o1 + 64 * 512 * 16 : o1;
            pace(t3, PG);
#pragma nounroll
            SPIN { get2(a[0], a[1], o1, o2, rsq); if (none((tagx(a[0], tag) | tagx(a[1], tag)) != 0u)) break; __builtin_amdgcn_s_sleep(8); }
            {
                float t = sum32(sum16x2(asf(a[0][0]) + (two ? asf(a[1][0]) : 0.f), asf(a[0][2]) + (two ? asf(a[1][2]) : 0.f)));
                t += dpp<0x128>(t);
                lwr<float>(L_RED + ((g & 1) * 8 + (l & 7)) * 32 + w * 4, t);
            }
            bar();
            STAMP(10);
            if (w == 0) {
                const f32x4 t2 = lrd<f32x4>(L_RED + ((g & 1) * 8 + (l & 7)) * 32), t4 = lrd<f32x4>(L_RED + ((g & 1) * 8 + (l & 7)) * 32 + 16);
                float glu = rbf(((t2[0] + t2[1]) + (t2[2] + t2[3])) + ((t4[0] + t4[1]) + (t4[2] + t4[3])));
                float xin = lo16(x[(g & 1) * 512 + 8 * r + (l & 7)]);
                unsigned o0 = WS_H5 + ((l >> 3) * 512 + 8 * r + (l & 7)) * 16;
                unsigned o_out = ((g & 1) * 512 + 8 * r + (l & 7)) * 2;
                PIN(glu); PIN(xin); PIN(o0); PIN(o_out);
                pace(hint_wait(rsq, o_t5, tag), P5);
                STAMP(11);
#pragma nounroll
                SPIN { CNT(3); get4s(a, o0, rsq, 65536, 131072, 196608); if (none(tagx4(a, tag) != 0u)) break; }
                STAMP(12);
                float t = sum32(sum16x2((asf(a[0][0]) + asf(a[1][0])) + (asf(a[2][0]) + asf(a[3][0])), (asf(a[0][2]) + asf(a[1][2])) + (asf(a[2][2]) + asf(a[3][2]))));
                t += dpp<0x128>(t);
                if (l < 32 && l15 < 8) __hip_atomic_store((GAS u16*)((GAS unsigned char*)out + o_out), (u16)pk(rbf(xin + rbf(t)) + glu, 0.f), __ATOMIC_RELAXED, __HIP_MEMORY_SCOPE_AGENT);
                if (r == 0 && l == 0) __hip_atomic_store((GAS unsigned*)ws, tag, __ATOMIC_RELAXED, __HIP_MEMORY_SCOPE_AGENT);
            }
        }
    } else {
        const int pr = xc * 8 + (yy >> 1), hg = yy & 1, b = pr >> 5, sp = pr & 31;
        unsigned ep, j0;
        unsigned o_t2 = WS_T2 + (xc * 128 + (16 * hg + 2 * w) * 4 + (l & 7)) * 128;
        asm volatile("global_load_dword %0, %1, %2 sc1" : "=v"(ep) : "v"(0), "s"(ws) : "memory");
        asm volatile("buffer_load_dword %0, %1, %2, 0 offen sc1" : "=v"(j0) : "v"(o_t2), "s"(rsq) : "memory");
        unsigned qoff = WS_H2 + ((((xc & 3) * 2 + b) * 32 + 16 * hg + 2 * w) * 128 + l) * 16;
        unsigned a_q0 = L_Q + 2 * w * 1024 + (((l >> 1) ^ (2 * w)) << 4) + (l & 1) * 8, a_q1 = L_Q + (2 * w + 1) * 1024 + (((l >> 1) ^ (2 * w + 1)) << 4) + (l & 1) * 8;
        unsigned o_kv[8];
#pragma unroll
        for (int i = 0; i < 8; ++i) { o_kv[i] = (pr * 128 + 64 * hg + 8 * w + i) * 1024 + ((l ^ (8 * (w & 1) + i)) << 4); PIN(o_kv[i]); }
        PIN(qoff); PIN(a_q0); PIN(a_q1);
        asm volatile("s_waitcnt vmcnt(0)" : "+v"(ep), "+v"(j0), "+v"(cp0), "+v"(cp1) :: "memory");
        const unsigned tag = mktag(ep);
        pace(t_in, PB);
#pragma unroll
        for (int i = 0; i < 8; ++i) glds(o_kv[i], ckv, L_R0 + (8 * w + i) * 1024);
        STAMP(1);
        bar0();
#pragma unroll
        for (int i = 0; i < 8; ++i) glds(o_kv[i] ^ 65536, ckv, L_R0 + (64 + 8 * w + i) * 1024);
        WAITVM(8);
        bar0();
        STAMP(2);
#define Q_ISSUE() \
            FENCE(); \
            WAITVM(2); \
            const v2u hv = hint_get(rsq, o_t2);        \
            STAMP(14); \
            pace(none(hv[0] != tag) ? hv[1] : hint_wait(rsq, o_t2, tag), P2); \
            STAMP(15); \
            v4u qa[4]; \
            asm volatile("buffer_load_dwordx4 %0, %4, %5, 0 offen sc1\n\tbuffer_load_dwordx4 %1, %4, %5, 0 offen offset:1024 sc1\n\t" \
                         "buffer_load_dwordx4 %2, %4, %5, 0 offen offset:2048 sc1\n\tbuffer_load_dwordx4 %3, %4, %5, 0 offen offset:3072 sc1" \
                         : "=&v"(qa[0]), "=&v"(qa[1]), "=&v"(qa[2]), "=&v"(qa[3]) : "v"(qoff), "s"(rsq) : "memory"); \
            bar0()
#define Q_LAND(n) do { \
            asm volatile("s_waitcnt vmcnt(" STR(n) ")" : "+v"(qa[0]), "+v"(qa[1]), "+v"(qa[2]), "+v"(qa[3]) :: "memory"); \
            if (__builtin_expect(!none(tagx4(qa, tag) != 0u), 0)) { _Pragma("nounroll") SPIN { __builtin_amdgcn_s_sleep(4); get4<1024>(qa, qoff, rsq); if (none(tagx4(qa, tag) != 0u)) break; } } \
            asm volatile("ds_write2_b32 %0, %2, %3 offset1:1\n\tds_write2_b32 %0, %4, %5 offset0:128 offset1:129\n\tds_write2_b32 %1, %6, %7 offset1:1\n\tds_write2_b32 %1, %8, %9 offset0:128 offset1:129" \
                         :: "v"(a_q0), "v"(a_q1), "v"(qa[0][0]), "v"(qa[0][2]), "v"(qa[1][0]), "v"(qa[1][2]), "v"(qa[2][0]), "v"(qa[2][2]), "v"(qa[3][0]), "v"(qa[3][2]) : "memory"); \
            STAMP(3); bar();   } while (0)
        unsigned t5 = 0;
        const int h = 4 * xc + (yy >> 2), cq = yy & 3;
        unsigned o_t4 = WS_T4 + ((h * 4 + cq) * 4 + (l & 3)) * 128;
        unsigned o_hg = (EXP & 2) ? 0 : WS_HG + ((4 * (w & 3) + g) * 1376 + 16 * min(un, 85) + l15) * 16;
        if (w >= 4) {
            unsigned a_r[4];
#pragma unroll
            for (int j = 0; j < 4; ++j) a_r[j] = l15 * 1024 + (((4 * j + g) ^ l15) << 4);
            const unsigned kb = L_R0 + (w - 4) * 16384;
            v4u cr[32], qf[16];
#pragma unroll
            for (int s = 0; s < 16; ++s) cr[s] = lrd<v4u>(a_r[s & 3] + kb + (s >> 2) * 256);
            unsigned a_mxw = L_MX + l15 * 16 + (w - 4) * 4, a_mxr = L_MX + l15 * 16;
            unsigned a_pw = L_P + l15 * 256 + (((2 * (w - 4) + (g >> 1)) ^ l15) << 4) + (g & 1) * 8;
            __builtin_amdgcn_s_setprio(1);
            PIN(a_mxw); PIN(a_mxr); PIN(a_pw); PIN(o_hg);
            Q_ISSUE();
#pragma unroll
            for (int s = 0; s < 16; ++s) cr[16 + s] = lrd<v4u>(a_r[s & 3] + kb + 65536 + (s >> 2) * 256);
            FENCE();
            Q_LAND(0);
            STAMP(4);
#pragma unroll
            for (int s = 0; s < 16; ++s) qf[s] = lrd<v4u>(a_r[s & 3] + L_Q + (s >> 2) * 256);
            FENCE();
            f32x4 s1 = ZF, s2 = ZF;
#pragma unroll
            for (int s = 0; s < 16; ++s) { s1 = mfma32(cr[s], qf[s], s1); s2 = mfma32(cr[16 + s], qf[s], s2); }
            asm volatile("s_nop 7" : "+v"(s1), "+v"(s2));
            lwr<float>(a_mxw, max32(max16(mx(mx3(mx3(s1[0], s1[1], s1[2]), s1[3], s2[0]), mx3(s2[1], s2[2], s2[3])))));
            bar();
            STAMP(12);
            const f32x4 m0 = lrd<f32x4>(a_mxr);
            const float mr = mx(mx3(m0[0], m0[1], m0[2]), m0[3]);
            v2u p1 = {pk(__builtin_amdgcn_exp2f(s1[0] - mr), __builtin_amdgcn_exp2f(s1[1] - mr)), pk(__builtin_amdgcn_exp2f(s1[2] - mr), __builtin_amdgcn_exp2f(s1[3] - mr))};
            lwr<v2u>(a_pw, p1);
            v2u p2 = {pk(__builtin_amdgcn_exp2f(s2[0] - mr), __builtin_amdgcn_exp2f(s2[1] - mr)), pk(__builtin_amdgcn_exp2f(s2[2] - mr), __builtin_amdgcn_exp2f(s2[3] - mr))};
            lwr<v2u>(a_pw ^ 128, p2);
            bar();
            STAMP(5);
            PIN(p1); PIN(p2);
            {
                const unsigned q0 = p1[0], q1 = p1[1], q2 = p2[0], q3 = p2[1];
                lwr<float>(a_mxw + 256, sum32(sum16(((lo16(q0) + hi16(q0)) + (lo16(q1) + hi16(q1))) + ((lo16(q2) + hi16(q2)) + (lo16(q3) + hi16(q3))))));
            }
            bar();
            __builtin_amdgcn_s_setprio(0);
            t5 = now();
            if (PH) pace(t5, PH);
            v4u ga, gb;
            asm volatile("buffer_load_dwordx4 %0, %3, %6, 0 offen sc1\n\tbuffer_load_dwordx4 %1, %4, %6, 0 offen sc1\n\tbuffer_load_dword %2, %5, %6, 0 offen sc1\n\ts_waitcnt vmcnt(0)"
                         : "=&v"(ga), "=&v"(gb), "=&v"(j0), "+v"(o_hg) : "v"(o_hg + 16 * 1376 * 16), "v"(o_t4), "s"(rsq) : "memory");
            if (un < 86) {
                if (!(EXP & 2) && __builtin_expect(!none((tagx(ga, tag) | tagx(gb, tag)) != 0u), 0)) {
#pragma nounroll
                    SPIN { __builtin_amdgcn_s_sleep(16); get2(ga, gb, o_hg, o_hg + 16 * 1376 * 16, rsq); if (none((tagx(ga, tag) | tagx(gb, tag)) != 0u)) break; }
                }
                lwr<float>(L_RED + (l & 31) * 32 + (w - 4) * 4, sum32(sum16x2(asf(ga[0]), asf(ga[2]))));
                lwr<float>(L_RED + (l & 31) * 32 + w * 4, sum32(sum16x2(asf(gb[0]), asf(gb[2]))));
            }
            pace(t5, PW);
#pragma unroll
            for (int i = 0; i < 8; ++i) {
                const unsigned ch = l15 ^ ((g << 2) | (i & 3));
                glds((h * 128 + 32 * (w - 4) + 4 * i + g) * 1024 + cq * 128 + (ch >> 3) * 512 + (ch & 7) * 16, wo, L_R1 + (32 * (w - 4) + 4 * i) * 256);
            }
        } else {
            unsigned a_t[2][4];
            {
                const unsigned q = l15 >> 2, p = l & 3;
#pragma unroll
                for (int j = 0; j < 8; ++j) {
                    const unsigned r0 = 8 * g + 4 * (j >> 2) + q, ch = 16 * w + 4 * p + (j & 3);
                    a_t[j >> 2][j & 3] = L_R0 + r0 * 1024 + ((ch ^ (r0 & 15)) << 4) + (p >> 1) * 8;
                }
            }
            v4u ct[8][4];
#pragma unroll
            for (int t = 0; t < 8; ++t)
#pragma unroll
                for (int s = 0; s < 2; ++s) ct[t][s] = trd2((a_t[0][t >> 1] ^ ((t & 1) * 8)) + s * 32768, (a_t[1][t >> 1] ^ ((t & 1) * 8)) + s * 32768);
            unsigned a_pr[4], a_mxr = L_MX + l15 * 16;
#pragma unroll
            for (int j = 0; j < 4; ++j) { a_pr[j] = L_P + l15 * 256 + (((4 * j + g) ^ l15) << 4); PIN(a_pr[j]); }
            unsigned o_t3 = tid < 4 ? WS_T3 + ((4 * hg + tid) * 128 + hg * 64 + pr) * 128 : OOB;
            unsigned o_h3 = WS_H3 + ((b * 32 + 16 * hg + (l >> 3)) * 4 + w) * H3S + sp * 384 + (l & 7) * 16, o_h3b = o_h3 + 8 * 4 * H3S;
            unsigned a_sw = L_R2 + w * 8192 + l15 * 144 + g * 16, a_sr = L_R2 + w * 8192 + (l >> 3) * 144 + (l & 7) * 16;
            PIN(a_sw); PIN(a_sr);
            unsigned o_wd[4], a_lr = L_MX + 256 + l15 * 16;
#pragma unroll
            for (int i = 0; i < 4; ++i) { const unsigned row = 4 * w + i; o_wd[i] = (EXP & 1) ? 0 : (16 * min(un, 85) + row) * 1024 + ((l ^ ((row & 7) << 1)) << 4); PIN(o_wd[i]); }
            PIN(a_mxr); PIN(o_t3); PIN(o_h3); PIN(o_h3b); PIN(a_lr);
            Q_ISSUE();
            v4u wst[4];
            asm volatile("global_load_dwordx4 %0, %4, %8\n\tglobal_load_dwordx4 %1, %5, %8\n\tglobal_load_dwordx4 %2, %6, %8\n\tglobal_load_dwordx4 %3, %7, %8"
                         : "=&v"(wst[0]), "=&v"(wst[1]), "=&v"(wst[2]), "=&v"(wst[3]) : "v"(o_wd[0]), "v"(o_wd[1]), "v"(o_wd[2]), "v"(o_wd[3]), "s"(wd) : "memory");
#pragma unroll
            for (int t = 0; t < 8; ++t)
#pragma unroll
                for (int s = 2; s < 4; ++s) ct[t][s] = trd2((a_t[0][t >> 1] ^ ((t & 1) * 8)) + s * 32768, (a_t[1][t >> 1] ^ ((t & 1) * 8)) + s * 32768);
            FENCE();
            Q_LAND(4);
            STAMP(4);
            hint_put(rs, o_t3, tag);
            bar0();
            const f32x4 m0 = lrd<f32x4>(a_mxr);
            float mr = mx(mx3(m0[0], m0[1], m0[2]), m0[3]);
            PIN(mr);
            bar();
            STAMP(5);
            v4u pf[4];
#pragma unroll
            for (int s = 0; s < 4; ++s) pf[s] = lrd<v4u>(a_pr[s]);
            FENCE();
            f32x4 o[8];
#define M0_(t) asm volatile("v_mfma_f32_16x16x32_bf16 %0, %1, %2, 0" : "=&v"(o[t]) : "v"(ct[t][0]), "v"(pf[0]))
#define MM(t, s) asm volatile("v_mfma_f32_16x16x32_bf16 %0, %1, %2, %0" : "+v"(o[t]) : "v"(ct[t][s]), "v"(pf[s]))
#define RDY(t) PIN(o[t])
#define SEQ() asm volatile("" ::: "memory")
#define CV(d, t, i) do { d = pk(o[t][i], o[t][i + 1]); PIN(d); } while (0)
#define PUT(n, k, A, B, C) lwr<v4u>(a_sw + (n) * 2304 + (k) * 64, v4u{A, B, C, tag})
#define GETL(n) do { x0 = lrd<v4u>(a_sr + (n) * 2304); x1 = lrd<v4u>(a_sr + (n) * 2304 + 8 * 144); } while (0)
#define OUTL(n) do { stx(rs, o_h3 + (n) * 128, x0); stx(rs, o_h3b + (n) * 128, x1); } while (0)
            unsigned d0, d1, d2, d3, d4, d5;
            v4u x0, x1;
            M0_(0); M0_(1); MM(0, 1); MM(1, 1); MM(0, 2); MM(1, 2); MM(0, 3); MM(1, 3);
            M0_(2); MM(2, 1); MM(2, 2); MM(2, 3);
            M0_(3); MM(3, 1); MM(3, 2);
            RDY(0); CV(d0, 0, 0); CV(d1, 0, 2);
            MM(3, 3); RDY(1); CV(d2, 1, 0); CV(d3, 1, 2);
            M0_(4); PUT(0, 0, d0, d1, d2); SEQ();
            MM(4, 1); RDY(2); CV(d4, 2, 0); CV(d5, 2, 2);
            MM(4, 2); PUT(0, 1, d3, d4, d5); GETL(0); SEQ();
            MM(4, 3); RDY(3); CV(d0, 3, 0); CV(d1, 3, 2);
            M0_(5);
            bar0();
            const f32x4 l0 = lrd<f32x4>(a_lr);
            MM(5, 1); MM(5, 2); MM(5, 3);
            RDY(4); CV(d2, 4, 0); CV(d3, 4, 2);
            STAMP2(3);
            M0_(6); OUTL(0); PUT(1, 0, d0, d1, d2); SEQ();
            STAMP2(4);
            MM(6, 1);
            float lsum = (l0[0] + l0[1]) + (l0[2] + l0[3]);
            PIN(lsum);
            MM(6, 2); MM(6, 3);
            RDY(5); CV(d4, 5, 0); CV(d5, 5, 2);
            M0_(7); PUT(1, 1, d3, d4, d5); GETL(1); SEQ();
            MM(7, 1); MM(7, 2); MM(7, 3);
            asm volatile("s_nop 1" : "+v"(o[6]));
            CV(d0, 6, 0); CV(d1, 6, 2);
            STAMP2(14);
            OUTL(1); SEQ();
            STAMP2(15);
            asm volatile("s_nop 3" : "+v"(o[7]));
            CV(d2, 7, 0); CV(d3, 7, 2);
            PUT(2, 0, d0, d1, d2); PUT(2, 1, d3, asu(mr), asu(lsum)); GETL(2); OUTL(2);
            FENCE();
            STAMP(6);
            asm volatile("s_waitcnt vmcnt(7)" : "+v"(wst[0]), "+v"(wst[1]), "+v"(wst[2]), "+v"(wst[3]) :: "memory");
#pragma unroll
            for (int i = 0; i < 4; ++i) lwr<v4u>(L_R0 + (4 * w + i) * 1024 + l * 16, wst[i]);
        }
        if (STOPAT <= 7) goto done;
        bar();
        if (un < 86) {
            if (w == 4) {
                const f32x4 gp = lrd<f32x4>(L_RED + (l & 31) * 32), up = lrd<f32x4>(L_RED + (l & 31) * 32 + 16);
                const float gg = rbf((gp[0] + gp[1]) + (gp[2] + gp[3])), uu = rbf((up[0] + up[1]) + (up[2] + up[3]));
                const float sg = rbf(__builtin_amdgcn_rcpf(1.f + __builtin_amdgcn_exp2f(gg * -1.4426950408889634f)));
                lwr<u16>(L_HH + (l & 31) * 2, (u16)pk(sg * uu, 0.f));
            }
            bar();
            const v2u ha = lrd<v2u>(L_HH + (l & 1) * 32 + g * 8);
            const unsigned q = l15 >> 2, p = l & 3, row = 4 * g + q;
            f32x4 o[4];
#pragma unroll
            for (int t = 0; t < 4; ++t) {
                const unsigned ch = 8 * w + 2 * t + (p >> 1);
                o[t] = mfma16(ha, trd(L_R0 + row * 1024 + ((ch ^ ((row & 7) << 1)) << 4) + (p & 1) * 8), ZF);
            }
            const float o0 = g == 0 ? o[0][0] : g == 1 ? o[1][0] : g == 2 ? o[2][0] : o[3][0];
            const float o1 = g == 0 ? o[0][1] : g == 1 ? o[1][1] : g == 2 ? o[2][1] : o[3][1];
            stx(rs, WS_H5 + ((32 + un) * 512 + 64 * w + l) * 16, v4u{asu(o0), tag, asu(o1), tag});
        }
        STAMP(7);
        {
            unsigned a0, a1;
            {
                const unsigned q = l15 >> 2, p = l & 3, ch = 2 * w + (p >> 1);
                a0 = L_R1 + (8 * g + q) * 256 + 16 * (ch ^ ((q << 2) | ((2 * g) & 3))) + 8 * (p & 1);
                a1 = L_R1 + (8 * g + q + 4) * 256 + 16 * (ch ^ ((q << 2) | ((2 * g + 1) & 3))) + 8 * (p & 1);
            }
            unsigned o_g4 = WS_H4 + ((h * 4 + g) * 128 + 16 * w + l15) * 16;
            unsigned o_t5 = tid < 16 ? WS_T5 + ((8 * (cq + 4 * (tid >> 3)) + (tid & 7)) * 32 + h) * 128 : OOB;
            unsigned o_h5 = l < 16 ? WS_H5 + (h * 512 + 64 * (cq + 4 * (w >> 2)) + 16 * (w & 3) + l) * 16 : OOB;
            unsigned a_qw = L_QS + (g & 1) * 272 + (16 * w + l15) * 2, a_qr = L_QS + (l & 1) * 272 + 16 * g;
            v4u pb = {0u, tag, 0u, tag}, fw[4], ga;
            WAITVM(0);
            bar();
            frags128(a0, a1, fw);
            PIN(fw[0]); PIN(fw[1]); PIN(fw[2]); PIN(fw[3]); PIN(o_g4); PIN(o_t5); PIN(o_h5); PIN(a_qw); PIN(a_qr); PIN(pb);
            STAMP(10);
            if (w == 4) pace(hint_wait(rsq, o_t4, tag), P4);
            bar0();
            STAMP(11);
            asm volatile("buffer_load_dwordx4 %0, %1, %2, 0 offen sc1" : "=&v"(ga) : "v"(o_g4), "s"(rsq) : "memory");
            hint_put(rs, o_t5, tag);
            asm volatile("s_waitcnt vmcnt(1)" : "+v"(ga) :: "memory");
            if (__builtin_expect(!none(tagx(ga, tag) != 0u), 0)) {
#pragma nounroll
                SPIN { __builtin_amdgcn_s_sleep(2); get1(ga, o_g4, rsq); if (none(tagx(ga, tag) != 0u)) break; }
            }
            STAMP(8);
            lwr<u16>(a_qw, (u16)pk(sum32(sum16x2(asf(ga[0]), asf(ga[2]))), 0.f));
            bar();
            v4u cf[4];
#pragma unroll
            for (int s = 0; s < 4; ++s) cf[s] = lrd<v4u>(a_qr + 64 * s);
            FENCE();
            const f32x4 acc = gemv4(cf, fw);
            pb[0] = asu(acc[0]); pb[2] = asu(acc[1]);
            stx(rs, o_h5, pb);
        }
        STAMP(9);
    }
done:
    STAMP(13);
    if (l == 0) {
        GAS unsigned long long* so = (GAS unsigned long long*)(ws + WS_STAMP) + ((size_t)id * 8 + w) * NST;
#pragma unroll
        for (int i = 0; i < NST; ++i) so[i] = st[i];
    }
}

__global__ void __launch_bounds__(256) k_report(const unsigned long long* stp, int cls, int k, int mode, int scale) {
    const unsigned long long tin = __builtin_amdgcn_s_memrealtime();
    __shared__ unsigned long long r0[256], r1[256], r2[256]; __shared__ unsigned cn[256];
    const int id = threadIdx.x; unsigned long long mn = ~0ull, mx = 0, sm = 0; unsigned c = 0;
    for (int w = 0; w < 8; ++w) { const unsigned long long a = stp[((size_t)id * 8 + w) * NST], v = stp[((size_t)id * 8 + w) * NST + k]; mn = a < mn ? a : mn;
        if (v && (cls == 2 || (cls == 0) == ((id >> 3) < 16))) { mx = v > mx ? v : mx; sm += v & 0xffffffffull; ++c; } }
    r0[id] = mn; r1[id] = mx; r2[id] = sm; cn[id] = c; __syncthreads();
    if (id == 0) { for (int i = 1; i < 256; ++i) { mn = r0[i] < mn ? r0[i] : mn; mx = r1[i] > mx ? r1[i] : mx; sm += r2[i]; c += cn[i]; }
        unsigned long long q = mode == 2 ? tin - mx : mode == 0 ? mx - mn : (sm / c - (mn & 0xffffffffull)); if (q > 20000) q = 20000;
        const unsigned long long t = __builtin_amdgcn_s_memrealtime() + q * scale; while (__builtin_amdgcn_s_memrealtime() < t) __builtin_amdgcn_s_sleep(1); }
}
extern "C" void kernel_launch(void* const* d_in, const int* in_sizes, int n_in, void* d_out, int out_size, void* d_ws, size_t ws_size,
                              hipStream_t stream) {
    (void)in_sizes; (void)n_in; (void)out_size; (void)ws_size;
    (void)hipFuncSetAttribute((const void*)mla_glu_decode, hipFuncAttributeMaxDynamicSharedMemorySize, L_END);
    mla_glu_decode<<<256, 512, L_END, stream>>>((const u16*)d_in[0], (const u16*)d_in[1], (const u16*)d_in[2], (const u16*)d_in[3], (const u16*)d_in[4],
                                               (const u16*)d_in[5], (const u16*)d_in[7], (const u16*)d_in[8], (const u16*)d_in[9], (u16*)d_out,
                                               (unsigned char*)d_ws);
    k_report<<<1, 256, 0, stream>>>((const unsigned long long*)((char*)d_ws + WS_STAMP), 0, 1, 0, 100);
}
```

```cpp
#include <hip/hip_runtime.h>
#include <hip/hip_bf16.h>

typedef unsigned short u16;
typedef unsigned v4u __attribute__((ext_vector_type(4)));
typedef unsigned v2u __attribute__((ext_vector_type(2)));
typedef float f32x4 __attribute__((ext_vector_type(4)));
typedef float f32x2 __attribute__((ext_vector_type(2)));
typedef short s16x4 __attribute__((ext_vector_type(4)));
typedef __bf16 bf16x8 __attribute__((ext_vector_type(8)));
typedef __bf16 bf16x2 __attribute__((ext_vector_type(2)));
#define LAS __attribute__((address_space(3)))
#define GAS __attribute__((address_space(1)))
#define DEVI __device__ __forceinline__

#define PB 95
#define P2 100
#define P3 180
#define P4 65
#define P5 62
#define PG 220
#define PW 60
#define PA 0
#define CODEB 14336
#define EXP 0
#define PH 0
#define H3S 12288
#define TLBW 1
#define STOPAT 99

constexpr int WS_T2 = 4096;
constexpr int WS_T3 = WS_T2 + 8 * 128 * 128;
constexpr int WS_T5 = WS_T3 + 8 * 128 * 128;
constexpr int WS_T4 = WS_T5 + 64 * 32 * 128;
constexpr int WS_H1 = WS_T4 + 32 * 16 * 128;
constexpr int WS_H4 = WS_H1 + 32 * 4 * 128 * 16;
constexpr int WS_H2 = WS_H4 + 32 * 4 * 128 * 16;
constexpr int WS_HG = WS_H2 + 4 * 2 * 32 * 128 * 16;
constexpr int WS_H5 = WS_HG + 2 * 16 * 1376 * 16;
constexpr int WS_H3 = WS_H5 + 118 * 512 * 16;
constexpr int WS_END = WS_H3 + 2 * 32 * 4 * H3S;
constexpr int WS_STAMP = 16 << 20;
constexpr int L_Q = 0;
constexpr int L_P = 16384;
constexpr int L_QS = L_P + 4096;
constexpr int L_OW = L_QS + 1024;
constexpr int L_MW = L_OW + 2304;
constexpr int L_MX = L_MW + 64;
constexpr int L_HH = L_MX + 512;
constexpr int L_TAG = L_HH + 64;
constexpr int L_JUNK = L_TAG + 64;
constexpr int L_RED = L_JUNK + 256;
constexpr int L_XN = 28672;
constexpr int L_R0 = 32768, L_R1 = 65536, L_R2 = 98304, L_R3 = 131072;
constexpr int L_END = 163840;
static_assert(L_RED + 1024 <= L_XN && L_XN + 2048 <= L_R0, "lds");
constexpr int SPIN_CAP = 1 << 16;

DEVI unsigned pk(float lo, float hi) { bf16x2 r = {(__bf16)lo, (__bf16)hi}; return __builtin_bit_cast(unsigned, r); }
DEVI float rbf(float f) { return (float)(__bf16)f; }
DEVI float lo16(unsigned w) { return __builtin_bit_cast(float, w << 16); }
DEVI float hi16(unsigned w) { return __builtin_bit_cast(float, w & 0xffff0000u); }
DEVI float asf(unsigned w) { return __builtin_bit_cast(float, w); }
DEVI unsigned asu(float w) { return __builtin_bit_cast(unsigned, w); }
DEVI float mx(float a, float b) { float r; asm("v_max_f32 %0, %1, %2" : "=v"(r) : "v"(a), "v"(b)); return r; }
DEVI float mx3(float a, float b, float c) { float r; asm("v_max3_f32 %0, %1, %2, %3" : "=v"(r) : "v"(a), "v"(b), "v"(c)); return r; }
DEVI f32x4 mfma32(v4u a, v4u b, f32x4 c) { return __builtin_amdgcn_mfma_f32_16x16x32_bf16(__builtin_bit_cast(bf16x8, a), __builtin_bit_cast(bf16x8, b), c, 0, 0, 0); }
DEVI f32x4 mfma16(v2u a, v2u b, f32x4 c) { return __builtin_amdgcn_mfma_f32_16x16x16bf16_1k(__builtin_bit_cast(s16x4, a), __builtin_bit_cast(s16x4, b), c, 0, 0, 0); }
template <class T> DEVI T lrd(unsigned off) { return *(LAS T*)(size_t)off; }
template <class T> DEVI void lwr(unsigned off, T v) { *(LAS T*)(size_t)off = v; }
DEVI v2u trd(unsigned off) { return __builtin_bit_cast(v2u, __builtin_amdgcn_ds_read_tr16_b64_v4i16((LAS s16x4*)(size_t)off)); }
DEVI v4u trd2(unsigned o0, unsigned o1) { v2u a = trd(o0), b = trd(o1); return v4u{a.x, a.y, b.x, b.y}; }
DEVI float sum16(float x) { auto r = __builtin_amdgcn_permlane16_swap(asu(x), asu(x), false, false); return asf(r[0]) + asf(r[1]); }
DEVI float sum32(float x) { auto r = __builtin_amdgcn_permlane32_swap(asu(x), asu(x), false, false); return asf(r[0]) + asf(r[1]); }
DEVI float max16(float x) { auto r = __builtin_amdgcn_permlane16_swap(asu(x), asu(x), false, false); return mx(asf(r[0]), asf(r[1])); }
DEVI float max32(float x) { auto r = __builtin_amdgcn_permlane32_swap(asu(x), asu(x), false, false); return mx(asf(r[0]), asf(r[1])); }
DEVI float odd16(float x) { auto r = __builtin_amdgcn_permlane16_swap(asu(x), asu(x), false, false); return asf(r[1]); }
DEVI float sum16x2(float x, float y) { auto r = __builtin_amdgcn_permlane16_swap(asu(x), asu(y), false, false); return asf(r[0]) + asf(r[1]); }
DEVI float sum32x2(float x, float y) { auto r = __builtin_amdgcn_permlane32_swap(asu(x), asu(y), false, false); return asf(r[0]) + asf(r[1]); }
template <int CTRL> DEVI float dpp(float x) { return asf((unsigned)__builtin_amdgcn_update_dpp(0, (int)asu(x), CTRL, 0xf, 0xf, true)); }
DEVI float wavesum(float x) {
    x += dpp<0xB1>(x); x += dpp<0x4E>(x); x += dpp<0x141>(x); x += dpp<0x140>(x);
    return sum32(sum16(x));
}
DEVI float dot2one(unsigned p, float c) { const unsigned one = 0x3f803f80u; return __builtin_amdgcn_fdot2_f32_bf16(__builtin_bit_cast(bf16x2, p), __builtin_bit_cast(bf16x2, one), c, false); }
#define STR_(x) #x
#define STR(x) STR_(x)
#define WAITVM(n) asm volatile("s_waitcnt vmcnt(" STR(n) ")" ::: "memory")
#define PIN(x) asm volatile("" : "+v"(x))
#define MFMA_SETTLE(x) asm volatile("s_nop 7" : "+v"(x))
#define UNDEF(x) asm volatile("" : "=v"(x))
#define FENCE() __builtin_amdgcn_sched_barrier(0)
#define SPIN for (int it_ = SPIN_CAP; it_ > 0; --it_)
DEVI void bar() { asm volatile("s_waitcnt lgkmcnt(0)\n\ts_barrier" ::: "memory"); }
DEVI void bar0() { asm volatile("s_barrier" ::: "memory"); }
#define USE_NT 0
template <bool NT = false> DEVI void glds(unsigned voff, const void* sbase, unsigned lds) {
    unsigned keep;
    if (NT && USE_NT) asm volatile("s_mov_b32 %0, m0\n\ts_mov_b32 m0, %3\n\ts_nop 0\n\tglobal_load_lds_dwordx4 %1, %2 nt\n\ts_mov_b32 m0, %0" : "=&s"(keep) : "v"(voff), "s"(sbase), "s"(lds) : "memory");
    else asm volatile("s_mov_b32 %0, m0\n\ts_mov_b32 m0, %3\n\ts_nop 0\n\tglobal_load_lds_dwordx4 %1, %2\n\ts_mov_b32 m0, %0" : "=&s"(keep) : "v"(voff), "s"(sbase), "s"(lds) : "memory");
}
typedef __amdgpu_buffer_rsrc_t rsrc_t;
constexpr unsigned OOB = 0x80000000u;
DEVI void stx(rsrc_t rs, unsigned off, v4u v) { __builtin_amdgcn_raw_buffer_store_b128(v, rs, off, 0, 16); }
DEVI bool none(bool bad) { asm volatile("" ::: "memory"); return __builtin_amdgcn_ballot_w64(bad) == 0ull; }
DEVI unsigned tagx(v4u a, unsigned tag) { unsigned r = (a[1] ^ tag) | (a[3] ^ tag); asm("" : "+v"(r)); return r; }
DEVI unsigned tagy(unsigned a, unsigned b, unsigned tag) { unsigned r = (a ^ tag) | (b ^ tag); asm("" : "+v"(r)); return r; }
DEVI unsigned tagx4(const v4u (&a)[4], unsigned tag) { return (tagx(a[0], tag) | tagx(a[1], tag)) | (tagx(a[2], tag) | tagx(a[3], tag)); }
DEVI void get1(v4u& a, unsigned off, v4u rq) { asm volatile("buffer_load_dwordx4 %0, %1, %2, 0 offen sc1\n\ts_waitcnt vmcnt(0)" : "+&v"(a) : "v"(off), "s"(rq) : "memory"); }
DEVI void get2(v4u& a, v4u& b, unsigned oa, unsigned ob, v4u rq) {
    asm volatile("buffer_load_dwordx4 %0, %2, %4, 0 offen sc1\n\tbuffer_load_dwordx4 %1, %3, %4, 0 offen sc1\n\ts_waitcnt vmcnt(0)" : "+&v"(a), "+&v"(b) : "v"(oa), "v"(ob), "s"(rq) : "memory");
}
template <int ST> DEVI void get4(v4u (&a)[4], unsigned off, v4u rq) {
    asm volatile("buffer_load_dwordx4 %0, %4, %5, 0 offen sc1\n\tbuffer_load_dwordx4 %1, %4, %5, 0 offen offset:%6 sc1\n\t"
                 "buffer_load_dwordx4 %2, %4, %5, 0 offen offset:%7 sc1\n\tbuffer_load_dwordx4 %3, %4, %5, 0 offen offset:%8 sc1\n\ts_waitcnt vmcnt(0)"
                 : "+&v"(a[0]), "+&v"(a[1]), "+&v"(a[2]), "+&v"(a[3]) : "v"(off), "s"(rq), "i"(ST), "i"(2 * ST), "i"(3 * ST) : "memory");
}
DEVI void get4s(v4u (&a)[4], unsigned off, v4u rq, unsigned s1, unsigned s2, unsigned s3) {
    asm volatile("buffer_load_dwordx4 %0, %4, %5, 0 offen sc1\n\tbuffer_load_dwordx4 %1, %4, %5, %6 offen sc1\n\t"
                 "buffer_load_dwordx4 %2, %4, %5, %7 offen sc1\n\tbuffer_load_dwordx4 %3, %4, %5, %8 offen sc1\n\ts_waitcnt vmcnt(0)"
                 : "+&v"(a[0]), "+&v"(a[1]), "+&v"(a[2]), "+&v"(a[3]) : "v"(off), "s"(rq), "s"(s1), "s"(s2), "s"(s3) : "memory");
}
DEVI unsigned now() { return (unsigned)__builtin_amdgcn_s_memrealtime(); }
DEVI void hint_put(rsrc_t rs, unsigned off, unsigned tag, unsigned t) { __builtin_amdgcn_raw_buffer_store_b64(v2u{tag, t}, rs, off, 0, 16); }
DEVI void hint_put(rsrc_t rs, unsigned off, unsigned tag) { hint_put(rs, off, tag, now()); }
DEVI v2u hint_get(v4u rq, unsigned off) { v2u v; asm volatile("buffer_load_dwordx2 %0, %1, %2, 0 offen sc1\n\ts_waitcnt vmcnt(0)" : "=&v"(v) : "v"(off), "s"(rq) : "memory"); return v; }
DEVI void pace(unsigned t, unsigned lead) {
#pragma nounroll
    for (int it = 256; it > 0; --it) { if (none(now() - t < lead)) break; }
}
DEVI unsigned hint_wait(v4u rq, unsigned off, unsigned tag) {
    v2u v;
#pragma nounroll
    SPIN { v = hint_get(rq, off); if (none(v[0] != tag)) break; __builtin_amdgcn_s_sleep(1); }
    return v[1];
}
DEVI unsigned mktag(unsigned ep) { return ep + 1u; }

#define STAMP(i)

#define CNT0(i)
#define CNT(i)
#define STAMPC(i)
#define STAMP2(i)
DEVI void frags128(unsigned a0, unsigned a1, v4u (&f)[4]) {
#pragma unroll
    for (int s = 0; s < 4; ++s) f[s] = trd2(a0 + s * 8192, a1 + s * 8192);
}
DEVI f32x4 gemv4(const v4u (&a)[4], const v4u (&b)[4]) {
    f32x4 c = {0.f, 0.f, 0.f, 0.f};
#pragma unroll
    for (int s = 0; s < 4; ++s) c = mfma32(a[s], b[s], c);
    return c;
}
DEVI void layernorm(v4u xv, int w, int l) {
    float v[8];
#pragma unroll
    for (int j = 0; j < 4; ++j) { v[2 * j] = lo16(xv[j]); v[2 * j + 1] = hi16(xv[j]); }
    const float mean = wavesum(((v[0] + v[1]) + (v[2] + v[3])) + ((v[4] + v[5]) + (v[6] + v[7]))) * (1.f / 512.f);
    float sq = 0.f;
#pragma unroll
    for (int j = 0; j < 8; ++j) { const float d = v[j] - mean; sq += d * d; }
    const float var = wavesum(sq) * (1.f / 512.f);
    const float mb = rbf(mean), ri = __builtin_amdgcn_rcpf(rbf(__builtin_amdgcn_sqrtf(rbf(rbf(var) + 1e-5f))));
    v4u o;
#pragma unroll
    for (int j = 0; j < 4; ++j) o[j] = pk(rbf(v[2 * j] - mb) * ri, rbf(v[2 * j + 1] - mb) * ri);
    lwr<v4u>(L_XN + w * 1024 + l * 16, o);
}

__global__ void __launch_bounds__(512, 2)
mla_glu_decode(const u16* __restrict__ x, const u16* __restrict__ ckv, const u16* __restrict__ wq, const u16* __restrict__ wuk,
               const u16* __restrict__ wuv, const u16* __restrict__ wo, const u16* __restrict__ wg, const u16* __restrict__ wu,
               const u16* __restrict__ wd, u16* __restrict__ out, unsigned char* __restrict__ ws) {
    STAMP(0);
    const int tid = threadIdx.x, l = tid & 63, w = __builtin_amdgcn_readfirstlane(tid >> 6);
    const int id = blockIdx.x, xc = id & 7, y = id >> 3, yy = y & 15;
    const int un = yy * 8 + xc;
    const unsigned g = l >> 4, l15 = l & 15;
    const unsigned t_in = now();
    unsigned cp0 = 0, cp1 = 0;
    if (y >= 16 && w == 2) {
        unsigned long long pc;
        asm volatile("s_getpc_b64 %0" : "=s"(pc));
        pc &= ~255ull;
        asm volatile("global_load_dword %0, %2, %4\n\tglobal_load_dword %1, %3, %4"
                     : "=&v"(cp0), "=&v"(cp1) : "v"(min(l * 128, CODEB - 128)), "v"(min(8192 + l * 128, CODEB - 128)), "s"(pc) : "memory");
    }
    asm volatile("" :: "s"(x), "s"(ckv), "s"(wq), "s"(wuk), "s"(wuv), "s"(wo), "s"(wg), "s"(wu), "s"(wd), "s"(out), "s"(ws));
    const rsrc_t rs = __builtin_amdgcn_make_buffer_rsrc(ws, 0, WS_END, 0x00020000);
    const v4u rsq = {(unsigned)(size_t)ws, (unsigned)((size_t)ws >> 32) & 0xffffu, (unsigned)WS_END, 0x00020000u};
    const f32x4 ZF = {0.f, 0.f, 0.f, 0.f};

    if (__builtin_expect(y < 16, 1)) {
        const int h = 4 * xc + (yy >> 2), cq = yy & 3;
        v4u xv; unsigned ep;
        if (w < 2) {
            asm volatile("global_load_dwordx4 %0, %1, %2" : "=v"(xv) : "v"(w * 1024 + l * 16), "s"(x) : "memory");
            asm volatile("global_load_dword %0, %1, %2 sc1" : "=v"(ep) : "v"(0), "s"(ws) : "memory");
        }
        const unsigned vb = (128 * cq + 16 * w + g) * 8192 + h * 256;
        unsigned sw[4];
#pragma unroll
        for (int i = 0; i < 4; ++i) sw[i] = (l15 ^ ((g << 2) | i)) << 4;
#pragma unroll
        for (int i = 0; i < 4; ++i) glds<true>(vb + i * 32768 + sw[i], wq, L_R0 + (16 * w + 4 * i) * 256);
        bar0();
#pragma unroll
        for (int i = 0; i < 4; ++i) glds<true>(vb + i * 32768 + ((l15 ^ (4 * i + g)) << 4), wuk, L_R1 + (16 * w + 4 * i) * 256);
        STAMP(1);
        if (w < 2) {
            asm volatile("s_waitcnt vmcnt(8)" : "+v"(xv), "+v"(ep) :: "memory");
            layernorm(xv, w, l);
            lwr<v4u>(L_TAG, v4u{mktag(ep), 0u, 0u, 0u});
        }
        bar();
        const unsigned tag = lrd<unsigned>(L_TAG);
        unsigned o_p1 = l < 16 ? WS_H1 + ((h * 4 + cq) * 128 + 16 * w + l) * 16 : OOB;
        unsigned o_g1 = WS_H1 + ((h * 4 + g) * 128 + 16 * w + l15) * 16;
        unsigned o_h2 = WS_H2 + ((((w & 3) * 2 + (l >> 5)) * 32 + h) * 128 + 32 * cq + (l & 31)) * 16;
        unsigned a_2w = l15 < 2 ? L_RED + l15 * 256 + (4 * w + g) * 8 : L_JUNK + (l & 31) * 8, a_2r = L_RED + l * 8;
        PIN(a_2w); PIN(a_2r);
        unsigned o_t2 = l < 8 ? WS_T2 + (l * 128 + h * 4 + cq) * 128 : OOB;
        unsigned a0, a1;
        {
            const unsigned q = l15 >> 2, p = l & 3, ch = 2 * w + (p >> 1);
            a0 = L_R0 + (8 * g + q) * 256 + 16 * (ch ^ ((q << 2) | ((2 * g) & 3))) + 8 * (p & 1);
            a1 = L_R0 + (8 * g + q + 4) * 256 + 16 * (ch ^ ((q << 2) | ((2 * g + 1) & 3))) + 8 * (p & 1);
        }
        unsigned a_qw = L_QS + (g & 1) * 272 + (16 * w + l15) * 2;
        unsigned a_qr = L_QS + (l & 1) * 272 + 16 * g;
        unsigned a_uk[4], o_gu[3], a_cnt = l == 0 ? L_TAG + 8 : L_JUNK + l * 4, one = 1u;
        v4u xf[4];
#pragma unroll
        for (int s = 0; s < 4; ++s) {
            xf[s] = lrd<v4u>(L_XN + (l & 1) * 1024 + (128 * cq + 32 * s + 8 * g) * 2);
            a_uk[s] = L_R1 + (16 * w + l15) * 256 + (((4 * s + g) ^ l15) << 4);
            PIN(xf[s]); PIN(a_uk[s]);
        }
        const int mat = un >> 6, mp = (un >> 2) & 15, fp = un & 3;
#pragma unroll
        for (int i = 0; i < 3; ++i) {
            const unsigned pc = min(8 * i + w, 21), idx = min(64 * pc + l, 1375u), row = idx / 43u, ch = idx - row * 43u;
            o_gu[i] = (32 * mp + row) * 2752 + fp * 688 + ch * 16; PIN(o_gu[i]);
        }
        v4u pb = {0u, tag, 0u, tag}, ga;
        PIN(a0); PIN(a1); PIN(o_p1); PIN(o_g1); PIN(o_h2); PIN(o_t2); PIN(a_qw); PIN(a_qr); PIN(pb); PIN(a_cnt); PIN(one);
        WAITVM(4);
        bar0();
        STAMP(2);
        {
            v4u f[4]; frags128(a0, a1, f);
            FENCE();
            const f32x4 acc = gemv4(xf, f);
            pb[0] = asu(acc[0]); pb[2] = asu(acc[1]);
            stx(rs, o_p1, pb);
        }
        STAMP(3);
        if (STOPAT <= 3) { WAITVM(0); STAMP(14); goto done; }
        __builtin_amdgcn_s_sleep(10);
        WAITVM(3);
        v4u fu[4];
        {
            unsigned arr;
            asm volatile("buffer_load_dwordx4 %1, %4, %5, 0 offen sc1\n\tds_add_rtn_u32 %0, %2, %3\n\ts_waitcnt lgkmcnt(0)" : "=&v"(arr), "=&v"(ga) : "v"(a_cnt), "v"(one), "v"(o_g1), "s"(rsq) : "memory");
            hint_put(rs, __builtin_amdgcn_readfirstlane(arr) == 7u ? o_t2 : OOB, tag);
            asm volatile("s_waitcnt vmcnt(1)" : "+v"(ga) :: "memory");
            STAMP(14);
#pragma unroll
            for (int s = 0; s < 4; ++s) fu[s] = lrd<v4u>(a_uk[s]);
            if (__builtin_expect(!none(tagx(ga, tag) != 0u), 0)) {
#pragma nounroll
                SPIN { __builtin_amdgcn_s_sleep(4); get1(ga, o_g1, rsq); if (none(tagx(ga, tag) != 0u)) break; }
            }
            lwr<u16>(a_qw, (u16)pk(sum32(sum16x2(asf(ga[0]), asf(ga[2]))), 0.f));
        }
        STAMP(4);
        bar();
        {
            v4u qf[4];
#pragma unroll
            for (int s = 0; s < 4; ++s) qf[s] = lrd<v4u>(a_qr + 64 * s);
            FENCE();
            const f32x4 acc = gemv4(fu, qf);
            constexpr float SC = 0.08838834764831845f * 1.4426950408889634f;
            lwr<v2u>(a_2w, v2u{pk(acc[0] * SC, acc[1] * SC), pk(acc[2] * SC, acc[3] * SC)});
            bar();
            if (w < 4) {
                const v2u t = lrd<v2u>(a_2r);
                pb[0] = t[0]; pb[2] = t[1];
                stx(rs, o_h2, pb);
            }
        }
        STAMP(5);
        if (STOPAT <= 5) goto done;
        const int r = xc * 8 + (yy >> 1);
        unsigned o_t3 = WS_T3 + (xc * 128 + (h >> 4) * 64 + 8 * w + (l & 7)) * 128, o_t5 = WS_T5 + (r * 32 + (l & 31)) * 128, j0, j1;
        asm volatile("buffer_load_dword %0, %2, %4, 0 offen sc1\n\tbuffer_load_dword %1, %3, %4, 0 offen sc1"
                     : "=&v"(j0), "=&v"(j1) : "v"(o_t3), "v"((w == 0 && !(yy & 1)) ? o_t5 : o_t3), "s"(rsq) : "memory");
        pace(t_in, PA);
#pragma unroll
        for (int i = 0; i < 3; ++i) glds(o_gu[i], mat ? wu : wg, L_R2 + min(8 * i + w, 21) * 1024);
#define V_ISSUE(i) glds<true>((EXP & 4) ? 0 : vb + (i) * 32768 + sw[i], wuv, L_R1 + (16 * w + 4 * (i)) * 256)
        V_ISSUE(0); V_ISSUE(1);
        {
            const v4u xg = lrd<v4u>(L_XN + (l & 1) * 1024 + (32 * mp + 8 * g) * 2);
            const unsigned ag = L_R2 + (8 * g + (l15 >> 2)) * 688 + (l & 3) * 8;
            const unsigned f = 16 * (8 * g + w) + l15;
            const unsigned o_hg = (g < 3 && f < 344) ? WS_HG + ((mat * 16 + mp) * 1376 + 344 * fp + f) * 16 : OOB;
            asm volatile("s_waitcnt vmcnt(2)" : "+v"(j0), "+v"(j1) :: "memory");
            bar();
            f32x4 o[3];
#pragma unroll
            for (int t = 0; t < 3; ++t) {
                const unsigned tl = min(8 * t + w, 21);
                o[t] = mfma32(xg, trd2(ag + tl * 32, ag + tl * 32 + 4 * 688), ZF);
            }
            const float o0 = g == 0 ? o[0][0] : g == 1 ? o[1][0] : o[2][0], o1 = g == 0 ? o[0][1] : g == 1 ? o[1][1] : o[2][1];
            stx(rs, o_hg, v4u{asu(o0), tag, asu(o1), tag});
        }
        V_ISSUE(2); V_ISSUE(3);
        STAMP(6);
        const unsigned u = l15 >= 12 ? 8 + l15 : (g & 1) ? 12 + (l15 & 7) : l15;
        const bool act = (g & 1) ? (l15 & 12) != 8 : l15 < 12;
        unsigned o_h3 = WS_H3 + (((w >> 2) * 32 + h) * 4 + cq) * H3S + (8 * (w & 3) + (g >> 1)) * 384 + u * 16;
        unsigned o_t4 = l < 4 ? WS_T4 + ((h * 4 + l) * 4 + cq) * 128 : OOB;
        unsigned o_p4 = l < 16 ? WS_H4 + ((h * 4 + cq) * 128 + 16 * w + l) * 16 : OOB;
        unsigned a_ow, a_ow2, a_ms;
        {
            const unsigned n = 6 * (u >> 2) + 2 * (g >> 1), n2 = 6 * (u >> 2) + 4, gg = u & 3;
            a_ow = (act && n < 32) ? L_OW + w * 272 + (32 * gg + 4 * ((n >> 2) ^ (gg >> 1)) + (n & 3)) * 2 : L_JUNK + l * 4;
            a_ow2 = (act && n2 < 32 && l < 32) ? L_OW + w * 272 + (32 * gg + 4 * ((n2 >> 2) ^ (gg >> 1)) + (n2 & 3)) * 2 : L_JUNK + l * 4;
            a_ms = (l & 31) == 12 ? L_MW + (g >> 1) * 32 + w * 4 : L_JUNK + l * 4;
        }
        unsigned a_or = L_OW + (l & 7) * 272 + 16 * g, a_mw = L_MW + (g & 1) * 16;
        v4u a[4];
        UNDEF(a[0]); UNDEF(a[1]); UNDEF(a[2]); UNDEF(a[3]);
        a_cnt += l == 0 ? 4 : 0;
        PIN(o_h3); PIN(o_t3); PIN(o_t4); PIN(o_p4); PIN(a_ow); PIN(a_ow2); PIN(a_or); PIN(a_mw); PIN(a_ms); PIN(a_cnt);
        WAITVM(0);
        bar();
        v4u fv[4];
        frags128(a0 + (L_R1 - L_R0), a1 + (L_R1 - L_R0), fv);
        PIN(fv[0]); PIN(fv[1]); PIN(fv[2]); PIN(fv[3]);
        STAMP(7);
        if (STOPAT <= 7) goto done;
        CNT0(2); CNT0(3);
        const unsigned t3 = hint_wait(rsq, o_t3, tag);
        STAMPC(1);
        pace(t3, P3);
        STAMP(15);
        {
#pragma nounroll
            SPIN { CNT(2); get4<768>(a, o_h3, rsq); if (none((tagy(a[0][3], a[1][3], tag) | tagy(a[2][3], a[3][3], tag)) != 0u)) break; __builtin_amdgcn_s_sleep(4); }
            STAMP(8);
            unsigned arr;
            unsigned long long tm;
            asm volatile("ds_add_rtn_u32 %0, %2, %3\n\ts_memrealtime %1" : "=&v"(arr), "=&s"(tm) : "v"(a_cnt), "v"(one) : "memory");
            float m[4], al[4];
#pragma unroll
            for (int k = 0; k < 4; ++k) m[k] = dpp<0x15c>(asf(a[k][1]));
            const float M = max32(mx(mx3(m[0], m[1], m[2]), m[3]));
#pragma unroll
            for (int k = 0; k < 4; ++k) al[k] = __builtin_amdgcn_exp2f(m[k] - M);
            FENCE();
            asm volatile("s_waitcnt lgkmcnt(0)" : "+v"(arr), "+s"(tm) :: "memory");
            hint_put(rs, __builtin_amdgcn_readfirstlane(arr) == 7u ? o_t4 : OOB, tag, (unsigned)tm);
            f32x2 c01, c23, c45;
            float Ls;
#pragma unroll
            for (int k = 0; k < 4; ++k) {
                const f32x2 aa = {al[k], al[k]}, v01 = {lo16(a[k][0]), hi16(a[k][0])}, v23 = {lo16(a[k][1]), hi16(a[k][1])}, v45 = {lo16(a[k][2]), hi16(a[k][2])};
                if (k == 0) { c01 = aa * v01; c23 = aa * v23; c45 = aa * v45; Ls = al[0] * asf(a[0][2]); }
                else { c01 = __builtin_elementwise_fma(aa, v01, c01); c23 = __builtin_elementwise_fma(aa, v23, c23); c45 = __builtin_elementwise_fma(aa, v45, c45); Ls = __builtin_fmaf(al[k], asf(a[k][2]), Ls); }
            }
            const float acc[6] = {c01[0], c01[1], c23[0], c23[1], c45[0], c45[1]};
            const float v4 = sum32x2(acc[4], Ls);
            lwr<unsigned>(a_ow, pk(sum32x2(acc[0], acc[2]), sum32x2(acc[1], acc[3])));
            lwr<unsigned>(a_ow2, pk(v4, sum32(acc[5])));
            lwr<float>(a_ms, l < 32 ? M : v4);
        }
        bar();
        {
            v4u of[4];
#pragma unroll
            for (int s = 0; s < 4; ++s) of[s] = lrd<v4u>(a_or + 64 * s);
            const f32x4 mw = lrd<f32x4>(a_mw), lw = lrd<f32x4>(a_mw + 32);
            FENCE();
            const f32x4 c = gemv4(of, fv);
            const float M = mx(mx3(mw[0], mw[1], mw[2]), mw[3]);
            float be[4];
#pragma unroll
            for (int r = 0; r < 4; ++r) be[r] = __builtin_amdgcn_exp2f(mw[r] - M);
            const float rd = __builtin_amdgcn_rcpf((be[0] * lw[0] + be[1] * lw[1]) + (be[2] * lw[2] + be[3] * lw[3]));
            const float v = ((be[0] * c[0] + be[1] * c[1]) + (be[2] * c[2] + be[3] * c[3])) * rd;
            auto sv = __builtin_amdgcn_permlane16_swap(asu(v), asu(v), false, false);
            pb[0] = sv[0]; pb[2] = sv[1];
            stx(rs, o_p4, pb);
        }
        STAMP(9);
        if ((yy & 1) == 0) {
            const unsigned o1 = WS_H5 + ((32 + (tid >> 3)) * 512 + 8 * r + (tid & 7)) * 16;
            const bool two = tid < 176;
            const unsigned o2 = two ? o1 + 64 * 512 * 16 : o1;
            pace(t3, PG);
#pragma nounroll
            SPIN { get2(a[0], a[1], o1, o2, rsq); if (none((tagx(a[0], tag) | tagx(a[1], tag)) != 0u)) break; __builtin_amdgcn_s_sleep(8); }
            {
                float t = sum32(sum16x2(asf(a[0][0]) + (two ? asf(a[1][0]) : 0.f), asf(a[0][2]) + (two ? asf(a[1][2]) : 0.f)));
                t += dpp<0x128>(t);
                lwr<float>(L_RED + ((g & 1) * 8 + (l & 7)) * 32 + w * 4, t);
            }
            bar();
            STAMP(10);
            if (w == 0) {
                const f32x4 t2 = lrd<f32x4>(L_RED + ((g & 1) * 8 + (l & 7)) * 32), t4 = lrd<f32x4>(L_RED + ((g & 1) * 8 + (l & 7)) * 32 + 16);
                float glu = rbf(((t2[0] + t2[1]) + (t2[2] + t2[3])) + ((t4[0] + t4[1]) + (t4[2] + t4[3])));
                float xin = lo16(x[(g & 1) * 512 + 8 * r + (l & 7)]);
                unsigned o0 = WS_H5 + ((l >> 3) * 512 + 8 * r + (l & 7)) * 16;
                unsigned o_out = ((g & 1) * 512 + 8 * r + (l & 7)) * 2;
                PIN(glu); PIN(xin); PIN(o0); PIN(o_out);
                pace(hint_wait(rsq, o_t5, tag), P5);
                STAMP(11);
#pragma nounroll
                SPIN { CNT(3); get4s(a, o0, rsq, 65536, 131072, 196608); if (none(tagx4(a, tag) != 0u)) break; }
                STAMP(12);
                float t = sum32(sum16x2((asf(a[0][0]) + asf(a[1][0])) + (asf(a[2][0]) + asf(a[3][0])), (asf(a[0][2]) + asf(a[1][2])) + (asf(a[2][2]) + asf(a[3][2]))));
                t += dpp<0x128>(t);
                if (l < 32 && l15 < 8) __hip_atomic_store((GAS u16*)((GAS unsigned char*)out + o_out), (u16)pk(rbf(xin + rbf(t)) + glu, 0.f), __ATOMIC_RELAXED, __HIP_MEMORY_SCOPE_AGENT);
                if (r == 0 && l == 0) __hip_atomic_store((GAS unsigned*)ws, tag, __ATOMIC_RELAXED, __HIP_MEMORY_SCOPE_AGENT);
            }
        }
    } else {
        const int pr = xc * 8 + (yy >> 1), hg = yy & 1, b = pr >> 5, sp = pr & 31;
        unsigned ep, j0;
        unsigned o_t2 = WS_T2 + (xc * 128 + (16 * hg + 2 * w) * 4 + (l & 7)) * 128;
        asm volatile("global_load_dword %0, %1, %2 sc1" : "=v"(ep) : "v"(0), "s"(ws) : "memory");
        asm volatile("buffer_load_dword %0, %1, %2, 0 offen sc1" : "=v"(j0) : "v"(o_t2), "s"(rsq) : "memory");
        if (TLBW && w == 1) {
            const unsigned k = l & 7, o19 = (k << 19) + (yy << 15), k3 = (l - 40) % 3u;
            const unsigned char* p = l < 8 ? (const unsigned char*)ckv + (o19 << 1) : l < 16 ? (const unsigned char*)wq + o19 : l < 24 ? (const unsigned char*)wuk + o19
                                   : l < 32 ? (const unsigned char*)wuv + o19 : l < 40 ? (const unsigned char*)wo + o19
                                   : l < 49 ? (const unsigned char*)(l < 43 ? wg : l < 46 ? wu : wd) + min((unsigned)((k3 << 19) + (yy << 15)), 1409020u)
                                   : l < 63 ? ws + min(((l - 49) << 19) + (yy << 15) + 2048, (unsigned)WS_END - 4u) : (const unsigned char*)out;
            asm volatile("global_load_dword %0, %1, off" : "=&v"(cp0) : "v"(p) : "memory");
        }
        unsigned qoff = WS_H2 + ((((xc & 3) * 2 + b) * 32 + 16 * hg + 2 * w) * 128 + l) * 16;
        unsigned a_q0 = L_Q + 2 * w * 1024 + (((l >> 1) ^ (2 * w)) << 4) + (l & 1) * 8, a_q1 = L_Q + (2 * w + 1) * 1024 + (((l >> 1) ^ (2 * w + 1)) << 4) + (l & 1) * 8;
        unsigned o_kv[8];
#pragma unroll
        for (int i = 0; i < 8; ++i) { o_kv[i] = (pr * 128 + 64 * hg + 8 * w + i) * 1024 + ((l ^ (8 * (w & 1) + i)) << 4); PIN(o_kv[i]); }
        PIN(qoff); PIN(a_q0); PIN(a_q1);
        asm volatile("s_waitcnt vmcnt(0)" : "+v"(ep), "+v"(j0), "+v"(cp0), "+v"(cp1) :: "memory");
        const unsigned tag = mktag(ep);
        pace(t_in, PB);
#pragma unroll
        for (int i = 0; i < 8; ++i) glds(o_kv[i], ckv, L_R0 + (8 * w + i) * 1024);
        STAMP(1);
        bar0();
#pragma unroll
        for (int i = 0; i < 8; ++i) glds(o_kv[i] ^ 65536, ckv, L_R0 + (64 + 8 * w + i) * 1024);
        WAITVM(8);
        bar0();
        STAMP(2);
#define Q_ISSUE() \
            FENCE(); \
            WAITVM(2); \
            const v2u hv = hint_get(rsq, o_t2);        \
            STAMP(14); \
            pace(none(hv[0] != tag) ? hv[1] : hint_wait(rsq, o_t2, tag), P2); \
            STAMP(15); \
            v4u qa[4]; \
            asm volatile("buffer_load_dwordx4 %0, %4, %5, 0 offen sc1\n\tbuffer_load_dwordx4 %1, %4, %5, 0 offen offset:1024 sc1\n\t" \
                         "buffer_load_dwordx4 %2, %4, %5, 0 offen offset:2048 sc1\n\tbuffer_load_dwordx4 %3, %4, %5, 0 offen offset:3072 sc1" \
                         : "=&v"(qa[0]), "=&v"(qa[1]), "=&v"(qa[2]), "=&v"(qa[3]) : "v"(qoff), "s"(rsq) : "memory"); \
            bar0()
#define Q_LAND(n) do { \
            asm volatile("s_waitcnt vmcnt(" STR(n) ")" : "+v"(qa[0]), "+v"(qa[1]), "+v"(qa[2]), "+v"(qa[3]) :: "memory"); \
            if (__builtin_expect(!none(tagx4(qa, tag) != 0u), 0)) { _Pragma("nounroll") SPIN { __builtin_amdgcn_s_sleep(4); get4<1024>(qa, qoff, rsq); if (none(tagx4(qa, tag) != 0u)) break; } } \
            asm volatile("ds_write2_b32 %0, %2, %3 offset1:1\n\tds_write2_b32 %0, %4, %5 offset0:128 offset1:129\n\tds_write2_b32 %1, %6, %7 offset1:1\n\tds_write2_b32 %1, %8, %9 offset0:128 offset1:129" \
                         :: "v"(a_q0), "v"(a_q1), "v"(qa[0][0]), "v"(qa[0][2]), "v"(qa[1][0]), "v"(qa[1][2]), "v"(qa[2][0]), "v"(qa[2][2]), "v"(qa[3][0]), "v"(qa[3][2]) : "memory"); \
            STAMP(3); bar();   } while (0)
        unsigned t5 = 0;
        const int h = 4 * xc + (yy >> 2), cq = yy & 3;
        unsigned o_t4 = WS_T4 + ((h * 4 + cq) * 4 + (l & 3)) * 128;
        unsigned o_hg = (EXP & 2) ? 0 : WS_HG + ((4 * (w & 3) + g) * 1376 + 16 * min(un, 85) + l15) * 16;
        if (w >= 4) {
            unsigned a_r[4];
#pragma unroll
            for (int j = 0; j < 4; ++j) a_r[j] = l15 * 1024 + (((4 * j + g) ^ l15) << 4);
            const unsigned kb = L_R0 + (w - 4) * 16384;
            v4u cr[32], qf[16];
#pragma unroll
            for (int s = 0; s < 16; ++s) cr[s] = lrd<v4u>(a_r[s & 3] + kb + (s >> 2) * 256);
            unsigned a_mxw = L_MX + l15 * 16 + (w - 4) * 4, a_mxr = L_MX + l15 * 16;
            unsigned a_pw = L_P + l15 * 256 + (((2 * (w - 4) + (g >> 1)) ^ l15) << 4) + (g & 1) * 8;
            __builtin_amdgcn_s_setprio(1);
            PIN(a_mxw); PIN(a_mxr); PIN(a_pw); PIN(o_hg);
            Q_ISSUE();
#pragma unroll
            for (int s = 0; s < 16; ++s) cr[16 + s] = lrd<v4u>(a_r[s & 3] + kb + 65536 + (s >> 2) * 256);
            FENCE();
            Q_LAND(0);
            STAMP(4);
#pragma unroll
            for (int s = 0; s < 16; ++s) qf[s] = lrd<v4u>(a_r[s & 3] + L_Q + (s >> 2) * 256);
            FENCE();
            f32x4 s1 = ZF, s2 = ZF;
#pragma unroll
            for (int s = 0; s < 16; ++s) { s1 = mfma32(cr[s], qf[s], s1); s2 = mfma32(cr[16 + s], qf[s], s2); }
            asm volatile("s_nop 7" : "+v"(s1), "+v"(s2));
            lwr<float>(a_mxw, max32(max16(mx(mx3(mx3(s1[0], s1[1], s1[2]), s1[3], s2[0]), mx3(s2[1], s2[2], s2[3])))));
            bar();
            STAMP(12);
            const f32x4 m0 = lrd<f32x4>(a_mxr);
            const float mr = mx(mx3(m0[0], m0[1], m0[2]), m0[3]);
            v2u p1 = {pk(__builtin_amdgcn_exp2f(s1[0] - mr), __builtin_amdgcn_exp2f(s1[1] - mr)), pk(__builtin_amdgcn_exp2f(s1[2] - mr), __builtin_amdgcn_exp2f(s1[3] - mr))};
            lwr<v2u>(a_pw, p1);
            v2u p2 = {pk(__builtin_amdgcn_exp2f(s2[0] - mr), __builtin_amdgcn_exp2f(s2[1] - mr)), pk(__builtin_amdgcn_exp2f(s2[2] - mr), __builtin_amdgcn_exp2f(s2[3] - mr))};
            lwr<v2u>(a_pw ^ 128, p2);
            bar();
            STAMP(5);
            PIN(p1); PIN(p2);
            {
                const unsigned q0 = p1[0], q1 = p1[1], q2 = p2[0], q3 = p2[1];
                lwr<float>(a_mxw + 256, sum32(sum16(((lo16(q0) + hi16(q0)) + (lo16(q1) + hi16(q1))) + ((lo16(q2) + hi16(q2)) + (lo16(q3) + hi16(q3))))));
            }
            bar();
            __builtin_amdgcn_s_setprio(0);
            t5 = now();
            if (PH) pace(t5, PH);
            v4u ga, gb;
            asm volatile("buffer_load_dwordx4 %0, %3, %6, 0 offen sc1\n\tbuffer_load_dwordx4 %1, %4, %6, 0 offen sc1\n\tbuffer_load_dword %2, %5, %6, 0 offen sc1\n\ts_waitcnt vmcnt(0)"
                         : "=&v"(ga), "=&v"(gb), "=&v"(j0), "+v"(o_hg) : "v"(o_hg + 16 * 1376 * 16), "v"(o_t4), "s"(rsq) : "memory");
            if (un < 86) {
                if (!(EXP & 2) && __builtin_expect(!none((tagx(ga, tag) | tagx(gb, tag)) != 0u), 0)) {
#pragma nounroll
                    SPIN { __builtin_amdgcn_s_sleep(16); get2(ga, gb, o_hg, o_hg + 16 * 1376 * 16, rsq); if (none((tagx(ga, tag) | tagx(gb, tag)) != 0u)) break; }
                }
                lwr<float>(L_RED + (l & 31) * 32 + (w - 4) * 4, sum32(sum16x2(asf(ga[0]), asf(ga[2]))));
                lwr<float>(L_RED + (l & 31) * 32 + w * 4, sum32(sum16x2(asf(gb[0]), asf(gb[2]))));
            }
            pace(t5, PW);
#pragma unroll
            for (int i = 0; i < 8; ++i) {
                const unsigned ch = l15 ^ ((g << 2) | (i & 3));
                glds((h * 128 + 32 * (w - 4) + 4 * i + g) * 1024 + cq * 128 + (ch >> 3) * 512 + (ch & 7) * 16, wo, L_R1 + (32 * (w - 4) + 4 * i) * 256);
            }
        } else {
            unsigned a_t[2][4];
            {
                const unsigned q = l15 >> 2, p = l & 3;
#pragma unroll
                for (int j = 0; j < 8; ++j) {
                    const unsigned r0 = 8 * g + 4 * (j >> 2) + q, ch = 16 * w + 4 * p + (j & 3);
                    a_t[j >> 2][j & 3] = L_R0 + r0 * 1024 + ((ch ^ (r0 & 15)) << 4) + (p >> 1) * 8;
                }
            }
            v4u ct[8][4];
#pragma unroll
            for (int t = 0; t < 8; ++t)
#pragma unroll
                for (int s = 0; s < 2; ++s) ct[t][s] = trd2((a_t[0][t >> 1] ^ ((t & 1) * 8)) + s * 32768, (a_t[1][t >> 1] ^ ((t & 1) * 8)) + s * 32768);
            unsigned a_pr[4], a_mxr = L_MX + l15 * 16;
#pragma unroll
            for (int j = 0; j < 4; ++j) { a_pr[j] = L_P + l15 * 256 + (((4 * j + g) ^ l15) << 4); PIN(a_pr[j]); }
            unsigned o_t3 = tid < 4 ? WS_T3 + ((4 * hg + tid) * 128 + hg * 64 + pr) * 128 : OOB;
            unsigned o_h3 = WS_H3 + ((b * 32 + 16 * hg + (l >> 3)) * 4 + w) * H3S + sp * 384 + (l & 7) * 16, o_h3b = o_h3 + 8 * 4 * H3S;
            unsigned a_sw = L_R2 + w * 8192 + l15 * 144 + g * 16, a_sr = L_R2 + w * 8192 + (l >> 3) * 144 + (l & 7) * 16;
            PIN(a_sw); PIN(a_sr);
            unsigned o_wd[4], a_lr = L_MX + 256 + l15 * 16;
#pragma unroll
            for (int i = 0; i < 4; ++i) { const unsigned row = 4 * w + i; o_wd[i] = (EXP & 1) ? 0 : (16 * min(un, 85) + row) * 1024 + ((l ^ ((row & 7) << 1)) << 4); PIN(o_wd[i]); }
            PIN(a_mxr); PIN(o_t3); PIN(o_h3); PIN(o_h3b); PIN(a_lr);
            Q_ISSUE();
            v4u wst[4];
            asm volatile("global_load_dwordx4 %0, %4, %8\n\tglobal_load_dwordx4 %1, %5, %8\n\tglobal_load_dwordx4 %2, %6, %8\n\tglobal_load_dwordx4 %3, %7, %8"
                         : "=&v"(wst[0]), "=&v"(wst[1]), "=&v"(wst[2]), "=&v"(wst[3]) : "v"(o_wd[0]), "v"(o_wd[1]), "v"(o_wd[2]), "v"(o_wd[3]), "s"(wd) : "memory");
#pragma unroll
            for (int t = 0; t < 8; ++t)
#pragma unroll
                for (int s = 2; s < 4; ++s) ct[t][s] = trd2((a_t[0][t >> 1] ^ ((t & 1) * 8)) + s * 32768, (a_t[1][t >> 1] ^ ((t & 1) * 8)) + s * 32768);
            FENCE();
            Q_LAND(4);
            STAMP(4);
            hint_put(rs, o_t3, tag);
            bar0();
            const f32x4 m0 = lrd<f32x4>(a_mxr);
            float mr = mx(mx3(m0[0], m0[1], m0[2]), m0[3]);
            PIN(mr);
            bar();
            STAMP(5);
            v4u pf[4];
#pragma unroll
            for (int s = 0; s < 4; ++s) pf[s] = lrd<v4u>(a_pr[s]);
            FENCE();
            f32x4 o[8];
#define M0_(t) asm volatile("v_mfma_f32_16x16x32_bf16 %0, %1, %2, 0" : "=&v"(o[t]) : "v"(ct[t][0]), "v"(pf[0]))
#define MM(t, s) asm volatile("v_mfma_f32_16x16x32_bf16 %0, %1, %2, %0" : "+v"(o[t]) : "v"(ct[t][s]), "v"(pf[s]))
#define RDY(t) PIN(o[t])
#define SEQ() asm volatile("" ::: "memory")
#define CV(d, t, i) do { d = pk(o[t][i], o[t][i + 1]); PIN(d); } while (0)
#define PUT(n, k, A, B, C) lwr<v4u>(a_sw + (n) * 2304 + (k) * 64, v4u{A, B, C, tag})
#define GETL(n) do { x0 = lrd<v4u>(a_sr + (n) * 2304); x1 = lrd<v4u>(a_sr + (n) * 2304 + 8 * 144); } while (0)
#define OUTL(n) do { stx(rs, o_h3 + (n) * 128, x0); stx(rs, o_h3b + (n) * 128, x1); } while (0)
            unsigned d0, d1, d2, d3, d4, d5;
            v4u x0, x1;
            M0_(0); M0_(1); MM(0, 1); MM(1, 1); MM(0, 2); MM(1, 2); MM(0, 3); MM(1, 3);
            M0_(2); MM(2, 1); MM(2, 2); MM(2, 3);
            M0_(3); MM(3, 1); MM(3, 2);
            RDY(0); CV(d0, 0, 0); CV(d1, 0, 2);
            MM(3, 3); RDY(1); CV(d2, 1, 0); CV(d3, 1, 2);
            M0_(4); PUT(0, 0, d0, d1, d2); SEQ();
            MM(4, 1); RDY(2); CV(d4, 2, 0); CV(d5, 2, 2);
            MM(4, 2); PUT(0, 1, d3, d4, d5); GETL(0); SEQ();
            MM(4, 3); RDY(3); CV(d0, 3, 0); CV(d1, 3, 2);
            M0_(5);
            bar0();
            const f32x4 l0 = lrd<f32x4>(a_lr);
            MM(5, 1); MM(5, 2); MM(5, 3);
            RDY(4); CV(d2, 4, 0); CV(d3, 4, 2);
            STAMP2(3);
            M0_(6); OUTL(0); PUT(1, 0, d0, d1, d2); SEQ();
            STAMP2(4);
            MM(6, 1);
            float lsum = (l0[0] + l0[1]) + (l0[2] + l0[3]);
            PIN(lsum);
            MM(6, 2); MM(6, 3);
            RDY(5); CV(d4, 5, 0); CV(d5, 5, 2);
            M0_(7); PUT(1, 1, d3, d4, d5); GETL(1); SEQ();
            MM(7, 1); MM(7, 2); MM(7, 3);
            asm volatile("s_nop 1" : "+v"(o[6]));
            CV(d0, 6, 0); CV(d1, 6, 2);
            STAMP2(14);
            OUTL(1); SEQ();
            STAMP2(15);
            asm volatile("s_nop 3" : "+v"(o[7]));
            CV(d2, 7, 0); CV(d3, 7, 2);
            PUT(2, 0, d0, d1, d2); PUT(2, 1, d3, asu(mr), asu(lsum)); GETL(2); OUTL(2);
            FENCE();
            STAMP(6);
            asm volatile("s_waitcnt vmcnt(7)" : "+v"(wst[0]), "+v"(wst[1]), "+v"(wst[2]), "+v"(wst[3]) :: "memory");
#pragma unroll
            for (int i = 0; i < 4; ++i) lwr<v4u>(L_R0 + (4 * w + i) * 1024 + l * 16, wst[i]);
        }
        if (STOPAT <= 7) goto done;
        bar();
        if (un < 86) {
            if (w == 4) {
                const f32x4 gp = lrd<f32x4>(L_RED + (l & 31) * 32), up = lrd<f32x4>(L_RED + (l & 31) * 32 + 16);
                const float gg = rbf((gp[0] + gp[1]) + (gp[2] + gp[3])), uu = rbf((up[0] + up[1]) + (up[2] + up[3]));
                const float sg = rbf(__builtin_amdgcn_rcpf(1.f + __builtin_amdgcn_exp2f(gg * -1.4426950408889634f)));
                lwr<u16>(L_HH + (l & 31) * 2, (u16)pk(sg * uu, 0.f));
            }
            bar();
            const v2u ha = lrd<v2u>(L_HH + (l & 1) * 32 + g * 8);
            const unsigned q = l15 >> 2, p = l & 3, row = 4 * g + q;
            f32x4 o[4];
#pragma unroll
            for (int t = 0; t < 4; ++t) {
                const unsigned ch = 8 * w + 2 * t + (p >> 1);
                o[t] = mfma16(ha, trd(L_R0 + row * 1024 + ((ch ^ ((row & 7) << 1)) << 4) + (p & 1) * 8), ZF);
            }
            const float o0 = g == 0 ? o[0][0] : g == 1 ? o[1][0] : g == 2 ? o[2][0] : o[3][0];
            const float o1 = g == 0 ? o[0][1] : g == 1 ? o[1][1] : g == 2 ? o[2][1] : o[3][1];
            stx(rs, WS_H5 + ((32 + un) * 512 + 64 * w + l) * 16, v4u{asu(o0), tag, asu(o1), tag});
        }
        STAMP(7);
        {
            unsigned a0, a1;
            {
                const unsigned q = l15 >> 2, p = l & 3, ch = 2 * w + (p >> 1);
                a0 = L_R1 + (8 * g + q) * 256 + 16 * (ch ^ ((q << 2) | ((2 * g) & 3))) + 8 * (p & 1);
                a1 = L_R1 + (8 * g + q + 4) * 256 + 16 * (ch ^ ((q << 2) | ((2 * g + 1) & 3))) + 8 * (p & 1);
            }
            unsigned o_g4 = WS_H4 + ((h * 4 + g) * 128 + 16 * w + l15) * 16;
            unsigned o_t5 = tid < 16 ? WS_T5 + ((8 * (cq + 4 * (tid >> 3)) + (tid & 7)) * 32 + h) * 128 : OOB;
            unsigned o_h5 = l < 16 ? WS_H5 + (h * 512 + 64 * (cq + 4 * (w >> 2)) + 16 * (w & 3) + l) * 16 : OOB;
            unsigned a_qw = L_QS + (g & 1) * 272 + (16 * w + l15) * 2, a_qr = L_QS + (l & 1) * 272 + 16 * g;
            v4u pb = {0u, tag, 0u, tag}, fw[4], ga;
            WAITVM(0);
            bar();
            frags128(a0, a1, fw);
            PIN(fw[0]); PIN(fw[1]); PIN(fw[2]); PIN(fw[3]); PIN(o_g4); PIN(o_t5); PIN(o_h5); PIN(a_qw); PIN(a_qr); PIN(pb);
            STAMP(10);
            if (w == 4) pace(hint_wait(rsq, o_t4, tag), P4);
            bar0();
            STAMP(11);
            asm volatile("buffer_load_dwordx4 %0, %1, %2, 0 offen sc1" : "=&v"(ga) : "v"(o_g4), "s"(rsq) : "memory");
            hint_put(rs, o_t5, tag);
            asm volatile("s_waitcnt vmcnt(1)" : "+v"(ga) :: "memory");
            if (__builtin_expect(!none(tagx(ga, tag) != 0u), 0)) {
#pragma nounroll
                SPIN { __builtin_amdgcn_s_sleep(2); get1(ga, o_g4, rsq); if (none(tagx(ga, tag) != 0u)) break; }
            }
            STAMP(8);
            lwr<u16>(a_qw, (u16)pk(sum32(sum16x2(asf(ga[0]), asf(ga[2]))), 0.f));
            bar();
            v4u cf[4];
#pragma unroll
            for (int s = 0; s < 4; ++s) cf[s] = lrd<v4u>(a_qr + 64 * s);
            FENCE();
            const f32x4 acc = gemv4(cf, fw);
            pb[0] = asu(acc[0]); pb[2] = asu(acc[1]);
            stx(rs, o_h5, pb);
        }
        STAMP(9);
    }
done:
    STAMP(13);
}

extern "C" void kernel_launch(void* const* d_in, const int* in_sizes, int n_in, void* d_out, int out_size, void* d_ws, size_t ws_size,
                              hipStream_t stream) {
    (void)in_sizes; (void)n_in; (void)out_size; (void)ws_size;
    (void)hipFuncSetAttribute((const void*)mla_glu_decode, hipFuncAttributeMaxDynamicSharedMemorySize, L_END);
    mla_glu_decode<<<256, 512, L_END, stream>>>((const u16*)d_in[0], (const u16*)d_in[1], (const u16*)d_in[2], (const u16*)d_in[3], (const u16*)d_in[4],
                                               (const u16*)d_in[5], (const u16*)d_in[7], (const u16*)d_in[8], (const u16*)d_in[9], (u16*)d_out,
                                               (unsigned char*)d_ws);
}
```

```cpp
#define STAMPS 1
#define SMASK 0x0001
#include <hip/hip_runtime.h>
#include <hip/hip_bf16.h>

typedef unsigned short u16;
typedef unsigned v4u __attribute__((ext_vector_type(4)));
typedef unsigned v2u __attribute__((ext_vector_type(2)));
typedef float f32x4 __attribute__((ext_vector_type(4)));
typedef float f32x2 __attribute__((ext_vector_type(2)));
typedef short s16x4 __attribute__((ext_vector_type(4)));
typedef __bf16 bf16x8 __attribute__((ext_vector_type(8)));
typedef __bf16 bf16x2 __attribute__((ext_vector_type(2)));
#define LAS __attribute__((address_space(3)))
#define GAS __attribute__((address_space(1)))
#define DEVI __device__ __forceinline__

#define PB 95
#define P2 100
#define P3 180
#define P4 65
#define P5 62
#define PG 220
#define PW 60
#define PA 0
#define CODEB 13696
#define EXP 0
#define PH 0
#define H3S 12288
#define STOPAT 99

constexpr int WS_T2 = 4096;
constexpr int WS_T3 = WS_T2 + 8 * 128 * 128;
constexpr int WS_T5 = WS_T3 + 8 * 128 * 128;
constexpr int WS_T4 = WS_T5 + 64 * 32 * 128;
constexpr int WS_H1 = WS_T4 + 32 * 16 * 128;
constexpr int WS_H4 = WS_H1 + 32 * 4 * 128 * 16;
constexpr int WS_H2 = WS_H4 + 32 * 4 * 128 * 16;
constexpr int WS_HG = WS_H2 + 4 * 2 * 32 * 128 * 16;
constexpr int WS_H5 = WS_HG + 2 * 16 * 1376 * 16;
constexpr int WS_H3 = WS_H5 + 118 * 512 * 16;
constexpr int WS_END = WS_H3 + 2 * 32 * 4 * H3S;
constexpr int WS_STAMP = 16 << 20;
constexpr int L_Q = 0;
constexpr int L_P = 16384;
constexpr int L_QS = L_P + 4096;
constexpr int L_OW = L_QS + 1024;
constexpr int L_MW = L_OW + 2304;
constexpr int L_MX = L_MW + 64;
constexpr int L_HH = L_MX + 512;
constexpr int L_TAG = L_HH + 64;
constexpr int L_JUNK = L_TAG + 64;
constexpr int L_RED = L_JUNK + 256;
constexpr int L_XN = 28672;
constexpr int L_R0 = 32768, L_R1 = 65536, L_R2 = 98304, L_R3 = 131072;
constexpr int L_END = 163840;
static_assert(L_RED + 1024 <= L_XN && L_XN + 2048 <= L_R0, "lds");
constexpr int SPIN_CAP = 1 << 16;

DEVI unsigned pk(float lo, float hi) { bf16x2 r = {(__bf16)lo, (__bf16)hi}; return __builtin_bit_cast(unsigned, r); }
DEVI float rbf(float f) { return (float)(__bf16)f; }
DEVI float lo16(unsigned w) { return __builtin_bit_cast(float, w << 16); }
DEVI float hi16(unsigned w) { return __builtin_bit_cast(float, w & 0xffff0000u); }
DEVI float asf(unsigned w) { return __builtin_bit_cast(float, w); }
DEVI unsigned asu(float w) { return __builtin_bit_cast(unsigned, w); }
DEVI float mx(float a, float b) { float r; asm("v_max_f32 %0, %1, %2" : "=v"(r) : "v"(a), "v"(b)); return r; }
DEVI float mx3(float a, float b, float c) { float r; asm("v_max3_f32 %0, %1, %2, %3" : "=v"(r) : "v"(a), "v"(b), "v"(c)); return r; }
DEVI f32x4 mfma32(v4u a, v4u b, f32x4 c) { return __builtin_amdgcn_mfma_f32_16x16x32_bf16(__builtin_bit_cast(bf16x8, a), __builtin_bit_cast(bf16x8, b), c, 0, 0, 0); }
DEVI f32x4 mfma16(v2u a, v2u b, f32x4 c) { return __builtin_amdgcn_mfma_f32_16x16x16bf16_1k(__builtin_bit_cast(s16x4, a), __builtin_bit_cast(s16x4, b), c, 0, 0, 0); }
template <class T> DEVI T lrd(unsigned off) { return *(LAS T*)(size_t)off; }
template <class T> DEVI void lwr(unsigned off, T v) { *(LAS T*)(size_t)off = v; }
DEVI v2u trd(unsigned off) { return __builtin_bit_cast(v2u, __builtin_amdgcn_ds_read_tr16_b64_v4i16((LAS s16x4*)(size_t)off)); }
DEVI v4u trd2(unsigned o0, unsigned o1) { v2u a = trd(o0), b = trd(o1); return v4u{a.x, a.y, b.x, b.y}; }
DEVI float sum16(float x) { auto r = __builtin_amdgcn_permlane16_swap(asu(x), asu(x), false, false); return asf(r[0]) + asf(r[1]); }
DEVI float sum32(float x) { auto r = __builtin_amdgcn_permlane32_swap(asu(x), asu(x), false, false); return asf(r[0]) + asf(r[1]); }
DEVI float max16(float x) { auto r = __builtin_amdgcn_permlane16_swap(asu(x), asu(x), false, false); return mx(asf(r[0]), asf(r[1])); }
DEVI float max32(float x) { auto r = __builtin_amdgcn_permlane32_swap(asu(x), asu(x), false, false); return mx(asf(r[0]), asf(r[1])); }
DEVI float odd16(float x) { auto r = __builtin_amdgcn_permlane16_swap(asu(x), asu(x), false, false); return asf(r[1]); }
DEVI float sum16x2(float x, float y) { auto r = __builtin_amdgcn_permlane16_swap(asu(x), asu(y), false, false); return asf(r[0]) + asf(r[1]); }
DEVI float sum32x2(float x, float y) { auto r = __builtin_amdgcn_permlane32_swap(asu(x), asu(y), false, false); return asf(r[0]) + asf(r[1]); }
template <int CTRL> DEVI float dpp(float x) { return asf((unsigned)__builtin_amdgcn_update_dpp(0, (int)asu(x), CTRL, 0xf, 0xf, true)); }
DEVI float wavesum(float x) {
    x += dpp<0xB1>(x); x += dpp<0x4E>(x); x += dpp<0x141>(x); x += dpp<0x140>(x);
    return sum32(sum16(x));
}
DEVI float dot2one(unsigned p, float c) { const unsigned one = 0x3f803f80u; return __builtin_amdgcn_fdot2_f32_bf16(__builtin_bit_cast(bf16x2, p), __builtin_bit_cast(bf16x2, one), c, false); }
#define STR_(x) #x
#define STR(x) STR_(x)
#define WAITVM(n) asm volatile("s_waitcnt vmcnt(" STR(n) ")" ::: "memory")
#define PIN(x) asm volatile("" : "+v"(x))
#define MFMA_SETTLE(x) asm volatile("s_nop 7" : "+v"(x))
#define UNDEF(x) asm volatile("" : "=v"(x))
#define FENCE() __builtin_amdgcn_sched_barrier(0)
#define SPIN for (int it_ = SPIN_CAP; it_ > 0; --it_)
DEVI void bar() { asm volatile("s_waitcnt lgkmcnt(0)\n\ts_barrier" ::: "memory"); }
DEVI void bar0() { asm volatile("s_barrier" ::: "memory"); }
#define USE_NT 0
template <bool NT = false> DEVI void glds(unsigned voff, const void* sbase, unsigned lds) {
    unsigned keep;
    if (NT && USE_NT) asm volatile("s_mov_b32 %0, m0\n\ts_mov_b32 m0, %3\n\ts_nop 0\n\tglobal_load_lds_dwordx4 %1, %2 nt\n\ts_mov_b32 m0, %0" : "=&s"(keep) : "v"(voff), "s"(sbase), "s"(lds) : "memory");
    else asm volatile("s_mov_b32 %0, m0\n\ts_mov_b32 m0, %3\n\ts_nop 0\n\tglobal_load_lds_dwordx4 %1, %2\n\ts_mov_b32 m0, %0" : "=&s"(keep) : "v"(voff), "s"(sbase), "s"(lds) : "memory");
}
typedef __amdgpu_buffer_rsrc_t rsrc_t;
constexpr unsigned OOB = 0x80000000u;
DEVI void stx(rsrc_t rs, unsigned off, v4u v) { __builtin_amdgcn_raw_buffer_store_b128(v, rs, off, 0, 16); }
DEVI bool none(bool bad) { asm volatile("" ::: "memory"); return __builtin_amdgcn_ballot_w64(bad) == 0ull; }
DEVI unsigned tagx(v4u a, unsigned tag) { unsigned r = (a[1] ^ tag) | (a[3] ^ tag); asm("" : "+v"(r)); return r; }
DEVI unsigned tagy(unsigned a, unsigned b, unsigned tag) { unsigned r = (a ^ tag) | (b ^ tag); asm("" : "+v"(r)); return r; }
DEVI unsigned tagx4(const v4u (&a)[4], unsigned tag) { return (tagx(a[0], tag) | tagx(a[1], tag)) | (tagx(a[2], tag) | tagx(a[3], tag)); }
DEVI void get1(v4u& a, unsigned off, v4u rq) { asm volatile("buffer_load_dwordx4 %0, %1, %2, 0 offen sc1\n\ts_waitcnt vmcnt(0)" : "+&v"(a) : "v"(off), "s"(rq) : "memory"); }
DEVI void get2(v4u& a, v4u& b, unsigned oa, unsigned ob, v4u rq) {
    asm volatile("buffer_load_dwordx4 %0, %2, %4, 0 offen sc1\n\tbuffer_load_dwordx4 %1, %3, %4, 0 offen sc1\n\ts_waitcnt vmcnt(0)" : "+&v"(a), "+&v"(b) : "v"(oa), "v"(ob), "s"(rq) : "memory");
}
template <int ST> DEVI void get4(v4u (&a)[4], unsigned off, v4u rq) {
    asm volatile("buffer_load_dwordx4 %0, %4, %5, 0 offen sc1\n\tbuffer_load_dwordx4 %1, %4, %5, 0 offen offset:%6 sc1\n\t"
                 "buffer_load_dwordx4 %2, %4, %5, 0 offen offset:%7 sc1\n\tbuffer_load_dwordx4 %3, %4, %5, 0 offen offset:%8 sc1\n\ts_waitcnt vmcnt(0)"
                 : "+&v"(a[0]), "+&v"(a[1]), "+&v"(a[2]), "+&v"(a[3]) : "v"(off), "s"(rq), "i"(ST), "i"(2 * ST), "i"(3 * ST) : "memory");
}
DEVI void get4s(v4u (&a)[4], unsigned off, v4u rq, unsigned s1, unsigned s2, unsigned s3) {
    asm volatile("buffer_load_dwordx4 %0, %4, %5, 0 offen sc1\n\tbuffer_load_dwordx4 %1, %4, %5, %6 offen sc1\n\t"
                 "buffer_load_dwordx4 %2, %4, %5, %7 offen sc1\n\tbuffer_load_dwordx4 %3, %4, %5, %8 offen sc1\n\ts_waitcnt vmcnt(0)"
                 : "+&v"(a[0]), "+&v"(a[1]), "+&v"(a[2]), "+&v"(a[3]) : "v"(off), "s"(rq), "s"(s1), "s"(s2), "s"(s3) : "memory");
}
DEVI unsigned now() { return (unsigned)__builtin_amdgcn_s_memrealtime(); }
DEVI void hint_put(rsrc_t rs, unsigned off, unsigned tag, unsigned t) { __builtin_amdgcn_raw_buffer_store_b64(v2u{tag, t}, rs, off, 0, 16); }
DEVI void hint_put(rsrc_t rs, unsigned off, unsigned tag) { hint_put(rs, off, tag, now()); }
DEVI v2u hint_get(v4u rq, unsigned off) { v2u v; asm volatile("buffer_load_dwordx2 %0, %1, %2, 0 offen sc1\n\ts_waitcnt vmcnt(0)" : "=&v"(v) : "v"(off), "s"(rq) : "memory"); return v; }
DEVI void pace(unsigned t, unsigned lead) {
#pragma nounroll
    for (int it = 256; it > 0; --it) { if (none(now() - t < lead)) break; }
}
DEVI unsigned hint_wait(v4u rq, unsigned off, unsigned tag) {
    v2u v;
#pragma nounroll
    SPIN { v = hint_get(rq, off); if (none(v[0] != tag)) break; __builtin_amdgcn_s_sleep(1); }
    return v[1];
}
DEVI unsigned mktag(unsigned ep) { return ep + 1u; }

#define NST 16
#define STAMP(i) do { if (!((SMASK >> (i)) & 1)) break; FENCE(); asm volatile("s_memrealtime %0\n\ts_waitcnt lgkmcnt(0)" : "=s"(st[i]) :: "memory"); FENCE(); } while (0)

#define CNT0(i)
#define CNT(i)
#define STAMPC(i)
#define STAMP2(i)
DEVI void frags128(unsigned a0, unsigned a1, v4u (&f)[4]) {
#pragma unroll
    for (int s = 0; s < 4; ++s) f[s] = trd2(a0 + s * 8192, a1 + s * 8192);
}
DEVI f32x4 gemv4(const v4u (&a)[4], const v4u (&b)[4]) {
    f32x4 c = {0.f, 0.f, 0.f, 0.f};
#pragma unroll
    for (int s = 0; s < 4; ++s) c = mfma32(a[s], b[s], c);
    return c;
}
DEVI void layernorm(v4u xv, int w, int l) {
    float v[8];
#pragma unroll
    for (int j = 0; j < 4; ++j) { v[2 * j] = lo16(xv[j]); v[2 * j + 1] = hi16(xv[j]); }
    const float mean = wavesum(((v[0] + v[1]) + (v[2] + v[3])) + ((v[4] + v[5]) + (v[6] + v[7]))) * (1.f / 512.f);
    float sq = 0.f;
#pragma unroll
    for (int j = 0; j < 8; ++j) { const float d = v[j] - mean; sq += d * d; }
    const float var = wavesum(sq) * (1.f / 512.f);
    const float mb = rbf(mean), ri = __builtin_amdgcn_rcpf(rbf(__builtin_amdgcn_sqrtf(rbf(rbf(var) + 1e-5f))));
    v4u o;
#pragma unroll
    for (int j = 0; j < 4; ++j) o[j] = pk(rbf(v[2 * j] - mb) * ri, rbf(v[2 * j + 1] - mb) * ri);
    lwr<v4u>(L_XN + w * 1024 + l * 16, o);
}

__global__ void __launch_bounds__(512, 2)
mla_glu_decode(const u16* __restrict__ x, const u16* __restrict__ ckv, const u16* __restrict__ wq, const u16* __restrict__ wuk,
               const u16* __restrict__ wuv, const u16* __restrict__ wo, const u16* __restrict__ wg, const u16* __restrict__ wu,
               const u16* __restrict__ wd, u16* __restrict__ out, unsigned char* __restrict__ ws) {
    unsigned long long st[NST];
#pragma unroll
    for (int i = 0; i < NST; ++i) st[i] = 0;
    STAMP(0);
    const int tid = threadIdx.x, l = tid & 63, w = __builtin_amdgcn_readfirstlane(tid >> 6);
    const int id = blockIdx.x, xc = id & 7, y = id >> 3, yy = y & 15;
    const int un = yy * 8 + xc;
    const unsigned g = l >> 4, l15 = l & 15;
    const unsigned t_in = now();
    unsigned cp0 = 0, cp1 = 0;
    if (y >= 16 && w == 2) {
        unsigned long long pc;
        asm volatile("s_getpc_b64 %0" : "=s"(pc));
        pc &= ~255ull;
        asm volatile("global_load_dword %0, %2, %4\n\tglobal_load_dword %1, %3, %4"
                     : "=&v"(cp0), "=&v"(cp1) : "v"(min(l * 128, CODEB - 128)), "v"(min(8192 + l * 128, CODEB - 128)), "s"(pc) : "memory");
    }
    asm volatile("" :: "s"(x), "s"(ckv), "s"(wq), "s"(wuk), "s"(wuv), "s"(wo), "s"(wg), "s"(wu), "s"(wd), "s"(out), "s"(ws));
    const rsrc_t rs = __builtin_amdgcn_make_buffer_rsrc(ws, 0, WS_END, 0x00020000);
    const v4u rsq = {(unsigned)(size_t)ws, (unsigned)((size_t)ws >> 32) & 0xffffu, (unsigned)WS_END, 0x00020000u};
    const f32x4 ZF = {0.f, 0.f, 0.f, 0.f};

    if (__builtin_expect(y < 16, 1)) {
        const int h = 4 * xc + (yy >> 2), cq = yy & 3;
        v4u xv; unsigned ep;
        if (w < 2) {
            asm volatile("global_load_dwordx4 %0, %1, %2" : "=v"(xv) : "v"(w * 1024 + l * 16), "s"(x) : "memory");
            asm volatile("global_load_dword %0, %1, %2 sc1" : "=v"(ep) : "v"(0), "s"(ws) : "memory");
        }
        const unsigned vb = (128 * cq + 16 * w + g) * 8192 + h * 256;
        unsigned sw[4];
#pragma unroll
        for (int i = 0; i < 4; ++i) sw[i] = (l15 ^ ((g << 2) | i)) << 4;
#pragma unroll
        for (int i = 0; i < 4; ++i) glds<true>(vb + i * 32768 + sw[i], wq, L_R0 + (16 * w + 4 * i) * 256);
        bar0();
#pragma unroll
        for (int i = 0; i < 4; ++i) glds<true>(vb + i * 32768 + ((l15 ^ (4 * i + g)) << 4), wuk, L_R1 + (16 * w + 4 * i) * 256);
        STAMP(1);
        if (w < 2) {
            asm volatile("s_waitcnt vmcnt(8)" : "+v"(xv), "+v"(ep) :: "memory");
            layernorm(xv, w, l);
            lwr<v4u>(L_TAG, v4u{mktag(ep), 0u, 0u, 0u});
        }
        bar();
        const unsigned tag = lrd<unsigned>(L_TAG);
        unsigned o_p1 = l < 16 ? WS_H1 + ((h * 4 + cq) * 128 + 16 * w + l) * 16 : OOB;
        unsigned o_g1 = WS_H1 + ((h * 4 + g) * 128 + 16 * w + l15) * 16;
        unsigned o_h2 = WS_H2 + ((((w & 3) * 2 + (l >> 5)) * 32 + h) * 128 + 32 * cq + (l & 31)) * 16;
        unsigned a_2w = l15 < 2 ? L_RED + l15 * 256 + (4 * w + g) * 8 : L_JUNK + (l & 31) * 8, a_2r = L_RED + l * 8;
        PIN(a_2w); PIN(a_2r);
        unsigned o_t2 = l < 8 ? WS_T2 + (l * 128 + h * 4 + cq) * 128 : OOB;
        unsigned a0, a1;
        {
            const unsigned q = l15 >> 2, p = l & 3, ch = 2 * w + (p >> 1);
            a0 = L_R0 + (8 * g + q) * 256 + 16 * (ch ^ ((q << 2) | ((2 * g) & 3))) + 8 * (p & 1);
            a1 = L_R0 + (8 * g + q + 4) * 256 + 16 * (ch ^ ((q << 2) | ((2 * g + 1) & 3))) + 8 * (p & 1);
        }
        unsigned a_qw = L_QS + (g & 1) * 272 + (16 * w + l15) * 2;
        unsigned a_qr = L_QS + (l & 1) * 272 + 16 * g;
        unsigned a_uk[4], o_gu[3], a_cnt = l == 0 ? L_TAG + 8 : L_JUNK + l * 4, one = 1u;
        v4u xf[4];
#pragma unroll
        for (int s = 0; s < 4; ++s) {
            xf[s] = lrd<v4u>(L_XN + (l & 1) * 1024 + (128 * cq + 32 * s + 8 * g) * 2);
            a_uk[s] = L_R1 + (16 * w + l15) * 256 + (((4 * s + g) ^ l15) << 4);
            PIN(xf[s]); PIN(a_uk[s]);
        }
        const int mat = un >> 6, mp = (un >> 2) & 15, fp = un & 3;
#pragma unroll
        for (int i = 0; i < 3; ++i) {
            const unsigned pc = min(8 * i + w, 21), idx = min(64 * pc + l, 1375u), row = idx / 43u, ch = idx - row * 43u;
            o_gu[i] = (32 * mp + row) * 2752 + fp * 688 + ch * 16; PIN(o_gu[i]);
        }
        v4u pb = {0u, tag, 0u, tag}, ga;
        PIN(a0); PIN(a1); PIN(o_p1); PIN(o_g1); PIN(o_h2); PIN(o_t2); PIN(a_qw); PIN(a_qr); PIN(pb); PIN(a_cnt); PIN(one);
        WAITVM(4);
        bar0();
        STAMP(2);
        {
            v4u f[4]; frags128(a0, a1, f);
            FENCE();
            const f32x4 acc = gemv4(xf, f);
            pb[0] = asu(acc[0]); pb[2] = asu(acc[1]);
            stx(rs, o_p1, pb);
        }
        STAMP(3);
        if (STOPAT <= 3) { WAITVM(0); STAMP(14); goto done; }
        __builtin_amdgcn_s_sleep(10);
        WAITVM(3);
        v4u fu[4];
        {
            unsigned arr;
            asm volatile("buffer_load_dwordx4 %1, %4, %5, 0 offen sc1\n\tds_add_rtn_u32 %0, %2, %3\n\ts_waitcnt lgkmcnt(0)" : "=&v"(arr), "=&v"(ga) : "v"(a_cnt), "v"(one), "v"(o_g1), "s"(rsq) : "memory");
            hint_put(rs, __builtin_amdgcn_readfirstlane(arr) == 7u ? o_t2 : OOB, tag);
            asm volatile("s_waitcnt vmcnt(1)" : "+v"(ga) :: "memory");
            STAMP(14);
#pragma unroll
            for (int s = 0; s < 4; ++s) fu[s] = lrd<v4u>(a_uk[s]);
            if (__builtin_expect(!none(tagx(ga, tag) != 0u), 0)) {
#pragma nounroll
                SPIN { __builtin_amdgcn_s_sleep(4); get1(ga, o_g1, rsq); if (none(tagx(ga, tag) != 0u)) break; }
            }
            lwr<u16>(a_qw, (u16)pk(sum32(sum16x2(asf(ga[0]), asf(ga[2]))), 0.f));
        }
        STAMP(4);
        bar();
        {
            v4u qf[4];
#pragma unroll
            for (int s = 0; s < 4; ++s) qf[s] = lrd<v4u>(a_qr + 64 * s);
            FENCE();
            const f32x4 acc = gemv4(fu, qf);
            constexpr float SC = 0.08838834764831845f * 1.4426950408889634f;
            lwr<v2u>(a_2w, v2u{pk(acc[0] * SC, acc[1] * SC), pk(acc[2] * SC, acc[3] * SC)});
            bar();
            if (w < 4) {
                const v2u t = lrd<v2u>(a_2r);
                pb[0] = t[0]; pb[2] = t[1];
                stx(rs, o_h2, pb);
            }
        }
        STAMP(5);
        if (STOPAT <= 5) goto done;
        const int r = xc * 8 + (yy >> 1);
        unsigned o_t3 = WS_T3 + (xc * 128 + (h >> 4) * 64 + 8 * w + (l & 7)) * 128, o_t5 = WS_T5 + (r * 32 + (l & 31)) * 128, j0, j1;
        asm volatile("buffer_load_dword %0, %2, %4, 0 offen sc1\n\tbuffer_load_dword %1, %3, %4, 0 offen sc1"
                     : "=&v"(j0), "=&v"(j1) : "v"(o_t3), "v"((w == 0 && !(yy & 1)) ? o_t5 : o_t3), "s"(rsq) : "memory");
        pace(t_in, PA);
#pragma unroll
        for (int i = 0; i < 3; ++i) glds(o_gu[i], mat ? wu : wg, L_R2 + min(8 * i + w, 21) * 1024);
#define V_ISSUE(i) glds<true>((EXP & 4) ? 0 : vb + (i) * 32768 + sw[i], wuv, L_R1 + (16 * w + 4 * (i)) * 256)
        V_ISSUE(0); V_ISSUE(1);
        {
            const v4u xg = lrd<v4u>(L_XN + (l & 1) * 1024 + (32 * mp + 8 * g) * 2);
            const unsigned ag = L_R2 + (8 * g + (l15 >> 2)) * 688 + (l & 3) * 8;
            const unsigned f = 16 * (8 * g + w) + l15;
            const unsigned o_hg = (g < 3 && f < 344) ? WS_HG + ((mat * 16 + mp) * 1376 + 344 * fp + f) * 16 : OOB;
            asm volatile("s_waitcnt vmcnt(2)" : "+v"(j0), "+v"(j1) :: "memory");
            bar();
            f32x4 o[3];
#pragma unroll
            for (int t = 0; t < 3; ++t) {
                const unsigned tl = min(8 * t + w, 21);
                o[t] = mfma32(xg, trd2(ag + tl * 32, ag + tl * 32 + 4 * 688), ZF);
            }
            const float o0 = g == 0 ? o[0][0] : g == 1 ? o[1][0] : o[2][0], o1 = g == 0 ? o[0][1] : g == 1 ? o[1][1] : o[2][1];
            stx(rs, o_hg, v4u{asu(o0), tag, asu(o1), tag});
        }
        V_ISSUE(2); V_ISSUE(3);
        STAMP(6);
        const unsigned u = l15 >= 12 ? 8 + l15 : (g & 1) ? 12 + (l15 & 7) : l15;
        const bool act = (g & 1) ? (l15 & 12) != 8 : l15 < 12;
        unsigned o_h3 = WS_H3 + (((w >> 2) * 32 + h) * 4 + cq) * H3S + (8 * (w & 3) + (g >> 1)) * 384 + u * 16;
        unsigned o_t4 = l < 4 ? WS_T4 + ((h * 4 + l) * 4 + cq) * 128 : OOB;
        unsigned o_p4 = l < 16 ? WS_H4 + ((h * 4 + cq) * 128 + 16 * w + l) * 16 : OOB;
        unsigned a_ow, a_ow2, a_ms;
        {
            const unsigned n = 6 * (u >> 2) + 2 * (g >> 1), n2 = 6 * (u >> 2) + 4, gg = u & 3;
            a_ow = (act && n < 32) ? L_OW + w * 272 + (32 * gg + 4 * ((n >> 2) ^ (gg >> 1)) + (n & 3)) * 2 : L_JUNK + l * 4;
            a_ow2 = (act && n2 < 32 && l < 32) ? L_OW + w * 272 + (32 * gg + 4 * ((n2 >> 2) ^ (gg >> 1)) + (n2 & 3)) * 2 : L_JUNK + l * 4;
            a_ms = (l & 31) == 12 ? L_MW + (g >> 1) * 32 + w * 4 : L_JUNK + l * 4;
        }
        unsigned a_or = L_OW + (l & 7) * 272 + 16 * g, a_mw = L_MW + (g & 1) * 16;
        v4u a[4];
        UNDEF(a[0]); UNDEF(a[1]); UNDEF(a[2]); UNDEF(a[3]);
        a_cnt += l == 0 ? 4 : 0;
        PIN(o_h3); PIN(o_t3); PIN(o_t4); PIN(o_p4); PIN(a_ow); PIN(a_ow2); PIN(a_or); PIN(a_mw); PIN(a_ms); PIN(a_cnt);
        WAITVM(0);
        bar();
        v4u fv[4];
        frags128(a0 + (L_R1 - L_R0), a1 + (L_R1 - L_R0), fv);
        PIN(fv[0]); PIN(fv[1]); PIN(fv[2]); PIN(fv[3]);
        STAMP(7);
        if (STOPAT <= 7) goto done;
        CNT0(2); CNT0(3);
        const unsigned t3 = hint_wait(rsq, o_t3, tag);
        STAMPC(1);
        pace(t3, P3);
        STAMP(15);
        {
#pragma nounroll
            SPIN { CNT(2); get4<768>(a, o_h3, rsq); if (none((tagy(a[0][3], a[1][3], tag) | tagy(a[2][3], a[3][3], tag)) != 0u)) break; __builtin_amdgcn_s_sleep(4); }
            STAMP(8);
            unsigned arr;
            unsigned long long tm;
            asm volatile("ds_add_rtn_u32 %0, %2, %3\n\ts_memrealtime %1" : "=&v"(arr), "=&s"(tm) : "v"(a_cnt), "v"(one) : "memory");
            float m[4], al[4];
#pragma unroll
            for (int k = 0; k < 4; ++k) m[k] = dpp<0x15c>(asf(a[k][1]));
            const float M = max32(mx(mx3(m[0], m[1], m[2]), m[3]));
#pragma unroll
            for (int k = 0; k < 4; ++k) al[k] = __builtin_amdgcn_exp2f(m[k] - M);
            FENCE();
            asm volatile("s_waitcnt lgkmcnt(0)" : "+v"(arr), "+s"(tm) :: "memory");
            hint_put(rs, __builtin_amdgcn_readfirstlane(arr) == 7u ? o_t4 : OOB, tag, (unsigned)tm);
            f32x2 c01, c23, c45;
            float Ls;
#pragma unroll
            for (int k = 0; k < 4; ++k) {
                const f32x2 aa = {al[k], al[k]}, v01 = {lo16(a[k][0]), hi16(a[k][0])}, v23 = {lo16(a[k][1]), hi16(a[k][1])}, v45 = {lo16(a[k][2]), hi16(a[k][2])};
                if (k == 0) { c01 = aa * v01; c23 = aa * v23; c45 = aa * v45; Ls = al[0] * asf(a[0][2]); }
                else { c01 = __builtin_elementwise_fma(aa, v01, c01); c23 = __builtin_elementwise_fma(aa, v23, c23); c45 = __builtin_elementwise_fma(aa, v45, c45); Ls = __builtin_fmaf(al[k], asf(a[k][2]), Ls); }
            }
            const float acc[6] = {c01[0], c01[1], c23[0], c23[1], c45[0], c45[1]};
            const float v4 = sum32x2(acc[4], Ls);
            lwr<unsigned>(a_ow, pk(sum32x2(acc[0], acc[2]), sum32x2(acc[1], acc[3])));
            lwr<unsigned>(a_ow2, pk(v4, sum32(acc[5])));
            lwr<float>(a_ms, l < 32 ? M : v4);
        }
        bar();
        {
            v4u of[4];
#pragma unroll
            for (int s = 0; s < 4; ++s) of[s] = lrd<v4u>(a_or + 64 * s);
            const f32x4 mw = lrd<f32x4>(a_mw), lw = lrd<f32x4>(a_mw + 32);
            FENCE();
            const f32x4 c = gemv4(of, fv);
            const float M = mx(mx3(mw[0], mw[1], mw[2]), mw[3]);
            float be[4];
#pragma unroll
            for (int r = 0; r < 4; ++r) be[r] = __builtin_amdgcn_exp2f(mw[r] - M);
            const float rd = __builtin_amdgcn_rcpf((be[0] * lw[0] + be[1] * lw[1]) + (be[2] * lw[2] + be[3] * lw[3]));
            const float v = ((be[0] * c[0] + be[1] * c[1]) + (be[2] * c[2] + be[3] * c[3])) * rd;
            auto sv = __builtin_amdgcn_permlane16_swap(asu(v), asu(v), false, false);
            pb[0] = sv[0]; pb[2] = sv[1];
            stx(rs, o_p4, pb);
        }
        STAMP(9);
        if ((yy & 1) == 0) {
            const unsigned o1 = WS_H5 + ((32 + (tid >> 3)) * 512 + 8 * r + (tid & 7)) * 16;
            const bool two = tid < 176;
            const unsigned o2 = two ? o1 + 64 * 512 * 16 : o1;
            pace(t3, PG);
#pragma nounroll
            SPIN { get2(a[0], a[1], o1, o2, rsq); if (none((tagx(a[0], tag) | tagx(a[1], tag)) != 0u)) break; __builtin_amdgcn_s_sleep(8); }
            {
                float t = sum32(sum16x2(asf(a[0][0]) + (two ? asf(a[1][0]) : 0.f), asf(a[0][2]) + (two ? asf(a[1][2]) : 0.f)));
                t += dpp<0x128>(t);
                lwr<float>(L_RED + ((g & 1) * 8 + (l & 7)) * 32 + w * 4, t);
            }
            bar();
            STAMP(10);
            if (w == 0) {
                const f32x4 t2 = lrd<f32x4>(L_RED + ((g & 1) * 8 + (l & 7)) * 32), t4 = lrd<f32x4>(L_RED + ((g & 1) * 8 + (l & 7)) * 32 + 16);
                float glu = rbf(((t2[0] + t2[1]) + (t2[2] + t2[3])) + ((t4[0] + t4[1]) + (t4[2] + t4[3])));
                float xin = lo16(x[(g & 1) * 512 + 8 * r + (l & 7)]);
                unsigned o0 = WS_H5 + ((l >> 3) * 512 + 8 * r + (l & 7)) * 16;
                unsigned o_out = ((g & 1) * 512 + 8 * r + (l & 7)) * 2;
                PIN(glu); PIN(xin); PIN(o0); PIN(o_out);
                pace(hint_wait(rsq, o_t5, tag), P5);
                STAMP(11);
#pragma nounroll
                SPIN { CNT(3); get4s(a, o0, rsq, 65536, 131072, 196608); if (none(tagx4(a, tag) != 0u)) break; }
                STAMP(12);
                float t = sum32(sum16x2((asf(a[0][0]) + asf(a[1][0])) + (asf(a[2][0]) + asf(a[3][0])), (asf(a[0][2]) + asf(a[1][2])) + (asf(a[2][2]) + asf(a[3][2]))));
                t += dpp<0x128>(t);
                if (l < 32 && l15 < 8) __hip_atomic_store((GAS u16*)((GAS unsigned char*)out + o_out), (u16)pk(rbf(xin + rbf(t)) + glu, 0.f), __ATOMIC_RELAXED, __HIP_MEMORY_SCOPE_AGENT);
                if (r == 0 && l == 0) __hip_atomic_store((GAS unsigned*)ws, tag, __ATOMIC_RELAXED, __HIP_MEMORY_SCOPE_AGENT);
            }
        }
    } else {
        const int pr = xc * 8 + (yy >> 1), hg = yy & 1, b = pr >> 5, sp = pr & 31;
        unsigned ep, j0;
        unsigned o_t2 = WS_T2 + (xc * 128 + (16 * hg + 2 * w) * 4 + (l & 7)) * 128;
        asm volatile("global_load_dword %0, %1, %2 sc1" : "=v"(ep) : "v"(0), "s"(ws) : "memory");
        asm volatile("buffer_load_dword %0, %1, %2, 0 offen sc1" : "=v"(j0) : "v"(o_t2), "s"(rsq) : "memory");
        unsigned qoff = WS_H2 + ((((xc & 3) * 2 + b) * 32 + 16 * hg + 2 * w) * 128 + l) * 16;
        unsigned a_q0 = L_Q + 2 * w * 1024 + (((l >> 1) ^ (2 * w)) << 4) + (l & 1) * 8, a_q1 = L_Q + (2 * w + 1) * 1024 + (((l >> 1) ^ (2 * w + 1)) << 4) + (l & 1) * 8;
        unsigned o_kv[8];
#pragma unroll
        for (int i = 0; i < 8; ++i) { o_kv[i] = (pr * 128 + 64 * hg + 8 * w + i) * 1024 + ((l ^ (8 * (w & 1) + i)) << 4); PIN(o_kv[i]); }
        PIN(qoff); PIN(a_q0); PIN(a_q1);
        asm volatile("s_waitcnt vmcnt(0)" : "+v"(ep), "+v"(j0), "+v"(cp0), "+v"(cp1) :: "memory");
        const unsigned tag = mktag(ep);
        pace(t_in, PB);
#pragma unroll
        for (int i = 0; i < 8; ++i) glds(o_kv[i], ckv, L_R0 + (8 * w + i) * 1024);
        STAMP(1);
        bar0();
#pragma unroll
        for (int i = 0; i < 8; ++i) glds(o_kv[i] ^ 65536, ckv, L_R0 + (64 + 8 * w + i) * 1024);
        WAITVM(8);
        bar0();
        STAMP(2);
#define Q_ISSUE() \
            FENCE(); \
            WAITVM(2); \
            const v2u hv = hint_get(rsq, o_t2);        \
            STAMP(14); \
            pace(none(hv[0] != tag) ? hv[1] : hint_wait(rsq, o_t2, tag), P2); \
            STAMP(15); \
            v4u qa[4]; \
            asm volatile("buffer_load_dwordx4 %0, %4, %5, 0 offen sc1\n\tbuffer_load_dwordx4 %1, %4, %5, 0 offen offset:1024 sc1\n\t" \
                         "buffer_load_dwordx4 %2, %4, %5, 0 offen offset:2048 sc1\n\tbuffer_load_dwordx4 %3, %4, %5, 0 offen offset:3072 sc1" \
                         : "=&v"(qa[0]), "=&v"(qa[1]), "=&v"(qa[2]), "=&v"(qa[3]) : "v"(qoff), "s"(rsq) : "memory"); \
            bar0()
#define Q_LAND(n) do { \
            asm volatile("s_waitcnt vmcnt(" STR(n) ")" : "+v"(qa[0]), "+v"(qa[1]), "+v"(qa[2]), "+v"(qa[3]) :: "memory"); \
            if (__builtin_expect(!none(tagx4(qa, tag) != 0u), 0)) { _Pragma("nounroll") SPIN { __builtin_amdgcn_s_sleep(4); get4<1024>(qa, qoff, rsq); if (none(tagx4(qa, tag) != 0u)) break; } } \
            asm volatile("ds_write2_b32 %0, %2, %3 offset1:1\n\tds_write2_b32 %0, %4, %5 offset0:128 offset1:129\n\tds_write2_b32 %1, %6, %7 offset1:1\n\tds_write2_b32 %1, %8, %9 offset0:128 offset1:129" \
                         :: "v"(a_q0), "v"(a_q1), "v"(qa[0][0]), "v"(qa[0][2]), "v"(qa[1][0]), "v"(qa[1][2]), "v"(qa[2][0]), "v"(qa[2][2]), "v"(qa[3][0]), "v"(qa[3][2]) : "memory"); \
            STAMP(3); bar();   } while (0)
        unsigned t5 = 0;
        const int h = 4 * xc + (yy >> 2), cq = yy & 3;
        unsigned o_t4 = WS_T4 + ((h * 4 + cq) * 4 + (l & 3)) * 128;
        unsigned o_hg = (EXP & 2) ? 0 : WS_HG + ((4 * (w & 3) + g) * 1376 + 16 * min(un, 85) + l15) * 16;
        if (w >= 4) {
            unsigned a_r[4];
#pragma unroll
            for (int j = 0; j < 4; ++j) a_r[j] = l15 * 1024 + (((4 * j + g) ^ l15) << 4);
            const unsigned kb = L_R0 + (w - 4) * 16384;
            v4u cr[32], qf[16];
#pragma unroll
            for (int s = 0; s < 16; ++s) cr[s] = lrd<v4u>(a_r[s & 3] + kb + (s >> 2) * 256);
            unsigned a_mxw = L_MX + l15 * 16 + (w - 4) * 4, a_mxr = L_MX + l15 * 16;
            unsigned a_pw = L_P + l15 * 256 + (((2 * (w - 4) + (g >> 1)) ^ l15) << 4) + (g & 1) * 8;
            __builtin_amdgcn_s_setprio(1);
            PIN(a_mxw); PIN(a_mxr); PIN(a_pw); PIN(o_hg);
            Q_ISSUE();
#pragma unroll
            for (int s = 0; s < 16; ++s) cr[16 + s] = lrd<v4u>(a_r[s & 3] + kb + 65536 + (s >> 2) * 256);
            FENCE();
            Q_LAND(0);
            STAMP(4);
#pragma unroll
            for (int s = 0; s < 16; ++s) qf[s] = lrd<v4u>(a_r[s & 3] + L_Q + (s >> 2) * 256);
            FENCE();
            f32x4 s1 = ZF, s2 = ZF;
#pragma unroll
            for (int s = 0; s < 16; ++s) { s1 = mfma32(cr[s], qf[s], s1); s2 = mfma32(cr[16 + s], qf[s], s2); }
            asm volatile("s_nop 7" : "+v"(s1), "+v"(s2));
            lwr<float>(a_mxw, max32(max16(mx(mx3(mx3(s1[0], s1[1], s1[2]), s1[3], s2[0]), mx3(s2[1], s2[2], s2[3])))));
            bar();
            STAMP(12);
            const f32x4 m0 = lrd<f32x4>(a_mxr);
            const float mr = mx(mx3(m0[0], m0[1], m0[2]), m0[3]);
            v2u p1 = {pk(__builtin_amdgcn_exp2f(s1[0] - mr), __builtin_amdgcn_exp2f(s1[1] - mr)), pk(__builtin_amdgcn_exp2f(s1[2] - mr), __builtin_amdgcn_exp2f(s1[3] - mr))};
            lwr<v2u>(a_pw, p1);
            v2u p2 = {pk(__builtin_amdgcn_exp2f(s2[0] - mr), __builtin_amdgcn_exp2f(s2[1] - mr)), pk(__builtin_amdgcn_exp2f(s2[2] - mr), __builtin_amdgcn_exp2f(s2[3] - mr))};
            lwr<v2u>(a_pw ^ 128, p2);
            bar();
            STAMP(5);
            PIN(p1); PIN(p2);
            {
                const unsigned q0 = p1[0], q1 = p1[1], q2 = p2[0], q3 = p2[1];
                lwr<float>(a_mxw + 256, sum32(sum16(((lo16(q0) + hi16(q0)) + (lo16(q1) + hi16(q1))) + ((lo16(q2) + hi16(q2)) + (lo16(q3) + hi16(q3))))));
            }
            bar();
            __builtin_amdgcn_s_setprio(0);
            t5 = now();
            if (PH) pace(t5, PH);
            v4u ga, gb;
            asm volatile("buffer_load_dwordx4 %0, %3, %6, 0 offen sc1\n\tbuffer_load_dwordx4 %1, %4, %6, 0 offen sc1\n\tbuffer_load_dword %2, %5, %6, 0 offen sc1\n\ts_waitcnt vmcnt(0)"
                         : "=&v"(ga), "=&v"(gb), "=&v"(j0), "+v"(o_hg) : "v"(o_hg + 16 * 1376 * 16), "v"(o_t4), "s"(rsq) : "memory");
            if (un < 86) {
                if (!(EXP & 2) && __builtin_expect(!none((tagx(ga, tag) | tagx(gb, tag)) != 0u), 0)) {
#pragma nounroll
                    SPIN { __builtin_amdgcn_s_sleep(16); get2(ga, gb, o_hg, o_hg + 16 * 1376 * 16, rsq); if (none((tagx(ga, tag) | tagx(gb, tag)) != 0u)) break; }
                }
                lwr<float>(L_RED + (l & 31) * 32 + (w - 4) * 4, sum32(sum16x2(asf(ga[0]), asf(ga[2]))));
                lwr<float>(L_RED + (l & 31) * 32 + w * 4, sum32(sum16x2(asf(gb[0]), asf(gb[2]))));
            }
            pace(t5, PW);
#pragma unroll
            for (int i = 0; i < 8; ++i) {
                const unsigned ch = l15 ^ ((g << 2) | (i & 3));
                glds((h * 128 + 32 * (w - 4) + 4 * i + g) * 1024 + cq * 128 + (ch >> 3) * 512 + (ch & 7) * 16, wo, L_R1 + (32 * (w - 4) + 4 * i) * 256);
            }
        } else {
            unsigned a_t[2][4];
            {
                const unsigned q = l15 >> 2, p = l & 3;
#pragma unroll
                for (int j = 0; j < 8; ++j) {
                    const unsigned r0 = 8 * g + 4 * (j >> 2) + q, ch = 16 * w + 4 * p + (j & 3);
                    a_t[j >> 2][j & 3] = L_R0 + r0 * 1024 + ((ch ^ (r0 & 15)) << 4) + (p >> 1) * 8;
                }
            }
            v4u ct[8][4];
#pragma unroll
            for (int t = 0; t < 8; ++t)
#pragma unroll
                for (int s = 0; s < 2; ++s) ct[t][s] = trd2((a_t[0][t >> 1] ^ ((t & 1) * 8)) + s * 32768, (a_t[1][t >> 1] ^ ((t & 1) * 8)) + s * 32768);
            unsigned a_pr[4], a_mxr = L_MX + l15 * 16;
#pragma unroll
            for (int j = 0; j < 4; ++j) { a_pr[j] = L_P + l15 * 256 + (((4 * j + g) ^ l15) << 4); PIN(a_pr[j]); }
            unsigned o_t3 = tid < 4 ? WS_T3 + ((4 * hg + tid) * 128 + hg * 64 + pr) * 128 : OOB;
            unsigned o_h3 = WS_H3 + ((b * 32 + 16 * hg + (l >> 3)) * 4 + w) * H3S + sp * 384 + (l & 7) * 16, o_h3b = o_h3 + 8 * 4 * H3S;
            unsigned a_sw = L_R2 + w * 8192 + l15 * 144 + g * 16, a_sr = L_R2 + w * 8192 + (l >> 3) * 144 + (l & 7) * 16;
            PIN(a_sw); PIN(a_sr);
            unsigned o_wd[4], a_lr = L_MX + 256 + l15 * 16;
#pragma unroll
            for (int i = 0; i < 4; ++i) { const unsigned row = 4 * w + i; o_wd[i] = (EXP & 1) ? 0 : (16 * min(un, 85) + row) * 1024 + ((l ^ ((row & 7) << 1)) << 4); PIN(o_wd[i]); }
            PIN(a_mxr); PIN(o_t3); PIN(o_h3); PIN(o_h3b); PIN(a_lr);
            Q_ISSUE();
            v4u wst[4];
            asm volatile("global_load_dwordx4 %0, %4, %8\n\tglobal_load_dwordx4 %1, %5, %8\n\tglobal_load_dwordx4 %2, %6, %8\n\tglobal_load_dwordx4 %3, %7, %8"
                         : "=&v"(wst[0]), "=&v"(wst[1]), "=&v"(wst[2]), "=&v"(wst[3]) : "v"(o_wd[0]), "v"(o_wd[1]), "v"(o_wd[2]), "v"(o_wd[3]), "s"(wd) : "memory");
#pragma unroll
            for (int t = 0; t < 8; ++t)
#pragma unroll
                for (int s = 2; s < 4; ++s) ct[t][s] = trd2((a_t[0][t >> 1] ^ ((t & 1) * 8)) + s * 32768, (a_t[1][t >> 1] ^ ((t & 1) * 8)) + s * 32768);
            FENCE();
            Q_LAND(4);
            STAMP(4);
            hint_put(rs, o_t3, tag);
            bar0();
            const f32x4 m0 = lrd<f32x4>(a_mxr);
            float mr = mx(mx3(m0[0], m0[1], m0[2]), m0[3]);
            PIN(mr);
            bar();
            STAMP(5);
            v4u pf[4];
#pragma unroll
            for (int s = 0; s < 4; ++s) pf[s] = lrd<v4u>(a_pr[s]);
            FENCE();
            f32x4 o[8];
#define M0_(t) asm volatile("v_mfma_f32_16x16x32_bf16 %0, %1, %2, 0" : "=&v"(o[t]) : "v"(ct[t][0]), "v"(pf[0]))
#define MM(t, s) asm volatile("v_mfma_f32_16x16x32_bf16 %0, %1, %2, %0" : "+v"(o[t]) : "v"(ct[t][s]), "v"(pf[s]))
#define RDY(t) PIN(o[t])
#define SEQ() asm volatile("" ::: "memory")
#define CV(d, t, i) do { d = pk(o[t][i], o[t][i + 1]); PIN(d); } while (0)
#define PUT(n, k, A, B, C) lwr<v4u>(a_sw + (n) * 2304 + (k) * 64, v4u{A, B, C, tag})
#define GETL(n) do { x0 = lrd<v4u>(a_sr + (n) * 2304); x1 = lrd<v4u>(a_sr + (n) * 2304 + 8 * 144); } while (0)
#define OUTL(n) do { stx(rs, o_h3 + (n) * 128, x0); stx(rs, o_h3b + (n) * 128, x1); } while (0)
            unsigned d0, d1, d2, d3, d4, d5;
            v4u x0, x1;
            M0_(0); M0_(1); MM(0, 1); MM(1, 1); MM(0, 2); MM(1, 2); MM(0, 3); MM(1, 3);
            M0_(2); MM(2, 1); MM(2, 2); MM(2, 3);
            M0_(3); MM(3, 1); MM(3, 2);
            RDY(0); CV(d0, 0, 0); CV(d1, 0, 2);
            MM(3, 3); RDY(1); CV(d2, 1, 0); CV(d3, 1, 2);
            M0_(4); PUT(0, 0, d0, d1, d2); SEQ();
            MM(4, 1); RDY(2); CV(d4, 2, 0); CV(d5, 2, 2);
            MM(4, 2); PUT(0, 1, d3, d4, d5); GETL(0); SEQ();
            MM(4, 3); RDY(3); CV(d0, 3, 0); CV(d1, 3, 2);
            M0_(5);
            bar0();
            const f32x4 l0 = lrd<f32x4>(a_lr);
            MM(5, 1); MM(5, 2); MM(5, 3);
            RDY(4); CV(d2, 4, 0); CV(d3, 4, 2);
            STAMP2(3);
            M0_(6); OUTL(0); PUT(1, 0, d0, d1, d2); SEQ();
            STAMP2(4);
            MM(6, 1);
            float lsum = (l0[0] + l0[1]) + (l0[2] + l0[3]);
            PIN(lsum);
            MM(6, 2); MM(6, 3);
            RDY(5); CV(d4, 5, 0); CV(d5, 5, 2);
            M0_(7); PUT(1, 1, d3, d4, d5); GETL(1); SEQ();
            MM(7, 1); MM(7, 2); MM(7, 3);
            asm volatile("s_nop 1" : "+v"(o[6]));
            CV(d0, 6, 0); CV(d1, 6, 2);
            STAMP2(14);
            OUTL(1); SEQ();
            STAMP2(15);
            asm volatile("s_nop 3" : "+v"(o[7]));
            CV(d2, 7, 0); CV(d3, 7, 2);
            PUT(2, 0, d0, d1, d2); PUT(2, 1, d3, asu(mr), asu(lsum)); GETL(2); OUTL(2);
            FENCE();
            STAMP(6);
            asm volatile("s_waitcnt vmcnt(7)" : "+v"(wst[0]), "+v"(wst[1]), "+v"(wst[2]), "+v"(wst[3]) :: "memory");
#pragma unroll
            for (int i = 0; i < 4; ++i) lwr<v4u>(L_R0 + (4 * w + i) * 1024 + l * 16, wst[i]);
        }
        if (STOPAT <= 7) goto done;
        bar();
        if (un < 86) {
            if (w == 4) {
                const f32x4 gp = lrd<f32x4>(L_RED + (l & 31) * 32), up = lrd<f32x4>(L_RED + (l & 31) * 32 + 16);
                const float gg = rbf((gp[0] + gp[1]) + (gp[2] + gp[3])), uu = rbf((up[0] + up[1]) + (up[2] + up[3]));
                const float sg = rbf(__builtin_amdgcn_rcpf(1.f + __builtin_amdgcn_exp2f(gg * -1.4426950408889634f)));
                lwr<u16>(L_HH + (l & 31) * 2, (u16)pk(sg * uu, 0.f));
            }
            bar();
            const v2u ha = lrd<v2u>(L_HH + (l & 1) * 32 + g * 8);
            const unsigned q = l15 >> 2, p = l & 3, row = 4 * g + q;
            f32x4 o[4];
#pragma unroll
            for (int t = 0; t < 4; ++t) {
                const unsigned ch = 8 * w + 2 * t + (p >> 1);
                o[t] = mfma16(ha, trd(L_R0 + row * 1024 + ((ch ^ ((row & 7) << 1)) << 4) + (p & 1) * 8), ZF);
            }
            const float o0 = g == 0 ? o[0][0] : g == 1 ? o[1][0] : g == 2 ? o[2][0] : o[3][0];
            const float o1 = g == 0 ? o[0][1] : g == 1 ? o[1][1] : g == 2 ? o[2][1] : o[3][1];
            stx(rs, WS_H5 + ((32 + un) * 512 + 64 * w + l) * 16, v4u{asu(o0), tag, asu(o1), tag});
        }
        STAMP(7);
        {
            unsigned a0, a1;
            {
                const unsigned q = l15 >> 2, p = l & 3, ch = 2 * w + (p >> 1);
                a0 = L_R1 + (8 * g + q) * 256 + 16 * (ch ^ ((q << 2) | ((2 * g) & 3))) + 8 * (p & 1);
                a1 = L_R1 + (8 * g + q + 4) * 256 + 16 * (ch ^ ((q << 2) | ((2 * g + 1) & 3))) + 8 * (p & 1);
            }
            unsigned o_g4 = WS_H4 + ((h * 4 + g) * 128 + 16 * w + l15) * 16;
            unsigned o_t5 = tid < 16 ? WS_T5 + ((8 * (cq + 4 * (tid >> 3)) + (tid & 7)) * 32 + h) * 128 : OOB;
            unsigned o_h5 = l < 16 ? WS_H5 + (h * 512 + 64 * (cq + 4 * (w >> 2)) + 16 * (w & 3) + l) * 16 : OOB;
            unsigned a_qw = L_QS + (g & 1) * 272 + (16 * w + l15) * 2, a_qr = L_QS + (l & 1) * 272 + 16 * g;
            v4u pb = {0u, tag, 0u, tag}, fw[4], ga;
            WAITVM(0);
            bar();
            frags128(a0, a1, fw);
            PIN(fw[0]); PIN(fw[1]); PIN(fw[2]); PIN(fw[3]); PIN(o_g4); PIN(o_t5); PIN(o_h5); PIN(a_qw); PIN(a_qr); PIN(pb);
            STAMP(10);
            if (w == 4) pace(hint_wait(rsq, o_t4, tag), P4);
            bar0();
            STAMP(11);
            asm volatile("buffer_load_dwordx4 %0, %1, %2, 0 offen sc1" : "=&v"(ga) : "v"(o_g4), "s"(rsq) : "memory");
            hint_put(rs, o_t5, tag);
            asm volatile("s_waitcnt vmcnt(1)" : "+v"(ga) :: "memory");
            if (__builtin_expect(!none(tagx(ga, tag) != 0u), 0)) {
#pragma nounroll
                SPIN { __builtin_amdgcn_s_sleep(2); get1(ga, o_g4, rsq); if (none(tagx(ga, tag) != 0u)) break; }
            }
            STAMP(8);
            lwr<u16>(a_qw, (u16)pk(sum32(sum16x2(asf(ga[0]), asf(ga[2]))), 0.f));
            bar();
            v4u cf[4];
#pragma unroll
            for (int s = 0; s < 4; ++s) cf[s] = lrd<v4u>(a_qr + 64 * s);
            FENCE();
            const f32x4 acc = gemv4(cf, fw);
            pb[0] = asu(acc[0]); pb[2] = asu(acc[1]);
            stx(rs, o_h5, pb);
        }
        STAMP(9);
    }
done:
    STAMP(13);
    if (l == 0) {
        GAS unsigned long long* so = (GAS unsigned long long*)(ws + WS_STAMP) + ((size_t)id * 8 + w) * NST;
#pragma unroll
        for (int i = 0; i < NST; ++i) so[i] = st[i];
    }
}

__global__ void __launch_bounds__(256) k_report(const unsigned long long* stp, int mask, int scale) {
    __shared__ unsigned long long r0[256];
    const int id = threadIdx.x; r0[id] = stp[(size_t)id * 8 * NST]; __syncthreads();
    if (id == 0) { unsigned long long mn = ~0ull, sm = 0; unsigned c = 0; for (int i = 0; i < 256; ++i) mn = r0[i] < mn ? r0[i] : mn;
        for (int i = 0; i < 256; ++i) if ((mask >> (i & 7)) & 1) { sm += r0[i] - mn; ++c; }
        unsigned long long q = sm * scale / c; if (q > 200000) q = 200000;
        const unsigned long long t = __builtin_amdgcn_s_memrealtime() + q; while (__builtin_amdgcn_s_memrealtime() < t) __builtin_amdgcn_s_sleep(1); }
}
extern "C" void kernel_launch(void* const* d_in, const int* in_sizes, int n_in, void* d_out, int out_size, void* d_ws, size_t ws_size,
                              hipStream_t stream) {
    (void)in_sizes; (void)n_in; (void)out_size; (void)ws_size;
    (void)hipFuncSetAttribute((const void*)mla_glu_decode, hipFuncAttributeMaxDynamicSharedMemorySize, L_END);
    mla_glu_decode<<<256, 512, L_END, stream>>>((const u16*)d_in[0], (const u16*)d_in[1], (const u16*)d_in[2], (const u16*)d_in[3], (const u16*)d_in[4],
                                               (const u16*)d_in[5], (const u16*)d_in[7], (const u16*)d_in[8], (const u16*)d_in[9], (u16*)d_out,
                                               (unsigned char*)d_ws);
    k_report<<<1, 256, 0, stream>>>((const unsigned long long*)((char*)d_ws + WS_STAMP), 12, 100);
}
```

```cpp
#include <hip/hip_runtime.h>
#include <hip/hip_bf16.h>

typedef unsigned short u16;
typedef unsigned v4u __attribute__((ext_vector_type(4)));
typedef unsigned v2u __attribute__((ext_vector_type(2)));
typedef float f32x4 __attribute__((ext_vector_type(4)));
typedef float f32x2 __attribute__((ext_vector_type(2)));
typedef short s16x4 __attribute__((ext_vector_type(4)));
typedef __bf16 bf16x8 __attribute__((ext_vector_type(8)));
typedef __bf16 bf16x2 __attribute__((ext_vector_type(2)));
#define LAS __attribute__((address_space(3)))
#define GAS __attribute__((address_space(1)))
#define DEVI __device__ __forceinline__

#define PB 120
#define P2 100
#define P3 110
#define P4 55
#define P5 50
#define PG 160
#define PW 60
#define PA 0
#define CODEB 14080
#define EXP 0
#define PH 0
#define H3S 12288
#define XMAP 0
#define VDEF 1
#define HP 1
#define PREH3 0
#define STOPAT 99

constexpr int WS_T2 = 4096;
constexpr int WS_T3 = WS_T2 + 8 * 128 * 128;
constexpr int WS_T5 = WS_T3 + 8 * 64 * 4 * 128;
constexpr int WS_T4 = WS_T5 + 64 * 32 * 128;
constexpr int WS_H1 = WS_T4 + 32 * 16 * 128;
constexpr int WS_H4 = WS_H1 + 32 * 4 * 128 * 16;
constexpr int WS_H2 = WS_H4 + 32 * 4 * 128 * 16;
constexpr int WS_HG = WS_H2 + 4 * 2 * 32 * 128 * 16;
constexpr int WS_H5 = WS_HG + 2 * 16 * 1376 * 16;
constexpr int WS_H3 = WS_H5 + 118 * 512 * 16;
constexpr int WS_END = WS_H3 + 2 * 32 * 4 * H3S;
constexpr int WS_STAMP = 16 << 20;
constexpr int L_Q = 0;
constexpr int L_P = 16384;
constexpr int L_QS = L_P + 4096;
constexpr int L_OW = L_QS + 1024;
constexpr int L_MW = L_OW + 2304;
constexpr int L_MX = L_MW + 64;
constexpr int L_HH = L_MX + 512;
constexpr int L_TAG = L_HH + 64;
constexpr int L_JUNK = L_TAG + 64;
constexpr int L_RED = L_JUNK + 256;
constexpr int L_XN = 28672;
constexpr int L_R0 = 32768, L_R1 = 65536, L_R2 = 98304, L_R3 = 131072;
constexpr int L_END = 163840;
static_assert(L_RED + 1024 <= L_XN && L_XN + 2048 <= L_R0, "lds");
constexpr int SPIN_CAP = 1 << 16;

DEVI unsigned pk(float lo, float hi) { bf16x2 r = {(__bf16)lo, (__bf16)hi}; return __builtin_bit_cast(unsigned, r); }
DEVI float rbf(float f) { return (float)(__bf16)f; }
DEVI float lo16(unsigned w) { return __builtin_bit_cast(float, w << 16); }
DEVI float hi16(unsigned w) { return __builtin_bit_cast(float, w & 0xffff0000u); }
DEVI float asf(unsigned w) { return __builtin_bit_cast(float, w); }
DEVI unsigned asu(float w) { return __builtin_bit_cast(unsigned, w); }
DEVI float mx(float a, float b) { float r; asm("v_max_f32 %0, %1, %2" : "=v"(r) : "v"(a), "v"(b)); return r; }
DEVI float mx3(float a, float b, float c) { float r; asm("v_max3_f32 %0, %1, %2, %3" : "=v"(r) : "v"(a), "v"(b), "v"(c)); return r; }
DEVI f32x4 mfma32(v4u a, v4u b, f32x4 c) { return __builtin_amdgcn_mfma_f32_16x16x32_bf16(__builtin_bit_cast(bf16x8, a), __builtin_bit_cast(bf16x8, b), c, 0, 0, 0); }
DEVI f32x4 mfma16(v2u a, v2u b, f32x4 c) { return __builtin_amdgcn_mfma_f32_16x16x16bf16_1k(__builtin_bit_cast(s16x4, a), __builtin_bit_cast(s16x4, b), c, 0, 0, 0); }
template <class T> DEVI T lrd(unsigned off) { return *(LAS T*)(size_t)off; }
template <class T> DEVI void lwr(unsigned off, T v) { *(LAS T*)(size_t)off = v; }
DEVI v2u trd(unsigned off) { return __builtin_bit_cast(v2u, __builtin_amdgcn_ds_read_tr16_b64_v4i16((LAS s16x4*)(size_t)off)); }
DEVI v4u trd2(unsigned o0, unsigned o1) { v2u a = trd(o0), b = trd(o1); return v4u{a.x, a.y, b.x, b.y}; }
DEVI float sum16(float x) { auto r = __builtin_amdgcn_permlane16_swap(asu(x), asu(x), false, false); return asf(r[0]) + asf(r[1]); }
DEVI float sum32(float x) { auto r = __builtin_amdgcn_permlane32_swap(asu(x), asu(x), false, false); return asf(r[0]) + asf(r[1]); }
DEVI float max16(float x) { auto r = __builtin_amdgcn_permlane16_swap(asu(x), asu(x), false, false); return mx(asf(r[0]), asf(r[1])); }
DEVI float max32(float x) { auto r = __builtin_amdgcn_permlane32_swap(asu(x), asu(x), false, false); return mx(asf(r[0]), asf(r[1])); }
DEVI float odd16(float x) { auto r = __builtin_amdgcn_permlane16_swap(asu(x), asu(x), false, false); return asf(r[1]); }
DEVI float sum16x2(float x, float y) { auto r = __builtin_amdgcn_permlane16_swap(asu(x), asu(y), false, false); return asf(r[0]) + asf(r[1]); }
DEVI float sum32x2(float x, float y) { auto r = __builtin_amdgcn_permlane32_swap(asu(x), asu(y), false, false); return asf(r[0]) + asf(r[1]); }
template <int CTRL> DEVI float dpp(float x) { return asf((unsigned)__builtin_amdgcn_update_dpp(0, (int)asu(x), CTRL, 0xf, 0xf, true)); }
DEVI float wavesum(float x) {
    x += dpp<0xB1>(x); x += dpp<0x4E>(x); x += dpp<0x141>(x); x += dpp<0x140>(x);
    return sum32(sum16(x));
}
DEVI float dot2one(unsigned p, float c) { const unsigned one = 0x3f803f80u; return __builtin_amdgcn_fdot2_f32_bf16(__builtin_bit_cast(bf16x2, p), __builtin_bit_cast(bf16x2, one), c, false); }
#define STR_(x) #x
#define STR(x) STR_(x)
#define WAITVM(n) asm volatile("s_waitcnt vmcnt(" STR(n) ")" ::: "memory")
#define PIN(x) asm volatile("" : "+v"(x))
#define MFMA_SETTLE(x) asm volatile("s_nop 7" : "+v"(x))
#define UNDEF(x) asm volatile("" : "=v"(x))
#define FENCE() __builtin_amdgcn_sched_barrier(0)
#define SPIN for (int it_ = SPIN_CAP; it_ > 0; --it_)
DEVI void bar() { asm volatile("s_waitcnt lgkmcnt(0)\n\ts_barrier" ::: "memory"); }
DEVI void bar0() { asm volatile("s_barrier" ::: "memory"); }
#define USE_NT 0
template <bool NT = false> DEVI void glds(unsigned voff, const void* sbase, unsigned lds) {
    unsigned keep;
    if (NT && USE_NT) asm volatile("s_mov_b32 %0, m0\n\ts_mov_b32 m0, %3\n\ts_nop 0\n\tglobal_load_lds_dwordx4 %1, %2 nt\n\ts_mov_b32 m0, %0" : "=&s"(keep) : "v"(voff), "s"(sbase), "s"(lds) : "memory");
    else asm volatile("s_mov_b32 %0, m0\n\ts_mov_b32 m0, %3\n\ts_nop 0\n\tglobal_load_lds_dwordx4 %1, %2\n\ts_mov_b32 m0, %0" : "=&s"(keep) : "v"(voff), "s"(sbase), "s"(lds) : "memory");
}
typedef __amdgpu_buffer_rsrc_t rsrc_t;
constexpr unsigned OOB = 0x80000000u;
DEVI void stx(rsrc_t rs, unsigned off, v4u v) { __builtin_amdgcn_raw_buffer_store_b128(v, rs, off, 0, 16); }
DEVI bool none(bool bad) { asm volatile("" ::: "memory"); return __builtin_amdgcn_ballot_w64(bad) == 0ull; }
DEVI unsigned tagx(v4u a, unsigned tag) { unsigned r = (a[1] ^ tag) | (a[3] ^ tag); asm("" : "+v"(r)); return r; }
DEVI unsigned tagy(unsigned a, unsigned b, unsigned tag) { unsigned r = (a ^ tag) | (b ^ tag); asm("" : "+v"(r)); return r; }
DEVI unsigned tagx4(const v4u (&a)[4], unsigned tag) { return (tagx(a[0], tag) | tagx(a[1], tag)) | (tagx(a[2], tag) | tagx(a[3], tag)); }
DEVI void get1(v4u& a, unsigned off, v4u rq) { asm volatile("buffer_load_dwordx4 %0, %1, %2, 0 offen sc1\n\ts_waitcnt vmcnt(0)" : "+&v"(a) : "v"(off), "s"(rq) : "memory"); }
DEVI void get2(v4u& a, v4u& b, unsigned oa, unsigned ob, v4u rq) {
    asm volatile("buffer_load_dwordx4 %0, %2, %4, 0 offen sc1\n\tbuffer_load_dwordx4 %1, %3, %4, 0 offen sc1\n\ts_waitcnt vmcnt(0)" : "+&v"(a), "+&v"(b) : "v"(oa), "v"(ob), "s"(rq) : "memory");
}
template <int ST> DEVI void get4(v4u (&a)[4], unsigned off, v4u rq) {
    asm volatile("buffer_load_dwordx4 %0, %4, %5, 0 offen sc1\n\tbuffer_load_dwordx4 %1, %4, %5, 0 offen offset:%6 sc1\n\t"
                 "buffer_load_dwordx4 %2, %4, %5, 0 offen offset:%7 sc1\n\tbuffer_load_dwordx4 %3, %4, %5, 0 offen offset:%8 sc1\n\ts_waitcnt vmcnt(0)"
                 : "+&v"(a[0]), "+&v"(a[1]), "+&v"(a[2]), "+&v"(a[3]) : "v"(off), "s"(rq), "i"(ST), "i"(2 * ST), "i"(3 * ST) : "memory");
}
DEVI void get4s(v4u (&a)[4], unsigned off, v4u rq, unsigned s1, unsigned s2, unsigned s3) {
    asm volatile("buffer_load_dwordx4 %0, %4, %5, 0 offen sc1\n\tbuffer_load_dwordx4 %1, %4, %5, %6 offen sc1\n\t"
                 "buffer_load_dwordx4 %2, %4, %5, %7 offen sc1\n\tbuffer_load_dwordx4 %3, %4, %5, %8 offen sc1\n\ts_waitcnt vmcnt(0)"
                 : "+&v"(a[0]), "+&v"(a[1]), "+&v"(a[2]), "+&v"(a[3]) : "v"(off), "s"(rq), "s"(s1), "s"(s2), "s"(s3) : "memory");
}
DEVI unsigned now() { return (unsigned)__builtin_amdgcn_s_memrealtime(); }
DEVI void hint_put(rsrc_t rs, unsigned off, unsigned tag, unsigned t) { __builtin_amdgcn_raw_buffer_store_b64(v2u{tag, t}, rs, off, 0, 16); }
DEVI void hint_put(rsrc_t rs, unsigned off, unsigned tag) { hint_put(rs, off, tag, now()); }
DEVI v2u hint_get(v4u rq, unsigned off) { v2u v; asm volatile("buffer_load_dwordx2 %0, %1, %2, 0 offen sc1\n\ts_waitcnt vmcnt(0)" : "=&v"(v) : "v"(off), "s"(rq) : "memory"); return v; }
DEVI void pace(unsigned t, unsigned lead) {
#pragma nounroll
    for (int it = 256; it > 0; --it) { if (none(now() - t < lead)) break; }
}
DEVI unsigned hint_wait(v4u rq, unsigned off, unsigned tag) {
    v2u v;
#pragma nounroll
    SPIN { v = hint_get(rq, off); if (none(v[0] != tag)) break; __builtin_amdgcn_s_sleep(1); }
    return v[1];
}
DEVI unsigned mktag(unsigned ep) { return ep + 1u; }

#define STAMP(i)

#define CNT0(i)
#define CNT(i)
#define STAMPC(i)
#define STAMP2(i)
DEVI void frags128(unsigned a0, unsigned a1, v4u (&f)[4]) {
#pragma unroll
    for (int s = 0; s < 4; ++s) f[s] = trd2(a0 + s * 8192, a1 + s * 8192);
}
DEVI f32x4 gemv4(const v4u (&a)[4], const v4u (&b)[4]) {
    f32x4 c = {0.f, 0.f, 0.f, 0.f};
#pragma unroll
    for (int s = 0; s < 4; ++s) c = mfma32(a[s], b[s], c);
    return c;
}
DEVI void layernorm(v4u xv, int w, int l) {
    float v[8];
#pragma unroll
    for (int j = 0; j < 4; ++j) { v[2 * j] = lo16(xv[j]); v[2 * j + 1] = hi16(xv[j]); }
    const float mean = wavesum(((v[0] + v[1]) + (v[2] + v[3])) + ((v[4] + v[5]) + (v[6] + v[7]))) * (1.f / 512.f);
    float sq = 0.f;
#pragma unroll
    for (int j = 0; j < 8; ++j) { const float d = v[j] - mean; sq += d * d; }
    const float var = wavesum(sq) * (1.f / 512.f);
    const float mb = rbf(mean), ri = __builtin_amdgcn_rcpf(rbf(__builtin_amdgcn_sqrtf(rbf(rbf(var) + 1e-5f))));
    v4u o;
#pragma unroll
    for (int j = 0; j < 4; ++j) o[j] = pk(rbf(v[2 * j] - mb) * ri, rbf(v[2 * j + 1] - mb) * ri);
    lwr<v4u>(L_XN + w * 1024 + l * 16, o);
}

__global__ void __launch_bounds__(512, 2)
mla_glu_decode(const u16* __restrict__ x, const u16* __restrict__ ckv, const u16* __restrict__ wq, const u16* __restrict__ wuk,
               const u16* __restrict__ wuv, const u16* __restrict__ wo, const u16* __restrict__ wg, const u16* __restrict__ wu,
               const u16* __restrict__ wd, u16* __restrict__ out, unsigned char* __restrict__ ws) {
    STAMP(0);
    const int tid = threadIdx.x, l = tid & 63, w = __builtin_amdgcn_readfirstlane(tid >> 6);
    const int id = blockIdx.x, ai = (id & 3) * 32 + (id >> 3);
    const int xc = XMAP ? ai >> 4 : id & 7, yy = XMAP ? ai & 15 : (id >> 3) & 15, y = XMAP ? (id & 4) * 4 : id >> 3;
    const int un = yy * 8 + xc;
    const unsigned g = l >> 4, l15 = l & 15;
    const unsigned t_in = now();
    unsigned cp0 = 0;
    if (w == 7) {
        unsigned long long pc;
        asm volatile("s_getpc_b64 %0" : "=s"(pc));
        pc &= ~255ull;
        asm volatile("global_load_dword %0, %1, %2" : "=&v"(cp0) : "v"(min(((id >> 3) + 32 * (l & 3)) * 128, CODEB - 128)), "s"(pc) : "memory");
    }
    asm volatile("" :: "s"(x), "s"(ckv), "s"(wq), "s"(wuk), "s"(wuv), "s"(wo), "s"(wg), "s"(wu), "s"(wd), "s"(out), "s"(ws));
    const rsrc_t rs = __builtin_amdgcn_make_buffer_rsrc(ws, 0, WS_END, 0x00020000);
    const v4u rsq = {(unsigned)(size_t)ws, (unsigned)((size_t)ws >> 32) & 0xffffu, (unsigned)WS_END, 0x00020000u};
    const f32x4 ZF = {0.f, 0.f, 0.f, 0.f};

    if (__builtin_expect(y < 16, 1)) {
        const int h = 4 * xc + (yy >> 2), cq = yy & 3;
        v4u xv; unsigned ep;
        if (w < 2) {
            asm volatile("global_load_dwordx4 %0, %1, %2" : "=v"(xv) : "v"(w * 1024 + l * 16), "s"(x) : "memory");
            asm volatile("global_load_dword %0, %1, %2 sc1" : "=v"(ep) : "v"(0), "s"(ws) : "memory");
        }
        const unsigned vb = (128 * cq + 16 * w + g) * 8192 + h * 256;
        unsigned sw[4];
#pragma unroll
        for (int i = 0; i < 4; ++i) sw[i] = (l15 ^ ((g << 2) | i)) << 4;
#pragma unroll
        for (int i = 0; i < 4; ++i) glds<true>(vb + i * 32768 + sw[i], wq, L_R0 + (16 * w + 4 * i) * 256);
        bar0();
#pragma unroll
        for (int i = 0; i < 4; ++i) glds<true>(vb + i * 32768 + ((l15 ^ (4 * i + g)) << 4), wuk, L_R1 + (16 * w + 4 * i) * 256);
        STAMP(1);
        if (w < 2) {
            asm volatile("s_waitcnt vmcnt(8)" : "+v"(xv), "+v"(ep) :: "memory");
            layernorm(xv, w, l);
            lwr<v4u>(L_TAG, v4u{mktag(ep), 0u, 0u, 0u});
        }
        bar();
        const unsigned tag = lrd<unsigned>(L_TAG);
        unsigned o_p1 = l < 16 ? WS_H1 + ((h * 4 + cq) * 128 + 16 * w + l) * 16 : OOB;
        unsigned o_g1 = WS_H1 + ((h * 4 + g) * 128 + 16 * w + l15) * 16;
        unsigned o_h2 = WS_H2 + ((((w & 3) * 2 + (l >> 5)) * 32 + h) * 128 + 32 * cq + (l & 31)) * 16;
        unsigned a_2w = l15 < 2 ? L_RED + l15 * 256 + (4 * w + g) * 8 : L_JUNK + (l & 31) * 8, a_2r = L_RED + l * 8;
        PIN(a_2w); PIN(a_2r);
        unsigned o_t2 = l < 8 ? WS_T2 + (l * 128 + h * 4 + cq) * 128 : OOB;
        unsigned a0, a1;
        {
            const unsigned q = l15 >> 2, p = l & 3, ch = 2 * w + (p >> 1);
            a0 = L_R0 + (8 * g + q) * 256 + 16 * (ch ^ ((q << 2) | ((2 * g) & 3))) + 8 * (p & 1);
            a1 = L_R0 + (8 * g + q + 4) * 256 + 16 * (ch ^ ((q << 2) | ((2 * g + 1) & 3))) + 8 * (p & 1);
        }
        unsigned a_qw = L_QS + (g & 1) * 272 + (16 * w + l15) * 2;
        unsigned a_qr = L_QS + (l & 1) * 272 + 16 * g;
        unsigned a_uk[4], o_gu[3], a_cnt = l == 0 ? L_TAG + 8 : L_JUNK + l * 4, one = 1u;
        v4u xf[4];
#pragma unroll
        for (int s = 0; s < 4; ++s) {
            xf[s] = lrd<v4u>(L_XN + (l & 1) * 1024 + (128 * cq + 32 * s + 8 * g) * 2);
            a_uk[s] = L_R1 + (16 * w + l15) * 256 + (((4 * s + g) ^ l15) << 4);
            PIN(xf[s]); PIN(a_uk[s]);
        }
        const int mat = un >> 6, mp = (un >> 2) & 15, fp = un & 3;
#pragma unroll
        for (int i = 0; i < 3; ++i) {
            const unsigned pc = min(8 * i + w, 21), idx = min(64 * pc + l, 1375u), row = idx / 43u, ch = idx - row * 43u;
            o_gu[i] = (32 * mp + row) * 2752 + fp * 688 + ch * 16; PIN(o_gu[i]);
        }
        v4u pb = {0u, tag, 0u, tag}, ga;
        PIN(a0); PIN(a1); PIN(o_p1); PIN(o_g1); PIN(o_h2); PIN(o_t2); PIN(a_qw); PIN(a_qr); PIN(pb); PIN(a_cnt); PIN(one);
        asm volatile("s_waitcnt vmcnt(4)" : "+v"(cp0) :: "memory");
        bar0();
        STAMP(2);
        {
            v4u f[4]; frags128(a0, a1, f);
            FENCE();
            const f32x4 acc = gemv4(xf, f);
            pb[0] = asu(acc[0]); pb[2] = asu(acc[1]);
            stx(rs, o_p1, pb);
        }
        STAMP(3);
        if (STOPAT <= 3) { WAITVM(0); STAMP(14); goto done; }
        __builtin_amdgcn_s_sleep(10);
        WAITVM(3);
        v4u fu[4];
        {
            unsigned arr;
            asm volatile("buffer_load_dwordx4 %1, %4, %5, 0 offen sc1\n\tds_add_rtn_u32 %0, %2, %3\n\ts_waitcnt lgkmcnt(0)" : "=&v"(arr), "=&v"(ga) : "v"(a_cnt), "v"(one), "v"(o_g1), "s"(rsq) : "memory");
            hint_put(rs, __builtin_amdgcn_readfirstlane(arr) == 7u ? o_t2 : OOB, tag);
            asm volatile("s_waitcnt vmcnt(1)" : "+v"(ga) :: "memory");
            STAMP(14);
#pragma unroll
            for (int s = 0; s < 4; ++s) fu[s] = lrd<v4u>(a_uk[s]);
            if (__builtin_expect(!none(tagx(ga, tag) != 0u), 0)) {
#pragma nounroll
                SPIN { __builtin_amdgcn_s_sleep(4); get1(ga, o_g1, rsq); if (none(tagx(ga, tag) != 0u)) break; }
            }
            lwr<u16>(a_qw, (u16)pk(sum32(sum16x2(asf(ga[0]), asf(ga[2]))), 0.f));
        }
        STAMP(4);
        bar();
        {
            v4u qf[4];
#pragma unroll
            for (int s = 0; s < 4; ++s) qf[s] = lrd<v4u>(a_qr + 64 * s);
            FENCE();
            const f32x4 acc = gemv4(fu, qf);
            constexpr float SC = 0.08838834764831845f * 1.4426950408889634f;
            lwr<v2u>(a_2w, v2u{pk(acc[0] * SC, acc[1] * SC), pk(acc[2] * SC, acc[3] * SC)});
            bar();
            if (w < 4) {
                const v2u t = lrd<v2u>(a_2r);
                pb[0] = t[0]; pb[2] = t[1];
                stx(rs, o_h2, pb);
            }
        }
        STAMP(5);
        if (STOPAT <= 5) goto done;
        const int r = xc * 8 + (yy >> 1);
        unsigned o_t3 = WS_T3 + ((xc * 4 + cq) * 64 + 8 * w + (l & 7)) * 128, o_t5 = WS_T5 + (r * 32 + (l & 31)) * 128, j0, j1;
        asm volatile("buffer_load_dword %0, %2, %4, 0 offen sc1\n\tbuffer_load_dword %1, %3, %4, 0 offen sc1"
                     : "=&v"(j0), "=&v"(j1) : "v"(o_t3), "v"((w == 0 && !(yy & 1)) ? o_t5 : o_t3), "s"(rsq) : "memory");
#pragma unroll
        for (int i = 0; i < 3; ++i) glds(o_gu[i], mat ? wu : wg, L_R2 + min(8 * i + w, 21) * 1024);
#define V_ISSUE(i) glds<true>((EXP & 4) ? 0 : vb + (i) * 32768 + sw[i], wuv, L_R1 + (16 * w + 4 * (i)) * 256)
        if (!VDEF) { V_ISSUE(0); V_ISSUE(1); }
        {
            const v4u xg = lrd<v4u>(L_XN + (l & 1) * 1024 + (32 * mp + 8 * g) * 2);
            const unsigned ag = L_R2 + (8 * g + (l15 >> 2)) * 688 + (l & 3) * 8;
            const unsigned f = 16 * (8 * g + w) + l15;
            const unsigned o_hg = (g < 3 && f < 344) ? WS_HG + ((mat * 16 + mp) * 1376 + 344 * fp + f) * 16 : OOB;
            if (VDEF) asm volatile("s_waitcnt vmcnt(0)" : "+v"(j0), "+v"(j1) :: "memory"); else asm volatile("s_waitcnt vmcnt(2)" : "+v"(j0), "+v"(j1) :: "memory");
            bar();
            f32x4 o[3];
#pragma unroll
            for (int t = 0; t < 3; ++t) {
                const unsigned tl = min(8 * t + w, 21);
                o[t] = mfma32(xg, trd2(ag + tl * 32, ag + tl * 32 + 4 * 688), ZF);
            }
            const float o0 = g == 0 ? o[0][0] : g == 1 ? o[1][0] : o[2][0], o1 = g == 0 ? o[0][1] : g == 1 ? o[1][1] : o[2][1];
            stx(rs, o_hg, v4u{asu(o0), tag, asu(o1), tag});
        }
        if (VDEF) {
            pace(hint_wait(rsq, WS_T2 + (xc * 128 + (l & 7) * 16 + 5) * 128, tag), PA);
            V_ISSUE(0); V_ISSUE(1);
        }
        V_ISSUE(2); V_ISSUE(3);
        STAMP(6);
        const unsigned u = l15 >= 12 ? 8 + l15 : (g & 1) ? 12 + (l15 & 7) : l15;
        const bool act = (g & 1) ? (l15 & 12) != 8 : l15 < 12;
        unsigned o_h3 = WS_H3 + (((w >> 2) * 32 + h) * 4 + cq) * H3S + (8 * (w & 3) + (g >> 1)) * 384 + u * 16;
        unsigned o_t4 = l < 4 ? WS_T4 + ((h * 4 + l) * 4 + cq) * 128 : OOB;
        unsigned o_p4 = l < 16 ? WS_H4 + ((h * 4 + cq) * 128 + 16 * w + l) * 16 : OOB;
        unsigned a_ow, a_ow2, a_ms;
        {
            const unsigned n = 6 * (u >> 2) + 2 * (g >> 1), n2 = 6 * (u >> 2) + 4, gg = u & 3;
            a_ow = (act && n < 32) ? L_OW + w * 272 + (32 * gg + 4 * ((n >> 2) ^ (gg >> 1)) + (n & 3)) * 2 : L_JUNK + l * 4;
            a_ow2 = (act && n2 < 32 && l < 32) ? L_OW + w * 272 + (32 * gg + 4 * ((n2 >> 2) ^ (gg >> 1)) + (n2 & 3)) * 2 : L_JUNK + l * 4;
            a_ms = (l & 31) == 12 ? L_MW + (g >> 1) * 32 + w * 4 : L_JUNK + l * 4;
        }
        unsigned a_or = L_OW + (l & 7) * 272 + 16 * g, a_mw = L_MW + (g & 1) * 16;
        v4u a[4];
        UNDEF(a[0]); UNDEF(a[1]); UNDEF(a[2]); UNDEF(a[3]);
        a_cnt += l == 0 ? 4 : 0;
        PIN(o_h3); PIN(o_t3); PIN(o_t4); PIN(o_p4); PIN(a_ow); PIN(a_ow2); PIN(a_or); PIN(a_mw); PIN(a_ms); PIN(a_cnt);
        WAITVM(0);
        bar();
        v4u fv[4];
        frags128(a0 + (L_R1 - L_R0), a1 + (L_R1 - L_R0), fv);
        PIN(fv[0]); PIN(fv[1]); PIN(fv[2]); PIN(fv[3]);
        STAMP(7);
        if (STOPAT <= 7) goto done;
        CNT0(2); CNT0(3);
        const unsigned t3 = hint_wait(rsq, o_t3, tag);
        STAMPC(1);
        pace(t3, P3);
        STAMP(15);
        {
#pragma nounroll
            SPIN { CNT(2); get4<768>(a, o_h3, rsq); if (none((tagy(a[0][3], a[1][3], tag) | tagy(a[2][3], a[3][3], tag)) != 0u)) break; __builtin_amdgcn_s_sleep(4); }
            STAMP(8);
            unsigned arr;
            unsigned long long tm;
            asm volatile("ds_add_rtn_u32 %0, %2, %3\n\ts_memrealtime %1" : "=&v"(arr), "=&s"(tm) : "v"(a_cnt), "v"(one) : "memory");
            float m[4], al[4];
#pragma unroll
            for (int k = 0; k < 4; ++k) m[k] = dpp<0x15c>(asf(a[k][1]));
            const float M = max32(mx(mx3(m[0], m[1], m[2]), m[3]));
#pragma unroll
            for (int k = 0; k < 4; ++k) al[k] = __builtin_amdgcn_exp2f(m[k] - M);
            FENCE();
            asm volatile("s_waitcnt lgkmcnt(0)" : "+v"(arr), "+s"(tm) :: "memory");
            hint_put(rs, __builtin_amdgcn_readfirstlane(arr) == 7u ? o_t4 : OOB, tag, (unsigned)tm);
            f32x2 c01, c23, c45;
            float Ls;
#pragma unroll
            for (int k = 0; k < 4; ++k) {
                const f32x2 aa = {al[k], al[k]}, v01 = {lo16(a[k][0]), hi16(a[k][0])}, v23 = {lo16(a[k][1]), hi16(a[k][1])}, v45 = {lo16(a[k][2]), hi16(a[k][2])};
                if (k == 0) { c01 = aa * v01; c23 = aa * v23; c45 = aa * v45; Ls = al[0] * asf(a[0][2]); }
                else { c01 = __builtin_elementwise_fma(aa, v01, c01); c23 = __builtin_elementwise_fma(aa, v23, c23); c45 = __builtin_elementwise_fma(aa, v45, c45); Ls = __builtin_fmaf(al[k], asf(a[k][2]), Ls); }
            }
            const float acc[6] = {c01[0], c01[1], c23[0], c23[1], c45[0], c45[1]};
            const float v4 = sum32x2(acc[4], Ls);
            lwr<unsigned>(a_ow, pk(sum32x2(acc[0], acc[2]), sum32x2(acc[1], acc[3])));
            lwr<unsigned>(a_ow2, pk(v4, sum32(acc[5])));
            lwr<float>(a_ms, l < 32 ? M : v4);
        }
        bar();
        {
            v4u of[4];
#pragma unroll
            for (int s = 0; s < 4; ++s) of[s] = lrd<v4u>(a_or + 64 * s);
            const f32x4 mw = lrd<f32x4>(a_mw), lw = lrd<f32x4>(a_mw + 32);
            FENCE();
            const f32x4 c = gemv4(of, fv);
            const float M = mx(mx3(mw[0], mw[1], mw[2]), mw[3]);
            float be[4];
#pragma unroll
            for (int r = 0; r < 4; ++r) be[r] = __builtin_amdgcn_exp2f(mw[r] - M);
            const float rd = __builtin_amdgcn_rcpf((be[0] * lw[0] + be[1] * lw[1]) + (be[2] * lw[2] + be[3] * lw[3]));
            const float v = ((be[0] * c[0] + be[1] * c[1]) + (be[2] * c[2] + be[3] * c[3])) * rd;
            auto sv = __builtin_amdgcn_permlane16_swap(asu(v), asu(v), false, false);
            pb[0] = sv[0]; pb[2] = sv[1];
            stx(rs, o_p4, pb);
        }
        STAMP(9);
        if ((yy & 1) == 0) {
            const unsigned o1 = WS_H5 + ((32 + (tid >> 3)) * 512 + 8 * r + (tid & 7)) * 16;
            const bool two = tid < 176;
            const unsigned o2 = two ? o1 + 64 * 512 * 16 : o1;
            pace(t3, PG);
#pragma nounroll
            SPIN { get2(a[0], a[1], o1, o2, rsq); if (none((tagx(a[0], tag) | tagx(a[1], tag)) != 0u)) break; __builtin_amdgcn_s_sleep(8); }
            {
                float t = sum32(sum16x2(asf(a[0][0]) + (two ? asf(a[1][0]) : 0.f), asf(a[0][2]) + (two ? asf(a[1][2]) : 0.f)));
                t += dpp<0x128>(t);
                lwr<float>(L_RED + ((g & 1) * 8 + (l & 7)) * 32 + w * 4, t);
            }
            bar();
            STAMP(10);
            if (w == 0) {
                const f32x4 t2 = lrd<f32x4>(L_RED + ((g & 1) * 8 + (l & 7)) * 32), t4 = lrd<f32x4>(L_RED + ((g & 1) * 8 + (l & 7)) * 32 + 16);
                float glu = rbf(((t2[0] + t2[1]) + (t2[2] + t2[3])) + ((t4[0] + t4[1]) + (t4[2] + t4[3])));
                float xin = lo16(x[(g & 1) * 512 + 8 * r + (l & 7)]);
                unsigned o0 = WS_H5 + ((l >> 3) * 512 + 8 * r + (l & 7)) * 16;
                unsigned o_out = ((g & 1) * 512 + 8 * r + (l & 7)) * 2;
                PIN(glu); PIN(xin); PIN(o0); PIN(o_out);
                pace(hint_wait(rsq, o_t5, tag), P5);
                STAMP(11);
#pragma nounroll
                SPIN { CNT(3); get4s(a, o0, rsq, 65536, 131072, 196608); if (none(tagx4(a, tag) != 0u)) break; }
                STAMP(12);
                float t = sum32(sum16x2((asf(a[0][0]) + asf(a[1][0])) + (asf(a[2][0]) + asf(a[3][0])), (asf(a[0][2]) + asf(a[1][2])) + (asf(a[2][2]) + asf(a[3][2]))));
                t += dpp<0x128>(t);
                if (l < 32 && l15 < 8) __hip_atomic_store((GAS u16*)((GAS unsigned char*)out + o_out), (u16)pk(rbf(xin + rbf(t)) + glu, 0.f), __ATOMIC_RELAXED, __HIP_MEMORY_SCOPE_AGENT);
                if (r == 0 && l == 0) __hip_atomic_store((GAS unsigned*)ws, tag, __ATOMIC_RELAXED, __HIP_MEMORY_SCOPE_AGENT);
            }
        }
    } else {
        const int pr = xc * 8 + (yy >> 1), hg = yy & 1, b = pr >> 5, sp = pr & 31;
        unsigned ep, j0;
        unsigned o_t2 = WS_T2 + (xc * 128 + (16 * hg + 2 * w) * 4 + (l & 7)) * 128;
        asm volatile("global_load_dword %0, %1, %2 sc1" : "=v"(ep) : "v"(0), "s"(ws) : "memory");
        asm volatile("buffer_load_dword %0, %1, %2, 0 offen sc1" : "=v"(j0) : "v"(o_t2), "s"(rsq) : "memory");
        unsigned qoff = WS_H2 + ((((xc & 3) * 2 + b) * 32 + 16 * hg + 2 * w) * 128 + l) * 16;
        unsigned a_q0 = L_Q + 2 * w * 1024 + (((l >> 1) ^ (2 * w)) << 4) + (l & 1) * 8, a_q1 = L_Q + (2 * w + 1) * 1024 + (((l >> 1) ^ (2 * w + 1)) << 4) + (l & 1) * 8;
        unsigned o_kv[8];
#pragma unroll
        for (int i = 0; i < 8; ++i) { o_kv[i] = (pr * 128 + 64 * hg + 8 * w + i) * 1024 + ((l ^ (8 * (w & 1) + i)) << 4); PIN(o_kv[i]); }
        PIN(qoff); PIN(a_q0); PIN(a_q1);
        asm volatile("s_waitcnt vmcnt(0)" : "+v"(ep), "+v"(j0), "+v"(cp0) :: "memory");
        const unsigned tag = mktag(ep);
        if (PREH3) {
            const unsigned o = WS_H3 + ((b * 32 + 16 * hg + 8 * (w >> 2) + (l >> 3)) * 4 + (w & 3)) * H3S + sp * 384 + (l & 7) * 16;
#pragma unroll
            for (int n = 0; n < 3; ++n) stx(rs, o + n * 128, v4u{0u, 0u, 0u, ~tag});
            WAITVM(0);
            bar0();
        }
        pace(t_in, PB);
#pragma unroll
        for (int i = 0; i < 8; ++i) glds(o_kv[i], ckv, L_R0 + (8 * w + i) * 1024);
        STAMP(1);
        bar0();
#pragma unroll
        for (int i = 0; i < 8; ++i) glds(o_kv[i] ^ 65536, ckv, L_R0 + (64 + 8 * w + i) * 1024);
        WAITVM(8);
        bar0();
        STAMP(2);
#define Q_ISSUE() \
            FENCE(); \
            WAITVM(2); \
            const v2u hv = hint_get(rsq, o_t2);        \
            STAMP(14); \
            pace(none(hv[0] != tag) ? hv[1] : hint_wait(rsq, o_t2, tag), P2); \
            STAMP(15); \
            v4u qa[4]; \
            asm volatile("buffer_load_dwordx4 %0, %4, %5, 0 offen sc1\n\tbuffer_load_dwordx4 %1, %4, %5, 0 offen offset:1024 sc1\n\t" \
                         "buffer_load_dwordx4 %2, %4, %5, 0 offen offset:2048 sc1\n\tbuffer_load_dwordx4 %3, %4, %5, 0 offen offset:3072 sc1" \
                         : "=&v"(qa[0]), "=&v"(qa[1]), "=&v"(qa[2]), "=&v"(qa[3]) : "v"(qoff), "s"(rsq) : "memory"); \
            bar0()
#define Q_LAND(n) do { \
            asm volatile("s_waitcnt vmcnt(" STR(n) ")" : "+v"(qa[0]), "+v"(qa[1]), "+v"(qa[2]), "+v"(qa[3]) :: "memory"); \
            if (__builtin_expect(!none(tagx4(qa, tag) != 0u), 0)) { _Pragma("nounroll") SPIN { __builtin_amdgcn_s_sleep(4); get4<1024>(qa, qoff, rsq); if (none(tagx4(qa, tag) != 0u)) break; } } \
            asm volatile("ds_write2_b32 %0, %2, %3 offset1:1\n\tds_write2_b32 %0, %4, %5 offset0:128 offset1:129\n\tds_write2_b32 %1, %6, %7 offset1:1\n\tds_write2_b32 %1, %8, %9 offset0:128 offset1:129" \
                         :: "v"(a_q0), "v"(a_q1), "v"(qa[0][0]), "v"(qa[0][2]), "v"(qa[1][0]), "v"(qa[1][2]), "v"(qa[2][0]), "v"(qa[2][2]), "v"(qa[3][0]), "v"(qa[3][2]) : "memory"); \
            STAMP(3); bar();   } while (0)
        unsigned t5 = 0;
        const int h = 4 * xc + (yy >> 2), cq = yy & 3;
        unsigned o_t4 = WS_T4 + ((h * 4 + cq) * 4 + (l & 3)) * 128;
        unsigned o_hg = (EXP & 2) ? 0 : WS_HG + ((4 * (w & 3) + g) * 1376 + 16 * min(un, 85) + l15) * 16;
        if (w >= 4) {
            unsigned a_r[4];
#pragma unroll
            for (int j = 0; j < 4; ++j) a_r[j] = l15 * 1024 + (((4 * j + g) ^ l15) << 4);
            const unsigned kb = L_R0 + (w - 4) * 16384;
            v4u cr[32], qf[16];
#pragma unroll
            for (int s = 0; s < 16; ++s) cr[s] = lrd<v4u>(a_r[s & 3] + kb + (s >> 2) * 256);
            unsigned a_mxw = L_MX + l15 * 16 + (w - 4) * 4, a_mxr = L_MX + l15 * 16;
            unsigned a_pw = L_P + l15 * 256 + (((2 * (w - 4) + (g >> 1)) ^ l15) << 4) + (g & 1) * 8;
            __builtin_amdgcn_s_setprio(1);
            PIN(a_mxw); PIN(a_mxr); PIN(a_pw); PIN(o_hg);
            Q_ISSUE();
#pragma unroll
            for (int s = 0; s < 16; ++s) cr[16 + s] = lrd<v4u>(a_r[s & 3] + kb + 65536 + (s >> 2) * 256);
            FENCE();
            Q_LAND(0);
            STAMP(4);
#pragma unroll
            for (int s = 0; s < 16; ++s) qf[s] = lrd<v4u>(a_r[s & 3] + L_Q + (s >> 2) * 256);
            FENCE();
            f32x4 s1 = ZF, s2 = ZF;
#pragma unroll
            for (int s = 0; s < 16; ++s) { s1 = mfma32(cr[s], qf[s], s1); s2 = mfma32(cr[16 + s], qf[s], s2); }
            asm volatile("s_nop 7" : "+v"(s1), "+v"(s2));
            lwr<float>(a_mxw, max32(max16(mx(mx3(mx3(s1[0], s1[1], s1[2]), s1[3], s2[0]), mx3(s2[1], s2[2], s2[3])))));
            bar();
            STAMP(12);
            const f32x4 m0 = lrd<f32x4>(a_mxr);
            const float mr = mx(mx3(m0[0], m0[1], m0[2]), m0[3]);
            v2u p1 = {pk(__builtin_amdgcn_exp2f(s1[0] - mr), __builtin_amdgcn_exp2f(s1[1] - mr)), pk(__builtin_amdgcn_exp2f(s1[2] - mr), __builtin_amdgcn_exp2f(s1[3] - mr))};
            lwr<v2u>(a_pw, p1);
            v2u p2 = {pk(__builtin_amdgcn_exp2f(s2[0] - mr), __builtin_amdgcn_exp2f(s2[1] - mr)), pk(__builtin_amdgcn_exp2f(s2[2] - mr), __builtin_amdgcn_exp2f(s2[3] - mr))};
            lwr<v2u>(a_pw ^ 128, p2);
            bar();
            STAMP(5);
            PIN(p1); PIN(p2);
            {
                const unsigned q0 = p1[0], q1 = p1[1], q2 = p2[0], q3 = p2[1];
                lwr<float>(a_mxw + 256, sum32(sum16(((lo16(q0) + hi16(q0)) + (lo16(q1) + hi16(q1))) + ((lo16(q2) + hi16(q2)) + (lo16(q3) + hi16(q3))))));
            }
            bar();
            __builtin_amdgcn_s_setprio(0);
            t5 = now();
            if (PH) pace(t5, PH);
            v4u ga, gb;
            asm volatile("buffer_load_dwordx4 %0, %3, %6, 0 offen sc1\n\tbuffer_load_dwordx4 %1, %4, %6, 0 offen sc1\n\tbuffer_load_dword %2, %5, %6, 0 offen sc1\n\ts_waitcnt vmcnt(0)"
                         : "=&v"(ga), "=&v"(gb), "=&v"(j0), "+v"(o_hg) : "v"(o_hg + 16 * 1376 * 16), "v"(o_t4), "s"(rsq) : "memory");
            if (un < 86) {
                if (!(EXP & 2) && __builtin_expect(!none((tagx(ga, tag) | tagx(gb, tag)) != 0u), 0)) {
#pragma nounroll
                    SPIN { __builtin_amdgcn_s_sleep(16); get2(ga, gb, o_hg, o_hg + 16 * 1376 * 16, rsq); if (none((tagx(ga, tag) | tagx(gb, tag)) != 0u)) break; }
                }
                lwr<float>(L_RED + (l & 31) * 32 + (w - 4) * 4, sum32(sum16x2(asf(ga[0]), asf(ga[2]))));
                lwr<float>(L_RED + (l & 31) * 32 + w * 4, sum32(sum16x2(asf(gb[0]), asf(gb[2]))));
            }
            pace(t5, PW);
#pragma unroll
            for (int i = 0; i < 8; ++i) {
                const unsigned ch = l15 ^ ((g << 2) | (i & 3));
                glds((h * 128 + 32 * (w - 4) + 4 * i + g) * 1024 + cq * 128 + (ch >> 3) * 512 + (ch & 7) * 16, wo, L_R1 + (32 * (w - 4) + 4 * i) * 256);
            }
        } else {
            unsigned a_t[2][4];
            {
                const unsigned q = l15 >> 2, p = l & 3;
#pragma unroll
                for (int j = 0; j < 8; ++j) {
                    const unsigned r0 = 8 * g + 4 * (j >> 2) + q, ch = 16 * w + 4 * p + (j & 3);
                    a_t[j >> 2][j & 3] = L_R0 + r0 * 1024 + ((ch ^ (r0 & 15)) << 4) + (p >> 1) * 8;
                }
            }
            v4u ct[8][4];
#pragma unroll
            for (int t = 0; t < 8; ++t)
#pragma unroll
                for (int s = 0; s < 2; ++s) ct[t][s] = trd2((a_t[0][t >> 1] ^ ((t & 1) * 8)) + s * 32768, (a_t[1][t >> 1] ^ ((t & 1) * 8)) + s * 32768);
            unsigned a_pr[4], a_mxr = L_MX + l15 * 16;
#pragma unroll
            for (int j = 0; j < 4; ++j) { a_pr[j] = L_P + l15 * 256 + (((4 * j + g) ^ l15) << 4); PIN(a_pr[j]); }
            unsigned o_t3 = l < 4 ? WS_T3 + (((4 * hg + l) * 4 + w) * 64 + pr) * 128 : OOB;
            unsigned o_h3 = WS_H3 + ((b * 32 + 16 * hg + (l >> 3)) * 4 + w) * H3S + sp * 384 + (l & 7) * 16, o_h3b = o_h3 + 8 * 4 * H3S;
            unsigned a_sw = L_R2 + w * 8192 + l15 * 144 + g * 16, a_sr = L_R2 + w * 8192 + (l >> 3) * 144 + (l & 7) * 16;
            PIN(a_sw); PIN(a_sr);
            unsigned o_wd[4], a_lr = L_MX + 256 + l15 * 16;
#pragma unroll
            for (int i = 0; i < 4; ++i) { const unsigned row = 4 * w + i; o_wd[i] = (EXP & 1) ? 0 : (16 * min(un, 85) + row) * 1024 + ((l ^ ((row & 7) << 1)) << 4); PIN(o_wd[i]); }
            PIN(a_mxr); PIN(o_t3); PIN(o_h3); PIN(o_h3b); PIN(a_lr);
            Q_ISSUE();
            v4u wst[4];
            asm volatile("global_load_dwordx4 %0, %4, %8\n\tglobal_load_dwordx4 %1, %5, %8\n\tglobal_load_dwordx4 %2, %6, %8\n\tglobal_load_dwordx4 %3, %7, %8"
                         : "=&v"(wst[0]), "=&v"(wst[1]), "=&v"(wst[2]), "=&v"(wst[3]) : "v"(o_wd[0]), "v"(o_wd[1]), "v"(o_wd[2]), "v"(o_wd[3]), "s"(wd) : "memory");
#pragma unroll
            for (int t = 0; t < 8; ++t)
#pragma unroll
                for (int s = 2; s < 4; ++s) ct[t][s] = trd2((a_t[0][t >> 1] ^ ((t & 1) * 8)) + s * 32768, (a_t[1][t >> 1] ^ ((t & 1) * 8)) + s * 32768);
            FENCE();
            Q_LAND(4);
            STAMP(4);
            bar0();
            const f32x4 m0 = lrd<f32x4>(a_mxr);
            float mr = mx(mx3(m0[0], m0[1], m0[2]), m0[3]);
            PIN(mr);
            bar();
            STAMP(5);
            v4u pf[4];
#pragma unroll
            for (int s = 0; s < 4; ++s) pf[s] = lrd<v4u>(a_pr[s]);
            if (HP == 0) hint_put(rs, o_t3, tag);
            FENCE();
            f32x4 o[8];
#define M0_(t) asm volatile("v_mfma_f32_16x16x32_bf16 %0, %1, %2, 0" : "=&v"(o[t]) : "v"(ct[t][0]), "v"(pf[0]))
#define MM(t, s) asm volatile("v_mfma_f32_16x16x32_bf16 %0, %1, %2, %0" : "+v"(o[t]) : "v"(ct[t][s]), "v"(pf[s]))
#define RDY(t) PIN(o[t])
#define SEQ() asm volatile("" ::: "memory")
#define CV(d, t, i) do { d = pk(o[t][i], o[t][i + 1]); PIN(d); } while (0)
#define PUT(n, k, A, B, C) lwr<v4u>(a_sw + (n) * 2304 + (k) * 64, v4u{A, B, C, tag})
#define GETL(n) do { x0 = lrd<v4u>(a_sr + (n) * 2304); x1 = lrd<v4u>(a_sr + (n) * 2304 + 8 * 144); } while (0)
#define OUTL(n) do { stx(rs, o_h3 + (n) * 128, x0); stx(rs, o_h3b + (n) * 128, x1); } while (0)
            unsigned d0, d1, d2, d3, d4, d5;
            v4u x0, x1;
            M0_(0); M0_(1); MM(0, 1); MM(1, 1); MM(0, 2); MM(1, 2); MM(0, 3); MM(1, 3);
            M0_(2); MM(2, 1); MM(2, 2); MM(2, 3);
            M0_(3); MM(3, 1); MM(3, 2);
            RDY(0); CV(d0, 0, 0); CV(d1, 0, 2);
            MM(3, 3); RDY(1); CV(d2, 1, 0); CV(d3, 1, 2);
            M0_(4); PUT(0, 0, d0, d1, d2); SEQ();
            MM(4, 1); RDY(2); CV(d4, 2, 0); CV(d5, 2, 2);
            MM(4, 2); PUT(0, 1, d3, d4, d5); GETL(0); SEQ();
            MM(4, 3); RDY(3); CV(d0, 3, 0); CV(d1, 3, 2);
            M0_(5);
            bar0();
            const f32x4 l0 = lrd<f32x4>(a_lr);
            MM(5, 1); MM(5, 2); MM(5, 3);
            RDY(4); CV(d2, 4, 0); CV(d3, 4, 2);
            STAMP2(3);
            M0_(6); OUTL(0); if (HP == 1) hint_put(rs, o_t3, tag); PUT(1, 0, d0, d1, d2); SEQ();
            STAMP2(4);
            MM(6, 1);
            float lsum = (l0[0] + l0[1]) + (l0[2] + l0[3]);
            PIN(lsum);
            MM(6, 2); MM(6, 3);
            RDY(5); CV(d4, 5, 0); CV(d5, 5, 2);
            M0_(7); PUT(1, 1, d3, d4, d5); GETL(1); SEQ();
            MM(7, 1); MM(7, 2); MM(7, 3);
            asm volatile("s_nop 1" : "+v"(o[6]));
            CV(d0, 6, 0); CV(d1, 6, 2);
            STAMP2(14);
            OUTL(1); if (HP == 2) hint_put(rs, o_t3, tag); SEQ();
            STAMP2(15);
            asm volatile("s_nop 3" : "+v"(o[7]));
            CV(d2, 7, 0); CV(d3, 7, 2);
            PUT(2, 0, d0, d1, d2); PUT(2, 1, d3, asu(mr), asu(lsum)); GETL(2); OUTL(2); if (HP == 3) hint_put(rs, o_t3, tag);
            FENCE();
            STAMP(6);
            asm volatile("s_waitcnt vmcnt(7)" : "+v"(wst[0]), "+v"(wst[1]), "+v"(wst[2]), "+v"(wst[3]) :: "memory");
#pragma unroll
            for (int i = 0; i < 4; ++i) lwr<v4u>(L_R0 + (4 * w + i) * 1024 + l * 16, wst[i]);
        }
        if (STOPAT <= 7) goto done;
        bar();
        if (un < 86) {
            if (w == 4) {
                const f32x4 gp = lrd<f32x4>(L_RED + (l & 31) * 32), up = lrd<f32x4>(L_RED + (l & 31) * 32 + 16);
                const float gg = rbf((gp[0] + gp[1]) + (gp[2] + gp[3])), uu = rbf((up[0] + up[1]) + (up[2] + up[3]));
                const float sg = rbf(__builtin_amdgcn_rcpf(1.f + __builtin_amdgcn_exp2f(gg * -1.4426950408889634f)));
                lwr<u16>(L_HH + (l & 31) * 2, (u16)pk(sg * uu, 0.f));
            }
            bar();
            const v2u ha = lrd<v2u>(L_HH + (l & 1) * 32 + g * 8);
            const unsigned q = l15 >> 2, p = l & 3, row = 4 * g + q;
            f32x4 o[4];
#pragma unroll
            for (int t = 0; t < 4; ++t) {
                const unsigned ch = 8 * w + 2 * t + (p >> 1);
                o[t] = mfma16(ha, trd(L_R0 + row * 1024 + ((ch ^ ((row & 7) << 1)) << 4) + (p & 1) * 8), ZF);
            }
            const float o0 = g == 0 ? o[0][0] : g == 1 ? o[1][0] : g == 2 ? o[2][0] : o[3][0];
            const float o1 = g == 0 ? o[0][1] : g == 1 ? o[1][1] : g == 2 ? o[2][1] : o[3][1];
            stx(rs, WS_H5 + ((32 + un) * 512 + 64 * w + l) * 16, v4u{asu(o0), tag, asu(o1), tag});
        }
        STAMP(7);
        {
            unsigned a0, a1;
            {
                const unsigned q = l15 >> 2, p = l & 3, ch = 2 * w + (p >> 1);
                a0 = L_R1 + (8 * g + q) * 256 + 16 * (ch ^ ((q << 2) | ((2 * g) & 3))) + 8 * (p & 1);
                a1 = L_R1 + (8 * g + q + 4) * 256 + 16 * (ch ^ ((q << 2) | ((2 * g + 1) & 3))) + 8 * (p & 1);
            }
            unsigned o_g4 = WS_H4 + ((h * 4 + g) * 128 + 16 * w + l15) * 16;
            unsigned o_t5 = tid < 16 ? WS_T5 + ((8 * (cq + 4 * (tid >> 3)) + (tid & 7)) * 32 + h) * 128 : OOB;
            unsigned o_h5 = l < 16 ? WS_H5 + (h * 512 + 64 * (cq + 4 * (w >> 2)) + 16 * (w & 3) + l) * 16 : OOB;
            unsigned a_qw = L_QS + (g & 1) * 272 + (16 * w + l15) * 2, a_qr = L_QS + (l & 1) * 272 + 16 * g;
            v4u pb = {0u, tag, 0u, tag}, fw[4], ga;
            WAITVM(0);
            bar();
            frags128(a0, a1, fw);
            PIN(fw[0]); PIN(fw[1]); PIN(fw[2]); PIN(fw[3]); PIN(o_g4); PIN(o_t5); PIN(o_h5); PIN(a_qw); PIN(a_qr); PIN(pb);
            STAMP(10);
            if (w == 4) pace(hint_wait(rsq, o_t4, tag), P4);
            bar0();
            STAMP(11);
            asm volatile("buffer_load_dwordx4 %0, %1, %2, 0 offen sc1" : "=&v"(ga) : "v"(o_g4), "s"(rsq) : "memory");
            hint_put(rs, o_t5, tag);
            asm volatile("s_waitcnt vmcnt(1)" : "+v"(ga) :: "memory");
            if (__builtin_expect(!none(tagx(ga, tag) != 0u), 0)) {
#pragma nounroll
                SPIN { __builtin_amdgcn_s_sleep(2); get1(ga, o_g4, rsq); if (none(tagx(ga, tag) != 0u)) break; }
            }
            STAMP(8);
            lwr<u16>(a_qw, (u16)pk(sum32(sum16x2(asf(ga[0]), asf(ga[2]))), 0.f));
            bar();
            v4u cf[4];
#pragma unroll
            for (int s = 0; s < 4; ++s) cf[s] = lrd<v4u>(a_qr + 64 * s);
            FENCE();
            const f32x4 acc = gemv4(cf, fw);
            pb[0] = asu(acc[0]); pb[2] = asu(acc[1]);
            stx(rs, o_h5, pb);
        }
        STAMP(9);
    }
done:
    STAMP(13);
}

extern "C" void kernel_launch(void* const* d_in, const int* in_sizes, int n_in, void* d_out, int out_size, void* d_ws, size_t ws_size,
                              hipStream_t stream) {
    (void)in_sizes; (void)n_in; (void)out_size; (void)ws_size;
    (void)hipFuncSetAttribute((const void*)mla_glu_decode, hipFuncAttributeMaxDynamicSharedMemorySize, L_END);
    mla_glu_decode<<<256, 512, L_END, stream>>>((const u16*)d_in[0], (const u16*)d_in[1], (const u16*)d_in[2], (const u16*)d_in[3], (const u16*)d_in[4],
                                               (const u16*)d_in[5], (const u16*)d_in[7], (const u16*)d_in[8], (const u16*)d_in[9], (u16*)d_out,
                                               (unsigned char*)d_ws);
}
```

```cpp
#include <hip/hip_runtime.h>
#include <hip/hip_bf16.h>

typedef unsigned short u16;
typedef unsigned v4u __attribute__((ext_vector_type(4)));
typedef unsigned v2u __attribute__((ext_vector_type(2)));
typedef float f32x4 __attribute__((ext_vector_type(4)));
typedef float f32x2 __attribute__((ext_vector_type(2)));
typedef short s16x4 __attribute__((ext_vector_type(4)));
typedef __bf16 bf16x8 __attribute__((ext_vector_type(8)));
typedef __bf16 bf16x2 __attribute__((ext_vector_type(2)));
#define LAS __attribute__((address_space(3)))
#define GAS __attribute__((address_space(1)))
#define DEVI __device__ __forceinline__

#define PB 95
#define P2 100
#define P3 60
#define P4 55
#define P5 50
#define PG 105
#define PW 60
#define PA 0
#define CODEB 14080
#define EXP 0
#define PH 0
#define H3S 12288
#define XMAP 0
#define VDEF 1
#define HP 2
#define PREH3 0
#define STOPAT 99

constexpr int WS_T2 = 4096;
constexpr int WS_T3 = WS_T2 + 8 * 128 * 128;
constexpr int WS_T5 = WS_T3 + 8 * 64 * 4 * 128;
constexpr int WS_T4 = WS_T5 + 64 * 32 * 128;
constexpr int WS_H1 = WS_T4 + 32 * 16 * 128;
constexpr int WS_H4 = WS_H1 + 32 * 4 * 128 * 16;
constexpr int WS_H2 = WS_H4 + 32 * 4 * 128 * 16;
constexpr int WS_HG = WS_H2 + 4 * 2 * 32 * 128 * 16;
constexpr int WS_H5 = WS_HG + 2 * 16 * 1376 * 16;
constexpr int WS_H3 = WS_H5 + 118 * 512 * 16;
constexpr int WS_END = WS_H3 + 2 * 32 * 4 * H3S;
constexpr int WS_STAMP = 16 << 20;
constexpr int L_Q = 0;
constexpr int L_P = 16384;
constexpr int L_QS = L_P + 4096;
constexpr int L_OW = L_QS + 1024;
constexpr int L_MW = L_OW + 2304;
constexpr int L_MX = L_MW + 64;
constexpr int L_HH = L_MX + 512;
constexpr int L_TAG = L_HH + 64;
constexpr int L_JUNK = L_TAG + 64;
constexpr int L_RED = L_JUNK + 256;
constexpr int L_XN = 28672;
constexpr int L_R0 = 32768, L_R1 = 65536, L_R2 = 98304, L_R3 = 131072;
constexpr int L_END = 163840;
static_assert(L_RED + 1024 <= L_XN && L_XN + 2048 <= L_R0, "lds");
constexpr int SPIN_CAP = 1 << 16;

DEVI unsigned pk(float lo, float hi) { bf16x2 r = {(__bf16)lo, (__bf16)hi}; return __builtin_bit_cast(unsigned, r); }
DEVI float rbf(float f) { return (float)(__bf16)f; }
DEVI float lo16(unsigned w) { return __builtin_bit_cast(float, w << 16); }
DEVI float hi16(unsigned w) { return __builtin_bit_cast(float, w & 0xffff0000u); }
DEVI float asf(unsigned w) { return __builtin_bit_cast(float, w); }
DEVI unsigned asu(float w) { return __builtin_bit_cast(unsigned, w); }
DEVI float mx(float a, float b) { float r; asm("v_max_f32 %0, %1, %2" : "=v"(r) : "v"(a), "v"(b)); return r; }
DEVI float mx3(float a, float b, float c) { float r; asm("v_max3_f32 %0, %1, %2, %3" : "=v"(r) : "v"(a), "v"(b), "v"(c)); return r; }
DEVI f32x4 mfma32(v4u a, v4u b, f32x4 c) { return __builtin_amdgcn_mfma_f32_16x16x32_bf16(__builtin_bit_cast(bf16x8, a), __builtin_bit_cast(bf16x8, b), c, 0, 0, 0); }
DEVI f32x4 mfma16(v2u a, v2u b, f32x4 c) { return __builtin_amdgcn_mfma_f32_16x16x16bf16_1k(__builtin_bit_cast(s16x4, a), __builtin_bit_cast(s16x4, b), c, 0, 0, 0); }
template <class T> DEVI T lrd(unsigned off) { return *(LAS T*)(size_t)off; }
template <class T> DEVI void lwr(unsigned off, T v) { *(LAS T*)(size_t)off = v; }
DEVI v2u trd(unsigned off) { return __builtin_bit_cast(v2u, __builtin_amdgcn_ds_read_tr16_b64_v4i16((LAS s16x4*)(size_t)off)); }
DEVI v4u trd2(unsigned o0, unsigned o1) { v2u a = trd(o0), b = trd(o1); return v4u{a.x, a.y, b.x, b.y}; }
DEVI float sum16(float x) { auto r = __builtin_amdgcn_permlane16_swap(asu(x), asu(x), false, false); return asf(r[0]) + asf(r[1]); }
DEVI float sum32(float x) { auto r = __builtin_amdgcn_permlane32_swap(asu(x), asu(x), false, false); return asf(r[0]) + asf(r[1]); }
DEVI float max16(float x) { auto r = __builtin_amdgcn_permlane16_swap(asu(x), asu(x), false, false); return mx(asf(r[0]), asf(r[1])); }
DEVI float max32(float x) { auto r = __builtin_amdgcn_permlane32_swap(asu(x), asu(x), false, false); return mx(asf(r[0]), asf(r[1])); }
DEVI float odd16(float x) { auto r = __builtin_amdgcn_permlane16_swap(asu(x), asu(x), false, false); return asf(r[1]); }
DEVI float sum16x2(float x, float y) { auto r = __builtin_amdgcn_permlane16_swap(asu(x), asu(y), false, false); return asf(r[0]) + asf(r[1]); }
DEVI float sum32x2(float x, float y) { auto r = __builtin_amdgcn_permlane32_swap(asu(x), asu(y), false, false); return asf(r[0]) + asf(r[1]); }
template <int CTRL> DEVI float dpp(float x) { return asf((unsigned)__builtin_amdgcn_update_dpp(0, (int)asu(x), CTRL, 0xf, 0xf, true)); }
DEVI float wavesum(float x) {
    x += dpp<0xB1>(x); x += dpp<0x4E>(x); x += dpp<0x141>(x); x += dpp<0x140>(x);
    return sum32(sum16(x));
}
DEVI float dot2one(unsigned p, float c) { const unsigned one = 0x3f803f80u; return __builtin_amdgcn_fdot2_f32_bf16(__builtin_bit_cast(bf16x2, p), __builtin_bit_cast(bf16x2, one), c, false); }
#define STR_(x) #x
#define STR(x) STR_(x)
#define WAITVM(n) asm volatile("s_waitcnt vmcnt(" STR(n) ")" ::: "memory")
#define PIN(x) asm volatile("" : "+v"(x))
#define MFMA_SETTLE(x) asm volatile("s_nop 7" : "+v"(x))
#define UNDEF(x) asm volatile("" : "=v"(x))
#define FENCE() __builtin_amdgcn_sched_barrier(0)
#define SPIN for (int it_ = SPIN_CAP; it_ > 0; --it_)
DEVI void bar() { asm volatile("s_waitcnt lgkmcnt(0)\n\ts_barrier" ::: "memory"); }
DEVI void bar0() { asm volatile("s_barrier" ::: "memory"); }
#define USE_NT 0
template <bool NT = false> DEVI void glds(unsigned voff, const void* sbase, unsigned lds) {
    unsigned keep;
    if (NT && USE_NT) asm volatile("s_mov_b32 %0, m0\n\ts_mov_b32 m0, %3\n\ts_nop 0\n\tglobal_load_lds_dwordx4 %1, %2 nt\n\ts_mov_b32 m0, %0" : "=&s"(keep) : "v"(voff), "s"(sbase), "s"(lds) : "memory");
    else asm volatile("s_mov_b32 %0, m0\n\ts_mov_b32 m0, %3\n\ts_nop 0\n\tglobal_load_lds_dwordx4 %1, %2\n\ts_mov_b32 m0, %0" : "=&s"(keep) : "v"(voff), "s"(sbase), "s"(lds) : "memory");
}
typedef __amdgpu_buffer_rsrc_t rsrc_t;
constexpr unsigned OOB = 0x80000000u;
DEVI void stx(rsrc_t rs, unsigned off, v4u v) { __builtin_amdgcn_raw_buffer_store_b128(v, rs, off, 0, 16); }
DEVI bool none(bool bad) { asm volatile("" ::: "memory"); return __builtin_amdgcn_ballot_w64(bad) == 0ull; }
DEVI unsigned tagx(v4u a, unsigned tag) { unsigned r = (a[1] ^ tag) | (a[3] ^ tag); asm("" : "+v"(r)); return r; }
DEVI unsigned tagy(unsigned a, unsigned b, unsigned tag) { unsigned r = (a ^ tag) | (b ^ tag); asm("" : "+v"(r)); return r; }
DEVI unsigned tagx4(const v4u (&a)[4], unsigned tag) { return (tagx(a[0], tag) | tagx(a[1], tag)) | (tagx(a[2], tag) | tagx(a[3], tag)); }
DEVI void get1(v4u& a, unsigned off, v4u rq) { asm volatile("buffer_load_dwordx4 %0, %1, %2, 0 offen sc1\n\ts_waitcnt vmcnt(0)" : "+&v"(a) : "v"(off), "s"(rq) : "memory"); }
DEVI void get2(v4u& a, v4u& b, unsigned oa, unsigned ob, v4u rq) {
    asm volatile("buffer_load_dwordx4 %0, %2, %4, 0 offen sc1\n\tbuffer_load_dwordx4 %1, %3, %4, 0 offen sc1\n\ts_waitcnt vmcnt(0)" : "+&v"(a), "+&v"(b) : "v"(oa), "v"(ob), "s"(rq) : "memory");
}
template <int ST> DEVI void get4(v4u (&a)[4], unsigned off, v4u rq) {
    asm volatile("buffer_load_dwordx4 %0, %4, %5, 0 offen sc1\n\tbuffer_load_dwordx4 %1, %4, %5, 0 offen offset:%6 sc1\n\t"
                 "buffer_load_dwordx4 %2, %4, %5, 0 offen offset:%7 sc1\n\tbuffer_load_dwordx4 %3, %4, %5, 0 offen offset:%8 sc1\n\ts_waitcnt vmcnt(0)"
                 : "+&v"(a[0]), "+&v"(a[1]), "+&v"(a[2]), "+&v"(a[3]) : "v"(off), "s"(rq), "i"(ST), "i"(2 * ST), "i"(3 * ST) : "memory");
}
DEVI void get4s(v4u (&a)[4], unsigned off, v4u rq, unsigned s1, unsigned s2, unsigned s3) {
    asm volatile("buffer_load_dwordx4 %0, %4, %5, 0 offen sc1\n\tbuffer_load_dwordx4 %1, %4, %5, %6 offen sc1\n\t"
                 "buffer_load_dwordx4 %2, %4, %5, %7 offen sc1\n\tbuffer_load_dwordx4 %3, %4, %5, %8 offen sc1\n\ts_waitcnt vmcnt(0)"
                 : "+&v"(a[0]), "+&v"(a[1]), "+&v"(a[2]), "+&v"(a[3]) : "v"(off), "s"(rq), "s"(s1), "s"(s2), "s"(s3) : "memory");
}
DEVI unsigned now() { return (unsigned)__builtin_amdgcn_s_memrealtime(); }
DEVI void hint_put(rsrc_t rs, unsigned off, unsigned tag, unsigned t) { __builtin_amdgcn_raw_buffer_store_b64(v2u{tag, t}, rs, off, 0, 16); }
DEVI void hint_put(rsrc_t rs, unsigned off, unsigned tag) { hint_put(rs, off, tag, now()); }
DEVI v2u hint_get(v4u rq, unsigned off) { v2u v; asm volatile("buffer_load_dwordx2 %0, %1, %2, 0 offen sc1\n\ts_waitcnt vmcnt(0)" : "=&v"(v) : "v"(off), "s"(rq) : "memory"); return v; }
DEVI void pace(unsigned t, unsigned lead) {
#pragma nounroll
    for (int it = 256; it > 0; --it) { if (none(now() - t < lead)) break; }
}
DEVI unsigned hint_wait(v4u rq, unsigned off, unsigned tag) {
    v2u v;
#pragma nounroll
    SPIN { v = hint_get(rq, off); if (none(v[0] != tag)) break; __builtin_amdgcn_s_sleep(1); }
    return v[1];
}
DEVI unsigned mktag(unsigned ep) { return ep + 1u; }

#define STAMP(i)

#define CNT0(i)
#define CNT(i)
#define STAMPC(i)
#define STAMP2(i)
DEVI void frags128(unsigned a0, unsigned a1, v4u (&f)[4]) {
#pragma unroll
    for (int s = 0; s < 4; ++s) f[s] = trd2(a0 + s * 8192, a1 + s * 8192);
}
DEVI f32x4 gemv4(const v4u (&a)[4], const v4u (&b)[4]) {
    f32x4 c = {0.f, 0.f, 0.f, 0.f};
#pragma unroll
    for (int s = 0; s < 4; ++s) c = mfma32(a[s], b[s], c);
    return c;
}
DEVI void layernorm(v4u xv, int w, int l) {
    float v[8];
#pragma unroll
    for (int j = 0; j < 4; ++j) { v[2 * j] = lo16(xv[j]); v[2 * j + 1] = hi16(xv[j]); }
    const float mean = wavesum(((v[0] + v[1]) + (v[2] + v[3])) + ((v[4] + v[5]) + (v[6] + v[7]))) * (1.f / 512.f);
    float sq = 0.f;
#pragma unroll
    for (int j = 0; j < 8; ++j) { const float d = v[j] - mean; sq += d * d; }
    const float var = wavesum(sq) * (1.f / 512.f);
    const float mb = rbf(mean), ri = __builtin_amdgcn_rcpf(rbf(__builtin_amdgcn_sqrtf(rbf(rbf(var) + 1e-5f))));
    v4u o;
#pragma unroll
    for (int j = 0; j < 4; ++j) o[j] = pk(rbf(v[2 * j] - mb) * ri, rbf(v[2 * j + 1] - mb) * ri);
    lwr<v4u>(L_XN + w * 1024 + l * 16, o);
}

__global__ void __launch_bounds__(512, 2)
mla_glu_decode(const u16* __restrict__ x, const u16* __restrict__ ckv, const u16* __restrict__ wq, const u16* __restrict__ wuk,
               const u16* __restrict__ wuv, const u16* __restrict__ wo, const u16* __restrict__ wg, const u16* __restrict__ wu,
               const u16* __restrict__ wd, u16* __restrict__ out, unsigned char* __restrict__ ws) {
    STAMP(0);
    const int tid = threadIdx.x, l = tid & 63, w = __builtin_amdgcn_readfirstlane(tid >> 6);
    const int id = blockIdx.x, ai = (id & 3) * 32 + (id >> 3);
    const int xc = XMAP ? ai >> 4 : id & 7, yy = XMAP ? ai & 15 : (id >> 3) & 15, y = XMAP ? (id & 4) * 4 : id >> 3;
    const int un = yy * 8 + xc;
    const unsigned g = l >> 4, l15 = l & 15;
    const unsigned t_in = now();
    unsigned cp0 = 0;
    if (w == 7) {
        unsigned long long pc;
        asm volatile("s_getpc_b64 %0" : "=s"(pc));
        pc &= ~255ull;
        asm volatile("global_load_dword %0, %1, %2" : "=&v"(cp0) : "v"(min(((id >> 3) + 32 * (l & 3)) * 128, CODEB - 128)), "s"(pc) : "memory");
    }
    asm volatile("" :: "s"(x), "s"(ckv), "s"(wq), "s"(wuk), "s"(wuv), "s"(wo), "s"(wg), "s"(wu), "s"(wd), "s"(out), "s"(ws));
    const rsrc_t rs = __builtin_amdgcn_make_buffer_rsrc(ws, 0, WS_END, 0x00020000);
    const v4u rsq = {(unsigned)(size_t)ws, (unsigned)((size_t)ws >> 32) & 0xffffu, (unsigned)WS_END, 0x00020000u};
    const f32x4 ZF = {0.f, 0.f, 0.f, 0.f};

    if (__builtin_expect(y < 16, 1)) {
        const int h = 4 * xc + (yy >> 2), cq = yy & 3;
        v4u xv; unsigned ep;
        if (w < 2) {
            asm volatile("global_load_dwordx4 %0, %1, %2" : "=v"(xv) : "v"(w * 1024 + l * 16), "s"(x) : "memory");
            asm volatile("global_load_dword %0, %1, %2 sc1" : "=v"(ep) : "v"(0), "s"(ws) : "memory");
        }
        const unsigned vb = (128 * cq + 16 * w + g) * 8192 + h * 256;
        unsigned sw[4];
#pragma unroll
        for (int i = 0; i < 4; ++i) sw[i] = (l15 ^ ((g << 2) | i)) << 4;
#pragma unroll
        for (int i = 0; i < 4; ++i) glds<true>(vb + i * 32768 + sw[i], wq, L_R0 + (16 * w + 4 * i) * 256);
        bar0();
#pragma unroll
        for (int i = 0; i < 4; ++i) glds<true>(vb + i * 32768 + ((l15 ^ (4 * i + g)) << 4), wuk, L_R1 + (16 * w + 4 * i) * 256);
        STAMP(1);
        if (w < 2) {
            asm volatile("s_waitcnt vmcnt(8)" : "+v"(xv), "+v"(ep) :: "memory");
            layernorm(xv, w, l);
            lwr<v4u>(L_TAG, v4u{mktag(ep), 0u, 0u, 0u});
        }
        bar();
        const unsigned tag = lrd<unsigned>(L_TAG);
        unsigned o_p1 = l < 16 ? WS_H1 + ((h * 4 + cq) * 128 + 16 * w + l) * 16 : OOB;
        unsigned o_g1 = WS_H1 + ((h * 4 + g) * 128 + 16 * w + l15) * 16;
        unsigned o_h2 = WS_H2 + ((((w & 3) * 2 + (l >> 5)) * 32 + h) * 128 + 32 * cq + (l & 31)) * 16;
        unsigned a_2w = l15 < 2 ? L_RED + l15 * 256 + (4 * w + g) * 8 : L_JUNK + (l & 31) * 8, a_2r = L_RED + l * 8;
        PIN(a_2w); PIN(a_2r);
        unsigned o_t2 = l < 8 ? WS_T2 + (l * 128 + h * 4 + cq) * 128 : OOB;
        unsigned a0, a1;
        {
            const unsigned q = l15 >> 2, p = l & 3, ch = 2 * w + (p >> 1);
            a0 = L_R0 + (8 * g + q) * 256 + 16 * (ch ^ ((q << 2) | ((2 * g) & 3))) + 8 * (p & 1);
            a1 = L_R0 + (8 * g + q + 4) * 256 + 16 * (ch ^ ((q << 2) | ((2 * g + 1) & 3))) + 8 * (p & 1);
        }
        unsigned a_qw = L_QS + (g & 1) * 272 + (16 * w + l15) * 2;
        unsigned a_qr = L_QS + (l & 1) * 272 + 16 * g;
        unsigned a_uk[4], o_gu[3], a_cnt = l == 0 ? L_TAG + 8 : L_JUNK + l * 4, one = 1u;
        v4u xf[4];
#pragma unroll
        for (int s = 0; s < 4; ++s) {
            xf[s] = lrd<v4u>(L_XN + (l & 1) * 1024 + (128 * cq + 32 * s + 8 * g) * 2);
            a_uk[s] = L_R1 + (16 * w + l15) * 256 + (((4 * s + g) ^ l15) << 4);
            PIN(xf[s]); PIN(a_uk[s]);
        }
        const int mat = un >> 6, mp = (un >> 2) & 15, fp = un & 3;
#pragma unroll
        for (int i = 0; i < 3; ++i) {
            const unsigned pc = min(8 * i + w, 21), idx = min(64 * pc + l, 1375u), row = idx / 43u, ch = idx - row * 43u;
            o_gu[i] = (32 * mp + row) * 2752 + fp * 688 + ch * 16; PIN(o_gu[i]);
        }
        v4u pb = {0u, tag, 0u, tag}, ga;
        PIN(a0); PIN(a1); PIN(o_p1); PIN(o_g1); PIN(o_h2); PIN(o_t2); PIN(a_qw); PIN(a_qr); PIN(pb); PIN(a_cnt); PIN(one);
        asm volatile("s_waitcnt vmcnt(4)" : "+v"(cp0) :: "memory");
        bar0();
        STAMP(2);
        {
            v4u f[4]; frags128(a0, a1, f);
            FENCE();
            const f32x4 acc = gemv4(xf, f);
            pb[0] = asu(acc[0]); pb[2] = asu(acc[1]);
            stx(rs, o_p1, pb);
        }
        STAMP(3);
        if (STOPAT <= 3) { WAITVM(0); STAMP(14); goto done; }
        __builtin_amdgcn_s_sleep(10);
        WAITVM(3);
        v4u fu[4];
        {
            unsigned arr;
            asm volatile("buffer_load_dwordx4 %1, %4, %5, 0 offen sc1\n\tds_add_rtn_u32 %0, %2, %3\n\ts_waitcnt lgkmcnt(0)" : "=&v"(arr), "=&v"(ga) : "v"(a_cnt), "v"(one), "v"(o_g1), "s"(rsq) : "memory");
            hint_put(rs, __builtin_amdgcn_readfirstlane(arr) == 7u ? o_t2 : OOB, tag);
            asm volatile("s_waitcnt vmcnt(1)" : "+v"(ga) :: "memory");
            STAMP(14);
#pragma unroll
            for (int s = 0; s < 4; ++s) fu[s] = lrd<v4u>(a_uk[s]);
            if (__builtin_expect(!none(tagx(ga, tag) != 0u), 0)) {
#pragma nounroll
                SPIN { __builtin_amdgcn_s_sleep(4); get1(ga, o_g1, rsq); if (none(tagx(ga, tag) != 0u)) break; }
            }
            lwr<u16>(a_qw, (u16)pk(sum32(sum16x2(asf(ga[0]), asf(ga[2]))), 0.f));
        }
        STAMP(4);
        bar();
        {
            v4u qf[4];
#pragma unroll
            for (int s = 0; s < 4; ++s) qf[s] = lrd<v4u>(a_qr + 64 * s);
            FENCE();
            const f32x4 acc = gemv4(fu, qf);
            constexpr float SC = 0.08838834764831845f * 1.4426950408889634f;
            lwr<v2u>(a_2w, v2u{pk(acc[0] * SC, acc[1] * SC), pk(acc[2] * SC, acc[3] * SC)});
            bar();
            if (w < 4) {
                const v2u t = lrd<v2u>(a_2r);
                pb[0] = t[0]; pb[2] = t[1];
                stx(rs, o_h2, pb);
            }
        }
        STAMP(5);
        if (STOPAT <= 5) goto done;
        const int r = xc * 8 + (yy >> 1);
        unsigned o_t3 = WS_T3 + ((xc * 4 + cq) * 64 + 8 * w + (l & 7)) * 128, o_t5 = WS_T5 + (r * 32 + (l & 31)) * 128, j0, j1;
        asm volatile("buffer_load_dword %0, %2, %4, 0 offen sc1\n\tbuffer_load_dword %1, %3, %4, 0 offen sc1"
                     : "=&v"(j0), "=&v"(j1) : "v"(o_t3), "v"((w == 0 && !(yy & 1)) ? o_t5 : o_t3), "s"(rsq) : "memory");
        unsigned trep = 0;
        if (VDEF == 2) trep = hint_wait(rsq, WS_T2 + (xc * 128 + (l & 7) * 16 + 5) * 128, tag);
#pragma unroll
        for (int i = 0; i < 3; ++i) glds(o_gu[i], mat ? wu : wg, L_R2 + min(8 * i + w, 21) * 1024);
#define V_ISSUE(i) glds<true>((EXP & 4) ? 0 : vb + (i) * 32768 + sw[i], wuv, L_R1 + (16 * w + 4 * (i)) * 256)
        if (!VDEF) { V_ISSUE(0); V_ISSUE(1); }
        {
            const v4u xg = lrd<v4u>(L_XN + (l & 1) * 1024 + (32 * mp + 8 * g) * 2);
            const unsigned ag = L_R2 + (8 * g + (l15 >> 2)) * 688 + (l & 3) * 8;
            const unsigned f = 16 * (8 * g + w) + l15;
            const unsigned o_hg = (g < 3 && f < 344) ? WS_HG + ((mat * 16 + mp) * 1376 + 344 * fp + f) * 16 : OOB;
            if (VDEF) asm volatile("s_waitcnt vmcnt(0)" : "+v"(j0), "+v"(j1) :: "memory"); else asm volatile("s_waitcnt vmcnt(2)" : "+v"(j0), "+v"(j1) :: "memory");
            bar();
            f32x4 o[3];
#pragma unroll
            for (int t = 0; t < 3; ++t) {
                const unsigned tl = min(8 * t + w, 21);
                o[t] = mfma32(xg, trd2(ag + tl * 32, ag + tl * 32 + 4 * 688), ZF);
            }
            const float o0 = g == 0 ? o[0][0] : g == 1 ? o[1][0] : o[2][0], o1 = g == 0 ? o[0][1] : g == 1 ? o[1][1] : o[2][1];
            stx(rs, o_hg, v4u{asu(o0), tag, asu(o1), tag});
        }
        if (VDEF) {
            pace(VDEF == 2 ? trep : hint_wait(rsq, WS_T2 + (xc * 128 + (l & 7) * 16 + 5) * 128, tag), PA);
            V_ISSUE(0); V_ISSUE(1);
        }
        V_ISSUE(2); V_ISSUE(3);
        STAMP(6);
        const unsigned u = l15 >= 12 ? 8 + l15 : (g & 1) ? 12 + (l15 & 7) : l15;
        const bool act = (g & 1) ? (l15 & 12) != 8 : l15 < 12;
        unsigned o_h3 = WS_H3 + (((w >> 2) * 32 + h) * 4 + cq) * H3S + (8 * (w & 3) + (g >> 1)) * 384 + u * 16;
        unsigned o_t4 = l < 4 ? WS_T4 + ((h * 4 + l) * 4 + cq) * 128 : OOB;
        unsigned o_p4 = l < 16 ? WS_H4 + ((h * 4 + cq) * 128 + 16 * w + l) * 16 : OOB;
        unsigned a_ow, a_ow2, a_ms;
        {
            const unsigned n = 6 * (u >> 2) + 2 * (g >> 1), n2 = 6 * (u >> 2) + 4, gg = u & 3;
            a_ow = (act && n < 32) ? L_OW + w * 272 + (32 * gg + 4 * ((n >> 2) ^ (gg >> 1)) + (n & 3)) * 2 : L_JUNK + l * 4;
            a_ow2 = (act && n2 < 32 && l < 32) ? L_OW + w * 272 + (32 * gg + 4 * ((n2 >> 2) ^ (gg >> 1)) + (n2 & 3)) * 2 : L_JUNK + l * 4;
            a_ms = (l & 31) == 12 ? L_MW + (g >> 1) * 32 + w * 4 : L_JUNK + l * 4;
        }
        unsigned a_or = L_OW + (l & 7) * 272 + 16 * g, a_mw = L_MW + (g & 1) * 16;
        v4u a[4];
        UNDEF(a[0]); UNDEF(a[1]); UNDEF(a[2]); UNDEF(a[3]);
        a_cnt += l == 0 ? 4 : 0;
        PIN(o_h3); PIN(o_t3); PIN(o_t4); PIN(o_p4); PIN(a_ow); PIN(a_ow2); PIN(a_or); PIN(a_mw); PIN(a_ms); PIN(a_cnt);
        WAITVM(0);
        bar();
        v4u fv[4];
        frags128(a0 + (L_R1 - L_R0), a1 + (L_R1 - L_R0), fv);
        PIN(fv[0]); PIN(fv[1]); PIN(fv[2]); PIN(fv[3]);
        STAMP(7);
        if (STOPAT <= 7) goto done;
        CNT0(2); CNT0(3);
        const unsigned t3 = hint_wait(rsq, o_t3, tag);
        STAMPC(1);
        pace(t3, P3);
        STAMP(15);
        {
#pragma nounroll
            SPIN { CNT(2); get4<768>(a, o_h3, rsq); if (none((tagy(a[0][3], a[1][3], tag) | tagy(a[2][3], a[3][3], tag)) != 0u)) break; __builtin_amdgcn_s_sleep(4); }
            STAMP(8);
            unsigned arr;
            unsigned long long tm;
            asm volatile("ds_add_rtn_u32 %0, %2, %3\n\ts_memrealtime %1" : "=&v"(arr), "=&s"(tm) : "v"(a_cnt), "v"(one) : "memory");
            float m[4], al[4];
#pragma unroll
            for (int k = 0; k < 4; ++k) m[k] = dpp<0x15c>(asf(a[k][1]));
            const float M = max32(mx(mx3(m[0], m[1], m[2]), m[3]));
#pragma unroll
            for (int k = 0; k < 4; ++k) al[k] = __builtin_amdgcn_exp2f(m[k] - M);
            FENCE();
            asm volatile("s_waitcnt lgkmcnt(0)" : "+v"(arr), "+s"(tm) :: "memory");
            hint_put(rs, __builtin_amdgcn_readfirstlane(arr) == 7u ? o_t4 : OOB, tag, (unsigned)tm);
            f32x2 c01, c23, c45;
            float Ls;
#pragma unroll
            for (int k = 0; k < 4; ++k) {
                const f32x2 aa = {al[k], al[k]}, v01 = {lo16(a[k][0]), hi16(a[k][0])}, v23 = {lo16(a[k][1]), hi16(a[k][1])}, v45 = {lo16(a[k][2]), hi16(a[k][2])};
                if (k == 0) { c01 = aa * v01; c23 = aa * v23; c45 = aa * v45; Ls = al[0] * asf(a[0][2]); }
                else { c01 = __builtin_elementwise_fma(aa, v01, c01); c23 = __builtin_elementwise_fma(aa, v23, c23); c45 = __builtin_elementwise_fma(aa, v45, c45); Ls = __builtin_fmaf(al[k], asf(a[k][2]), Ls); }
            }
            const float acc[6] = {c01[0], c01[1], c23[0], c23[1], c45[0], c45[1]};
            const float v4 = sum32x2(acc[4], Ls);
            lwr<unsigned>(a_ow, pk(sum32x2(acc[0], acc[2]), sum32x2(acc[1], acc[3])));
            lwr<unsigned>(a_ow2, pk(v4, sum32(acc[5])));
            lwr<float>(a_ms, l < 32 ? M : v4);
        }
        bar();
        {
            v4u of[4];
#pragma unroll
            for (int s = 0; s < 4; ++s) of[s] = lrd<v4u>(a_or + 64 * s);
            const f32x4 mw = lrd<f32x4>(a_mw), lw = lrd<f32x4>(a_mw + 32);
            FENCE();
            const f32x4 c = gemv4(of, fv);
            const float M = mx(mx3(mw[0], mw[1], mw[2]), mw[3]);
            float be[4];
#pragma unroll
            for (int r = 0; r < 4; ++r) be[r] = __builtin_amdgcn_exp2f(mw[r] - M);
            const float rd = __builtin_amdgcn_rcpf((be[0] * lw[0] + be[1] * lw[1]) + (be[2] * lw[2] + be[3] * lw[3]));
            const float v = ((be[0] * c[0] + be[1] * c[1]) + (be[2] * c[2] + be[3] * c[3])) * rd;
            auto sv = __builtin_amdgcn_permlane16_swap(asu(v), asu(v), false, false);
            pb[0] = sv[0]; pb[2] = sv[1];
            stx(rs, o_p4, pb);
        }
        STAMP(9);
        if ((yy & 1) == 0) {
            const unsigned o1 = WS_H5 + ((32 + (tid >> 3)) * 512 + 8 * r + (tid & 7)) * 16;
            const bool two = tid < 176;
            const unsigned o2 = two ? o1 + 64 * 512 * 16 : o1;
            pace(t3, PG);
#pragma nounroll
            SPIN { get2(a[0], a[1], o1, o2, rsq); if (none((tagx(a[0], tag) | tagx(a[1], tag)) != 0u)) break; __builtin_amdgcn_s_sleep(8); }
            {
                float t = sum32(sum16x2(asf(a[0][0]) + (two ? asf(a[1][0]) : 0.f), asf(a[0][2]) + (two ? asf(a[1][2]) : 0.f)));
                t += dpp<0x128>(t);
                lwr<float>(L_RED + ((g & 1) * 8 + (l & 7)) * 32 + w * 4, t);
            }
            bar();
            STAMP(10);
            if (w == 0) {
                const f32x4 t2 = lrd<f32x4>(L_RED + ((g & 1) * 8 + (l & 7)) * 32), t4 = lrd<f32x4>(L_RED + ((g & 1) * 8 + (l & 7)) * 32 + 16);
                float glu = rbf(((t2[0] + t2[1]) + (t2[2] + t2[3])) + ((t4[0] + t4[1]) + (t4[2] + t4[3])));
                float xin = lo16(x[(g & 1) * 512 + 8 * r + (l & 7)]);
                unsigned o0 = WS_H5 + ((l >> 3) * 512 + 8 * r + (l & 7)) * 16;
                unsigned o_out = ((g & 1) * 512 + 8 * r + (l & 7)) * 2;
                PIN(glu); PIN(xin); PIN(o0); PIN(o_out);
                pace(hint_wait(rsq, o_t5, tag), P5);
                STAMP(11);
#pragma nounroll
                SPIN { CNT(3); get4s(a, o0, rsq, 65536, 131072, 196608); if (none(tagx4(a, tag) != 0u)) break; }
                STAMP(12);
                float t = sum32(sum16x2((asf(a[0][0]) + asf(a[1][0])) + (asf(a[2][0]) + asf(a[3][0])), (asf(a[0][2]) + asf(a[1][2])) + (asf(a[2][2]) + asf(a[3][2]))));
                t += dpp<0x128>(t);
                if (l < 32 && l15 < 8) *(GAS u16*)((GAS unsigned char*)out + o_out) = (u16)pk(rbf(xin + rbf(t)) + glu, 0.f);
                if (r == 0 && l == 0) __hip_atomic_store((GAS unsigned*)ws, tag, __ATOMIC_RELAXED, __HIP_MEMORY_SCOPE_AGENT);
            }
        }
    } else {
        const int pr = xc * 8 + (yy >> 1), hg = yy & 1, b = pr >> 5, sp = pr & 31;
        unsigned ep, j0;
        unsigned o_t2 = WS_T2 + (xc * 128 + (16 * hg + 2 * w) * 4 + (l & 7)) * 128;
        asm volatile("global_load_dword %0, %1, %2 sc1" : "=v"(ep) : "v"(0), "s"(ws) : "memory");
        asm volatile("buffer_load_dword %0, %1, %2, 0 offen sc1" : "=v"(j0) : "v"(o_t2), "s"(rsq) : "memory");
        unsigned qoff = WS_H2 + ((((xc & 3) * 2 + b) * 32 + 16 * hg + 2 * w) * 128 + l) * 16;
        unsigned a_q0 = L_Q + 2 * w * 1024 + (((l >> 1) ^ (2 * w)) << 4) + (l & 1) * 8, a_q1 = L_Q + (2 * w + 1) * 1024 + (((l >> 1) ^ (2 * w + 1)) << 4) + (l & 1) * 8;
        unsigned o_kv[8];
#pragma unroll
        for (int i = 0; i < 8; ++i) { o_kv[i] = (pr * 128 + 64 * hg + 8 * w + i) * 1024 + ((l ^ (8 * (w & 1) + i)) << 4); PIN(o_kv[i]); }
        PIN(qoff); PIN(a_q0); PIN(a_q1);
        asm volatile("s_waitcnt vmcnt(0)" : "+v"(ep), "+v"(j0), "+v"(cp0) :: "memory");
        const unsigned tag = mktag(ep);
        if (PREH3) {
            const unsigned o = WS_H3 + ((b * 32 + 16 * hg + 8 * (w >> 2) + (l >> 3)) * 4 + (w & 3)) * H3S + sp * 384 + (l & 7) * 16;
#pragma unroll
            for (int n = 0; n < 3; ++n) stx(rs, o + n * 128, v4u{0u, 0u, 0u, ~tag});
            WAITVM(0);
            bar0();
        }
        pace(t_in, PB);
#pragma unroll
        for (int i = 0; i < 8; ++i) glds(o_kv[i], ckv, L_R0 + (8 * w + i) * 1024);
        STAMP(1);
        bar0();
#pragma unroll
        for (int i = 0; i < 8; ++i) glds(o_kv[i] ^ 65536, ckv, L_R0 + (64 + 8 * w + i) * 1024);
        WAITVM(8);
        bar0();
        STAMP(2);
#define Q_ISSUE() \
            FENCE(); \
            WAITVM(2); \
            const v2u hv = hint_get(rsq, o_t2);        \
            STAMP(14); \
            pace(none(hv[0] != tag) ? hv[1] : hint_wait(rsq, o_t2, tag), P2); \
            STAMP(15); \
            v4u qa[4]; \
            asm volatile("buffer_load_dwordx4 %0, %4, %5, 0 offen sc1\n\tbuffer_load_dwordx4 %1, %4, %5, 0 offen offset:1024 sc1\n\t" \
                         "buffer_load_dwordx4 %2, %4, %5, 0 offen offset:2048 sc1\n\tbuffer_load_dwordx4 %3, %4, %5, 0 offen offset:3072 sc1" \
                         : "=&v"(qa[0]), "=&v"(qa[1]), "=&v"(qa[2]), "=&v"(qa[3]) : "v"(qoff), "s"(rsq) : "memory"); \
            bar0()
#define Q_LAND(n) do { \
            asm volatile("s_waitcnt vmcnt(" STR(n) ")" : "+v"(qa[0]), "+v"(qa[1]), "+v"(qa[2]), "+v"(qa[3]) :: "memory"); \
            if (__builtin_expect(!none(tagx4(qa, tag) != 0u), 0)) { _Pragma("nounroll") SPIN { __builtin_amdgcn_s_sleep(4); get4<1024>(qa, qoff, rsq); if (none(tagx4(qa, tag) != 0u)) break; } } \
            asm volatile("ds_write2_b32 %0, %2, %3 offset1:1\n\tds_write2_b32 %0, %4, %5 offset0:128 offset1:129\n\tds_write2_b32 %1, %6, %7 offset1:1\n\tds_write2_b32 %1, %8, %9 offset0:128 offset1:129" \
                         :: "v"(a_q0), "v"(a_q1), "v"(qa[0][0]), "v"(qa[0][2]), "v"(qa[1][0]), "v"(qa[1][2]), "v"(qa[2][0]), "v"(qa[2][2]), "v"(qa[3][0]), "v"(qa[3][2]) : "memory"); \
            STAMP(3); bar();   } while (0)
        unsigned t5 = 0;
        const int h = 4 * xc + (yy >> 2), cq = yy & 3;
        unsigned o_t4 = WS_T4 + ((h * 4 + cq) * 4 + (l & 3)) * 128;
        unsigned o_hg = (EXP & 2) ? 0 : WS_HG + ((4 * (w & 3) + g) * 1376 + 16 * min(un, 85) + l15) * 16;
        if (w >= 4) {
            unsigned a_r[4];
#pragma unroll
            for (int j = 0; j < 4; ++j) a_r[j] = l15 * 1024 + (((4 * j + g) ^ l15) << 4);
            const unsigned kb = L_R0 + (w - 4) * 16384;
            v4u cr[32], qf[16];
#pragma unroll
            for (int s = 0; s < 16; ++s) cr[s] = lrd<v4u>(a_r[s & 3] + kb + (s >> 2) * 256);
            unsigned a_mxw = L_MX + l15 * 16 + (w - 4) * 4, a_mxr = L_MX + l15 * 16;
            unsigned a_pw = L_P + l15 * 256 + (((2 * (w - 4) + (g >> 1)) ^ l15) << 4) + (g & 1) * 8;
            __builtin_amdgcn_s_setprio(1);
            PIN(a_mxw); PIN(a_mxr); PIN(a_pw); PIN(o_hg);
            Q_ISSUE();
#pragma unroll
            for (int s = 0; s < 16; ++s) cr[16 + s] = lrd<v4u>(a_r[s & 3] + kb + 65536 + (s >> 2) * 256);
            FENCE();
            Q_LAND(0);
            STAMP(4);
#pragma unroll
            for (int s = 0; s < 16; ++s) qf[s] = lrd<v4u>(a_r[s & 3] + L_Q + (s >> 2) * 256);
            FENCE();
            f32x4 s1 = ZF, s2 = ZF;
#pragma unroll
            for (int s = 0; s < 16; ++s) { s1 = mfma32(cr[s], qf[s], s1); s2 = mfma32(cr[16 + s], qf[s], s2); }
            asm volatile("s_nop 7" : "+v"(s1), "+v"(s2));
            lwr<float>(a_mxw, max32(max16(mx(mx3(mx3(s1[0], s1[1], s1[2]), s1[3], s2[0]), mx3(s2[1], s2[2], s2[3])))));
            bar();
            STAMP(12);
            const f32x4 m0 = lrd<f32x4>(a_mxr);
            const float mr = mx(mx3(m0[0], m0[1], m0[2]), m0[3]);
            v2u p1 = {pk(__builtin_amdgcn_exp2f(s1[0] - mr), __builtin_amdgcn_exp2f(s1[1] - mr)), pk(__builtin_amdgcn_exp2f(s1[2] - mr), __builtin_amdgcn_exp2f(s1[3] - mr))};
            lwr<v2u>(a_pw, p1);
            v2u p2 = {pk(__builtin_amdgcn_exp2f(s2[0] - mr), __builtin_amdgcn_exp2f(s2[1] - mr)), pk(__builtin_amdgcn_exp2f(s2[2] - mr), __builtin_amdgcn_exp2f(s2[3] - mr))};
            lwr<v2u>(a_pw ^ 128, p2);
            bar();
            STAMP(5);
            PIN(p1); PIN(p2);
            {
                const unsigned q0 = p1[0], q1 = p1[1], q2 = p2[0], q3 = p2[1];
                lwr<float>(a_mxw + 256, sum32(sum16(((lo16(q0) + hi16(q0)) + (lo16(q1) + hi16(q1))) + ((lo16(q2) + hi16(q2)) + (lo16(q3) + hi16(q3))))));
            }
            bar();
            __builtin_amdgcn_s_setprio(0);
            t5 = now();
            if (PH) pace(t5, PH);
            v4u ga, gb;
            asm volatile("buffer_load_dwordx4 %0, %3, %6, 0 offen sc1\n\tbuffer_load_dwordx4 %1, %4, %6, 0 offen sc1\n\tbuffer_load_dword %2, %5, %6, 0 offen sc1\n\ts_waitcnt vmcnt(0)"
                         : "=&v"(ga), "=&v"(gb), "=&v"(j0), "+v"(o_hg) : "v"(o_hg + 16 * 1376 * 16), "v"(o_t4), "s"(rsq) : "memory");
            if (un < 86) {
                if (!(EXP & 2) && __builtin_expect(!none((tagx(ga, tag) | tagx(gb, tag)) != 0u), 0)) {
#pragma nounroll
                    SPIN { __builtin_amdgcn_s_sleep(16); get2(ga, gb, o_hg, o_hg + 16 * 1376 * 16, rsq); if (none((tagx(ga, tag) | tagx(gb, tag)) != 0u)) break; }
                }
                lwr<float>(L_RED + (l & 31) * 32 + (w - 4) * 4, sum32(sum16x2(asf(ga[0]), asf(ga[2]))));
                lwr<float>(L_RED + (l & 31) * 32 + w * 4, sum32(sum16x2(asf(gb[0]), asf(gb[2]))));
            }
            pace(t5, PW);
#pragma unroll
            for (int i = 0; i < 8; ++i) {
                const unsigned ch = l15 ^ ((g << 2) | (i & 3));
                glds((h * 128 + 32 * (w - 4) + 4 * i + g) * 1024 + cq * 128 + (ch >> 3) * 512 + (ch & 7) * 16, wo, L_R1 + (32 * (w - 4) + 4 * i) * 256);
            }
        } else {
            unsigned a_t[2][4];
            {
                const unsigned q = l15 >> 2, p = l & 3;
#pragma unroll
                for (int j = 0; j < 8; ++j) {
                    const unsigned r0 = 8 * g + 4 * (j >> 2) + q, ch = 16 * w + 4 * p + (j & 3);
                    a_t[j >> 2][j & 3] = L_R0 + r0 * 1024 + ((ch ^ (r0 & 15)) << 4) + (p >> 1) * 8;
                }
            }
            v4u ct[8][4];
#pragma unroll
            for (int t = 0; t < 8; ++t)
#pragma unroll
                for (int s = 0; s < 2; ++s) ct[t][s] = trd2((a_t[0][t >> 1] ^ ((t & 1) * 8)) + s * 32768, (a_t[1][t >> 1] ^ ((t & 1) * 8)) + s * 32768);
            unsigned a_pr[4], a_mxr = L_MX + l15 * 16;
#pragma unroll
            for (int j = 0; j < 4; ++j) { a_pr[j] = L_P + l15 * 256 + (((4 * j + g) ^ l15) << 4); PIN(a_pr[j]); }
            unsigned o_t3 = l < 4 ? WS_T3 + (((4 * hg + l) * 4 + w) * 64 + pr) * 128 : OOB;
            unsigned o_h3 = WS_H3 + ((b * 32 + 16 * hg + (l >> 3)) * 4 + w) * H3S + sp * 384 + (l & 7) * 16, o_h3b = o_h3 + 8 * 4 * H3S;
            unsigned a_sw = L_R2 + w * 8192 + l15 * 144 + g * 16, a_sr = L_R2 + w * 8192 + (l >> 3) * 144 + (l & 7) * 16;
            PIN(a_sw); PIN(a_sr);
            unsigned o_wd[4], a_lr = L_MX + 256 + l15 * 16;
#pragma unroll
            for (int i = 0; i < 4; ++i) { const unsigned row = 4 * w + i; o_wd[i] = (EXP & 1) ? 0 : (16 * min(un, 85) + row) * 1024 + ((l ^ ((row & 7) << 1)) << 4); PIN(o_wd[i]); }
            PIN(a_mxr); PIN(o_t3); PIN(o_h3); PIN(o_h3b); PIN(a_lr);
            Q_ISSUE();
            v4u wst[4];
            asm volatile("global_load_dwordx4 %0, %4, %8\n\tglobal_load_dwordx4 %1, %5, %8\n\tglobal_load_dwordx4 %2, %6, %8\n\tglobal_load_dwordx4 %3, %7, %8"
                         : "=&v"(wst[0]), "=&v"(wst[1]), "=&v"(wst[2]), "=&v"(wst[3]) : "v"(o_wd[0]), "v"(o_wd[1]), "v"(o_wd[2]), "v"(o_wd[3]), "s"(wd) : "memory");
#pragma unroll
            for (int t = 0; t < 8; ++t)
#pragma unroll
                for (int s = 2; s < 4; ++s) ct[t][s] = trd2((a_t[0][t >> 1] ^ ((t & 1) * 8)) + s * 32768, (a_t[1][t >> 1] ^ ((t & 1) * 8)) + s * 32768);
            FENCE();
            Q_LAND(4);
            STAMP(4);
            bar0();
            const f32x4 m0 = lrd<f32x4>(a_mxr);
            float mr = mx(mx3(m0[0], m0[1], m0[2]), m0[3]);
            PIN(mr);
            bar();
            STAMP(5);
            v4u pf[4];
#pragma unroll
            for (int s = 0; s < 4; ++s) pf[s] = lrd<v4u>(a_pr[s]);
            if (HP == 0) hint_put(rs, o_t3, tag);
            FENCE();
            f32x4 o[8];
#define M0_(t) asm volatile("v_mfma_f32_16x16x32_bf16 %0, %1, %2, 0" : "=&v"(o[t]) : "v"(ct[t][0]), "v"(pf[0]))
#define MM(t, s) asm volatile("v_mfma_f32_16x16x32_bf16 %0, %1, %2, %0" : "+v"(o[t]) : "v"(ct[t][s]), "v"(pf[s]))
#define RDY(t) PIN(o[t])
#define SEQ() asm volatile("" ::: "memory")
#define CV(d, t, i) do { d = pk(o[t][i], o[t][i + 1]); PIN(d); } while (0)
#define PUT(n, k, A, B, C) lwr<v4u>(a_sw + (n) * 2304 + (k) * 64, v4u{A, B, C, tag})
#define GETL(n) do { x0 = lrd<v4u>(a_sr + (n) * 2304); x1 = lrd<v4u>(a_sr + (n) * 2304 + 8 * 144); } while (0)
#define OUTL(n) do { stx(rs, o_h3 + (n) * 128, x0); stx(rs, o_h3b + (n) * 128, x1); } while (0)
            unsigned d0, d1, d2, d3, d4, d5;
            v4u x0, x1;
            M0_(0); M0_(1); MM(0, 1); MM(1, 1); MM(0, 2); MM(1, 2); MM(0, 3); MM(1, 3);
            M0_(2); MM(2, 1); MM(2, 2); MM(2, 3);
            M0_(3); MM(3, 1); MM(3, 2);
            RDY(0); CV(d0, 0, 0); CV(d1, 0, 2);
            MM(3, 3); RDY(1); CV(d2, 1, 0); CV(d3, 1, 2);
            M0_(4); PUT(0, 0, d0, d1, d2); SEQ();
            MM(4, 1); RDY(2); CV(d4, 2, 0); CV(d5, 2, 2);
            MM(4, 2); PUT(0, 1, d3, d4, d5); GETL(0); SEQ();
            MM(4, 3); RDY(3); CV(d0, 3, 0); CV(d1, 3, 2);
            M0_(5);
            bar0();
            const f32x4 l0 = lrd<f32x4>(a_lr);
            MM(5, 1); MM(5, 2); MM(5, 3);
            RDY(4); CV(d2, 4, 0); CV(d3, 4, 2);
            STAMP2(3);
            M0_(6); OUTL(0); if (HP == 1) hint_put(rs, o_t3, tag); PUT(1, 0, d0, d1, d2); SEQ();
            STAMP2(4);
            MM(6, 1);
            float lsum = (l0[0] + l0[1]) + (l0[2] + l0[3]);
            PIN(lsum);
            MM(6, 2); MM(6, 3);
            RDY(5); CV(d4, 5, 0); CV(d5, 5, 2);
            M0_(7); PUT(1, 1, d3, d4, d5); GETL(1); SEQ();
            MM(7, 1); MM(7, 2); MM(7, 3);
            asm volatile("s_nop 1" : "+v"(o[6]));
            CV(d0, 6, 0); CV(d1, 6, 2);
            STAMP2(14);
            OUTL(1); if (HP == 2) hint_put(rs, o_t3, tag); SEQ();
            STAMP2(15);
            asm volatile("s_nop 3" : "+v"(o[7]));
            CV(d2, 7, 0); CV(d3, 7, 2);
            PUT(2, 0, d0, d1, d2); PUT(2, 1, d3, asu(mr), asu(lsum)); GETL(2); OUTL(2); if (HP == 3) hint_put(rs, o_t3, tag);
            FENCE();
            STAMP(6);
            asm volatile("s_waitcnt vmcnt(7)" : "+v"(wst[0]), "+v"(wst[1]), "+v"(wst[2]), "+v"(wst[3]) :: "memory");
#pragma unroll
            for (int i = 0; i < 4; ++i) lwr<v4u>(L_R0 + (4 * w + i) * 1024 + l * 16, wst[i]);
        }
        if (STOPAT <= 7) goto done;
        bar();
        if (un < 86) {
            if (w == 4) {
                const f32x4 gp = lrd<f32x4>(L_RED + (l & 31) * 32), up = lrd<f32x4>(L_RED + (l & 31) * 32 + 16);
                const float gg = rbf((gp[0] + gp[1]) + (gp[2] + gp[3])), uu = rbf((up[0] + up[1]) + (up[2] + up[3]));
                const float sg = rbf(__builtin_amdgcn_rcpf(1.f + __builtin_amdgcn_exp2f(gg * -1.4426950408889634f)));
                lwr<u16>(L_HH + (l & 31) * 2, (u16)pk(sg * uu, 0.f));
            }
            bar();
            const v2u ha = lrd<v2u>(L_HH + (l & 1) * 32 + g * 8);
            const unsigned q = l15 >> 2, p = l & 3, row = 4 * g + q;
            f32x4 o[4];
#pragma unroll
            for (int t = 0; t < 4; ++t) {
                const unsigned ch = 8 * w + 2 * t + (p >> 1);
                o[t] = mfma16(ha, trd(L_R0 + row * 1024 + ((ch ^ ((row & 7) << 1)) << 4) + (p & 1) * 8), ZF);
            }
            const float o0 = g == 0 ? o[0][0] : g == 1 ? o[1][0] : g == 2 ? o[2][0] : o[3][0];
            const float o1 = g == 0 ? o[0][1] : g == 1 ? o[1][1] : g == 2 ? o[2][1] : o[3][1];
            stx(rs, WS_H5 + ((32 + un) * 512 + 64 * w + l) * 16, v4u{asu(o0), tag, asu(o1), tag});
        }
        STAMP(7);
        {
            unsigned a0, a1;
            {
                const unsigned q = l15 >> 2, p = l & 3, ch = 2 * w + (p >> 1);
                a0 = L_R1 + (8 * g + q) * 256 + 16 * (ch ^ ((q << 2) | ((2 * g) & 3))) + 8 * (p & 1);
                a1 = L_R1 + (8 * g + q + 4) * 256 + 16 * (ch ^ ((q << 2) | ((2 * g + 1) & 3))) + 8 * (p & 1);
            }
            unsigned o_g4 = WS_H4 + ((h * 4 + g) * 128 + 16 * w + l15) * 16;
            unsigned o_t5 = tid < 16 ? WS_T5 + ((8 * (cq + 4 * (tid >> 3)) + (tid & 7)) * 32 + h) * 128 : OOB;
            unsigned o_h5 = l < 16 ? WS_H5 + (h * 512 + 64 * (cq + 4 * (w >> 2)) + 16 * (w & 3) + l) * 16 : OOB;
            unsigned a_qw = L_QS + (g & 1) * 272 + (16 * w + l15) * 2, a_qr = L_QS + (l & 1) * 272 + 16 * g;
            v4u pb = {0u, tag, 0u, tag}, fw[4], ga;
            WAITVM(0);
            bar();
            frags128(a0, a1, fw);
            PIN(fw[0]); PIN(fw[1]); PIN(fw[2]); PIN(fw[3]); PIN(o_g4); PIN(o_t5); PIN(o_h5); PIN(a_qw); PIN(a_qr); PIN(pb);
            STAMP(10);
            if (w == 4) pace(hint_wait(rsq, o_t4, tag), P4);
            bar0();
            STAMP(11);
            asm volatile("buffer_load_dwordx4 %0, %1, %2, 0 offen sc1" : "=&v"(ga) : "v"(o_g4), "s"(rsq) : "memory");
            hint_put(rs, o_t5, tag);
            asm volatile("s_waitcnt vmcnt(1)" : "+v"(ga) :: "memory");
            if (__builtin_expect(!none(tagx(ga, tag) != 0u), 0)) {
#pragma nounroll
                SPIN { __builtin_amdgcn_s_sleep(2); get1(ga, o_g4, rsq); if (none(tagx(ga, tag) != 0u)) break; }
            }
            STAMP(8);
            lwr<u16>(a_qw, (u16)pk(sum32(sum16x2(asf(ga[0]), asf(ga[2]))), 0.f));
            bar();
            v4u cf[4];
#pragma unroll
            for (int s = 0; s < 4; ++s) cf[s] = lrd<v4u>(a_qr + 64 * s);
            FENCE();
            const f32x4 acc = gemv4(cf, fw);
            pb[0] = asu(acc[0]); pb[2] = asu(acc[1]);
            stx(rs, o_h5, pb);
        }
        STAMP(9);
    }
done:
    STAMP(13);
}

extern "C" void kernel_launch(void* const* d_in, const int* in_sizes, int n_in, void* d_out, int out_size, void* d_ws, size_t ws_size,
                              hipStream_t stream) {
    (void)in_sizes; (void)n_in; (void)out_size; (void)ws_size;
    (void)hipFuncSetAttribute((const void*)mla_glu_decode, hipFuncAttributeMaxDynamicSharedMemorySize, L_END);
    mla_glu_decode<<<256, 512, L_END, stream>>>((const u16*)d_in[0], (const u16*)d_in[1], (const u16*)d_in[2], (const u16*)d_in[3], (const u16*)d_in[4],
                                               (const u16*)d_in[5], (const u16*)d_in[7], (const u16*)d_in[8], (const u16*)d_in[9], (u16*)d_out,
                                               (unsigned char*)d_ws);
}
```

```cpp
#include <hip/hip_runtime.h>
#include <hip/hip_bf16.h>

typedef unsigned short u16;
typedef unsigned v4u __attribute__((ext_vector_type(4)));
typedef unsigned v2u __attribute__((ext_vector_type(2)));
typedef float f32x4 __attribute__((ext_vector_type(4)));
typedef float f32x2 __attribute__((ext_vector_type(2)));
typedef short s16x4 __attribute__((ext_vector_type(4)));
typedef __bf16 bf16x8 __attribute__((ext_vector_type(8)));
typedef __bf16 bf16x2 __attribute__((ext_vector_type(2)));
#define LAS __attribute__((address_space(3)))
#define GAS __attribute__((address_space(1)))
#define DEVI __device__ __forceinline__

#define PB 60
#define P2 100
#define P3 60
#define P4 55
#define P5 50
#define PG 105
#define PW 60
#define PA 0
#define CODEB 14080
#define EXP 0
#define PH 0
#define H3S 12288
#define XMAP 0
#define VDEF 1
#define HP 2
#define PREH3 0
#define STOPAT 99

constexpr int WS_T2 = 4096;
constexpr int WS_T3 = WS_T2 + 8 * 128 * 128;
constexpr int WS_T5 = WS_T3 + 8 * 64 * 4 * 128;
constexpr int WS_T4 = WS_T5 + 64 * 32 * 128;
constexpr int WS_H1 = WS_T4 + 32 * 16 * 128;
constexpr int WS_H4 = WS_H1 + 32 * 4 * 128 * 16;
constexpr int WS_H2 = WS_H4 + 32 * 4 * 128 * 16;
constexpr int WS_HG = WS_H2 + 4 * 2 * 32 * 128 * 16;
constexpr int WS_H5 = WS_HG + 2 * 16 * 1376 * 16;
constexpr int WS_H3 = WS_H5 + 118 * 512 * 16;
constexpr int WS_END = WS_H3 + 2 * 32 * 4 * H3S;
constexpr int WS_STAMP = 16 << 20;
constexpr int L_Q = 0;
constexpr int L_P = 16384;
constexpr int L_QS = L_P + 4096;
constexpr int L_OW = L_QS + 1024;
constexpr int L_MW = L_OW + 2304;
constexpr int L_MX = L_MW + 64;
constexpr int L_HH = L_MX + 512;
constexpr int L_TAG = L_HH + 64;
constexpr int L_JUNK = L_TAG + 64;
constexpr int L_RED = L_JUNK + 256;
constexpr int L_XN = 28672;
constexpr int L_R0 = 32768, L_R1 = 65536, L_R2 = 98304, L_R3 = 131072;
constexpr int L_END = 163840;
static_assert(L_RED + 1024 <= L_XN && L_XN + 2048 <= L_R0, "lds");
constexpr int SPIN_CAP = 1 << 16;

DEVI unsigned pk(float lo, float hi) { bf16x2 r = {(__bf16)lo, (__bf16)hi}; return __builtin_bit_cast(unsigned, r); }
DEVI float rbf(float f) { return (float)(__bf16)f; }
DEVI float lo16(unsigned w) { return __builtin_bit_cast(float, w << 16); }
DEVI float hi16(unsigned w) { return __builtin_bit_cast(float, w & 0xffff0000u); }
DEVI float asf(unsigned w) { return __builtin_bit_cast(float, w); }
DEVI unsigned asu(float w) { return __builtin_bit_cast(unsigned, w); }
DEVI float mx(float a, float b) { float r; asm("v_max_f32 %0, %1, %2" : "=v"(r) : "v"(a), "v"(b)); return r; }
DEVI float mx3(float a, float b, float c) { float r; asm("v_max3_f32 %0, %1, %2, %3" : "=v"(r) : "v"(a), "v"(b), "v"(c)); return r; }
DEVI f32x4 mfma32(v4u a, v4u b, f32x4 c) { return __builtin_amdgcn_mfma_f32_16x16x32_bf16(__builtin_bit_cast(bf16x8, a), __builtin_bit_cast(bf16x8, b), c, 0, 0, 0); }
DEVI f32x4 mfma16(v2u a, v2u b, f32x4 c) { return __builtin_amdgcn_mfma_f32_16x16x16bf16_1k(__builtin_bit_cast(s16x4, a), __builtin_bit_cast(s16x4, b), c, 0, 0, 0); }
template <class T> DEVI T lrd(unsigned off) { return *(LAS T*)(size_t)off; }
template <class T> DEVI void lwr(unsigned off, T v) { *(LAS T*)(size_t)off = v; }
DEVI v2u trd(unsigned off) { return __builtin_bit_cast(v2u, __builtin_amdgcn_ds_read_tr16_b64_v4i16((LAS s16x4*)(size_t)off)); }
DEVI v4u trd2(unsigned o0, unsigned o1) { v2u a = trd(o0), b = trd(o1); return v4u{a.x, a.y, b.x, b.y}; }
DEVI float sum16(float x) { auto r = __builtin_amdgcn_permlane16_swap(asu(x), asu(x), false, false); return asf(r[0]) + asf(r[1]); }
DEVI float sum32(float x) { auto r = __builtin_amdgcn_permlane32_swap(asu(x), asu(x), false, false); return asf(r[0]) + asf(r[1]); }
DEVI float max16(float x) { auto r = __builtin_amdgcn_permlane16_swap(asu(x), asu(x), false, false); return mx(asf(r[0]), asf(r[1])); }
DEVI float max32(float x) { auto r = __builtin_amdgcn_permlane32_swap(asu(x), asu(x), false, false); return mx(asf(r[0]), asf(r[1])); }
DEVI float odd16(float x) { auto r = __builtin_amdgcn_permlane16_swap(asu(x), asu(x), false, false); return asf(r[1]); }
DEVI float sum16x2(float x, float y) { auto r = __builtin_amdgcn_permlane16_swap(asu(x), asu(y), false, false); return asf(r[0]) + asf(r[1]); }
DEVI float sum32x2(float x, float y) { auto r = __builtin_amdgcn_permlane32_swap(asu(x), asu(y), false, false); return asf(r[0]) + asf(r[1]); }
template <int CTRL> DEVI float dpp(float x) { return asf((unsigned)__builtin_amdgcn_update_dpp(0, (int)asu(x), CTRL, 0xf, 0xf, true)); }
DEVI float wavesum(float x) {
    x += dpp<0xB1>(x); x += dpp<0x4E>(x); x += dpp<0x141>(x); x += dpp<0x140>(x);
    return sum32(sum16(x));
}
DEVI float dot2one(unsigned p, float c) { const unsigned one = 0x3f803f80u; return __builtin_amdgcn_fdot2_f32_bf16(__builtin_bit_cast(bf16x2, p), __builtin_bit_cast(bf16x2, one), c, false); }
#define STR_(x) #x
#define STR(x) STR_(x)
#define WAITVM(n) asm volatile("s_waitcnt vmcnt(" STR(n) ")" ::: "memory")
#define PIN(x) asm volatile("" : "+v"(x))
#define MFMA_SETTLE(x) asm volatile("s_nop 7" : "+v"(x))
#define UNDEF(x) asm volatile("" : "=v"(x))
#define FENCE() __builtin_amdgcn_sched_barrier(0)
#define SPIN for (int it_ = SPIN_CAP; it_ > 0; --it_)
DEVI void bar() { asm volatile("s_waitcnt lgkmcnt(0)\n\ts_barrier" ::: "memory"); }
DEVI void bar0() { asm volatile("s_barrier" ::: "memory"); }
#define USE_NT 0
template <bool NT = false> DEVI void glds(unsigned voff, const void* sbase, unsigned lds) {
    unsigned keep;
    if (NT && USE_NT) asm volatile("s_mov_b32 %0, m0\n\ts_mov_b32 m0, %3\n\ts_nop 0\n\tglobal_load_lds_dwordx4 %1, %2 nt\n\ts_mov_b32 m0, %0" : "=&s"(keep) : "v"(voff), "s"(sbase), "s"(lds) : "memory");
    else asm volatile("s_mov_b32 %0, m0\n\ts_mov_b32 m0, %3\n\ts_nop 0\n\tglobal_load_lds_dwordx4 %1, %2\n\ts_mov_b32 m0, %0" : "=&s"(keep) : "v"(voff), "s"(sbase), "s"(lds) : "memory");
}
typedef __amdgpu_buffer_rsrc_t rsrc_t;
constexpr unsigned OOB = 0x80000000u;
DEVI void stx(rsrc_t rs, unsigned off, v4u v) { __builtin_amdgcn_raw_buffer_store_b128(v, rs, off, 0, 16); }
DEVI bool none(bool bad) { asm volatile("" ::: "memory"); return __builtin_amdgcn_ballot_w64(bad) == 0ull; }
DEVI unsigned tagx(v4u a, unsigned tag) { unsigned r = (a[1] ^ tag) | (a[3] ^ tag); asm("" : "+v"(r)); return r; }
DEVI unsigned tagy(unsigned a, unsigned b, unsigned tag) { unsigned r = (a ^ tag) | (b ^ tag); asm("" : "+v"(r)); return r; }
DEVI unsigned tagx4(const v4u (&a)[4], unsigned tag) { return (tagx(a[0], tag) | tagx(a[1], tag)) | (tagx(a[2], tag) | tagx(a[3], tag)); }
DEVI void get1(v4u& a, unsigned off, v4u rq) { asm volatile("buffer_load_dwordx4 %0, %1, %2, 0 offen sc1\n\ts_waitcnt vmcnt(0)" : "+&v"(a) : "v"(off), "s"(rq) : "memory"); }
DEVI void get2(v4u& a, v4u& b, unsigned oa, unsigned ob, v4u rq) {
    asm volatile("buffer_load_dwordx4 %0, %2, %4, 0 offen sc1\n\tbuffer_load_dwordx4 %1, %3, %4, 0 offen sc1\n\ts_waitcnt vmcnt(0)" : "+&v"(a), "+&v"(b) : "v"(oa), "v"(ob), "s"(rq) : "memory");
}
template <int ST> DEVI void get4(v4u (&a)[4], unsigned off, v4u rq) {
    asm volatile("buffer_load_dwordx4 %0, %4, %5, 0 offen sc1\n\tbuffer_load_dwordx4 %1, %4, %5, 0 offen offset:%6 sc1\n\t"
                 "buffer_load_dwordx4 %2, %4, %5, 0 offen offset:%7 sc1\n\tbuffer_load_dwordx4 %3, %4, %5, 0 offen offset:%8 sc1\n\ts_waitcnt vmcnt(0)"
                 : "+&v"(a[0]), "+&v"(a[1]), "+&v"(a[2]), "+&v"(a[3]) : "v"(off), "s"(rq), "i"(ST), "i"(2 * ST), "i"(3 * ST) : "memory");
}
DEVI void get4s(v4u (&a)[4], unsigned off, v4u rq, unsigned s1, unsigned s2, unsigned s3) {
    asm volatile("buffer_load_dwordx4 %0, %4, %5, 0 offen sc1\n\tbuffer_load_dwordx4 %1, %4, %5, %6 offen sc1\n\t"
                 "buffer_load_dwordx4 %2, %4, %5, %7 offen sc1\n\tbuffer_load_dwordx4 %3, %4, %5, %8 offen sc1\n\ts_waitcnt vmcnt(0)"
                 : "+&v"(a[0]), "+&v"(a[1]), "+&v"(a[2]), "+&v"(a[3]) : "v"(off), "s"(rq), "s"(s1), "s"(s2), "s"(s3) : "memory");
}
DEVI unsigned now() { return (unsigned)__builtin_amdgcn_s_memrealtime(); }
DEVI void hint_put(rsrc_t rs, unsigned off, unsigned tag, unsigned t) { __builtin_amdgcn_raw_buffer_store_b64(v2u{tag, t}, rs, off, 0, 16); }
DEVI void hint_put(rsrc_t rs, unsigned off, unsigned tag) { hint_put(rs, off, tag, now()); }
DEVI v2u hint_get(v4u rq, unsigned off) { v2u v; asm volatile("buffer_load_dwordx2 %0, %1, %2, 0 offen sc1\n\ts_waitcnt vmcnt(0)" : "=&v"(v) : "v"(off), "s"(rq) : "memory"); return v; }
DEVI void pace(unsigned t, unsigned lead) {
#pragma nounroll
    for (int it = 256; it > 0; --it) { if (none(now() - t < lead)) break; }
}
DEVI unsigned hint_wait(v4u rq, unsigned off, unsigned tag) {
    v2u v;
#pragma nounroll
    SPIN { v = hint_get(rq, off); if (none(v[0] != tag)) break; __builtin_amdgcn_s_sleep(1); }
    return v[1];
}
DEVI unsigned mktag(unsigned ep) { return ep + 1u; }

#define STAMP(i)

#define CNT0(i)
#define CNT(i)
#define STAMPC(i)
#define STAMP2(i)
DEVI void frags128(unsigned a0, unsigned a1, v4u (&f)[4]) {
#pragma unroll
    for (int s = 0; s < 4; ++s) f[s] = trd2(a0 + s * 8192, a1 + s * 8192);
}
DEVI f32x4 gemv4(const v4u (&a)[4], const v4u (&b)[4]) {
    f32x4 c = {0.f, 0.f, 0.f, 0.f};
#pragma unroll
    for (int s = 0; s < 4; ++s) c = mfma32(a[s], b[s], c);
    return c;
}
DEVI void layernorm(v4u xv, int w, int l) {
    float v[8];
#pragma unroll
    for (int j = 0; j < 4; ++j) { v[2 * j] = lo16(xv[j]); v[2 * j + 1] = hi16(xv[j]); }
    const float mean = wavesum(((v[0] + v[1]) + (v[2] + v[3])) + ((v[4] + v[5]) + (v[6] + v[7]))) * (1.f / 512.f);
    float sq = 0.f;
#pragma unroll
    for (int j = 0; j < 8; ++j) { const float d = v[j] - mean; sq += d * d; }
    const float var = wavesum(sq) * (1.f / 512.f);
    const float mb = rbf(mean), ri = __builtin_amdgcn_rcpf(rbf(__builtin_amdgcn_sqrtf(rbf(rbf(var) + 1e-5f))));
    v4u o;
#pragma unroll
    for (int j = 0; j < 4; ++j) o[j] = pk(rbf(v[2 * j] - mb) * ri, rbf(v[2 * j + 1] - mb) * ri);
    lwr<v4u>(L_XN + w * 1024 + l * 16, o);
}

__global__ void __launch_bounds__(512, 2)
mla_glu_decode(const u16* __restrict__ x, const u16* __restrict__ ckv, const u16* __restrict__ wq, const u16* __restrict__ wuk,
               const u16* __restrict__ wuv, const u16* __restrict__ wo, const u16* __restrict__ wg, const u16* __restrict__ wu,
               const u16* __restrict__ wd, u16* __restrict__ out, unsigned char* __restrict__ ws) {
    STAMP(0);
    const int tid = threadIdx.x, l = tid & 63, w = __builtin_amdgcn_readfirstlane(tid >> 6);
    const int id = blockIdx.x, ai = (id & 3) * 32 + (id >> 3);
    const int xc = XMAP ? ai >> 4 : id & 7, yy = XMAP ? ai & 15 : (id >> 3) & 15, y = XMAP ? (id & 4) * 4 : id >> 3;
    const int un = yy * 8 + xc;
    const unsigned g = l >> 4, l15 = l & 15;
    unsigned cp0 = 0;
    if (w == 7) {
        unsigned long long pc;
        asm volatile("s_getpc_b64 %0" : "=s"(pc));
        pc &= ~255ull;
        asm volatile("global_load_dword %0, %1, %2" : "=&v"(cp0) : "v"(min(((id >> 3) + 32 * (l & 3)) * 128, CODEB - 128)), "s"(pc) : "memory");
    }
    unsigned vb = (128 * (yy & 3) + 16 * w + g) * 8192 + (4 * xc + (yy >> 2)) * 256, sw[4], o_q[4];
#pragma unroll
    for (int i = 0; i < 4; ++i) { sw[i] = (l15 ^ ((g << 2) | i)) << 4; o_q[i] = vb + i * 32768 + sw[i]; PIN(o_q[i]); }
    asm volatile("" :: "s"(x), "s"(ckv), "s"(wq), "s"(wuk), "s"(wuv), "s"(wo), "s"(wg), "s"(wu), "s"(wd), "s"(out), "s"(ws));
    const rsrc_t rs = __builtin_amdgcn_make_buffer_rsrc(ws, 0, WS_END, 0x00020000);
    const v4u rsq = {(unsigned)(size_t)ws, (unsigned)((size_t)ws >> 32) & 0xffffu, (unsigned)WS_END, 0x00020000u};
    const f32x4 ZF = {0.f, 0.f, 0.f, 0.f};

    if (__builtin_expect(y < 16, 1)) {
        const int h = 4 * xc + (yy >> 2), cq = yy & 3;
        v4u xv; unsigned ep;
        if (w < 2) {
            asm volatile("global_load_dwordx4 %0, %1, %2" : "=v"(xv) : "v"(w * 1024 + l * 16), "s"(x) : "memory");
            asm volatile("global_load_dword %0, %1, %2 sc1" : "=v"(ep) : "v"(0), "s"(ws) : "memory");
        }
#pragma unroll
        for (int i = 0; i < 4; ++i) glds<true>(o_q[i], wq, L_R0 + (16 * w + 4 * i) * 256);
        bar0();
#pragma unroll
        for (int i = 0; i < 4; ++i) glds<true>(vb + i * 32768 + ((l15 ^ (4 * i + g)) << 4), wuk, L_R1 + (16 * w + 4 * i) * 256);
        STAMP(1);
        if (w < 2) {
            asm volatile("s_waitcnt vmcnt(8)" : "+v"(xv), "+v"(ep) :: "memory");
            layernorm(xv, w, l);
            lwr<v4u>(L_TAG, v4u{mktag(ep), 0u, 0u, 0u});
        }
        bar();
        const unsigned tag = lrd<unsigned>(L_TAG);
        unsigned o_p1 = l < 16 ? WS_H1 + ((h * 4 + cq) * 128 + 16 * w + l) * 16 : OOB;
        unsigned o_g1 = WS_H1 + ((h * 4 + g) * 128 + 16 * w + l15) * 16;
        unsigned o_h2 = WS_H2 + ((((w & 3) * 2 + (l >> 5)) * 32 + h) * 128 + 32 * cq + (l & 31)) * 16;
        unsigned a_2w = l15 < 2 ? L_RED + l15 * 256 + (4 * w + g) * 8 : L_JUNK + (l & 31) * 8, a_2r = L_RED + l * 8;
        PIN(a_2w); PIN(a_2r);
        unsigned o_t2 = l < 8 ? WS_T2 + (l * 128 + h * 4 + cq) * 128 : OOB;
        unsigned a0, a1;
        {
            const unsigned q = l15 >> 2, p = l & 3, ch = 2 * w + (p >> 1);
            a0 = L_R0 + (8 * g + q) * 256 + 16 * (ch ^ ((q << 2) | ((2 * g) & 3))) + 8 * (p & 1);
            a1 = L_R0 + (8 * g + q + 4) * 256 + 16 * (ch ^ ((q << 2) | ((2 * g + 1) & 3))) + 8 * (p & 1);
        }
        unsigned a_qw = L_QS + (g & 1) * 272 + (16 * w + l15) * 2;
        unsigned a_qr = L_QS + (l & 1) * 272 + 16 * g;
        unsigned a_uk[4], o_gu[3], a_cnt = l == 0 ? L_TAG + 8 : L_JUNK + l * 4, one = 1u;
        v4u xf[4];
#pragma unroll
        for (int s = 0; s < 4; ++s) {
            xf[s] = lrd<v4u>(L_XN + (l & 1) * 1024 + (128 * cq + 32 * s + 8 * g) * 2);
            a_uk[s] = L_R1 + (16 * w + l15) * 256 + (((4 * s + g) ^ l15) << 4);
            PIN(xf[s]); PIN(a_uk[s]);
        }
        const int mat = un >> 6, mp = (un >> 2) & 15, fp = un & 3;
#pragma unroll
        for (int i = 0; i < 3; ++i) {
            const unsigned pc = min(8 * i + w, 21), idx = min(64 * pc + l, 1375u), row = idx / 43u, ch = idx - row * 43u;
            o_gu[i] = (32 * mp + row) * 2752 + fp * 688 + ch * 16; PIN(o_gu[i]);
        }
        v4u pb = {0u, tag, 0u, tag}, ga;
        PIN(a0); PIN(a1); PIN(o_p1); PIN(o_g1); PIN(o_h2); PIN(o_t2); PIN(a_qw); PIN(a_qr); PIN(pb); PIN(a_cnt); PIN(one);
        asm volatile("s_waitcnt vmcnt(4)" : "+v"(cp0) :: "memory");
        bar0();
        STAMP(2);
        {
            v4u f[4]; frags128(a0, a1, f);
            FENCE();
            const f32x4 acc = gemv4(xf, f);
            pb[0] = asu(acc[0]); pb[2] = asu(acc[1]);
            stx(rs, o_p1, pb);
        }
        STAMP(3);
        if (STOPAT <= 3) { WAITVM(0); STAMP(14); goto done; }
        __builtin_amdgcn_s_sleep(10);
        WAITVM(3);
        v4u fu[4];
        {
            unsigned arr;
            asm volatile("buffer_load_dwordx4 %1, %4, %5, 0 offen sc1\n\tds_add_rtn_u32 %0, %2, %3\n\ts_waitcnt lgkmcnt(0)" : "=&v"(arr), "=&v"(ga) : "v"(a_cnt), "v"(one), "v"(o_g1), "s"(rsq) : "memory");
            hint_put(rs, __builtin_amdgcn_readfirstlane(arr) == 7u ? o_t2 : OOB, tag);
            asm volatile("s_waitcnt vmcnt(1)" : "+v"(ga) :: "memory");
            STAMP(14);
#pragma unroll
            for (int s = 0; s < 4; ++s) fu[s] = lrd<v4u>(a_uk[s]);
            if (__builtin_expect(!none(tagx(ga, tag) != 0u), 0)) {
#pragma nounroll
                SPIN { __builtin_amdgcn_s_sleep(4); get1(ga, o_g1, rsq); if (none(tagx(ga, tag) != 0u)) break; }
            }
            lwr<u16>(a_qw, (u16)pk(sum32(sum16x2(asf(ga[0]), asf(ga[2]))), 0.f));
        }
        STAMP(4);
        bar();
        {
            v4u qf[4];
#pragma unroll
            for (int s = 0; s < 4; ++s) qf[s] = lrd<v4u>(a_qr + 64 * s);
            FENCE();
            const f32x4 acc = gemv4(fu, qf);
            constexpr float SC = 0.08838834764831845f * 1.4426950408889634f;
            lwr<v2u>(a_2w, v2u{pk(acc[0] * SC, acc[1] * SC), pk(acc[2] * SC, acc[3] * SC)});
            bar();
            if (w < 4) {
                const v2u t = lrd<v2u>(a_2r);
                pb[0] = t[0]; pb[2] = t[1];
                stx(rs, o_h2, pb);
            }
        }
        STAMP(5);
        if (STOPAT <= 5) goto done;
        const int r = xc * 8 + (yy >> 1);
        unsigned o_t3 = WS_T3 + ((xc * 4 + cq) * 64 + 8 * w + (l & 7)) * 128, o_t5 = WS_T5 + (r * 32 + (l & 31)) * 128, j0, j1;
        asm volatile("buffer_load_dword %0, %2, %4, 0 offen sc1\n\tbuffer_load_dword %1, %3, %4, 0 offen sc1"
                     : "=&v"(j0), "=&v"(j1) : "v"(o_t3), "v"((w == 0 && !(yy & 1)) ? o_t5 : o_t3), "s"(rsq) : "memory");
        unsigned trep = 0;
        if (VDEF == 2) trep = hint_wait(rsq, WS_T2 + (xc * 128 + (l & 7) * 16 + 5) * 128, tag);
#pragma unroll
        for (int i = 0; i < 3; ++i) glds(o_gu[i], mat ? wu : wg, L_R2 + min(8 * i + w, 21) * 1024);
#define V_ISSUE(i) glds<true>((EXP & 4) ? 0 : vb + (i) * 32768 + sw[i], wuv, L_R1 + (16 * w + 4 * (i)) * 256)
        if (!VDEF) { V_ISSUE(0); V_ISSUE(1); }
        {
            const v4u xg = lrd<v4u>(L_XN + (l & 1) * 1024 + (32 * mp + 8 * g) * 2);
            const unsigned ag = L_R2 + (8 * g + (l15 >> 2)) * 688 + (l & 3) * 8;
            const unsigned f = 16 * (8 * g + w) + l15;
            const unsigned o_hg = (g < 3 && f < 344) ? WS_HG + ((mat * 16 + mp) * 1376 + 344 * fp + f) * 16 : OOB;
            if (VDEF) asm volatile("s_waitcnt vmcnt(0)" : "+v"(j0), "+v"(j1) :: "memory"); else asm volatile("s_waitcnt vmcnt(2)" : "+v"(j0), "+v"(j1) :: "memory");
            bar();
            f32x4 o[3];
#pragma unroll
            for (int t = 0; t < 3; ++t) {
                const unsigned tl = min(8 * t + w, 21);
                o[t] = mfma32(xg, trd2(ag + tl * 32, ag + tl * 32 + 4 * 688), ZF);
            }
            const float o0 = g == 0 ? o[0][0] : g == 1 ? o[1][0] : o[2][0], o1 = g == 0 ? o[0][1] : g == 1 ? o[1][1] : o[2][1];
            stx(rs, o_hg, v4u{asu(o0), tag, asu(o1), tag});
        }
        if (VDEF) {
            pace(VDEF == 2 ? trep : hint_wait(rsq, WS_T2 + (xc * 128 + (l & 7) * 16 + 5) * 128, tag), PA);
            V_ISSUE(0); V_ISSUE(1);
        }
        V_ISSUE(2); V_ISSUE(3);
        STAMP(6);
        const unsigned u = l15 >= 12 ? 8 + l15 : (g & 1) ? 12 + (l15 & 7) : l15;
        const bool act = (g & 1) ? (l15 & 12) != 8 : l15 < 12;
        unsigned o_h3 = WS_H3 + (((w >> 2) * 32 + h) * 4 + cq) * H3S + (8 * (w & 3) + (g >> 1)) * 384 + u * 16;
        unsigned o_t4 = l < 4 ? WS_T4 + ((h * 4 + l) * 4 + cq) * 128 : OOB;
        unsigned o_p4 = l < 16 ? WS_H4 + ((h * 4 + cq) * 128 + 16 * w + l) * 16 : OOB;
        unsigned a_ow, a_ow2, a_ms;
        {
            const unsigned n = 6 * (u >> 2) + 2 * (g >> 1), n2 = 6 * (u >> 2) + 4, gg = u & 3;
            a_ow = (act && n < 32) ? L_OW + w * 272 + (32 * gg + 4 * ((n >> 2) ^ (gg >> 1)) + (n & 3)) * 2 : L_JUNK + l * 4;
            a_ow2 = (act && n2 < 32 && l < 32) ? L_OW + w * 272 + (32 * gg + 4 * ((n2 >> 2) ^ (gg >> 1)) + (n2 & 3)) * 2 : L_JUNK + l * 4;
            a_ms = (l & 31) == 12 ? L_MW + (g >> 1) * 32 + w * 4 : L_JUNK + l * 4;
        }
        unsigned a_or = L_OW + (l & 7) * 272 + 16 * g, a_mw = L_MW + (g & 1) * 16;
        v4u a[4];
        UNDEF(a[0]); UNDEF(a[1]); UNDEF(a[2]); UNDEF(a[3]);
        a_cnt += l == 0 ? 4 : 0;
        PIN(o_h3); PIN(o_t3); PIN(o_t4); PIN(o_p4); PIN(a_ow); PIN(a_ow2); PIN(a_or); PIN(a_mw); PIN(a_ms); PIN(a_cnt);
        WAITVM(0);
        bar();
        v4u fv[4];
        frags128(a0 + (L_R1 - L_R0), a1 + (L_R1 - L_R0), fv);
        PIN(fv[0]); PIN(fv[1]); PIN(fv[2]); PIN(fv[3]);
        STAMP(7);
        if (STOPAT <= 7) goto done;
        CNT0(2); CNT0(3);
        const unsigned t3 = hint_wait(rsq, o_t3, tag);
        STAMPC(1);
        pace(t3, P3);
        STAMP(15);
        {
#pragma nounroll
            SPIN { CNT(2); get4<768>(a, o_h3, rsq); if (none((tagy(a[0][3], a[1][3], tag) | tagy(a[2][3], a[3][3], tag)) != 0u)) break; __builtin_amdgcn_s_sleep(4); }
            STAMP(8);
            unsigned arr;
            unsigned long long tm;
            asm volatile("ds_add_rtn_u32 %0, %2, %3\n\ts_memrealtime %1" : "=&v"(arr), "=&s"(tm) : "v"(a_cnt), "v"(one) : "memory");
            float m[4], al[4];
#pragma unroll
            for (int k = 0; k < 4; ++k) m[k] = dpp<0x15c>(asf(a[k][1]));
            const float M = max32(mx(mx3(m[0], m[1], m[2]), m[3]));
#pragma unroll
            for (int k = 0; k < 4; ++k) al[k] = __builtin_amdgcn_exp2f(m[k] - M);
            FENCE();
            asm volatile("s_waitcnt lgkmcnt(0)" : "+v"(arr), "+s"(tm) :: "memory");
            hint_put(rs, __builtin_amdgcn_readfirstlane(arr) == 7u ? o_t4 : OOB, tag, (unsigned)tm);
            f32x2 c01, c23, c45;
            float Ls;
#pragma unroll
            for (int k = 0; k < 4; ++k) {
                const f32x2 aa = {al[k], al[k]}, v01 = {lo16(a[k][0]), hi16(a[k][0])}, v23 = {lo16(a[k][1]), hi16(a[k][1])}, v45 = {lo16(a[k][2]), hi16(a[k][2])};
                if (k == 0) { c01 = aa * v01; c23 = aa * v23; c45 = aa * v45; Ls = al[0] * asf(a[0][2]); }
                else { c01 = __builtin_elementwise_fma(aa, v01, c01); c23 = __builtin_elementwise_fma(aa, v23, c23); c45 = __builtin_elementwise_fma(aa, v45, c45); Ls = __builtin_fmaf(al[k], asf(a[k][2]), Ls); }
            }
            const float acc[6] = {c01[0], c01[1], c23[0], c23[1], c45[0], c45[1]};
            const float v4 = sum32x2(acc[4], Ls);
            lwr<unsigned>(a_ow, pk(sum32x2(acc[0], acc[2]), sum32x2(acc[1], acc[3])));
            lwr<unsigned>(a_ow2, pk(v4, sum32(acc[5])));
            lwr<float>(a_ms, l < 32 ? M : v4);
        }
        bar();
        {
            v4u of[4];
#pragma unroll
            for (int s = 0; s < 4; ++s) of[s] = lrd<v4u>(a_or + 64 * s);
            const f32x4 mw = lrd<f32x4>(a_mw), lw = lrd<f32x4>(a_mw + 32);
            FENCE();
            const f32x4 c = gemv4(of, fv);
            const float M = mx(mx3(mw[0], mw[1], mw[2]), mw[3]);
            float be[4];
#pragma unroll
            for (int r = 0; r < 4; ++r) be[r] = __builtin_amdgcn_exp2f(mw[r] - M);
            const float rd = __builtin_amdgcn_rcpf((be[0] * lw[0] + be[1] * lw[1]) + (be[2] * lw[2] + be[3] * lw[3]));
            const float v = ((be[0] * c[0] + be[1] * c[1]) + (be[2] * c[2] + be[3] * c[3])) * rd;
            auto sv = __builtin_amdgcn_permlane16_swap(asu(v), asu(v), false, false);
            pb[0] = sv[0]; pb[2] = sv[1];
            stx(rs, o_p4, pb);
        }
        STAMP(9);
        if ((yy & 1) == 0) {
            const unsigned o1 = WS_H5 + ((32 + (tid >> 3)) * 512 + 8 * r + (tid & 7)) * 16;
            const bool two = tid < 176;
            const unsigned o2 = two ? o1 + 64 * 512 * 16 : o1;
            pace(t3, PG);
#pragma nounroll
            SPIN { get2(a[0], a[1], o1, o2, rsq); if (none((tagx(a[0], tag) | tagx(a[1], tag)) != 0u)) break; __builtin_amdgcn_s_sleep(8); }
            {
                float t = sum32(sum16x2(asf(a[0][0]) + (two ? asf(a[1][0]) : 0.f), asf(a[0][2]) + (two ? asf(a[1][2]) : 0.f)));
                t += dpp<0x128>(t);
                lwr<float>(L_RED + ((g & 1) * 8 + (l & 7)) * 32 + w * 4, t);
            }
            bar();
            STAMP(10);
            if (w == 0) {
                const f32x4 t2 = lrd<f32x4>(L_RED + ((g & 1) * 8 + (l & 7)) * 32), t4 = lrd<f32x4>(L_RED + ((g & 1) * 8 + (l & 7)) * 32 + 16);
                float glu = rbf(((t2[0] + t2[1]) + (t2[2] + t2[3])) + ((t4[0] + t4[1]) + (t4[2] + t4[3])));
                float xin = lo16(x[(g & 1) * 512 + 8 * r + (l & 7)]);
                unsigned o0 = WS_H5 + ((l >> 3) * 512 + 8 * r + (l & 7)) * 16;
                unsigned o_out = ((g & 1) * 512 + 8 * r + (l & 7)) * 2;
                PIN(glu); PIN(xin); PIN(o0); PIN(o_out);
                pace(hint_wait(rsq, o_t5, tag), P5);
                STAMP(11);
                asm volatile("buffer_load_dwordx4 %0, %4, %5, 0 offen sc1\n\tbuffer_load_dwordx4 %1, %4, %5, %6 offen sc1\n\t"
                             "buffer_load_dwordx4 %2, %4, %5, %7 offen sc1\n\tbuffer_load_dwordx4 %3, %4, %5, %8 offen sc1"
                             : "=&v"(a[0]), "=&v"(a[1]), "=&v"(a[2]), "=&v"(a[3]) : "v"(o0), "s"(rsq), "s"(65536), "s"(131072), "s"(196608) : "memory");
                __builtin_amdgcn_raw_buffer_store_b32(tag, rs, (r == 0 && l == 0) ? 0u : OOB, 0, 16);
                asm volatile("s_waitcnt vmcnt(1)" : "+v"(a[0]), "+v"(a[1]), "+v"(a[2]), "+v"(a[3]) :: "memory");
                if (__builtin_expect(!none(tagx4(a, tag) != 0u), 0)) {
#pragma nounroll
                    SPIN { CNT(3); get4s(a, o0, rsq, 65536, 131072, 196608); if (none(tagx4(a, tag) != 0u)) break; }
                }
                STAMP(12);
                float t = sum32(sum16x2((asf(a[0][0]) + asf(a[1][0])) + (asf(a[2][0]) + asf(a[3][0])), (asf(a[0][2]) + asf(a[1][2])) + (asf(a[2][2]) + asf(a[3][2]))));
                t += dpp<0x128>(t);
                if (l < 32 && l15 < 8) *(GAS u16*)((GAS unsigned char*)out + o_out) = (u16)pk(rbf(xin + rbf(t)) + glu, 0.f);
            }
        }
    } else {
        const int pr = xc * 8 + (yy >> 1), hg = yy & 1, b = pr >> 5, sp = pr & 31;
        const unsigned t_in = now();
        unsigned ep, j0;
        unsigned o_t2 = WS_T2 + (xc * 128 + (16 * hg + 2 * w) * 4 + (l & 7)) * 128;
        asm volatile("global_load_dword %0, %1, %2 sc1" : "=v"(ep) : "v"(0), "s"(ws) : "memory");
        asm volatile("buffer_load_dword %0, %1, %2, 0 offen sc1" : "=v"(j0) : "v"(o_t2), "s"(rsq) : "memory");
        unsigned qoff = WS_H2 + ((((xc & 3) * 2 + b) * 32 + 16 * hg + 2 * w) * 128 + l) * 16;
        unsigned a_q0 = L_Q + 2 * w * 1024 + (((l >> 1) ^ (2 * w)) << 4) + (l & 1) * 8, a_q1 = L_Q + (2 * w + 1) * 1024 + (((l >> 1) ^ (2 * w + 1)) << 4) + (l & 1) * 8;
        unsigned o_kv[8];
#pragma unroll
        for (int i = 0; i < 8; ++i) { o_kv[i] = (pr * 128 + 64 * hg + 8 * w + i) * 1024 + ((l ^ (8 * (w & 1) + i)) << 4); PIN(o_kv[i]); }
        PIN(qoff); PIN(a_q0); PIN(a_q1);
        asm volatile("s_waitcnt vmcnt(0)" : "+v"(ep), "+v"(j0), "+v"(cp0) :: "memory");
        const unsigned tag = mktag(ep);
        pace(t_in, PB);
#pragma unroll
        for (int i = 0; i < 8; ++i) glds(o_kv[i], ckv, L_R0 + (8 * w + i) * 1024);
        STAMP(1);
        bar0();
#pragma unroll
        for (int i = 0; i < 8; ++i) glds(o_kv[i] ^ 65536, ckv, L_R0 + (64 + 8 * w + i) * 1024);
        WAITVM(8);
        bar0();
        STAMP(2);
#define Q_ISSUE() \
            FENCE(); \
            WAITVM(2); \
            const v2u hv = hint_get(rsq, o_t2);        \
            STAMP(14); \
            pace(none(hv[0] != tag) ? hv[1] : hint_wait(rsq, o_t2, tag), P2); \
            STAMP(15); \
            v4u qa[4]; \
            asm volatile("buffer_load_dwordx4 %0, %4, %5, 0 offen sc1\n\tbuffer_load_dwordx4 %1, %4, %5, 0 offen offset:1024 sc1\n\t" \
                         "buffer_load_dwordx4 %2, %4, %5, 0 offen offset:2048 sc1\n\tbuffer_load_dwordx4 %3, %4, %5, 0 offen offset:3072 sc1" \
                         : "=&v"(qa[0]), "=&v"(qa[1]), "=&v"(qa[2]), "=&v"(qa[3]) : "v"(qoff), "s"(rsq) : "memory"); \
            bar0()
#define Q_LAND(n) do { \
            asm volatile("s_waitcnt vmcnt(" STR(n) ")" : "+v"(qa[0]), "+v"(qa[1]), "+v"(qa[2]), "+v"(qa[3]) :: "memory"); \
            if (__builtin_expect(!none(tagx4(qa, tag) != 0u), 0)) { _Pragma("nounroll") SPIN { __builtin_amdgcn_s_sleep(4); get4<1024>(qa, qoff, rsq); if (none(tagx4(qa, tag) != 0u)) break; } } \
            asm volatile("ds_write2_b32 %0, %2, %3 offset1:1\n\tds_write2_b32 %0, %4, %5 offset0:128 offset1:129\n\tds_write2_b32 %1, %6, %7 offset1:1\n\tds_write2_b32 %1, %8, %9 offset0:128 offset1:129" \
                         :: "v"(a_q0), "v"(a_q1), "v"(qa[0][0]), "v"(qa[0][2]), "v"(qa[1][0]), "v"(qa[1][2]), "v"(qa[2][0]), "v"(qa[2][2]), "v"(qa[3][0]), "v"(qa[3][2]) : "memory"); \
            STAMP(3); bar();   } while (0)
        unsigned t5 = 0;
        const int h = 4 * xc + (yy >> 2), cq = yy & 3;
        unsigned o_t4 = WS_T4 + ((h * 4 + cq) * 4 + (l & 3)) * 128;
        unsigned o_hg = (EXP & 2) ? 0 : WS_HG + ((4 * (w & 3) + g) * 1376 + 16 * min(un, 85) + l15) * 16;
        if (w >= 4) {
            unsigned a_r[4];
#pragma unroll
            for (int j = 0; j < 4; ++j) a_r[j] = l15 * 1024 + (((4 * j + g) ^ l15) << 4);
            const unsigned kb = L_R0 + (w - 4) * 16384;
            v4u cr[32], qf[16];
#pragma unroll
            for (int s = 0; s < 16; ++s) cr[s] = lrd<v4u>(a_r[s & 3] + kb + (s >> 2) * 256);
            unsigned a_mxw = L_MX + l15 * 16 + (w - 4) * 4, a_mxr = L_MX + l15 * 16;
            unsigned a_pw = L_P + l15 * 256 + (((2 * (w - 4) + (g >> 1)) ^ l15) << 4) + (g & 1) * 8;
            __builtin_amdgcn_s_setprio(1);
            PIN(a_mxw); PIN(a_mxr); PIN(a_pw); PIN(o_hg);
            Q_ISSUE();
#pragma unroll
            for (int s = 0; s < 16; ++s) cr[16 + s] = lrd<v4u>(a_r[s & 3] + kb + 65536 + (s >> 2) * 256);
            FENCE();
            Q_LAND(0);
            STAMP(4);
#pragma unroll
            for (int s = 0; s < 16; ++s) qf[s] = lrd<v4u>(a_r[s & 3] + L_Q + (s >> 2) * 256);
            FENCE();
            f32x4 s1 = ZF, s2 = ZF;
#pragma unroll
            for (int s = 0; s < 16; ++s) { s1 = mfma32(cr[s], qf[s], s1); s2 = mfma32(cr[16 + s], qf[s], s2); }
            asm volatile("s_nop 7" : "+v"(s1), "+v"(s2));
            lwr<float>(a_mxw, max32(max16(mx(mx3(mx3(s1[0], s1[1], s1[2]), s1[3], s2[0]), mx3(s2[1], s2[2], s2[3])))));
            bar();
            STAMP(12);
            const f32x4 m0 = lrd<f32x4>(a_mxr);
            const float mr = mx(mx3(m0[0], m0[1], m0[2]), m0[3]);
            v2u p1 = {pk(__builtin_amdgcn_exp2f(s1[0] - mr), __builtin_amdgcn_exp2f(s1[1] - mr)), pk(__builtin_amdgcn_exp2f(s1[2] - mr), __builtin_amdgcn_exp2f(s1[3] - mr))};
            lwr<v2u>(a_pw, p1);
            v2u p2 = {pk(__builtin_amdgcn_exp2f(s2[0] - mr), __builtin_amdgcn_exp2f(s2[1] - mr)), pk(__builtin_amdgcn_exp2f(s2[2] - mr), __builtin_amdgcn_exp2f(s2[3] - mr))};
            lwr<v2u>(a_pw ^ 128, p2);
            bar();
            STAMP(5);
            PIN(p1); PIN(p2);
            {
                const unsigned q0 = p1[0], q1 = p1[1], q2 = p2[0], q3 = p2[1];
                lwr<float>(a_mxw + 256, sum32(sum16(((lo16(q0) + hi16(q0)) + (lo16(q1) + hi16(q1))) + ((lo16(q2) + hi16(q2)) + (lo16(q3) + hi16(q3))))));
            }
            bar();
            __builtin_amdgcn_s_setprio(0);
            t5 = now();
            if (PH) pace(t5, PH);
            v4u ga, gb;
            asm volatile("buffer_load_dwordx4 %0, %3, %6, 0 offen sc1\n\tbuffer_load_dwordx4 %1, %4, %6, 0 offen sc1\n\tbuffer_load_dword %2, %5, %6, 0 offen sc1\n\ts_waitcnt vmcnt(0)"
                         : "=&v"(ga), "=&v"(gb), "=&v"(j0), "+v"(o_hg) : "v"(o_hg + 16 * 1376 * 16), "v"(o_t4), "s"(rsq) : "memory");
            if (un < 86) {
                if (!(EXP & 2) && __builtin_expect(!none((tagx(ga, tag) | tagx(gb, tag)) != 0u), 0)) {
#pragma nounroll
                    SPIN { __builtin_amdgcn_s_sleep(16); get2(ga, gb, o_hg, o_hg + 16 * 1376 * 16, rsq); if (none((tagx(ga, tag) | tagx(gb, tag)) != 0u)) break; }
                }
                lwr<float>(L_RED + (l & 31) * 32 + (w - 4) * 4, sum32(sum16x2(asf(ga[0]), asf(ga[2]))));
                lwr<float>(L_RED + (l & 31) * 32 + w * 4, sum32(sum16x2(asf(gb[0]), asf(gb[2]))));
            }
            pace(t5, PW);
#pragma unroll
            for (int i = 0; i < 8; ++i) {
                const unsigned ch = l15 ^ ((g << 2) | (i & 3));
                glds((h * 128 + 32 * (w - 4) + 4 * i + g) * 1024 + cq * 128 + (ch >> 3) * 512 + (ch & 7) * 16, wo, L_R1 + (32 * (w - 4) + 4 * i) * 256);
            }
        } else {
            unsigned a_t[2][4];
            {
                const unsigned q = l15 >> 2, p = l & 3;
#pragma unroll
                for (int j = 0; j < 8; ++j) {
                    const unsigned r0 = 8 * g + 4 * (j >> 2) + q, ch = 16 * w + 4 * p + (j & 3);
                    a_t[j >> 2][j & 3] = L_R0 + r0 * 1024 + ((ch ^ (r0 & 15)) << 4) + (p >> 1) * 8;
                }
            }
            v4u ct[8][4];
#pragma unroll
            for (int t = 0; t < 8; ++t)
#pragma unroll
                for (int s = 0; s < 2; ++s) ct[t][s] = trd2((a_t[0][t >> 1] ^ ((t & 1) * 8)) + s * 32768, (a_t[1][t >> 1] ^ ((t & 1) * 8)) + s * 32768);
            unsigned a_pr[4], a_mxr = L_MX + l15 * 16;
#pragma unroll
            for (int j = 0; j < 4; ++j) { a_pr[j] = L_P + l15 * 256 + (((4 * j + g) ^ l15) << 4); PIN(a_pr[j]); }
            unsigned o_t3 = l < 4 ? WS_T3 + (((4 * hg + l) * 4 + w) * 64 + pr) * 128 : OOB;
            unsigned o_h3 = WS_H3 + ((b * 32 + 16 * hg + (l >> 3)) * 4 + w) * H3S + sp * 384 + (l & 7) * 16, o_h3b = o_h3 + 8 * 4 * H3S;
            unsigned a_sw = L_R2 + w * 8192 + l15 * 144 + g * 16, a_sr = L_R2 + w * 8192 + (l >> 3) * 144 + (l & 7) * 16;
            PIN(a_sw); PIN(a_sr);
            unsigned o_wd[4], a_lr = L_MX + 256 + l15 * 16;
#pragma unroll
            for (int i = 0; i < 4; ++i) { const unsigned row = 4 * w + i; o_wd[i] = (EXP & 1) ? 0 : (16 * min(un, 85) + row) * 1024 + ((l ^ ((row & 7) << 1)) << 4); PIN(o_wd[i]); }
            PIN(a_mxr); PIN(o_t3); PIN(o_h3); PIN(o_h3b); PIN(a_lr);
            Q_ISSUE();
            v4u wst[4];
            asm volatile("global_load_dwordx4 %0, %4, %8\n\tglobal_load_dwordx4 %1, %5, %8\n\tglobal_load_dwordx4 %2, %6, %8\n\tglobal_load_dwordx4 %3, %7, %8"
                         : "=&v"(wst[0]), "=&v"(wst[1]), "=&v"(wst[2]), "=&v"(wst[3]) : "v"(o_wd[0]), "v"(o_wd[1]), "v"(o_wd[2]), "v"(o_wd[3]), "s"(wd) : "memory");
#pragma unroll
            for (int t = 0; t < 8; ++t)
#pragma unroll
                for (int s = 2; s < 4; ++s) ct[t][s] = trd2((a_t[0][t >> 1] ^ ((t & 1) * 8)) + s * 32768, (a_t[1][t >> 1] ^ ((t & 1) * 8)) + s * 32768);
            FENCE();
            Q_LAND(4);
            STAMP(4);
            bar0();
            const f32x4 m0 = lrd<f32x4>(a_mxr);
            float mr = mx(mx3(m0[0], m0[1], m0[2]), m0[3]);
            PIN(mr);
            bar();
            STAMP(5);
            v4u pf[4];
#pragma unroll
            for (int s = 0; s < 4; ++s) pf[s] = lrd<v4u>(a_pr[s]);
            if (HP == 0) hint_put(rs, o_t3, tag);
            FENCE();
            f32x4 o[8];
#define M0_(t) asm volatile("v_mfma_f32_16x16x32_bf16 %0, %1, %2, 0" : "=&v"(o[t]) : "v"(ct[t][0]), "v"(pf[0]))
#define MM(t, s) asm volatile("v_mfma_f32_16x16x32_bf16 %0, %1, %2, %0" : "+v"(o[t]) : "v"(ct[t][s]), "v"(pf[s]))
#define RDY(t) PIN(o[t])
#define SEQ() asm volatile("" ::: "memory")
#define CV(d, t, i) do { d = pk(o[t][i], o[t][i + 1]); PIN(d); } while (0)
#define PUT(n, k, A, B, C) lwr<v4u>(a_sw + (n) * 2304 + (k) * 64, v4u{A, B, C, tag})
#define GETL(n) do { x0 = lrd<v4u>(a_sr + (n) * 2304); x1 = lrd<v4u>(a_sr + (n) * 2304 + 8 * 144); } while (0)
#define OUTL(n) do { stx(rs, o_h3 + (n) * 128, x0); stx(rs, o_h3b + (n) * 128, x1); } while (0)
            unsigned d0, d1, d2, d3, d4, d5;
            v4u x0, x1;
            M0_(0); M0_(1); MM(0, 1); MM(1, 1); MM(0, 2); MM(1, 2); MM(0, 3); MM(1, 3);
            M0_(2); MM(2, 1); MM(2, 2); MM(2, 3);
            M0_(3); MM(3, 1); MM(3, 2);
            RDY(0); CV(d0, 0, 0); CV(d1, 0, 2);
            MM(3, 3); RDY(1); CV(d2, 1, 0); CV(d3, 1, 2);
            M0_(4); PUT(0, 0, d0, d1, d2); SEQ();
            MM(4, 1); RDY(2); CV(d4, 2, 0); CV(d5, 2, 2);
            MM(4, 2); PUT(0, 1, d3, d4, d5); GETL(0); SEQ();
            MM(4, 3); RDY(3); CV(d0, 3, 0); CV(d1, 3, 2);
            M0_(5);
            bar0();
            const f32x4 l0 = lrd<f32x4>(a_lr);
            MM(5, 1); MM(5, 2); MM(5, 3);
            RDY(4); CV(d2, 4, 0); CV(d3, 4, 2);
            STAMP2(3);
            M0_(6); OUTL(0); if (HP == 1) hint_put(rs, o_t3, tag); PUT(1, 0, d0, d1, d2); SEQ();
            STAMP2(4);
            MM(6, 1);
            float lsum = (l0[0] + l0[1]) + (l0[2] + l0[3]);
            PIN(lsum);
            MM(6, 2); MM(6, 3);
            RDY(5); CV(d4, 5, 0); CV(d5, 5, 2);
            M0_(7); PUT(1, 1, d3, d4, d5); GETL(1); SEQ();
            MM(7, 1); MM(7, 2); MM(7, 3);
            asm volatile("s_nop 1" : "+v"(o[6]));
            CV(d0, 6, 0); CV(d1, 6, 2);
            STAMP2(14);
            OUTL(1); if (HP == 2) hint_put(rs, o_t3, tag); SEQ();
            STAMP2(15);
            asm volatile("s_nop 3" : "+v"(o[7]));
            CV(d2, 7, 0); CV(d3, 7, 2);
            PUT(2, 0, d0, d1, d2); PUT(2, 1, d3, asu(mr), asu(lsum)); GETL(2); OUTL(2); if (HP == 3) hint_put(rs, o_t3, tag);
            FENCE();
            STAMP(6);
            asm volatile("s_waitcnt vmcnt(7)" : "+v"(wst[0]), "+v"(wst[1]), "+v"(wst[2]), "+v"(wst[3]) :: "memory");
#pragma unroll
            for (int i = 0; i < 4; ++i) lwr<v4u>(L_R0 + (4 * w + i) * 1024 + l * 16, wst[i]);
        }
        if (STOPAT <= 7) goto done;
        bar();
        if (un < 86) {
            if (w == 4) {
                const f32x4 gp = lrd<f32x4>(L_RED + (l & 31) * 32), up = lrd<f32x4>(L_RED + (l & 31) * 32 + 16);
                const float gg = rbf((gp[0] + gp[1]) + (gp[2] + gp[3])), uu = rbf((up[0] + up[1]) + (up[2] + up[3]));
                const float sg = rbf(__builtin_amdgcn_rcpf(1.f + __builtin_amdgcn_exp2f(gg * -1.4426950408889634f)));
                lwr<u16>(L_HH + (l & 31) * 2, (u16)pk(sg * uu, 0.f));
            }
            bar();
            const v2u ha = lrd<v2u>(L_HH + (l & 1) * 32 + g * 8);
            const unsigned q = l15 >> 2, p = l & 3, row = 4 * g + q;
            f32x4 o[4];
#pragma unroll
            for (int t = 0; t < 4; ++t) {
                const unsigned ch = 8 * w + 2 * t + (p >> 1);
                o[t] = mfma16(ha, trd(L_R0 + row * 1024 + ((ch ^ ((row & 7) << 1)) << 4) + (p & 1) * 8), ZF);
            }
            const float o0 = g == 0 ? o[0][0] : g == 1 ? o[1][0] : g == 2 ? o[2][0] : o[3][0];
            const float o1 = g == 0 ? o[0][1] : g == 1 ? o[1][1] : g == 2 ? o[2][1] : o[3][1];
            stx(rs, WS_H5 + ((32 + un) * 512 + 64 * w + l) * 16, v4u{asu(o0), tag, asu(o1), tag});
        }
        STAMP(7);
        {
            unsigned a0, a1;
            {
                const unsigned q = l15 >> 2, p = l & 3, ch = 2 * w + (p >> 1);
                a0 = L_R1 + (8 * g + q) * 256 + 16 * (ch ^ ((q << 2) | ((2 * g) & 3))) + 8 * (p & 1);
                a1 = L_R1 + (8 * g + q + 4) * 256 + 16 * (ch ^ ((q << 2) | ((2 * g + 1) & 3))) + 8 * (p & 1);
            }
            unsigned o_g4 = WS_H4 + ((h * 4 + g) * 128 + 16 * w + l15) * 16;
            unsigned o_t5 = tid < 16 ? WS_T5 + ((8 * (cq + 4 * (tid >> 3)) + (tid & 7)) * 32 + h) * 128 : OOB;
            unsigned o_h5 = l < 16 ? WS_H5 + (h * 512 + 64 * (cq + 4 * (w >> 2)) + 16 * (w & 3) + l) * 16 : OOB;
            unsigned a_qw = L_QS + (g & 1) * 272 + (16 * w + l15) * 2, a_qr = L_QS + (l & 1) * 272 + 16 * g;
            v4u pb = {0u, tag, 0u, tag}, fw[4], ga;
            WAITVM(0);
            bar();
            frags128(a0, a1, fw);
            PIN(fw[0]); PIN(fw[1]); PIN(fw[2]); PIN(fw[3]); PIN(o_g4); PIN(o_t5); PIN(o_h5); PIN(a_qw); PIN(a_qr); PIN(pb);
            STAMP(10);
            if (w == 4) pace(hint_wait(rsq, o_t4, tag), P4);
            bar0();
            STAMP(11);
            asm volatile("buffer_load_dwordx4 %0, %1, %2, 0 offen sc1" : "=&v"(ga) : "v"(o_g4), "s"(rsq) : "memory");
            hint_put(rs, o_t5, tag);
            asm volatile("s_waitcnt vmcnt(1)" : "+v"(ga) :: "memory");
            if (__builtin_expect(!none(tagx(ga, tag) != 0u), 0)) {
#pragma nounroll
                SPIN { __builtin_amdgcn_s_sleep(2); get1(ga, o_g4, rsq); if (none(tagx(ga, tag) != 0u)) break; }
            }
            STAMP(8);
            lwr<u16>(a_qw, (u16)pk(sum32(sum16x2(asf(ga[0]), asf(ga[2]))), 0.f));
            bar();
            v4u cf[4];
#pragma unroll
            for (int s = 0; s < 4; ++s) cf[s] = lrd<v4u>(a_qr + 64 * s);
            FENCE();
            const f32x4 acc = gemv4(cf, fw);
            pb[0] = asu(acc[0]); pb[2] = asu(acc[1]);
            stx(rs, o_h5, pb);
        }
        STAMP(9);
    }
done:
    STAMP(13);
}

extern "C" void kernel_launch(void* const* d_in, const int* in_sizes, int n_in, void* d_out, int out_size, void* d_ws, size_t ws_size,
                              hipStream_t stream) {
    (void)in_sizes; (void)n_in; (void)out_size; (void)ws_size;
    (void)hipFuncSetAttribute((const void*)mla_glu_decode, hipFuncAttributeMaxDynamicSharedMemorySize, L_END);
    mla_glu_decode<<<256, 512, L_END, stream>>>((const u16*)d_in[0], (const u16*)d_in[1], (const u16*)d_in[2], (const u16*)d_in[3], (const u16*)d_in[4],
                                               (const u16*)d_in[5], (const u16*)d_in[7], (const u16*)d_in[8], (const u16*)d_in[9], (u16*)d_out,
                                               (unsigned char*)d_ws);
}
```

```cpp
#include <hip/hip_runtime.h>
#include <hip/hip_bf16.h>

typedef unsigned short u16;
typedef unsigned v4u __attribute__((ext_vector_type(4)));
typedef unsigned v2u __attribute__((ext_vector_type(2)));
typedef float f32x4 __attribute__((ext_vector_type(4)));
typedef float f32x2 __attribute__((ext_vector_type(2)));
typedef short s16x4 __attribute__((ext_vector_type(4)));
typedef __bf16 bf16x8 __attribute__((ext_vector_type(8)));
typedef __bf16 bf16x2 __attribute__((ext_vector_type(2)));
#define LAS __attribute__((address_space(3)))
#define GAS __attribute__((address_space(1)))
#define DEVI __device__ __forceinline__

#define PB 85
#define P2 100
#define P3 60
#define P4 55
#define P5 50
#define PG 105
#define PW 60
#define PA 0
#define CODEB 14080
#define EXP 0
#define PH 0
#define H3S 12288
#define XMAP 0
#define VDEF 1
#define HP 2
#define PREH3 0
#define STOPAT 99

constexpr int WS_T2 = 4096;
constexpr int WS_T3 = WS_T2 + 8 * 128 * 128;
constexpr int WS_T5 = WS_T3 + 8 * 64 * 4 * 128;
constexpr int WS_T4 = WS_T5 + 64 * 32 * 128;
constexpr int WS_H1 = WS_T4 + 32 * 16 * 128;
constexpr int WS_H4 = WS_H1 + 32 * 4 * 128 * 16;
constexpr int WS_H2 = WS_H4 + 32 * 4 * 128 * 16;
constexpr int WS_HG = WS_H2 + 4 * 2 * 32 * 128 * 16;
constexpr int WS_H5 = WS_HG + 2 * 16 * 1376 * 16;
constexpr int WS_H3 = WS_H5 + 118 * 512 * 16;
constexpr int WS_END = WS_H3 + 2 * 32 * 4 * H3S;
constexpr int WS_STAMP = 16 << 20;
constexpr int L_Q = 0;
constexpr int L_P = 16384;
constexpr int L_QS = L_P + 4096;
constexpr int L_OW = L_QS + 1024;
constexpr int L_MW = L_OW + 2304;
constexpr int L_MX = L_MW + 64;
constexpr int L_HH = L_MX + 512;
constexpr int L_TAG = L_HH + 64;
constexpr int L_JUNK = L_TAG + 64;
constexpr int L_RED = L_JUNK + 256;
constexpr int L_XN = 28672;
constexpr int L_R0 = 32768, L_R1 = 65536, L_R2 = 98304, L_R3 = 131072;
constexpr int L_END = 163840;
static_assert(L_RED + 1024 <= L_XN && L_XN + 2048 <= L_R0, "lds");
constexpr int SPIN_CAP = 1 << 16;

DEVI unsigned pk(float lo, float hi) { bf16x2 r = {(__bf16)lo, (__bf16)hi}; return __builtin_bit_cast(unsigned, r); }
DEVI float rbf(float f) { return (float)(__bf16)f; }
DEVI float lo16(unsigned w) { return __builtin_bit_cast(float, w << 16); }
DEVI float hi16(unsigned w) { return __builtin_bit_cast(float, w & 0xffff0000u); }
DEVI float asf(unsigned w) { return __builtin_bit_cast(float, w); }
DEVI unsigned asu(float w) { return __builtin_bit_cast(unsigned, w); }
DEVI float mx(float a, float b) { float r; asm("v_max_f32 %0, %1, %2" : "=v"(r) : "v"(a), "v"(b)); return r; }
DEVI float mx3(float a, float b, float c) { float r; asm("v_max3_f32 %0, %1, %2, %3" : "=v"(r) : "v"(a), "v"(b), "v"(c)); return r; }
DEVI f32x4 mfma32(v4u a, v4u b, f32x4 c) { return __builtin_amdgcn_mfma_f32_16x16x32_bf16(__builtin_bit_cast(bf16x8, a), __builtin_bit_cast(bf16x8, b), c, 0, 0, 0); }
DEVI f32x4 mfma16(v2u a, v2u b, f32x4 c) { return __builtin_amdgcn_mfma_f32_16x16x16bf16_1k(__builtin_bit_cast(s16x4, a), __builtin_bit_cast(s16x4, b), c, 0, 0, 0); }
template <class T> DEVI T lrd(unsigned off) { return *(LAS T*)(size_t)off; }
template <class T> DEVI void lwr(unsigned off, T v) { *(LAS T*)(size_t)off = v; }
DEVI v2u trd(unsigned off) { return __builtin_bit_cast(v2u, __builtin_amdgcn_ds_read_tr16_b64_v4i16((LAS s16x4*)(size_t)off)); }
DEVI v4u trd2(unsigned o0, unsigned o1) { v2u a = trd(o0), b = trd(o1); return v4u{a.x, a.y, b.x, b.y}; }
DEVI float sum16(float x) { auto r = __builtin_amdgcn_permlane16_swap(asu(x), asu(x), false, false); return asf(r[0]) + asf(r[1]); }
DEVI float sum32(float x) { auto r = __builtin_amdgcn_permlane32_swap(asu(x), asu(x), false, false); return asf(r[0]) + asf(r[1]); }
DEVI float max16(float x) { auto r = __builtin_amdgcn_permlane16_swap(asu(x), asu(x), false, false); return mx(asf(r[0]), asf(r[1])); }
DEVI float max32(float x) { auto r = __builtin_amdgcn_permlane32_swap(asu(x), asu(x), false, false); return mx(asf(r[0]), asf(r[1])); }
DEVI float odd16(float x) { auto r = __builtin_amdgcn_permlane16_swap(asu(x), asu(x), false, false); return asf(r[1]); }
DEVI float sum16x2(float x, float y) { auto r = __builtin_amdgcn_permlane16_swap(asu(x), asu(y), false, false); return asf(r[0]) + asf(r[1]); }
DEVI float sum32x2(float x, float y) { auto r = __builtin_amdgcn_permlane32_swap(asu(x), asu(y), false, false); return asf(r[0]) + asf(r[1]); }
template <int CTRL> DEVI float dpp(float x) { return asf((unsigned)__builtin_amdgcn_update_dpp(0, (int)asu(x), CTRL, 0xf, 0xf, true)); }
DEVI float wavesum(float x) {
    x += dpp<0xB1>(x); x += dpp<0x4E>(x); x += dpp<0x141>(x); x += dpp<0x140>(x);
    return sum32(sum16(x));
}
DEVI float dot2one(unsigned p, float c) { const unsigned one = 0x3f803f80u; return __builtin_amdgcn_fdot2_f32_bf16(__builtin_bit_cast(bf16x2, p), __builtin_bit_cast(bf16x2, one), c, false); }
#define STR_(x) #x
#define STR(x) STR_(x)
#define WAITVM(n) asm volatile("s_waitcnt vmcnt(" STR(n) ")" ::: "memory")
#define PIN(x) asm volatile("" : "+v"(x))
#define MFMA_SETTLE(x) asm volatile("s_nop 7" : "+v"(x))
#define UNDEF(x) asm volatile("" : "=v"(x))
#define FENCE() __builtin_amdgcn_sched_barrier(0)
#define SPIN for (int it_ = SPIN_CAP; it_ > 0; --it_)
DEVI void bar() { asm volatile("s_waitcnt lgkmcnt(0)\n\ts_barrier" ::: "memory"); }
DEVI void bar0() { asm volatile("s_barrier" ::: "memory"); }
#define USE_NT 0
template <bool NT = false> DEVI void glds(unsigned voff, const void* sbase, unsigned lds) {
    unsigned keep;
    if (NT && USE_NT) asm volatile("s_mov_b32 %0, m0\n\ts_mov_b32 m0, %3\n\ts_nop 0\n\tglobal_load_lds_dwordx4 %1, %2 nt\n\ts_mov_b32 m0, %0" : "=&s"(keep) : "v"(voff), "s"(sbase), "s"(lds) : "memory");
    else asm volatile("s_mov_b32 %0, m0\n\ts_mov_b32 m0, %3\n\ts_nop 0\n\tglobal_load_lds_dwordx4 %1, %2\n\ts_mov_b32 m0, %0" : "=&s"(keep) : "v"(voff), "s"(sbase), "s"(lds) : "memory");
}
typedef __amdgpu_buffer_rsrc_t rsrc_t;
constexpr unsigned OOB = 0x80000000u;
DEVI void stx(rsrc_t rs, unsigned off, v4u v) { __builtin_amdgcn_raw_buffer_store_b128(v, rs, off, 0, 16); }
DEVI bool none(bool bad) { asm volatile("" ::: "memory"); return __builtin_amdgcn_ballot_w64(bad) == 0ull; }
DEVI unsigned tagx(v4u a, unsigned tag) { unsigned r = (a[1] ^ tag) | (a[3] ^ tag); asm("" : "+v"(r)); return r; }
DEVI unsigned tagy(unsigned a, unsigned b, unsigned tag) { unsigned r = (a ^ tag) | (b ^ tag); asm("" : "+v"(r)); return r; }
DEVI unsigned tagx4(const v4u (&a)[4], unsigned tag) { return (tagx(a[0], tag) | tagx(a[1], tag)) | (tagx(a[2], tag) | tagx(a[3], tag)); }
DEVI void get1(v4u& a, unsigned off, v4u rq) { asm volatile("buffer_load_dwordx4 %0, %1, %2, 0 offen sc1\n\ts_waitcnt vmcnt(0)" : "+&v"(a) : "v"(off), "s"(rq) : "memory"); }
DEVI void get2(v4u& a, v4u& b, unsigned oa, unsigned ob, v4u rq) {
    asm volatile("buffer_load_dwordx4 %0, %2, %4, 0 offen sc1\n\tbuffer_load_dwordx4 %1, %3, %4, 0 offen sc1\n\ts_waitcnt vmcnt(0)" : "+&v"(a), "+&v"(b) : "v"(oa), "v"(ob), "s"(rq) : "memory");
}
template <int ST> DEVI void get4(v4u (&a)[4], unsigned off, v4u rq) {
    asm volatile("buffer_load_dwordx4 %0, %4, %5, 0 offen sc1\n\tbuffer_load_dwordx4 %1, %4, %5, 0 offen offset:%6 sc1\n\t"
                 "buffer_load_dwordx4 %2, %4, %5, 0 offen offset:%7 sc1\n\tbuffer_load_dwordx4 %3, %4, %5, 0 offen offset:%8 sc1\n\ts_waitcnt vmcnt(0)"
                 : "+&v"(a[0]), "+&v"(a[1]), "+&v"(a[2]), "+&v"(a[3]) : "v"(off), "s"(rq), "i"(ST), "i"(2 * ST), "i"(3 * ST) : "memory");
}
DEVI void get4s(v4u (&a)[4], unsigned off, v4u rq, unsigned s1, unsigned s2, unsigned s3) {
    asm volatile("buffer_load_dwordx4 %0, %4, %5, 0 offen sc1\n\tbuffer_load_dwordx4 %1, %4, %5, %6 offen sc1\n\t"
                 "buffer_load_dwordx4 %2, %4, %5, %7 offen sc1\n\tbuffer_load_dwordx4 %3, %4, %5, %8 offen sc1\n\ts_waitcnt vmcnt(0)"
                 : "+&v"(a[0]), "+&v"(a[1]), "+&v"(a[2]), "+&v"(a[3]) : "v"(off), "s"(rq), "s"(s1), "s"(s2), "s"(s3) : "memory");
}
DEVI unsigned now() { return (unsigned)__builtin_amdgcn_s_memrealtime(); }
DEVI void hint_put(rsrc_t rs, unsigned off, unsigned tag, unsigned t) { __builtin_amdgcn_raw_buffer_store_b64(v2u{tag, t}, rs, off, 0, 16); }
DEVI void hint_put(rsrc_t rs, unsigned off, unsigned tag) { hint_put(rs, off, tag, now()); }
DEVI v2u hint_get(v4u rq, unsigned off) { v2u v; asm volatile("buffer_load_dwordx2 %0, %1, %2, 0 offen sc1\n\ts_waitcnt vmcnt(0)" : "=&v"(v) : "v"(off), "s"(rq) : "memory"); return v; }
DEVI void pace(unsigned t, unsigned lead) {
#pragma nounroll
    for (int it = 256; it > 0; --it) { if (none(now() - t < lead)) break; }
}
DEVI unsigned hint_wait(v4u rq, unsigned off, unsigned tag) {
    v2u v;
#pragma nounroll
    SPIN { v = hint_get(rq, off); if (none(v[0] != tag)) break; __builtin_amdgcn_s_sleep(1); }
    return v[1];
}
DEVI unsigned mktag(unsigned ep) { return ep + 1u; }

#define STAMP(i)

#define CNT0(i)
#define CNT(i)
#define STAMPC(i)
#define STAMP2(i)
DEVI void frags128(unsigned a0, unsigned a1, v4u (&f)[4]) {
#pragma unroll
    for (int s = 0; s < 4; ++s) f[s] = trd2(a0 + s * 8192, a1 + s * 8192);
}
DEVI f32x4 gemv4(const v4u (&a)[4], const v4u (&b)[4]) {
    f32x4 c = {0.f, 0.f, 0.f, 0.f};
#pragma unroll
    for (int s = 0; s < 4; ++s) c = mfma32(a[s], b[s], c);
    return c;
}
DEVI void layernorm(v4u xv, int w, int l) {
    float v[8];
#pragma unroll
    for (int j = 0; j < 4; ++j) { v[2 * j] = lo16(xv[j]); v[2 * j + 1] = hi16(xv[j]); }
    const float mean = wavesum(((v[0] + v[1]) + (v[2] + v[3])) + ((v[4] + v[5]) + (v[6] + v[7]))) * (1.f / 512.f);
    float sq = 0.f;
#pragma unroll
    for (int j = 0; j < 8; ++j) { const float d = v[j] - mean; sq += d * d; }
    const float var = wavesum(sq) * (1.f / 512.f);
    const float mb = rbf(mean), ri = __builtin_amdgcn_rcpf(rbf(__builtin_amdgcn_sqrtf(rbf(rbf(var) + 1e-5f))));
    v4u o;
#pragma unroll
    for (int j = 0; j < 4; ++j) o[j] = pk(rbf(v[2 * j] - mb) * ri, rbf(v[2 * j + 1] - mb) * ri);
    lwr<v4u>(L_XN + w * 1024 + l * 16, o);
}

__global__ void __launch_bounds__(512, 2)
mla_glu_decode(const u16* __restrict__ x, const u16* __restrict__ ckv, const u16* __restrict__ wq, const u16* __restrict__ wuk,
               const u16* __restrict__ wuv, const u16* __restrict__ wo, const u16* __restrict__ wg, const u16* __restrict__ wu,
               const u16* __restrict__ wd, u16* __restrict__ out, unsigned char* __restrict__ ws) {
    STAMP(0);
    const int tid = threadIdx.x, l = tid & 63, w = __builtin_amdgcn_readfirstlane(tid >> 6);
    const int id = blockIdx.x, ai = (id & 3) * 32 + (id >> 3);
    const int xc = XMAP ? ai >> 4 : id & 7, yy = XMAP ? ai & 15 : (id >> 3) & 15, y = XMAP ? (id & 4) * 4 : id >> 3;
    const int un = yy * 8 + xc;
    const unsigned g = l >> 4, l15 = l & 15;
    unsigned cp0 = 0;
    if (w == 7) {
        unsigned long long pc;
        asm volatile("s_getpc_b64 %0" : "=s"(pc));
        pc &= ~255ull;
        asm volatile("global_load_dword %0, %1, %2" : "=&v"(cp0) : "v"(min(((id >> 3) + 32 * (l & 3)) * 128, CODEB - 128)), "s"(pc) : "memory");
    }
    unsigned vb = (128 * (yy & 3) + 16 * w + g) * 8192 + (4 * xc + (yy >> 2)) * 256, sw[4], o_q[4];
#pragma unroll
    for (int i = 0; i < 4; ++i) { sw[i] = (l15 ^ ((g << 2) | i)) << 4; o_q[i] = vb + i * 32768 + sw[i]; PIN(o_q[i]); }
    asm volatile("" :: "s"(x), "s"(ckv), "s"(wq), "s"(wuk), "s"(wuv), "s"(wo), "s"(wg), "s"(wu), "s"(wd), "s"(out), "s"(ws));
    const rsrc_t rs = __builtin_amdgcn_make_buffer_rsrc(ws, 0, WS_END, 0x00020000);
    const v4u rsq = {(unsigned)(size_t)ws, (unsigned)((size_t)ws >> 32) & 0xffffu, (unsigned)WS_END, 0x00020000u};
    const f32x4 ZF = {0.f, 0.f, 0.f, 0.f};

    if (__builtin_expect(y < 16, 1)) {
        const int h = 4 * xc + (yy >> 2), cq = yy & 3;
        v4u xv; unsigned ep;
        if (w < 2) {
            asm volatile("global_load_dwordx4 %0, %1, %2" : "=v"(xv) : "v"(w * 1024 + l * 16), "s"(x) : "memory");
            asm volatile("global_load_dword %0, %1, %2 sc1" : "=v"(ep) : "v"(0), "s"(ws) : "memory");
        }
#pragma unroll
        for (int i = 0; i < 4; ++i) glds<true>(o_q[i], wq, L_R0 + (16 * w + 4 * i) * 256);
        bar0();
#pragma unroll
        for (int i = 0; i < 4; ++i) glds<true>(vb + i * 32768 + ((l15 ^ (4 * i + g)) << 4), wuk, L_R1 + (16 * w + 4 * i) * 256);
        STAMP(1);
        if (w < 2) {
            asm volatile("s_waitcnt vmcnt(8)" : "+v"(xv), "+v"(ep) :: "memory");
            layernorm(xv, w, l);
            lwr<v4u>(L_TAG, v4u{mktag(ep), 0u, 0u, 0u});
        }
        bar();
        const unsigned tag = lrd<unsigned>(L_TAG);
        unsigned o_p1 = l < 16 ? WS_H1 + ((h * 4 + cq) * 128 + 16 * w + l) * 16 : OOB;
        unsigned o_g1 = WS_H1 + ((h * 4 + g) * 128 + 16 * w + l15) * 16;
        unsigned o_h2 = WS_H2 + ((((w & 3) * 2 + (l >> 5)) * 32 + h) * 128 + 32 * cq + (l & 31)) * 16;
        unsigned a_2w = l15 < 2 ? L_RED + l15 * 256 + (4 * w + g) * 8 : L_JUNK + (l & 31) * 8, a_2r = L_RED + l * 8;
        PIN(a_2w); PIN(a_2r);
        unsigned o_t2 = l < 8 ? WS_T2 + (l * 128 + h * 4 + cq) * 128 : OOB;
        unsigned a0, a1;
        {
            const unsigned q = l15 >> 2, p = l & 3, ch = 2 * w + (p >> 1);
            a0 = L_R0 + (8 * g + q) * 256 + 16 * (ch ^ ((q << 2) | ((2 * g) & 3))) + 8 * (p & 1);
            a1 = L_R0 + (8 * g + q + 4) * 256 + 16 * (ch ^ ((q << 2) | ((2 * g + 1) & 3))) + 8 * (p & 1);
        }
        unsigned a_qw = L_QS + (g & 1) * 272 + (16 * w + l15) * 2;
        unsigned a_qr = L_QS + (l & 1) * 272 + 16 * g;
        unsigned a_uk[4], o_gu[3], a_cnt = l == 0 ? L_TAG + 8 : L_JUNK + l * 4, one = 1u;
        v4u xf[4];
#pragma unroll
        for (int s = 0; s < 4; ++s) {
            xf[s] = lrd<v4u>(L_XN + (l & 1) * 1024 + (128 * cq + 32 * s + 8 * g) * 2);
            a_uk[s] = L_R1 + (16 * w + l15) * 256 + (((4 * s + g) ^ l15) << 4);
            PIN(xf[s]); PIN(a_uk[s]);
        }
        const int mat = un >> 6, mp = (un >> 2) & 15, fp = un & 3;
#pragma unroll
        for (int i = 0; i < 3; ++i) {
            const unsigned pc = min(8 * i + w, 21), idx = min(64 * pc + l, 1375u), row = idx / 43u, ch = idx - row * 43u;
            o_gu[i] = (32 * mp + row) * 2752 + fp * 688 + ch * 16; PIN(o_gu[i]);
        }
        v4u pb = {0u, tag, 0u, tag}, ga;
        PIN(a0); PIN(a1); PIN(o_p1); PIN(o_g1); PIN(o_h2); PIN(o_t2); PIN(a_qw); PIN(a_qr); PIN(pb); PIN(a_cnt); PIN(one);
        asm volatile("s_waitcnt vmcnt(4)" : "+v"(cp0) :: "memory");
        bar0();
        STAMP(2);
        {
            v4u f[4]; frags128(a0, a1, f);
            FENCE();
            const f32x4 acc = gemv4(xf, f);
            pb[0] = asu(acc[0]); pb[2] = asu(acc[1]);
            stx(rs, o_p1, pb);
        }
        STAMP(3);
        if (STOPAT <= 3) { WAITVM(0); STAMP(14); goto done; }
        __builtin_amdgcn_s_sleep(10);
        WAITVM(3);
        v4u fu[4];
        {
            unsigned arr;
            asm volatile("buffer_load_dwordx4 %1, %4, %5, 0 offen sc1\n\tds_add_rtn_u32 %0, %2, %3\n\ts_waitcnt lgkmcnt(0)" : "=&v"(arr), "=&v"(ga) : "v"(a_cnt), "v"(one), "v"(o_g1), "s"(rsq) : "memory");
            hint_put(rs, __builtin_amdgcn_readfirstlane(arr) == 7u ? o_t2 : OOB, tag);
            asm volatile("s_waitcnt vmcnt(1)" : "+v"(ga) :: "memory");
            STAMP(14);
#pragma unroll
            for (int s = 0; s < 4; ++s) fu[s] = lrd<v4u>(a_uk[s]);
            if (__builtin_expect(!none(tagx(ga, tag) != 0u), 0)) {
#pragma nounroll
                SPIN { __builtin_amdgcn_s_sleep(4); get1(ga, o_g1, rsq); if (none(tagx(ga, tag) != 0u)) break; }
            }
            lwr<u16>(a_qw, (u16)pk(sum32(sum16x2(asf(ga[0]), asf(ga[2]))), 0.f));
        }
        STAMP(4);
        bar();
        {
            v4u qf[4];
#pragma unroll
            for (int s = 0; s < 4; ++s) qf[s] = lrd<v4u>(a_qr + 64 * s);
            FENCE();
            const f32x4 acc = gemv4(fu, qf);
            constexpr float SC = 0.08838834764831845f * 1.4426950408889634f;
            lwr<v2u>(a_2w, v2u{pk(acc[0] * SC, acc[1] * SC), pk(acc[2] * SC, acc[3] * SC)});
            bar();
            if (w < 4) {
                const v2u t = lrd<v2u>(a_2r);
                pb[0] = t[0]; pb[2] = t[1];
                stx(rs, o_h2, pb);
            }
        }
        STAMP(5);
        if (STOPAT <= 5) goto done;
        const int r = xc * 8 + (yy >> 1);
        unsigned o_t3 = WS_T3 + ((xc * 4 + cq) * 64 + 8 * w + (l & 7)) * 128, o_t5 = WS_T5 + (r * 32 + (l & 31)) * 128, j0, j1;
        asm volatile("buffer_load_dword %0, %2, %4, 0 offen sc1\n\tbuffer_load_dword %1, %3, %4, 0 offen sc1"
                     : "=&v"(j0), "=&v"(j1) : "v"(o_t3), "v"((w == 0 && !(yy & 1)) ? o_t5 : o_t3), "s"(rsq) : "memory");
        unsigned trep = 0;
        if (VDEF == 2) trep = hint_wait(rsq, WS_T2 + (xc * 128 + (l & 7) * 16 + 5) * 128, tag);
#pragma unroll
        for (int i = 0; i < 3; ++i) glds(o_gu[i], mat ? wu : wg, L_R2 + min(8 * i + w, 21) * 1024);
#define V_ISSUE(i) glds<true>((EXP & 4) ? 0 : vb + (i) * 32768 + sw[i], wuv, L_R1 + (16 * w + 4 * (i)) * 256)
        if (!VDEF) { V_ISSUE(0); V_ISSUE(1); }
        {
            const v4u xg = lrd<v4u>(L_XN + (l & 1) * 1024 + (32 * mp + 8 * g) * 2);
            const unsigned ag = L_R2 + (8 * g + (l15 >> 2)) * 688 + (l & 3) * 8;
            const unsigned f = 16 * (8 * g + w) + l15;
            const unsigned o_hg = (g < 3 && f < 344) ? WS_HG + ((mat * 16 + mp) * 1376 + 344 * fp + f) * 16 : OOB;
            if (VDEF) asm volatile("s_waitcnt vmcnt(0)" : "+v"(j0), "+v"(j1) :: "memory"); else asm volatile("s_waitcnt vmcnt(2)" : "+v"(j0), "+v"(j1) :: "memory");
            bar();
            f32x4 o[3];
#pragma unroll
            for (int t = 0; t < 3; ++t) {
                const unsigned tl = min(8 * t + w, 21);
                o[t] = mfma32(xg, trd2(ag + tl * 32, ag + tl * 32 + 4 * 688), ZF);
            }
            const float o0 = g == 0 ? o[0][0] : g == 1 ? o[1][0] : o[2][0], o1 = g == 0 ? o[0][1] : g == 1 ? o[1][1] : o[2][1];
            stx(rs, o_hg, v4u{asu(o0), tag, asu(o1), tag});
        }
        if (VDEF) {
            pace(VDEF == 2 ? trep : hint_wait(rsq, WS_T2 + (xc * 128 + (l & 7) * 16 + 5) * 128, tag), PA);
            V_ISSUE(0); V_ISSUE(1);
        }
        V_ISSUE(2); V_ISSUE(3);
        STAMP(6);
        const unsigned u = l15 >= 12 ? 8 + l15 : (g & 1) ? 12 + (l15 & 7) : l15;
        const bool act = (g & 1) ? (l15 & 12) != 8 : l15 < 12;
        unsigned o_h3 = WS_H3 + (((w >> 2) * 32 + h) * 4 + cq) * H3S + (8 * (w & 3) + (g >> 1)) * 384 + u * 16;
        unsigned o_t4 = l < 4 ? WS_T4 + ((h * 4 + l) * 4 + cq) * 128 : OOB;
        unsigned o_p4 = l < 16 ? WS_H4 + ((h * 4 + cq) * 128 + 16 * w + l) * 16 : OOB;
        unsigned a_ow, a_ow2, a_ms;
        {
            const unsigned n = 6 * (u >> 2) + 2 * (g >> 1), n2 = 6 * (u >> 2) + 4, gg = u & 3;
            a_ow = (act && n < 32) ? L_OW + w * 272 + (32 * gg + 4 * ((n >> 2) ^ (gg >> 1)) + (n & 3)) * 2 : L_JUNK + l * 4;
            a_ow2 = (act && n2 < 32 && l < 32) ? L_OW + w * 272 + (32 * gg + 4 * ((n2 >> 2) ^ (gg >> 1)) + (n2 & 3)) * 2 : L_JUNK + l * 4;
            a_ms = (l & 31) == 12 ? L_MW + (g >> 1) * 32 + w * 4 : L_JUNK + l * 4;
        }
        unsigned a_or = L_OW + (l & 7) * 272 + 16 * g, a_mw = L_MW + (g & 1) * 16;
        v4u a[4];
        UNDEF(a[0]); UNDEF(a[1]); UNDEF(a[2]); UNDEF(a[3]);
        a_cnt += l == 0 ? 4 : 0;
        PIN(o_h3); PIN(o_t3); PIN(o_t4); PIN(o_p4); PIN(a_ow); PIN(a_ow2); PIN(a_or); PIN(a_mw); PIN(a_ms); PIN(a_cnt);
        WAITVM(0);
        bar();
        v4u fv[4];
        frags128(a0 + (L_R1 - L_R0), a1 + (L_R1 - L_R0), fv);
        PIN(fv[0]); PIN(fv[1]); PIN(fv[2]); PIN(fv[3]);
        STAMP(7);
        if (STOPAT <= 7) goto done;
        CNT0(2); CNT0(3);
        const unsigned t3 = hint_wait(rsq, o_t3, tag);
        STAMPC(1);
        pace(t3, P3);
        STAMP(15);
        {
#pragma nounroll
            SPIN { CNT(2); get4<768>(a, o_h3, rsq); if (none((tagy(a[0][3], a[1][3], tag) | tagy(a[2][3], a[3][3], tag)) != 0u)) break; __builtin_amdgcn_s_sleep(4); }
            STAMP(8);
            unsigned arr;
            unsigned long long tm;
            asm volatile("ds_add_rtn_u32 %0, %2, %3\n\ts_memrealtime %1" : "=&v"(arr), "=&s"(tm) : "v"(a_cnt), "v"(one) : "memory");
            float m[4], al[4];
#pragma unroll
            for (int k = 0; k < 4; ++k) m[k] = dpp<0x15c>(asf(a[k][1]));
            const float M = max32(mx(mx3(m[0], m[1], m[2]), m[3]));
#pragma unroll
            for (int k = 0; k < 4; ++k) al[k] = __builtin_amdgcn_exp2f(m[k] - M);
            FENCE();
            asm volatile("s_waitcnt lgkmcnt(0)" : "+v"(arr), "+s"(tm) :: "memory");
            hint_put(rs, __builtin_amdgcn_readfirstlane(arr) == 7u ? o_t4 : OOB, tag, (unsigned)tm);
            f32x2 c01, c23, c45;
            float Ls;
#pragma unroll
            for (int k = 0; k < 4; ++k) {
                const f32x2 aa = {al[k], al[k]}, v01 = {lo16(a[k][0]), hi16(a[k][0])}, v23 = {lo16(a[k][1]), hi16(a[k][1])}, v45 = {lo16(a[k][2]), hi16(a[k][2])};
                if (k == 0) { c01 = aa * v01; c23 = aa * v23; c45 = aa * v45; Ls = al[0] * asf(a[0][2]); }
                else { c01 = __builtin_elementwise_fma(aa, v01, c01); c23 = __builtin_elementwise_fma(aa, v23, c23); c45 = __builtin_elementwise_fma(aa, v45, c45); Ls = __builtin_fmaf(al[k], asf(a[k][2]), Ls); }
            }
            const float acc[6] = {c01[0], c01[1], c23[0], c23[1], c45[0], c45[1]};
            const float v4 = sum32x2(acc[4], Ls);
            lwr<unsigned>(a_ow, pk(sum32x2(acc[0], acc[2]), sum32x2(acc[1], acc[3])));
            lwr<unsigned>(a_ow2, pk(v4, sum32(acc[5])));
            lwr<float>(a_ms, l < 32 ? M : v4);
        }
        bar();
        {
            v4u of[4];
#pragma unroll
            for (int s = 0; s < 4; ++s) of[s] = lrd<v4u>(a_or + 64 * s);
            const f32x4 mw = lrd<f32x4>(a_mw), lw = lrd<f32x4>(a_mw + 32);
            FENCE();
            const f32x4 c = gemv4(of, fv);
            const float M = mx(mx3(mw[0], mw[1], mw[2]), mw[3]);
            float be[4];
#pragma unroll
            for (int r = 0; r < 4; ++r) be[r] = __builtin_amdgcn_exp2f(mw[r] - M);
            const float rd = __builtin_amdgcn_rcpf((be[0] * lw[0] + be[1] * lw[1]) + (be[2] * lw[2] + be[3] * lw[3]));
            const float v = ((be[0] * c[0] + be[1] * c[1]) + (be[2] * c[2] + be[3] * c[3])) * rd;
            auto sv = __builtin_amdgcn_permlane16_swap(asu(v), asu(v), false, false);
            pb[0] = sv[0]; pb[2] = sv[1];
            stx(rs, o_p4, pb);
        }
        STAMP(9);
        if ((yy & 1) == 0) {
            const unsigned o1 = WS_H5 + ((32 + (tid >> 3)) * 512 + 8 * r + (tid & 7)) * 16;
            const bool two = tid < 176;
            const unsigned o2 = two ? o1 + 64 * 512 * 16 : o1;
            pace(t3, PG);
#pragma nounroll
            SPIN { get2(a[0], a[1], o1, o2, rsq); if (none((tagx(a[0], tag) | tagx(a[1], tag)) != 0u)) break; __builtin_amdgcn_s_sleep(8); }
            {
                float t = sum32(sum16x2(asf(a[0][0]) + (two ? asf(a[1][0]) : 0.f), asf(a[0][2]) + (two ? asf(a[1][2]) : 0.f)));
                t += dpp<0x128>(t);
                lwr<float>(L_RED + ((g & 1) * 8 + (l & 7)) * 32 + w * 4, t);
            }
            bar();
            STAMP(10);
            if (w == 0) {
                const f32x4 t2 = lrd<f32x4>(L_RED + ((g & 1) * 8 + (l & 7)) * 32), t4 = lrd<f32x4>(L_RED + ((g & 1) * 8 + (l & 7)) * 32 + 16);
                float glu = rbf(((t2[0] + t2[1]) + (t2[2] + t2[3])) + ((t4[0] + t4[1]) + (t4[2] + t4[3])));
                float xin = lo16(x[(g & 1) * 512 + 8 * r + (l & 7)]);
                unsigned o0 = WS_H5 + ((l >> 3) * 512 + 8 * r + (l & 7)) * 16;
                unsigned o_out = ((g & 1) * 512 + 8 * r + (l & 7)) * 2;
                PIN(glu); PIN(xin); PIN(o0); PIN(o_out);
                pace(hint_wait(rsq, o_t5, tag), P5);
                STAMP(11);
                asm volatile("buffer_load_dwordx4 %0, %4, %5, 0 offen sc1\n\tbuffer_load_dwordx4 %1, %4, %5, %6 offen sc1\n\t"
                             "buffer_load_dwordx4 %2, %4, %5, %7 offen sc1\n\tbuffer_load_dwordx4 %3, %4, %5, %8 offen sc1"
                             : "=&v"(a[0]), "=&v"(a[1]), "=&v"(a[2]), "=&v"(a[3]) : "v"(o0), "s"(rsq), "s"(65536), "s"(131072), "s"(196608) : "memory");
                __builtin_amdgcn_raw_buffer_store_b32(tag, rs, (r == 0 && l == 0) ? 0u : OOB, 0, 16);
                asm volatile("s_waitcnt vmcnt(1)" : "+v"(a[0]), "+v"(a[1]), "+v"(a[2]), "+v"(a[3]) :: "memory");
                if (__builtin_expect(!none(tagx4(a, tag) != 0u), 0)) {
#pragma nounroll
                    SPIN { CNT(3); get4s(a, o0, rsq, 65536, 131072, 196608); if (none(tagx4(a, tag) != 0u)) break; }
                }
                STAMP(12);
                float t = sum32(sum16x2((asf(a[0][0]) + asf(a[1][0])) + (asf(a[2][0]) + asf(a[3][0])), (asf(a[0][2]) + asf(a[1][2])) + (asf(a[2][2]) + asf(a[3][2]))));
                t += dpp<0x128>(t);
                {
                    const unsigned v = pk(rbf(xin + rbf(t)) + glu, 0.f);
                    const unsigned p = __builtin_amdgcn_perm((unsigned)__builtin_amdgcn_update_dpp(0, (int)v, 0x101, 0xf, 0xf, true), v, 0x05040100u);
                    const v4u o = {p, (unsigned)__builtin_amdgcn_update_dpp(0, (int)p, 0x102, 0xf, 0xf, true), (unsigned)__builtin_amdgcn_update_dpp(0, (int)p, 0x104, 0xf, 0xf, true),
                                   (unsigned)__builtin_amdgcn_update_dpp(0, (int)p, 0x106, 0xf, 0xf, true)};
                    __builtin_amdgcn_raw_buffer_store_b128(o, __builtin_amdgcn_make_buffer_rsrc(out, 0, 2048, 0x00020000), (l == 0 || l == 16) ? o_out : OOB, 0, 0);
                }
            }
        }
    } else {
        const int pr = xc * 8 + (yy >> 1), hg = yy & 1, b = pr >> 5, sp = pr & 31;
        const unsigned t_in = now();
        unsigned ep, j0;
        unsigned o_t2 = WS_T2 + (xc * 128 + (16 * hg + 2 * w) * 4 + (l & 7)) * 128;
        asm volatile("global_load_dword %0, %1, %2 sc1" : "=v"(ep) : "v"(0), "s"(ws) : "memory");
        asm volatile("buffer_load_dword %0, %1, %2, 0 offen sc1" : "=v"(j0) : "v"(o_t2), "s"(rsq) : "memory");
        unsigned qoff = WS_H2 + ((((xc & 3) * 2 + b) * 32 + 16 * hg + 2 * w) * 128 + l) * 16;
        unsigned a_q0 = L_Q + 2 * w * 1024 + (((l >> 1) ^ (2 * w)) << 4) + (l & 1) * 8, a_q1 = L_Q + (2 * w + 1) * 1024 + (((l >> 1) ^ (2 * w + 1)) << 4) + (l & 1) * 8;
        unsigned o_kv[8];
#pragma unroll
        for (int i = 0; i < 8; ++i) { o_kv[i] = (pr * 128 + 64 * hg + 8 * w + i) * 1024 + ((l ^ (8 * (w & 1) + i)) << 4); PIN(o_kv[i]); }
        PIN(qoff); PIN(a_q0); PIN(a_q1);
        asm volatile("s_waitcnt vmcnt(0)" : "+v"(ep), "+v"(j0), "+v"(cp0) :: "memory");
        const unsigned tag = mktag(ep);
        pace(t_in, PB);
#pragma unroll
        for (int i = 0; i < 8; ++i) glds(o_kv[i], ckv, L_R0 + (8 * w + i) * 1024);
        STAMP(1);
        bar0();
#pragma unroll
        for (int i = 0; i < 8; ++i) glds(o_kv[i] ^ 65536, ckv, L_R0 + (64 + 8 * w + i) * 1024);
        WAITVM(8);
        bar0();
        STAMP(2);
#define Q_ISSUE() \
            FENCE(); \
            WAITVM(2); \
            const v2u hv = hint_get(rsq, o_t2);        \
            STAMP(14); \
            pace(none(hv[0] != tag) ? hv[1] : hint_wait(rsq, o_t2, tag), P2); \
            STAMP(15); \
            v4u qa[4]; \
            asm volatile("buffer_load_dwordx4 %0, %4, %5, 0 offen sc1\n\tbuffer_load_dwordx4 %1, %4, %5, 0 offen offset:1024 sc1\n\t" \
                         "buffer_load_dwordx4 %2, %4, %5, 0 offen offset:2048 sc1\n\tbuffer_load_dwordx4 %3, %4, %5, 0 offen offset:3072 sc1" \
                         : "=&v"(qa[0]), "=&v"(qa[1]), "=&v"(qa[2]), "=&v"(qa[3]) : "v"(qoff), "s"(rsq) : "memory"); \
            bar0()
#define Q_LAND(n) do { \
            asm volatile("s_waitcnt vmcnt(" STR(n) ")" : "+v"(qa[0]), "+v"(qa[1]), "+v"(qa[2]), "+v"(qa[3]) :: "memory"); \
            if (__builtin_expect(!none(tagx4(qa, tag) != 0u), 0)) { _Pragma("nounroll") SPIN { __builtin_amdgcn_s_sleep(4); get4<1024>(qa, qoff, rsq); if (none(tagx4(qa, tag) != 0u)) break; } } \
            asm volatile("ds_write2_b32 %0, %2, %3 offset1:1\n\tds_write2_b32 %0, %4, %5 offset0:128 offset1:129\n\tds_write2_b32 %1, %6, %7 offset1:1\n\tds_write2_b32 %1, %8, %9 offset0:128 offset1:129" \
                         :: "v"(a_q0), "v"(a_q1), "v"(qa[0][0]), "v"(qa[0][2]), "v"(qa[1][0]), "v"(qa[1][2]), "v"(qa[2][0]), "v"(qa[2][2]), "v"(qa[3][0]), "v"(qa[3][2]) : "memory"); \
            STAMP(3); bar();   } while (0)
        unsigned t5 = 0;
        const int h = 4 * xc + (yy >> 2), cq = yy & 3;
        unsigned o_t4 = WS_T4 + ((h * 4 + cq) * 4 + (l & 3)) * 128;
        unsigned o_hg = (EXP & 2) ? 0 : WS_HG + ((4 * (w & 3) + g) * 1376 + 16 * min(un, 85) + l15) * 16;
        if (w >= 4) {
            unsigned a_r[4];
#pragma unroll
            for (int j = 0; j < 4; ++j) a_r[j] = l15 * 1024 + (((4 * j + g) ^ l15) << 4);
            const unsigned kb = L_R0 + (w - 4) * 16384;
            v4u cr[32], qf[16];
#pragma unroll
            for (int s = 0; s < 16; ++s) cr[s] = lrd<v4u>(a_r[s & 3] + kb + (s >> 2) * 256);
            unsigned a_mxw = L_MX + l15 * 16 + (w - 4) * 4, a_mxr = L_MX + l15 * 16;
            unsigned a_pw = L_P + l15 * 256 + (((2 * (w - 4) + (g >> 1)) ^ l15) << 4) + (g & 1) * 8;
            __builtin_amdgcn_s_setprio(1);
            PIN(a_mxw); PIN(a_mxr); PIN(a_pw); PIN(o_hg);
            Q_ISSUE();
#pragma unroll
            for (int s = 0; s < 16; ++s) cr[16 + s] = lrd<v4u>(a_r[s & 3] + kb + 65536 + (s >> 2) * 256);
            FENCE();
            Q_LAND(0);
            STAMP(4);
#pragma unroll
            for (int s = 0; s < 16; ++s) qf[s] = lrd<v4u>(a_r[s & 3] + L_Q + (s >> 2) * 256);
            FENCE();
            f32x4 s1 = ZF, s2 = ZF;
#pragma unroll
            for (int s = 0; s < 16; ++s) { s1 = mfma32(cr[s], qf[s], s1); s2 = mfma32(cr[16 + s], qf[s], s2); }
            asm volatile("s_nop 7" : "+v"(s1), "+v"(s2));
            lwr<float>(a_mxw, max32(max16(mx(mx3(mx3(s1[0], s1[1], s1[2]), s1[3], s2[0]), mx3(s2[1], s2[2], s2[3])))));
            bar();
            STAMP(12);
            const f32x4 m0 = lrd<f32x4>(a_mxr);
            const float mr = mx(mx3(m0[0], m0[1], m0[2]), m0[3]);
            v2u p1 = {pk(__builtin_amdgcn_exp2f(s1[0] - mr), __builtin_amdgcn_exp2f(s1[1] - mr)), pk(__builtin_amdgcn_exp2f(s1[2] - mr), __builtin_amdgcn_exp2f(s1[3] - mr))};
            lwr<v2u>(a_pw, p1);
            v2u p2 = {pk(__builtin_amdgcn_exp2f(s2[0] - mr), __builtin_amdgcn_exp2f(s2[1] - mr)), pk(__builtin_amdgcn_exp2f(s2[2] - mr), __builtin_amdgcn_exp2f(s2[3] - mr))};
            lwr<v2u>(a_pw ^ 128, p2);
            bar();
            STAMP(5);
            PIN(p1); PIN(p2);
            {
                const unsigned q0 = p1[0], q1 = p1[1], q2 = p2[0], q3 = p2[1];
                lwr<float>(a_mxw + 256, sum32(sum16(((lo16(q0) + hi16(q0)) + (lo16(q1) + hi16(q1))) + ((lo16(q2) + hi16(q2)) + (lo16(q3) + hi16(q3))))));
            }
            bar();
            __builtin_amdgcn_s_setprio(0);
            t5 = now();
            if (PH) pace(t5, PH);
            v4u ga, gb;
            asm volatile("buffer_load_dwordx4 %0, %3, %6, 0 offen sc1\n\tbuffer_load_dwordx4 %1, %4, %6, 0 offen sc1\n\tbuffer_load_dword %2, %5, %6, 0 offen sc1\n\ts_waitcnt vmcnt(0)"
                         : "=&v"(ga), "=&v"(gb), "=&v"(j0), "+v"(o_hg) : "v"(o_hg + 16 * 1376 * 16), "v"(o_t4), "s"(rsq) : "memory");
            if (un < 86) {
                if (!(EXP & 2) && __builtin_expect(!none((tagx(ga, tag) | tagx(gb, tag)) != 0u), 0)) {
#pragma nounroll
                    SPIN { __builtin_amdgcn_s_sleep(16); get2(ga, gb, o_hg, o_hg + 16 * 1376 * 16, rsq); if (none((tagx(ga, tag) | tagx(gb, tag)) != 0u)) break; }
                }
                lwr<float>(L_RED + (l & 31) * 32 + (w - 4) * 4, sum32(sum16x2(asf(ga[0]), asf(ga[2]))));
                lwr<float>(L_RED + (l & 31) * 32 + w * 4, sum32(sum16x2(asf(gb[0]), asf(gb[2]))));
            }
            pace(t5, PW);
#pragma unroll
            for (int i = 0; i < 8; ++i) {
                const unsigned ch = l15 ^ ((g << 2) | (i & 3));
                glds((h * 128 + 32 * (w - 4) + 4 * i + g) * 1024 + cq * 128 + (ch >> 3) * 512 + (ch & 7) * 16, wo, L_R1 + (32 * (w - 4) + 4 * i) * 256);
            }
        } else {
            unsigned a_t[2][4];
            {
                const unsigned q = l15 >> 2, p = l & 3;
#pragma unroll
                for (int j = 0; j < 8; ++j) {
                    const unsigned r0 = 8 * g + 4 * (j >> 2) + q, ch = 16 * w + 4 * p + (j & 3);
                    a_t[j >> 2][j & 3] = L_R0 + r0 * 1024 + ((ch ^ (r0 & 15)) << 4) + (p >> 1) * 8;
                }
            }
            v4u ct[8][4];
#pragma unroll
            for (int t = 0; t < 8; ++t)
#pragma unroll
                for (int s = 0; s < 2; ++s) ct[t][s] = trd2((a_t[0][t >> 1] ^ ((t & 1) * 8)) + s * 32768, (a_t[1][t >> 1] ^ ((t & 1) * 8)) + s * 32768);
            unsigned a_pr[4], a_mxr = L_MX + l15 * 16;
#pragma unroll
            for (int j = 0; j < 4; ++j) { a_pr[j] = L_P + l15 * 256 + (((4 * j + g) ^ l15) << 4); PIN(a_pr[j]); }
            unsigned o_t3 = l < 4 ? WS_T3 + (((4 * hg + l) * 4 + w) * 64 + pr) * 128 : OOB;
            unsigned o_h3 = WS_H3 + ((b * 32 + 16 * hg + (l >> 3)) * 4 + w) * H3S + sp * 384 + (l & 7) * 16, o_h3b = o_h3 + 8 * 4 * H3S;
            unsigned a_sw = L_R2 + w * 8192 + l15 * 144 + g * 16, a_sr = L_R2 + w * 8192 + (l >> 3) * 144 + (l & 7) * 16;
            PIN(a_sw); PIN(a_sr);
            unsigned o_wd[4], a_lr = L_MX + 256 + l15 * 16;
#pragma unroll
            for (int i = 0; i < 4; ++i) { const unsigned row = 4 * w + i; o_wd[i] = (EXP & 1) ? 0 : (16 * min(un, 85) + row) * 1024 + ((l ^ ((row & 7) << 1)) << 4); PIN(o_wd[i]); }
            PIN(a_mxr); PIN(o_t3); PIN(o_h3); PIN(o_h3b); PIN(a_lr);
            Q_ISSUE();
            v4u wst[4];
            asm volatile("global_load_dwordx4 %0, %4, %8\n\tglobal_load_dwordx4 %1, %5, %8\n\tglobal_load_dwordx4 %2, %6, %8\n\tglobal_load_dwordx4 %3, %7, %8"
                         : "=&v"(wst[0]), "=&v"(wst[1]), "=&v"(wst[2]), "=&v"(wst[3]) : "v"(o_wd[0]), "v"(o_wd[1]), "v"(o_wd[2]), "v"(o_wd[3]), "s"(wd) : "memory");
#pragma unroll
            for (int t = 0; t < 8; ++t)
#pragma unroll
                for (int s = 2; s < 4; ++s) ct[t][s] = trd2((a_t[0][t >> 1] ^ ((t & 1) * 8)) + s * 32768, (a_t[1][t >> 1] ^ ((t & 1) * 8)) + s * 32768);
            FENCE();
            Q_LAND(4);
            STAMP(4);
            bar0();
            const f32x4 m0 = lrd<f32x4>(a_mxr);
            float mr = mx(mx3(m0[0], m0[1], m0[2]), m0[3]);
            PIN(mr);
            bar();
            STAMP(5);
            v4u pf[4];
#pragma unroll
            for (int s = 0; s < 4; ++s) pf[s] = lrd<v4u>(a_pr[s]);
            if (HP == 0) hint_put(rs, o_t3, tag);
            FENCE();
            f32x4 o[8];
#define M0_(t) asm volatile("v_mfma_f32_16x16x32_bf16 %0, %1, %2, 0" : "=&v"(o[t]) : "v"(ct[t][0]), "v"(pf[0]))
#define MM(t, s) asm volatile("v_mfma_f32_16x16x32_bf16 %0, %1, %2, %0" : "+v"(o[t]) : "v"(ct[t][s]), "v"(pf[s]))
#define RDY(t) PIN(o[t])
#define SEQ() asm volatile("" ::: "memory")
#define CV(d, t, i) do { d = pk(o[t][i], o[t][i + 1]); PIN(d); } while (0)
#define PUT(n, k, A, B, C) lwr<v4u>(a_sw + (n) * 2304 + (k) * 64, v4u{A, B, C, tag})
#define GETL(n) do { x0 = lrd<v4u>(a_sr + (n) * 2304); x1 = lrd<v4u>(a_sr + (n) * 2304 + 8 * 144); } while (0)
#define OUTL(n) do { stx(rs, o_h3 + (n) * 128, x0); stx(rs, o_h3b + (n) * 128, x1); } while (0)
            unsigned d0, d1, d2, d3, d4, d5;
            v4u x0, x1;
            M0_(0); M0_(1); MM(0, 1); MM(1, 1); MM(0, 2); MM(1, 2); MM(0, 3); MM(1, 3);
            M0_(2); MM(2, 1); MM(2, 2); MM(2, 3);
            M0_(3); MM(3, 1); MM(3, 2);
            RDY(0); CV(d0, 0, 0); CV(d1, 0, 2);
            MM(3, 3); RDY(1); CV(d2, 1, 0); CV(d3, 1, 2);
            M0_(4); PUT(0, 0, d0, d1, d2); SEQ();
            MM(4, 1); RDY(2); CV(d4, 2, 0); CV(d5, 2, 2);
            MM(4, 2); PUT(0, 1, d3, d4, d5); GETL(0); SEQ();
            MM(4, 3); RDY(3); CV(d0, 3, 0); CV(d1, 3, 2);
            M0_(5);
            bar0();
            const f32x4 l0 = lrd<f32x4>(a_lr);
            MM(5, 1); MM(5, 2); MM(5, 3);
            RDY(4); CV(d2, 4, 0); CV(d3, 4, 2);
            STAMP2(3);
            M0_(6); OUTL(0); if (HP == 1) hint_put(rs, o_t3, tag); PUT(1, 0, d0, d1, d2); SEQ();
            STAMP2(4);
            MM(6, 1);
            float lsum = (l0[0] + l0[1]) + (l0[2] + l0[3]);
            PIN(lsum);
            MM(6, 2); MM(6, 3);
            RDY(5); CV(d4, 5, 0); CV(d5, 5, 2);
            M0_(7); PUT(1, 1, d3, d4, d5); GETL(1); SEQ();
            MM(7, 1); MM(7, 2); MM(7, 3);
            asm volatile("s_nop 1" : "+v"(o[6]));
            CV(d0, 6, 0); CV(d1, 6, 2);
            STAMP2(14);
            OUTL(1); if (HP == 2) hint_put(rs, o_t3, tag); SEQ();
            STAMP2(15);
            asm volatile("s_nop 3" : "+v"(o[7]));
            CV(d2, 7, 0); CV(d3, 7, 2);
            PUT(2, 0, d0, d1, d2); PUT(2, 1, d3, asu(mr), asu(lsum)); GETL(2); OUTL(2); if (HP == 3) hint_put(rs, o_t3, tag);
            FENCE();
            STAMP(6);
            asm volatile("s_waitcnt vmcnt(7)" : "+v"(wst[0]), "+v"(wst[1]), "+v"(wst[2]), "+v"(wst[3]) :: "memory");
#pragma unroll
            for (int i = 0; i < 4; ++i) lwr<v4u>(L_R0 + (4 * w + i) * 1024 + l * 16, wst[i]);
        }
        if (STOPAT <= 7) goto done;
        bar();
        if (un < 86) {
            if (w == 4) {
                const f32x4 gp = lrd<f32x4>(L_RED + (l & 31) * 32), up = lrd<f32x4>(L_RED + (l & 31) * 32 + 16);
                const float gg = rbf((gp[0] + gp[1]) + (gp[2] + gp[3])), uu = rbf((up[0] + up[1]) + (up[2] + up[3]));
                const float sg = rbf(__builtin_amdgcn_rcpf(1.f + __builtin_amdgcn_exp2f(gg * -1.4426950408889634f)));
                lwr<u16>(L_HH + (l & 31) * 2, (u16)pk(sg * uu, 0.f));
            }
            bar();
            const v2u ha = lrd<v2u>(L_HH + (l & 1) * 32 + g * 8);
            const unsigned q = l15 >> 2, p = l & 3, row = 4 * g + q;
            f32x4 o[4];
#pragma unroll
            for (int t = 0; t < 4; ++t) {
                const unsigned ch = 8 * w + 2 * t + (p >> 1);
                o[t] = mfma16(ha, trd(L_R0 + row * 1024 + ((ch ^ ((row & 7) << 1)) << 4) + (p & 1) * 8), ZF);
            }
            const float o0 = g == 0 ? o[0][0] : g == 1 ? o[1][0] : g == 2 ? o[2][0] : o[3][0];
            const float o1 = g == 0 ? o[0][1] : g == 1 ? o[1][1] : g == 2 ? o[2][1] : o[3][1];
            stx(rs, WS_H5 + ((32 + un) * 512 + 64 * w + l) * 16, v4u{asu(o0), tag, asu(o1), tag});
        }
        STAMP(7);
        {
            unsigned a0, a1;
            {
                const unsigned q = l15 >> 2, p = l & 3, ch = 2 * w + (p >> 1);
                a0 = L_R1 + (8 * g + q) * 256 + 16 * (ch ^ ((q << 2) | ((2 * g) & 3))) + 8 * (p & 1);
                a1 = L_R1 + (8 * g + q + 4) * 256 + 16 * (ch ^ ((q << 2) | ((2 * g + 1) & 3))) + 8 * (p & 1);
            }
            unsigned o_g4 = WS_H4 + ((h * 4 + g) * 128 + 16 * w + l15) * 16;
            unsigned o_t5 = tid < 16 ? WS_T5 + ((8 * (cq + 4 * (tid >> 3)) + (tid & 7)) * 32 + h) * 128 : OOB;
            unsigned o_h5 = l < 16 ? WS_H5 + (h * 512 + 64 * (cq + 4 * (w >> 2)) + 16 * (w & 3) + l) * 16 : OOB;
            unsigned a_qw = L_QS + (g & 1) * 272 + (16 * w + l15) * 2, a_qr = L_QS + (l & 1) * 272 + 16 * g;
            v4u pb = {0u, tag, 0u, tag}, fw[4], ga;
            WAITVM(0);
            bar();
            frags128(a0, a1, fw);
            PIN(fw[0]); PIN(fw[1]); PIN(fw[2]); PIN(fw[3]); PIN(o_g4); PIN(o_t5); PIN(o_h5); PIN(a_qw); PIN(a_qr); PIN(pb);
            STAMP(10);
            if (w == 4) pace(hint_wait(rsq, o_t4, tag), P4);
            bar0();
            STAMP(11);
            asm volatile("buffer_load_dwordx4 %0, %1, %2, 0 offen sc1" : "=&v"(ga) : "v"(o_g4), "s"(rsq) : "memory");
            hint_put(rs, o_t5, tag);
            asm volatile("s_waitcnt vmcnt(1)" : "+v"(ga) :: "memory");
            if (__builtin_expect(!none(tagx(ga, tag) != 0u), 0)) {
#pragma nounroll
                SPIN { __builtin_amdgcn_s_sleep(2); get1(ga, o_g4, rsq); if (none(tagx(ga, tag) != 0u)) break; }
            }
            STAMP(8);
            lwr<u16>(a_qw, (u16)pk(sum32(sum16x2(asf(ga[0]), asf(ga[2]))), 0.f));
            bar();
            v4u cf[4];
#pragma unroll
            for (int s = 0; s < 4; ++s) cf[s] = lrd<v4u>(a_qr + 64 * s);
            FENCE();
            const f32x4 acc = gemv4(cf, fw);
            pb[0] = asu(acc[0]); pb[2] = asu(acc[1]);
            stx(rs, o_h5, pb);
        }
        STAMP(9);
    }
done:
    STAMP(13);
}

extern "C" void kernel_launch(void* const* d_in, const int* in_sizes, int n_in, void* d_out, int out_size, void* d_ws, size_t ws_size,
                              hipStream_t stream) {
    (void)in_sizes; (void)n_in; (void)out_size; (void)ws_size;
    (void)hipFuncSetAttribute((const void*)mla_glu_decode, hipFuncAttributeMaxDynamicSharedMemorySize, L_END);
    mla_glu_decode<<<256, 512, L_END, stream>>>((const u16*)d_in[0], (const u16*)d_in[1], (const u16*)d_in[2], (const u16*)d_in[3], (const u16*)d_in[4],
                                               (const u16*)d_in[5], (const u16*)d_in[7], (const u16*)d_in[8], (const u16*)d_in[9], (u16*)d_out,
                                               (unsigned char*)d_ws);
}
```

```cpp
#include <hip/hip_runtime.h>
#include <hip/hip_bf16.h>

typedef unsigned short u16;
typedef unsigned v4u __attribute__((ext_vector_type(4)));
typedef unsigned v2u __attribute__((ext_vector_type(2)));
typedef float f32x4 __attribute__((ext_vector_type(4)));
typedef float f32x2 __attribute__((ext_vector_type(2)));
typedef short s16x4 __attribute__((ext_vector_type(4)));
typedef __bf16 bf16x8 __attribute__((ext_vector_type(8)));
typedef __bf16 bf16x2 __attribute__((ext_vector_type(2)));
#define LAS __attribute__((address_space(3)))
#define GAS __attribute__((address_space(1)))
#define DEVI __device__ __forceinline__

#define PB 85
#define P2 100
#define P3 60
#define P4 55
#define P5 50
#define PG 105
#define PW 60
#define PA 0
#define CODEB 14080
#define EXP 0
#define PH 0
#define H3S 12288
#define XMAP 0
#define VDEF 1
#define HP 2
#define PREH3 0
#define NCOPY 1
#define STOPAT 99

constexpr int WS_T2 = 4096;
constexpr int WS_T3 = WS_T2 + 8 * 128 * 128;
constexpr int WS_T5 = WS_T3 + 8 * 64 * 4 * 128;
constexpr int WS_T4 = WS_T5 + 64 * 32 * 128;
constexpr int WS_H1 = WS_T4 + 32 * 16 * 128;
constexpr int WS_H4 = WS_H1 + 32 * 4 * 128 * 16;
constexpr int WS_H2 = WS_H4 + 32 * 4 * 128 * 16;
constexpr int WS_HG = WS_H2 + 4 * 2 * 32 * 128 * 16;
constexpr int WS_H5 = WS_HG + 2 * 16 * 1376 * 16;
constexpr int WS_H3 = WS_H5 + 118 * 512 * 16;
constexpr int WS_END = WS_H3 + 2 * 32 * 4 * H3S;
constexpr int WS_STAMP = 16 << 20;
constexpr int L_Q = 0;
constexpr int L_P = 16384;
constexpr int L_QS = L_P + 4096;
constexpr int L_OW = L_QS + 1024;
constexpr int L_MW = L_OW + 2304;
constexpr int L_MX = L_MW + 64;
constexpr int L_HH = L_MX + 512;
constexpr int L_TAG = L_HH + 64;
constexpr int L_JUNK = L_TAG + 64;
constexpr int L_RED = L_JUNK + 256;
constexpr int L_XN = 28672;
constexpr int L_R0 = 32768, L_R1 = 65536, L_R2 = 98304, L_R3 = 131072;
constexpr int L_END = 163840;
static_assert(L_RED + 1024 <= L_XN && L_XN + 2048 <= L_R0, "lds");
constexpr int SPIN_CAP = 1 << 16;

DEVI unsigned pk(float lo, float hi) { bf16x2 r = {(__bf16)lo, (__bf16)hi}; return __builtin_bit_cast(unsigned, r); }
DEVI float rbf(float f) { return (float)(__bf16)f; }
DEVI float lo16(unsigned w) { return __builtin_bit_cast(float, w << 16); }
DEVI float hi16(unsigned w) { return __builtin_bit_cast(float, w & 0xffff0000u); }
DEVI float asf(unsigned w) { return __builtin_bit_cast(float, w); }
DEVI unsigned asu(float w) { return __builtin_bit_cast(unsigned, w); }
DEVI float mx(float a, float b) { float r; asm("v_max_f32 %0, %1, %2" : "=v"(r) : "v"(a), "v"(b)); return r; }
DEVI float mx3(float a, float b, float c) { float r; asm("v_max3_f32 %0, %1, %2, %3" : "=v"(r) : "v"(a), "v"(b), "v"(c)); return r; }
DEVI f32x4 mfma32(v4u a, v4u b, f32x4 c) { return __builtin_amdgcn_mfma_f32_16x16x32_bf16(__builtin_bit_cast(bf16x8, a), __builtin_bit_cast(bf16x8, b), c, 0, 0, 0); }
DEVI f32x4 mfma16(v2u a, v2u b, f32x4 c) { return __builtin_amdgcn_mfma_f32_16x16x16bf16_1k(__builtin_bit_cast(s16x4, a), __builtin_bit_cast(s16x4, b), c, 0, 0, 0); }
template <class T> DEVI T lrd(unsigned off) { return *(LAS T*)(size_t)off; }
template <class T> DEVI void lwr(unsigned off, T v) { *(LAS T*)(size_t)off = v; }
DEVI v2u trd(unsigned off) { return __builtin_bit_cast(v2u, __builtin_amdgcn_ds_read_tr16_b64_v4i16((LAS s16x4*)(size_t)off)); }
DEVI v4u trd2(unsigned o0, unsigned o1) { v2u a = trd(o0), b = trd(o1); return v4u{a.x, a.y, b.x, b.y}; }
DEVI float sum16(float x) { auto r = __builtin_amdgcn_permlane16_swap(asu(x), asu(x), false, false); return asf(r[0]) + asf(r[1]); }
DEVI float sum32(float x) { auto r = __builtin_amdgcn_permlane32_swap(asu(x), asu(x), false, false); return asf(r[0]) + asf(r[1]); }
DEVI float max16(float x) { auto r = __builtin_amdgcn_permlane16_swap(asu(x), asu(x), false, false); return mx(asf(r[0]), asf(r[1])); }
DEVI float max32(float x) { auto r = __builtin_amdgcn_permlane32_swap(asu(x), asu(x), false, false); return mx(asf(r[0]), asf(r[1])); }
DEVI float odd16(float x) { auto r = __builtin_amdgcn_permlane16_swap(asu(x), asu(x), false, false); return asf(r[1]); }
DEVI float sum16x2(float x, float y) { auto r = __builtin_amdgcn_permlane16_swap(asu(x), asu(y), false, false); return asf(r[0]) + asf(r[1]); }
DEVI float sum32x2(float x, float y) { auto r = __builtin_amdgcn_permlane32_swap(asu(x), asu(y), false, false); return asf(r[0]) + asf(r[1]); }
template <int CTRL> DEVI float dpp(float x) { return asf((unsigned)__builtin_amdgcn_update_dpp(0, (int)asu(x), CTRL, 0xf, 0xf, true)); }
DEVI float wavesum(float x) {
    x += dpp<0xB1>(x); x += dpp<0x4E>(x); x += dpp<0x141>(x); x += dpp<0x140>(x);
    return sum32(sum16(x));
}
DEVI float dot2one(unsigned p, float c) { const unsigned one = 0x3f803f80u; return __builtin_amdgcn_fdot2_f32_bf16(__builtin_bit_cast(bf16x2, p), __builtin_bit_cast(bf16x2, one), c, false); }
#define STR_(x) #x
#define STR(x) STR_(x)
#define WAITVM(n) asm volatile("s_waitcnt vmcnt(" STR(n) ")" ::: "memory")
#define PIN(x) asm volatile("" : "+v"(x))
#define MFMA_SETTLE(x) asm volatile("s_nop 7" : "+v"(x))
#define UNDEF(x) asm volatile("" : "=v"(x))
#define FENCE() __builtin_amdgcn_sched_barrier(0)
#define SPIN for (int it_ = SPIN_CAP; it_ > 0; --it_)
DEVI void bar() { asm volatile("s_waitcnt lgkmcnt(0)\n\ts_barrier" ::: "memory"); }
DEVI void bar0() { asm volatile("s_barrier" ::: "memory"); }
#define USE_NT 0
template <bool NT = false> DEVI void glds(unsigned voff, const void* sbase, unsigned lds) {
    unsigned keep;
    if (NT && USE_NT) asm volatile("s_mov_b32 %0, m0\n\ts_mov_b32 m0, %3\n\ts_nop 0\n\tglobal_load_lds_dwordx4 %1, %2 nt\n\ts_mov_b32 m0, %0" : "=&s"(keep) : "v"(voff), "s"(sbase), "s"(lds) : "memory");
    else asm volatile("s_mov_b32 %0, m0\n\ts_mov_b32 m0, %3\n\ts_nop 0\n\tglobal_load_lds_dwordx4 %1, %2\n\ts_mov_b32 m0, %0" : "=&s"(keep) : "v"(voff), "s"(sbase), "s"(lds) : "memory");
}
typedef __amdgpu_buffer_rsrc_t rsrc_t;
constexpr unsigned OOB = 0x80000000u;
DEVI void stx(rsrc_t rs, unsigned off, v4u v) { __builtin_amdgcn_raw_buffer_store_b128(v, rs, off, 0, 16); }
DEVI bool none(bool bad) { asm volatile("" ::: "memory"); return __builtin_amdgcn_ballot_w64(bad) == 0ull; }
DEVI unsigned tagx(v4u a, unsigned tag) { unsigned r = (a[1] ^ tag) | (a[3] ^ tag); asm("" : "+v"(r)); return r; }
DEVI unsigned tagy(unsigned a, unsigned b, unsigned tag) { unsigned r = (a ^ tag) | (b ^ tag); asm("" : "+v"(r)); return r; }
DEVI unsigned tagx4(const v4u (&a)[4], unsigned tag) { return (tagx(a[0], tag) | tagx(a[1], tag)) | (tagx(a[2], tag) | tagx(a[3], tag)); }
DEVI void get1(v4u& a, unsigned off, v4u rq) { asm volatile("buffer_load_dwordx4 %0, %1, %2, 0 offen sc1\n\ts_waitcnt vmcnt(0)" : "+&v"(a) : "v"(off), "s"(rq) : "memory"); }
DEVI void get2(v4u& a, v4u& b, unsigned oa, unsigned ob, v4u rq) {
    asm volatile("buffer_load_dwordx4 %0, %2, %4, 0 offen sc1\n\tbuffer_load_dwordx4 %1, %3, %4, 0 offen sc1\n\ts_waitcnt vmcnt(0)" : "+&v"(a), "+&v"(b) : "v"(oa), "v"(ob), "s"(rq) : "memory");
}
template <int ST> DEVI void get4(v4u (&a)[4], unsigned off, v4u rq) {
    asm volatile("buffer_load_dwordx4 %0, %4, %5, 0 offen sc1\n\tbuffer_load_dwordx4 %1, %4, %5, 0 offen offset:%6 sc1\n\t"
                 "buffer_load_dwordx4 %2, %4, %5, 0 offen offset:%7 sc1\n\tbuffer_load_dwordx4 %3, %4, %5, 0 offen offset:%8 sc1\n\ts_waitcnt vmcnt(0)"
                 : "+&v"(a[0]), "+&v"(a[1]), "+&v"(a[2]), "+&v"(a[3]) : "v"(off), "s"(rq), "i"(ST), "i"(2 * ST), "i"(3 * ST) : "memory");
}
DEVI void get4s(v4u (&a)[4], unsigned off, v4u rq, unsigned s1, unsigned s2, unsigned s3) {
    asm volatile("buffer_load_dwordx4 %0, %4, %5, 0 offen sc1\n\tbuffer_load_dwordx4 %1, %4, %5, %6 offen sc1\n\t"
                 "buffer_load_dwordx4 %2, %4, %5, %7 offen sc1\n\tbuffer_load_dwordx4 %3, %4, %5, %8 offen sc1\n\ts_waitcnt vmcnt(0)"
                 : "+&v"(a[0]), "+&v"(a[1]), "+&v"(a[2]), "+&v"(a[3]) : "v"(off), "s"(rq), "s"(s1), "s"(s2), "s"(s3) : "memory");
}
DEVI unsigned now() { return (unsigned)__builtin_amdgcn_s_memrealtime(); }
DEVI void hint_put(rsrc_t rs, unsigned off, unsigned tag, unsigned t) { __builtin_amdgcn_raw_buffer_store_b64(v2u{tag, t}, rs, off, 0, 16); }
DEVI void hint_put(rsrc_t rs, unsigned off, unsigned tag) { hint_put(rs, off, tag, now()); }
DEVI v2u hint_get(v4u rq, unsigned off) { v2u v; asm volatile("buffer_load_dwordx2 %0, %1, %2, 0 offen sc1\n\ts_waitcnt vmcnt(0)" : "=&v"(v) : "v"(off), "s"(rq) : "memory"); return v; }
DEVI void pace(unsigned t, unsigned lead) {
#pragma nounroll
    for (int it = 256; it > 0; --it) { if (none(now() - t < lead)) break; }
}
DEVI unsigned hint_wait(v4u rq, unsigned off, unsigned tag) {
    v2u v;
#pragma nounroll
    SPIN { v = hint_get(rq, off); if (none(v[0] != tag)) break; __builtin_amdgcn_s_sleep(1); }
    return v[1];
}
DEVI unsigned mktag(unsigned ep) { return ep + 1u; }

#define STAMP(i)

#define CNT0(i)
#define CNT(i)
#define STAMPC(i)
#define STAMP2(i)
DEVI void frags128(unsigned a0, unsigned a1, v4u (&f)[4]) {
#pragma unroll
    for (int s = 0; s < 4; ++s) f[s] = trd2(a0 + s * 8192, a1 + s * 8192);
}
DEVI f32x4 gemv4(const v4u (&a)[4], const v4u (&b)[4]) {
    f32x4 c = {0.f, 0.f, 0.f, 0.f};
#pragma unroll
    for (int s = 0; s < 4; ++s) c = mfma32(a[s], b[s], c);
    return c;
}
DEVI void layernorm(v4u xv, int w, int l) {
    float v[8];
#pragma unroll
    for (int j = 0; j < 4; ++j) { v[2 * j] = lo16(xv[j]); v[2 * j + 1] = hi16(xv[j]); }
    const float mean = wavesum(((v[0] + v[1]) + (v[2] + v[3])) + ((v[4] + v[5]) + (v[6] + v[7]))) * (1.f / 512.f);
    float sq = 0.f;
#pragma unroll
    for (int j = 0; j < 8; ++j) { const float d = v[j] - mean; sq += d * d; }
    const float var = wavesum(sq) * (1.f / 512.f);
    const float mb = rbf(mean), ri = __builtin_amdgcn_rcpf(rbf(__builtin_amdgcn_sqrtf(rbf(rbf(var) + 1e-5f))));
    v4u o;
#pragma unroll
    for (int j = 0; j < 4; ++j) o[j] = pk(rbf(v[2 * j] - mb) * ri, rbf(v[2 * j + 1] - mb) * ri);
    lwr<v4u>(L_XN + w * 1024 + l * 16, o);
}

__global__ void __launch_bounds__(512, 2)
mla_glu_decode(const u16* __restrict__ x, const u16* __restrict__ ckv, const u16* __restrict__ wq, const u16* __restrict__ wuk,
               const u16* __restrict__ wuv, const u16* __restrict__ wo, const u16* __restrict__ wg, const u16* __restrict__ wu,
               const u16* __restrict__ wd, u16* __restrict__ out, unsigned char* __restrict__ ws) {
    STAMP(0);
    const int tid = threadIdx.x, l = tid & 63, w = __builtin_amdgcn_readfirstlane(tid >> 6);
    const int id = blockIdx.x, ai = (id & 3) * 32 + (id >> 3);
    const int xc = XMAP ? ai >> 4 : id & 7, yy = XMAP ? ai & 15 : (id >> 3) & 15, y = XMAP ? (id & 4) * 4 : id >> 3;
    const int un = yy * 8 + xc;
    const unsigned g = l >> 4, l15 = l & 15;
    unsigned cp0 = 0;
    if (w == 7) {
        unsigned long long pc;
        asm volatile("s_getpc_b64 %0" : "=s"(pc));
        pc &= ~255ull;
        asm volatile("global_load_dword %0, %1, %2" : "=&v"(cp0) : "v"(min(((id >> 3) + 32 * (l & 3)) * 128, CODEB - 128)), "s"(pc) : "memory");
    }
    unsigned vb = (128 * (yy & 3) + 16 * w + g) * 8192 + (4 * xc + (yy >> 2)) * 256, sw[4], o_q[4];
#pragma unroll
    for (int i = 0; i < 4; ++i) { sw[i] = (l15 ^ ((g << 2) | i)) << 4; o_q[i] = vb + i * 32768 + sw[i]; PIN(o_q[i]); }
    asm volatile("" :: "s"(x), "s"(ckv), "s"(wq), "s"(wuk), "s"(wuv), "s"(wo), "s"(wg), "s"(wu), "s"(wd), "s"(out), "s"(ws));
    const rsrc_t rs = __builtin_amdgcn_make_buffer_rsrc(ws, 0, WS_END, 0x00020000);
    const v4u rsq = {(unsigned)(size_t)ws, (unsigned)((size_t)ws >> 32) & 0xffffu, (unsigned)WS_END, 0x00020000u};
    const f32x4 ZF = {0.f, 0.f, 0.f, 0.f};

    if (__builtin_expect(y < 16, 1)) {
        const int h = 4 * xc + (yy >> 2), cq = yy & 3;
        v4u xv; unsigned ep;
        if (w < 2) {
            asm volatile("global_load_dwordx4 %0, %1, %2" : "=v"(xv) : "v"(w * 1024 + l * 16), "s"(x) : "memory");
            asm volatile("global_load_dword %0, %1, %2 sc1" : "=v"(ep) : "v"(0), "s"(ws) : "memory");
        }
#pragma unroll
        for (int i = 0; i < 4; ++i) glds<true>(o_q[i], wq, L_R0 + (16 * w + 4 * i) * 256);
        bar0();
#pragma unroll
        for (int i = 0; i < 4; ++i) glds<true>(vb + i * 32768 + ((l15 ^ (4 * i + g)) << 4), wuk, L_R1 + (16 * w + 4 * i) * 256);
        STAMP(1);
        if (w < 2) {
            asm volatile("s_waitcnt vmcnt(8)" : "+v"(xv), "+v"(ep) :: "memory");
            layernorm(xv, w, l);
            lwr<v4u>(L_TAG, v4u{mktag(ep), 0u, 0u, 0u});
        }
        bar();
        const unsigned tag = lrd<unsigned>(L_TAG);
        unsigned o_p1 = l < 16 ? WS_H1 + ((h * 4 + cq) * 128 + 16 * w + l) * 16 : OOB;
        unsigned o_g1 = WS_H1 + ((h * 4 + g) * 128 + 16 * w + l15) * 16;
        unsigned o_h2 = WS_H2 + ((((w & (NCOPY - 1)) * 2 + (l >> 5)) * 32 + h) * 128 + 32 * cq + (l & 31)) * 16;
        unsigned a_2w = l15 < 2 ? L_RED + l15 * 256 + (4 * w + g) * 8 : L_JUNK + (l & 31) * 8, a_2r = L_RED + l * 8;
        PIN(a_2w); PIN(a_2r);
        unsigned o_t2 = l < 8 ? WS_T2 + (l * 128 + h * 4 + cq) * 128 : OOB;
        unsigned a0, a1;
        {
            const unsigned q = l15 >> 2, p = l & 3, ch = 2 * w + (p >> 1);
            a0 = L_R0 + (8 * g + q) * 256 + 16 * (ch ^ ((q << 2) | ((2 * g) & 3))) + 8 * (p & 1);
            a1 = L_R0 + (8 * g + q + 4) * 256 + 16 * (ch ^ ((q << 2) | ((2 * g + 1) & 3))) + 8 * (p & 1);
        }
        unsigned a_qw = L_QS + (g & 1) * 272 + (16 * w + l15) * 2;
        unsigned a_qr = L_QS + (l & 1) * 272 + 16 * g;
        unsigned a_uk[4], o_gu[3], a_cnt = l == 0 ? L_TAG + 8 : L_JUNK + l * 4, one = 1u;
        v4u xf[4];
#pragma unroll
        for (int s = 0; s < 4; ++s) {
            xf[s] = lrd<v4u>(L_XN + (l & 1) * 1024 + (128 * cq + 32 * s + 8 * g) * 2);
            a_uk[s] = L_R1 + (16 * w + l15) * 256 + (((4 * s + g) ^ l15) << 4);
            PIN(xf[s]); PIN(a_uk[s]);
        }
        const int mat = un >> 6, mp = (un >> 2) & 15, fp = un & 3;
#pragma unroll
        for (int i = 0; i < 3; ++i) {
            const unsigned pc = min(8 * i + w, 21), idx = min(64 * pc + l, 1375u), row = idx / 43u, ch = idx - row * 43u;
            o_gu[i] = (32 * mp + row) * 2752 + fp * 688 + ch * 16; PIN(o_gu[i]);
        }
        v4u pb = {0u, tag, 0u, tag}, ga;
        PIN(a0); PIN(a1); PIN(o_p1); PIN(o_g1); PIN(o_h2); PIN(o_t2); PIN(a_qw); PIN(a_qr); PIN(pb); PIN(a_cnt); PIN(one);
        asm volatile("s_waitcnt vmcnt(4)" : "+v"(cp0) :: "memory");
        bar0();
        STAMP(2);
        {
            v4u f[4]; frags128(a0, a1, f);
            FENCE();
            const f32x4 acc = gemv4(xf, f);
            pb[0] = asu(acc[0]); pb[2] = asu(acc[1]);
            stx(rs, o_p1, pb);
        }
        STAMP(3);
        if (STOPAT <= 3) { WAITVM(0); STAMP(14); goto done; }
        __builtin_amdgcn_s_sleep(10);
        WAITVM(3);
        v4u fu[4];
        {
            unsigned arr;
            asm volatile("buffer_load_dwordx4 %1, %4, %5, 0 offen sc1\n\tds_add_rtn_u32 %0, %2, %3\n\ts_waitcnt lgkmcnt(0)" : "=&v"(arr), "=&v"(ga) : "v"(a_cnt), "v"(one), "v"(o_g1), "s"(rsq) : "memory");
            hint_put(rs, __builtin_amdgcn_readfirstlane(arr) == 7u ? o_t2 : OOB, tag);
            asm volatile("s_waitcnt vmcnt(1)" : "+v"(ga) :: "memory");
            STAMP(14);
#pragma unroll
            for (int s = 0; s < 4; ++s) fu[s] = lrd<v4u>(a_uk[s]);
            if (__builtin_expect(!none(tagx(ga, tag) != 0u), 0)) {
#pragma nounroll
                SPIN { __builtin_amdgcn_s_sleep(4); get1(ga, o_g1, rsq); if (none(tagx(ga, tag) != 0u)) break; }
            }
            lwr<u16>(a_qw, (u16)pk(sum32(sum16x2(asf(ga[0]), asf(ga[2]))), 0.f));
        }
        STAMP(4);
        bar();
        {
            v4u qf[4];
#pragma unroll
            for (int s = 0; s < 4; ++s) qf[s] = lrd<v4u>(a_qr + 64 * s);
            FENCE();
            const f32x4 acc = gemv4(fu, qf);
            constexpr float SC = 0.08838834764831845f * 1.4426950408889634f;
            lwr<v2u>(a_2w, v2u{pk(acc[0] * SC, acc[1] * SC), pk(acc[2] * SC, acc[3] * SC)});
            bar();
            if (w < NCOPY) {
                const v2u t = lrd<v2u>(a_2r);
                pb[0] = t[0]; pb[2] = t[1];
                stx(rs, o_h2, pb);
            }
        }
        STAMP(5);
        if (STOPAT <= 5) goto done;
        const int r = xc * 8 + (yy >> 1);
        unsigned o_t3 = WS_T3 + ((xc * 4 + cq) * 64 + 8 * w + (l & 7)) * 128, o_t5 = WS_T5 + (r * 32 + (l & 31)) * 128, j0, j1;
        asm volatile("buffer_load_dword %0, %2, %4, 0 offen sc1\n\tbuffer_load_dword %1, %3, %4, 0 offen sc1"
                     : "=&v"(j0), "=&v"(j1) : "v"(o_t3), "v"((w == 0 && !(yy & 1)) ? o_t5 : o_t3), "s"(rsq) : "memory");
        unsigned trep = 0;
        if (VDEF == 2) trep = hint_wait(rsq, WS_T2 + (xc * 128 + (l & 7) * 16 + 5) * 128, tag);
#pragma unroll
        for (int i = 0; i < 3; ++i) glds(o_gu[i], mat ? wu : wg, L_R2 + min(8 * i + w, 21) * 1024);
#define V_ISSUE(i) glds<true>((EXP & 4) ? 0 : vb + (i) * 32768 + sw[i], wuv, L_R1 + (16 * w + 4 * (i)) * 256)
        if (!VDEF) { V_ISSUE(0); V_ISSUE(1); }
        {
            const v4u xg = lrd<v4u>(L_XN + (l & 1) * 1024 + (32 * mp + 8 * g) * 2);
            const unsigned ag = L_R2 + (8 * g + (l15 >> 2)) * 688 + (l & 3) * 8;
            const unsigned f = 16 * (8 * g + w) + l15;
            const unsigned o_hg = (g < 3 && f < 344) ? WS_HG + ((mat * 16 + mp) * 1376 + 344 * fp + f) * 16 : OOB;
            if (VDEF) asm volatile("s_waitcnt vmcnt(0)" : "+v"(j0), "+v"(j1) :: "memory"); else asm volatile("s_waitcnt vmcnt(2)" : "+v"(j0), "+v"(j1) :: "memory");
            bar();
            f32x4 o[3];
#pragma unroll
            for (int t = 0; t < 3; ++t) {
                const unsigned tl = min(8 * t + w, 21);
                o[t] = mfma32(xg, trd2(ag + tl * 32, ag + tl * 32 + 4 * 688), ZF);
            }
            const float o0 = g == 0 ? o[0][0] : g == 1 ? o[1][0] : o[2][0], o1 = g == 0 ? o[0][1] : g == 1 ? o[1][1] : o[2][1];
            stx(rs, o_hg, v4u{asu(o0), tag, asu(o1), tag});
        }
        if (VDEF) {
            pace(VDEF == 2 ? trep : hint_wait(rsq, WS_T2 + (xc * 128 + (l & 7) * 16 + 5) * 128, tag), PA);
            V_ISSUE(0); V_ISSUE(1);
        }
        V_ISSUE(2); V_ISSUE(3);
        STAMP(6);
        const unsigned u = l15 >= 12 ? 8 + l15 : (g & 1) ? 12 + (l15 & 7) : l15;
        const bool act = (g & 1) ? (l15 & 12) != 8 : l15 < 12;
        unsigned o_h3 = WS_H3 + (((8 * w + (g >> 1)) * 2 + (h >> 4)) * 4 + cq) * 6144 + (u >> 3) * 2048 + (h & 15) * 128 + (u & 7) * 16;
        unsigned o_t4 = l < 4 ? WS_T4 + ((h * 4 + l) * 4 + cq) * 128 : OOB;
        unsigned o_p4 = l < 16 ? WS_H4 + ((h * 4 + cq) * 128 + 16 * w + l) * 16 : OOB;
        unsigned a_ow, a_ow2, a_ms;
        {
            const unsigned n = 6 * (u >> 2) + 2 * (g >> 1), n2 = 6 * (u >> 2) + 4, gg = u & 3;
            a_ow = (act && n < 32) ? L_OW + w * 272 + (32 * gg + 4 * ((n >> 2) ^ (gg >> 1)) + (n & 3)) * 2 : L_JUNK + l * 4;
            a_ow2 = (act && n2 < 32 && l < 32) ? L_OW + w * 272 + (32 * gg + 4 * ((n2 >> 2) ^ (gg >> 1)) + (n2 & 3)) * 2 : L_JUNK + l * 4;
            a_ms = (l & 31) == 12 ? L_MW + (g >> 1) * 32 + w * 4 : L_JUNK + l * 4;
        }
        unsigned a_or = L_OW + (l & 7) * 272 + 16 * g, a_mw = L_MW + (g & 1) * 16;
        v4u a[4];
        UNDEF(a[0]); UNDEF(a[1]); UNDEF(a[2]); UNDEF(a[3]);
        a_cnt += l == 0 ? 4 : 0;
        PIN(o_h3); PIN(o_t3); PIN(o_t4); PIN(o_p4); PIN(a_ow); PIN(a_ow2); PIN(a_or); PIN(a_mw); PIN(a_ms); PIN(a_cnt);
        WAITVM(0);
        bar();
        v4u fv[4];
        frags128(a0 + (L_R1 - L_R0), a1 + (L_R1 - L_R0), fv);
        PIN(fv[0]); PIN(fv[1]); PIN(fv[2]); PIN(fv[3]);
        STAMP(7);
        if (STOPAT <= 7) goto done;
        CNT0(2); CNT0(3);
        const unsigned t3 = hint_wait(rsq, o_t3, tag);
        STAMPC(1);
        pace(t3, P3);
        STAMP(15);
        {
#pragma nounroll
            SPIN { CNT(2); get4s(a, o_h3, rsq, 98304, 196608, 294912); if (none((tagy(a[0][3], a[1][3], tag) | tagy(a[2][3], a[3][3], tag)) != 0u)) break; __builtin_amdgcn_s_sleep(4); }
            STAMP(8);
            unsigned arr;
            unsigned long long tm;
            asm volatile("ds_add_rtn_u32 %0, %2, %3\n\ts_memrealtime %1" : "=&v"(arr), "=&s"(tm) : "v"(a_cnt), "v"(one) : "memory");
            float m[4], al[4];
#pragma unroll
            for (int k = 0; k < 4; ++k) m[k] = dpp<0x15c>(asf(a[k][1]));
            const float M = max32(mx(mx3(m[0], m[1], m[2]), m[3]));
#pragma unroll
            for (int k = 0; k < 4; ++k) al[k] = __builtin_amdgcn_exp2f(m[k] - M);
            FENCE();
            asm volatile("s_waitcnt lgkmcnt(0)" : "+v"(arr), "+s"(tm) :: "memory");
            hint_put(rs, __builtin_amdgcn_readfirstlane(arr) == 7u ? o_t4 : OOB, tag, (unsigned)tm);
            f32x2 c01, c23, c45;
            float Ls;
#pragma unroll
            for (int k = 0; k < 4; ++k) {
                const f32x2 aa = {al[k], al[k]}, v01 = {lo16(a[k][0]), hi16(a[k][0])}, v23 = {lo16(a[k][1]), hi16(a[k][1])}, v45 = {lo16(a[k][2]), hi16(a[k][2])};
                if (k == 0) { c01 = aa * v01; c23 = aa * v23; c45 = aa * v45; Ls = al[0] * asf(a[0][2]); }
                else { c01 = __builtin_elementwise_fma(aa, v01, c01); c23 = __builtin_elementwise_fma(aa, v23, c23); c45 = __builtin_elementwise_fma(aa, v45, c45); Ls = __builtin_fmaf(al[k], asf(a[k][2]), Ls); }
            }
            const float acc[6] = {c01[0], c01[1], c23[0], c23[1], c45[0], c45[1]};
            const float v4 = sum32x2(acc[4], Ls);
            lwr<unsigned>(a_ow, pk(sum32x2(acc[0], acc[2]), sum32x2(acc[1], acc[3])));
            lwr<unsigned>(a_ow2, pk(v4, sum32(acc[5])));
            lwr<float>(a_ms, l < 32 ? M : v4);
        }
        bar();
        {
            v4u of[4];
#pragma unroll
            for (int s = 0; s < 4; ++s) of[s] = lrd<v4u>(a_or + 64 * s);
            const f32x4 mw = lrd<f32x4>(a_mw), lw = lrd<f32x4>(a_mw + 32);
            FENCE();
            const f32x4 c = gemv4(of, fv);
            const float M = mx(mx3(mw[0], mw[1], mw[2]), mw[3]);
            float be[4];
#pragma unroll
            for (int r = 0; r < 4; ++r) be[r] = __builtin_amdgcn_exp2f(mw[r] - M);
            const float rd = __builtin_amdgcn_rcpf((be[0] * lw[0] + be[1] * lw[1]) + (be[2] * lw[2] + be[3] * lw[3]));
            const float v = ((be[0] * c[0] + be[1] * c[1]) + (be[2] * c[2] + be[3] * c[3])) * rd;
            auto sv = __builtin_amdgcn_permlane16_swap(asu(v), asu(v), false, false);
            pb[0] = sv[0]; pb[2] = sv[1];
            stx(rs, o_p4, pb);
        }
        STAMP(9);
        if ((yy & 1) == 0) {
            const unsigned o1 = WS_H5 + ((32 + (tid >> 3)) * 512 + 8 * r + (tid & 7)) * 16;
            const bool two = tid < 176;
            const unsigned o2 = two ? o1 + 64 * 512 * 16 : o1;
            pace(t3, PG);
#pragma nounroll
            SPIN { get2(a[0], a[1], o1, o2, rsq); if (none((tagx(a[0], tag) | tagx(a[1], tag)) != 0u)) break; __builtin_amdgcn_s_sleep(8); }
            {
                float t = sum32(sum16x2(asf(a[0][0]) + (two ? asf(a[1][0]) : 0.f), asf(a[0][2]) + (two ? asf(a[1][2]) : 0.f)));
                t += dpp<0x128>(t);
                lwr<float>(L_RED + ((g & 1) * 8 + (l & 7)) * 32 + w * 4, t);
            }
            bar();
            STAMP(10);
            if (w == 0) {
                const f32x4 t2 = lrd<f32x4>(L_RED + ((g & 1) * 8 + (l & 7)) * 32), t4 = lrd<f32x4>(L_RED + ((g & 1) * 8 + (l & 7)) * 32 + 16);
                float glu = rbf(((t2[0] + t2[1]) + (t2[2] + t2[3])) + ((t4[0] + t4[1]) + (t4[2] + t4[3])));
                float xin = lo16(x[(g & 1) * 512 + 8 * r + (l & 7)]);
                unsigned o0 = WS_H5 + ((l >> 3) * 512 + 8 * r + (l & 7)) * 16;
                unsigned o_out = ((g & 1) * 512 + 8 * r + (l & 7)) * 2;
                PIN(glu); PIN(xin); PIN(o0); PIN(o_out);
                pace(hint_wait(rsq, o_t5, tag), P5);
                STAMP(11);
                asm volatile("buffer_load_dwordx4 %0, %4, %5, 0 offen sc1\n\tbuffer_load_dwordx4 %1, %4, %5, %6 offen sc1\n\t"
                             "buffer_load_dwordx4 %2, %4, %5, %7 offen sc1\n\tbuffer_load_dwordx4 %3, %4, %5, %8 offen sc1"
                             : "=&v"(a[0]), "=&v"(a[1]), "=&v"(a[2]), "=&v"(a[3]) : "v"(o0), "s"(rsq), "s"(65536), "s"(131072), "s"(196608) : "memory");
                __builtin_amdgcn_raw_buffer_store_b32(tag, rs, (r == 0 && l == 0) ? 0u : OOB, 0, 16);
                asm volatile("s_waitcnt vmcnt(1)" : "+v"(a[0]), "+v"(a[1]), "+v"(a[2]), "+v"(a[3]) :: "memory");
                if (__builtin_expect(!none(tagx4(a, tag) != 0u), 0)) {
#pragma nounroll
                    SPIN { CNT(3); get4s(a, o0, rsq, 65536, 131072, 196608); if (none(tagx4(a, tag) != 0u)) break; }
                }
                STAMP(12);
                float t = sum32(sum16x2((asf(a[0][0]) + asf(a[1][0])) + (asf(a[2][0]) + asf(a[3][0])), (asf(a[0][2]) + asf(a[1][2])) + (asf(a[2][2]) + asf(a[3][2]))));
                t += dpp<0x128>(t);
                {
                    const unsigned v = pk(rbf(xin + rbf(t)) + glu, 0.f);
                    const unsigned p = __builtin_amdgcn_perm((unsigned)__builtin_amdgcn_update_dpp(0, (int)v, 0x101, 0xf, 0xf, true), v, 0x05040100u);
                    const v4u o = {p, (unsigned)__builtin_amdgcn_update_dpp(0, (int)p, 0x102, 0xf, 0xf, true), (unsigned)__builtin_amdgcn_update_dpp(0, (int)p, 0x104, 0xf, 0xf, true),
                                   (unsigned)__builtin_amdgcn_update_dpp(0, (int)p, 0x106, 0xf, 0xf, true)};
                    __builtin_amdgcn_raw_buffer_store_b128(o, __builtin_amdgcn_make_buffer_rsrc(out, 0, 2048, 0x00020000), (l == 0 || l == 16) ? o_out : OOB, 0, 0);
                }
            }
        }
    } else {
        const int pr = xc * 8 + (yy >> 1), hg = yy & 1, b = pr >> 5, sp = pr & 31;
        const unsigned t_in = now();
        unsigned ep, j0;
        unsigned o_t2 = WS_T2 + (xc * 128 + (16 * hg + 2 * w) * 4 + (l & 7)) * 128;
        asm volatile("global_load_dword %0, %1, %2 sc1" : "=v"(ep) : "v"(0), "s"(ws) : "memory");
        asm volatile("buffer_load_dword %0, %1, %2, 0 offen sc1" : "=v"(j0) : "v"(o_t2), "s"(rsq) : "memory");
        unsigned qoff = WS_H2 + ((((xc & (NCOPY - 1)) * 2 + b) * 32 + 16 * hg + 2 * w) * 128 + l) * 16;
        unsigned a_q0 = L_Q + 2 * w * 1024 + (((l >> 1) ^ (2 * w)) << 4) + (l & 1) * 8, a_q1 = L_Q + (2 * w + 1) * 1024 + (((l >> 1) ^ (2 * w + 1)) << 4) + (l & 1) * 8;
        unsigned o_kv[8];
#pragma unroll
        for (int i = 0; i < 8; ++i) { o_kv[i] = (pr * 128 + 64 * hg + 8 * w + i) * 1024 + ((l ^ (8 * (w & 1) + i)) << 4); PIN(o_kv[i]); }
        PIN(qoff); PIN(a_q0); PIN(a_q1);
        asm volatile("s_waitcnt vmcnt(0)" : "+v"(ep), "+v"(j0), "+v"(cp0) :: "memory");
        const unsigned tag = mktag(ep);
        pace(t_in, PB);
#pragma unroll
        for (int i = 0; i < 8; ++i) glds(o_kv[i], ckv, L_R0 + (8 * w + i) * 1024);
        STAMP(1);
        bar0();
#pragma unroll
        for (int i = 0; i < 8; ++i) glds(o_kv[i] ^ 65536, ckv, L_R0 + (64 + 8 * w + i) * 1024);
        WAITVM(8);
        bar0();
        STAMP(2);
#define Q_ISSUE() \
            FENCE(); \
            WAITVM(2); \
            const v2u hv = hint_get(rsq, o_t2);        \
            STAMP(14); \
            pace(none(hv[0] != tag) ? hv[1] : hint_wait(rsq, o_t2, tag), P2); \
            STAMP(15); \
            v4u qa[4]; \
            asm volatile("buffer_load_dwordx4 %0, %4, %5, 0 offen sc1\n\tbuffer_load_dwordx4 %1, %4, %5, 0 offen offset:1024 sc1\n\t" \
                         "buffer_load_dwordx4 %2, %4, %5, 0 offen offset:2048 sc1\n\tbuffer_load_dwordx4 %3, %4, %5, 0 offen offset:3072 sc1" \
                         : "=&v"(qa[0]), "=&v"(qa[1]), "=&v"(qa[2]), "=&v"(qa[3]) : "v"(qoff), "s"(rsq) : "memory"); \
            bar0()
#define Q_LAND(n) do { \
            asm volatile("s_waitcnt vmcnt(" STR(n) ")" : "+v"(qa[0]), "+v"(qa[1]), "+v"(qa[2]), "+v"(qa[3]) :: "memory"); \
            if (__builtin_expect(!none(tagx4(qa, tag) != 0u), 0)) { _Pragma("nounroll") SPIN { __builtin_amdgcn_s_sleep(4); get4<1024>(qa, qoff, rsq); if (none(tagx4(qa, tag) != 0u)) break; } } \
            asm volatile("ds_write2_b32 %0, %2, %3 offset1:1\n\tds_write2_b32 %0, %4, %5 offset0:128 offset1:129\n\tds_write2_b32 %1, %6, %7 offset1:1\n\tds_write2_b32 %1, %8, %9 offset0:128 offset1:129" \
                         :: "v"(a_q0), "v"(a_q1), "v"(qa[0][0]), "v"(qa[0][2]), "v"(qa[1][0]), "v"(qa[1][2]), "v"(qa[2][0]), "v"(qa[2][2]), "v"(qa[3][0]), "v"(qa[3][2]) : "memory"); \
            STAMP(3); bar();   } while (0)
        unsigned t5 = 0;
        const int h = 4 * xc + (yy >> 2), cq = yy & 3;
        unsigned o_t4 = WS_T4 + ((h * 4 + cq) * 4 + (l & 3)) * 128;
        unsigned o_hg = (EXP & 2) ? 0 : WS_HG + ((4 * (w & 3) + g) * 1376 + 16 * min(un, 85) + l15) * 16;
        if (w >= 4) {
            unsigned a_r[4];
#pragma unroll
            for (int j = 0; j < 4; ++j) a_r[j] = l15 * 1024 + (((4 * j + g) ^ l15) << 4);
            const unsigned kb = L_R0 + (w - 4) * 16384;
            v4u cr[32], qf[16];
#pragma unroll
            for (int s = 0; s < 16; ++s) cr[s] = lrd<v4u>(a_r[s & 3] + kb + (s >> 2) * 256);
            unsigned a_mxw = L_MX + l15 * 16 + (w - 4) * 4, a_mxr = L_MX + l15 * 16;
            unsigned a_pw = L_P + l15 * 256 + (((2 * (w - 4) + (g >> 1)) ^ l15) << 4) + (g & 1) * 8;
            __builtin_amdgcn_s_setprio(1);
            PIN(a_mxw); PIN(a_mxr); PIN(a_pw); PIN(o_hg);
            Q_ISSUE();
#pragma unroll
            for (int s = 0; s < 16; ++s) cr[16 + s] = lrd<v4u>(a_r[s & 3] + kb + 65536 + (s >> 2) * 256);
            FENCE();
            Q_LAND(0);
            STAMP(4);
#pragma unroll
            for (int s = 0; s < 16; ++s) qf[s] = lrd<v4u>(a_r[s & 3] + L_Q + (s >> 2) * 256);
            FENCE();
            f32x4 s1 = ZF, s2 = ZF;
#pragma unroll
            for (int s = 0; s < 16; ++s) { s1 = mfma32(cr[s], qf[s], s1); s2 = mfma32(cr[16 + s], qf[s], s2); }
            asm volatile("s_nop 7" : "+v"(s1), "+v"(s2));
            lwr<float>(a_mxw, max32(max16(mx(mx3(mx3(s1[0], s1[1], s1[2]), s1[3], s2[0]), mx3(s2[1], s2[2], s2[3])))));
            bar();
            STAMP(12);
            const f32x4 m0 = lrd<f32x4>(a_mxr);
            const float mr = mx(mx3(m0[0], m0[1], m0[2]), m0[3]);
            v2u p1 = {pk(__builtin_amdgcn_exp2f(s1[0] - mr), __builtin_amdgcn_exp2f(s1[1] - mr)), pk(__builtin_amdgcn_exp2f(s1[2] - mr), __builtin_amdgcn_exp2f(s1[3] - mr))};
            lwr<v2u>(a_pw, p1);
            v2u p2 = {pk(__builtin_amdgcn_exp2f(s2[0] - mr), __builtin_amdgcn_exp2f(s2[1] - mr)), pk(__builtin_amdgcn_exp2f(s2[2] - mr), __builtin_amdgcn_exp2f(s2[3] - mr))};
            lwr<v2u>(a_pw ^ 128, p2);
            bar();
            STAMP(5);
            PIN(p1); PIN(p2);
            {
                const unsigned q0 = p1[0], q1 = p1[1], q2 = p2[0], q3 = p2[1];
                lwr<float>(a_mxw + 256, sum32(sum16(((lo16(q0) + hi16(q0)) + (lo16(q1) + hi16(q1))) + ((lo16(q2) + hi16(q2)) + (lo16(q3) + hi16(q3))))));
            }
            bar();
            __builtin_amdgcn_s_setprio(0);
            t5 = now();
            if (PH) pace(t5, PH);
            v4u ga, gb;
            asm volatile("buffer_load_dwordx4 %0, %3, %6, 0 offen sc1\n\tbuffer_load_dwordx4 %1, %4, %6, 0 offen sc1\n\tbuffer_load_dword %2, %5, %6, 0 offen sc1\n\ts_waitcnt vmcnt(0)"
                         : "=&v"(ga), "=&v"(gb), "=&v"(j0), "+v"(o_hg) : "v"(o_hg + 16 * 1376 * 16), "v"(o_t4), "s"(rsq) : "memory");
            if (un < 86) {
                if (!(EXP & 2) && __builtin_expect(!none((tagx(ga, tag) | tagx(gb, tag)) != 0u), 0)) {
#pragma nounroll
                    SPIN { __builtin_amdgcn_s_sleep(16); get2(ga, gb, o_hg, o_hg + 16 * 1376 * 16, rsq); if (none((tagx(ga, tag) | tagx(gb, tag)) != 0u)) break; }
                }
                lwr<float>(L_RED + (l & 31) * 32 + (w - 4) * 4, sum32(sum16x2(asf(ga[0]), asf(ga[2]))));
                lwr<float>(L_RED + (l & 31) * 32 + w * 4, sum32(sum16x2(asf(gb[0]), asf(gb[2]))));
            }
            pace(t5, PW);
#pragma unroll
            for (int i = 0; i < 8; ++i) {
                const unsigned ch = l15 ^ ((g << 2) | (i & 3));
                glds((h * 128 + 32 * (w - 4) + 4 * i + g) * 1024 + cq * 128 + (ch >> 3) * 512 + (ch & 7) * 16, wo, L_R1 + (32 * (w - 4) + 4 * i) * 256);
            }
        } else {
            unsigned a_t[2][4];
            {
                const unsigned q = l15 >> 2, p = l & 3;
#pragma unroll
                for (int j = 0; j < 8; ++j) {
                    const unsigned r0 = 8 * g + 4 * (j >> 2) + q, ch = 16 * w + 4 * p + (j & 3);
                    a_t[j >> 2][j & 3] = L_R0 + r0 * 1024 + ((ch ^ (r0 & 15)) << 4) + (p >> 1) * 8;
                }
            }
            v4u ct[8][4];
#pragma unroll
            for (int t = 0; t < 8; ++t)
#pragma unroll
                for (int s = 0; s < 2; ++s) ct[t][s] = trd2((a_t[0][t >> 1] ^ ((t & 1) * 8)) + s * 32768, (a_t[1][t >> 1] ^ ((t & 1) * 8)) + s * 32768);
            unsigned a_pr[4], a_mxr = L_MX + l15 * 16;
#pragma unroll
            for (int j = 0; j < 4; ++j) { a_pr[j] = L_P + l15 * 256 + (((4 * j + g) ^ l15) << 4); PIN(a_pr[j]); }
            unsigned o_t3 = l < 4 ? WS_T3 + (((4 * hg + l) * 4 + w) * 64 + pr) * 128 : OOB;
            unsigned o_h3 = WS_H3 + ((pr * 2 + hg) * 4 + w) * 6144 + l * 16, o_h3b = o_h3 + 2048;
            unsigned a_sw = L_R2 + w * 8192 + l15 * 144 + g * 16, a_sr = L_R2 + w * 8192 + (l >> 3) * 144 + (l & 7) * 16;
            PIN(a_sw); PIN(a_sr);
            unsigned o_wd[4], a_lr = L_MX + 256 + l15 * 16;
#pragma unroll
            for (int i = 0; i < 4; ++i) { const unsigned row = 4 * w + i; o_wd[i] = (EXP & 1) ? 0 : (16 * min(un, 85) + row) * 1024 + ((l ^ ((row & 7) << 1)) << 4); PIN(o_wd[i]); }
            PIN(a_mxr); PIN(o_t3); PIN(o_h3); PIN(o_h3b); PIN(a_lr);
            Q_ISSUE();
            v4u wst[4];
            asm volatile("global_load_dwordx4 %0, %4, %8\n\tglobal_load_dwordx4 %1, %5, %8\n\tglobal_load_dwordx4 %2, %6, %8\n\tglobal_load_dwordx4 %3, %7, %8"
                         : "=&v"(wst[0]), "=&v"(wst[1]), "=&v"(wst[2]), "=&v"(wst[3]) : "v"(o_wd[0]), "v"(o_wd[1]), "v"(o_wd[2]), "v"(o_wd[3]), "s"(wd) : "memory");
#pragma unroll
            for (int t = 0; t < 8; ++t)
#pragma unroll
                for (int s = 2; s < 4; ++s) ct[t][s] = trd2((a_t[0][t >> 1] ^ ((t & 1) * 8)) + s * 32768, (a_t[1][t >> 1] ^ ((t & 1) * 8)) + s * 32768);
            FENCE();
            Q_LAND(4);
            STAMP(4);
            bar0();
            const f32x4 m0 = lrd<f32x4>(a_mxr);
            float mr = mx(mx3(m0[0], m0[1], m0[2]), m0[3]);
            PIN(mr);
            bar();
            STAMP(5);
            v4u pf[4];
#pragma unroll
            for (int s = 0; s < 4; ++s) pf[s] = lrd<v4u>(a_pr[s]);
            if (HP == 0) hint_put(rs, o_t3, tag);
            FENCE();
            f32x4 o[8];
#define M0_(t) asm volatile("v_mfma_f32_16x16x32_bf16 %0, %1, %2, 0" : "=&v"(o[t]) : "v"(ct[t][0]), "v"(pf[0]))
#define MM(t, s) asm volatile("v_mfma_f32_16x16x32_bf16 %0, %1, %2, %0" : "+v"(o[t]) : "v"(ct[t][s]), "v"(pf[s]))
#define RDY(t) PIN(o[t])
#define SEQ() asm volatile("" ::: "memory")
#define CV(d, t, i) do { d = pk(o[t][i], o[t][i + 1]); PIN(d); } while (0)
#define PUT(n, k, A, B, C) lwr<v4u>(a_sw + (n) * 2304 + (k) * 64, v4u{A, B, C, tag})
#define GETL(n) do { x0 = lrd<v4u>(a_sr + (n) * 2304); x1 = lrd<v4u>(a_sr + (n) * 2304 + 8 * 144); } while (0)
#define OUTL(n) do { if ((n) < 2) { stx(rs, o_h3 + (n) * 2048, x0); stx(rs, o_h3 + (n) * 2048 + 1024, x1); } else { stx(rs, o_h3b + 2048, x0); stx(rs, o_h3b + 3072, x1); } } while (0)
            unsigned d0, d1, d2, d3, d4, d5;
            v4u x0, x1;
            M0_(0); M0_(1); MM(0, 1); MM(1, 1); MM(0, 2); MM(1, 2); MM(0, 3); MM(1, 3);
            M0_(2); MM(2, 1); MM(2, 2); MM(2, 3);
            M0_(3); MM(3, 1); MM(3, 2);
            RDY(0); CV(d0, 0, 0); CV(d1, 0, 2);
            MM(3, 3); RDY(1); CV(d2, 1, 0); CV(d3, 1, 2);
            M0_(4); PUT(0, 0, d0, d1, d2); SEQ();
            MM(4, 1); RDY(2); CV(d4, 2, 0); CV(d5, 2, 2);
            MM(4, 2); PUT(0, 1, d3, d4, d5); GETL(0); SEQ();
            MM(4, 3); RDY(3); CV(d0, 3, 0); CV(d1, 3, 2);
            M0_(5);
            bar0();
            const f32x4 l0 = lrd<f32x4>(a_lr);
            MM(5, 1); MM(5, 2); MM(5, 3);
            RDY(4); CV(d2, 4, 0); CV(d3, 4, 2);
            STAMP2(3);
            M0_(6); OUTL(0); if (HP == 1) hint_put(rs, o_t3, tag); PUT(1, 0, d0, d1, d2); SEQ();
            STAMP2(4);
            MM(6, 1);
            float lsum = (l0[0] + l0[1]) + (l0[2] + l0[3]);
            PIN(lsum);
            MM(6, 2); MM(6, 3);
            RDY(5); CV(d4, 5, 0); CV(d5, 5, 2);
            M0_(7); PUT(1, 1, d3, d4, d5); GETL(1); SEQ();
            MM(7, 1); MM(7, 2); MM(7, 3);
            asm volatile("s_nop 1" : "+v"(o[6]));
            CV(d0, 6, 0); CV(d1, 6, 2);
            STAMP2(14);
            OUTL(1); if (HP == 2) hint_put(rs, o_t3, tag); SEQ();
            STAMP2(15);
            asm volatile("s_nop 3" : "+v"(o[7]));
            CV(d2, 7, 0); CV(d3, 7, 2);
            PUT(2, 0, d0, d1, d2); PUT(2, 1, d3, asu(mr), asu(lsum)); GETL(2); OUTL(2); if (HP == 3) hint_put(rs, o_t3, tag);
            FENCE();
            STAMP(6);
            asm volatile("s_waitcnt vmcnt(7)" : "+v"(wst[0]), "+v"(wst[1]), "+v"(wst[2]), "+v"(wst[3]) :: "memory");
#pragma unroll
            for (int i = 0; i < 4; ++i) lwr<v4u>(L_R0 + (4 * w + i) * 1024 + l * 16, wst[i]);
        }
        if (STOPAT <= 7) goto done;
        bar();
        if (un < 86) {
            if (w == 4) {
                const f32x4 gp = lrd<f32x4>(L_RED + (l & 31) * 32), up = lrd<f32x4>(L_RED + (l & 31) * 32 + 16);
                const float gg = rbf((gp[0] + gp[1]) + (gp[2] + gp[3])), uu = rbf((up[0] + up[1]) + (up[2] + up[3]));
                const float sg = rbf(__builtin_amdgcn_rcpf(1.f + __builtin_amdgcn_exp2f(gg * -1.4426950408889634f)));
                lwr<u16>(L_HH + (l & 31) * 2, (u16)pk(sg * uu, 0.f));
            }
            bar();
            const v2u ha = lrd<v2u>(L_HH + (l & 1) * 32 + g * 8);
            const unsigned q = l15 >> 2, p = l & 3, row = 4 * g + q;
            f32x4 o[4];
#pragma unroll
            for (int t = 0; t < 4; ++t) {
                const unsigned ch = 8 * w + 2 * t + (p >> 1);
                o[t] = mfma16(ha, trd(L_R0 + row * 1024 + ((ch ^ ((row & 7) << 1)) << 4) + (p & 1) * 8), ZF);
            }
            const float o0 = g == 0 ? o[0][0] : g == 1 ? o[1][0] : g == 2 ? o[2][0] : o[3][0];
            const float o1 = g == 0 ? o[0][1] : g == 1 ? o[1][1] : g == 2 ? o[2][1] : o[3][1];
            stx(rs, WS_H5 + ((32 + un) * 512 + 64 * w + l) * 16, v4u{asu(o0), tag, asu(o1), tag});
        }
        STAMP(7);
        {
            unsigned a0, a1;
            {
                const unsigned q = l15 >> 2, p = l & 3, ch = 2 * w + (p >> 1);
                a0 = L_R1 + (8 * g + q) * 256 + 16 * (ch ^ ((q << 2) | ((2 * g) & 3))) + 8 * (p & 1);
                a1 = L_R1 + (8 * g + q + 4) * 256 + 16 * (ch ^ ((q << 2) | ((2 * g + 1) & 3))) + 8 * (p & 1);
            }
            unsigned o_g4 = WS_H4 + ((h * 4 + g) * 128 + 16 * w + l15) * 16;
            unsigned o_t5 = tid < 16 ? WS_T5 + ((8 * (cq + 4 * (tid >> 3)) + (tid & 7)) * 32 + h) * 128 : OOB;
            unsigned o_h5 = l < 16 ? WS_H5 + (h * 512 + 64 * (cq + 4 * (w >> 2)) + 16 * (w & 3) + l) * 16 : OOB;
            unsigned a_qw = L_QS + (g & 1) * 272 + (16 * w + l15) * 2, a_qr = L_QS + (l & 1) * 272 + 16 * g;
            v4u pb = {0u, tag, 0u, tag}, fw[4], ga;
            WAITVM(0);
            bar();
            frags128(a0, a1, fw);
            PIN(fw[0]); PIN(fw[1]); PIN(fw[2]); PIN(fw[3]); PIN(o_g4); PIN(o_t5); PIN(o_h5); PIN(a_qw); PIN(a_qr); PIN(pb);
            STAMP(10);
            if (w == 4) pace(hint_wait(rsq, o_t4, tag), P4);
            bar0();
            STAMP(11);
            asm volatile("buffer_load_dwordx4 %0, %1, %2, 0 offen sc1" : "=&v"(ga) : "v"(o_g4), "s"(rsq) : "memory");
            hint_put(rs, o_t5, tag);
            asm volatile("s_waitcnt vmcnt(1)" : "+v"(ga) :: "memory");
            if (__builtin_expect(!none(tagx(ga, tag) != 0u), 0)) {
#pragma nounroll
                SPIN { __builtin_amdgcn_s_sleep(2); get1(ga, o_g4, rsq); if (none(tagx(ga, tag) != 0u)) break; }
            }
            STAMP(8);
            lwr<u16>(a_qw, (u16)pk(sum32(sum16x2(asf(ga[0]), asf(ga[2]))), 0.f));
            bar();
            v4u cf[4];
#pragma unroll
            for (int s = 0; s < 4; ++s) cf[s] = lrd<v4u>(a_qr + 64 * s);
            FENCE();
            const f32x4 acc = gemv4(cf, fw);
            pb[0] = asu(acc[0]); pb[2] = asu(acc[1]);
            stx(rs, o_h5, pb);
        }
        STAMP(9);
    }
done:
    STAMP(13);
}

extern "C" void kernel_launch(void* const* d_in, const int* in_sizes, int n_in, void* d_out, int out_size, void* d_ws, size_t ws_size,
                              hipStream_t stream) {
    (void)in_sizes; (void)n_in; (void)out_size; (void)ws_size;
    (void)hipFuncSetAttribute((const void*)mla_glu_decode, hipFuncAttributeMaxDynamicSharedMemorySize, L_END);
    mla_glu_decode<<<256, 512, L_END, stream>>>((const u16*)d_in[0], (const u16*)d_in[1], (const u16*)d_in[2], (const u16*)d_in[3], (const u16*)d_in[4],
                                               (const u16*)d_in[5], (const u16*)d_in[7], (const u16*)d_in[8], (const u16*)d_in[9], (u16*)d_out,
                                               (unsigned char*)d_ws);
}
```

```cpp
#define STAMPS 1
#define SMASK 0x0001
#define COUNTS
#include <hip/hip_runtime.h>
#include <hip/hip_bf16.h>

typedef unsigned short u16;
typedef unsigned v4u __attribute__((ext_vector_type(4)));
typedef unsigned v2u __attribute__((ext_vector_type(2)));
typedef float f32x4 __attribute__((ext_vector_type(4)));
typedef float f32x2 __attribute__((ext_vector_type(2)));
typedef short s16x4 __attribute__((ext_vector_type(4)));
typedef __bf16 bf16x8 __attribute__((ext_vector_type(8)));
typedef __bf16 bf16x2 __attribute__((ext_vector_type(2)));
#define LAS __attribute__((address_space(3)))
#define GAS __attribute__((address_space(1)))
#define DEVI __device__ __forceinline__

#define PB 85
#define P2 100
#define P3 125
#define P4 55
#define P5 50
#define PG 185
#define PW 60
#define PA 0
#define CODEB 14080
#define EXP 0
#define PH 0
#define H3S 12288
#define XMAP 0
#define VDEF 1
#define HP 5
#define PREH3 0
#define NCOPY 1
#define P3A 75
#define SMODE 0
#define STOPAT 99

constexpr int WS_T2 = 4096;
constexpr int WS_T3 = WS_T2 + 8 * 128 * 128;
constexpr int WS_T3B = WS_T3 + 8 * 64 * 4 * 128;
constexpr int WS_T5 = WS_T3B + 8 * 64 * 4 * 128;
constexpr int WS_T4 = WS_T5 + 64 * 32 * 128;
constexpr int WS_H1 = WS_T4 + 32 * 16 * 128;
constexpr int WS_H4 = WS_H1 + 32 * 4 * 128 * 16;
constexpr int WS_H2 = WS_H4 + 32 * 4 * 128 * 16;
constexpr int WS_HG = WS_H2 + 4 * 2 * 32 * 128 * 16;
constexpr int WS_H5 = WS_HG + 2 * 16 * 1376 * 16;
constexpr int WS_H3 = WS_H5 + 118 * 512 * 16;
constexpr int WS_END = WS_H3 + 128 * 4 * 4096;
constexpr int WS_STAMP = 16 << 20;
constexpr int L_Q = 0;
constexpr int L_P = 16384;
constexpr int L_QS = L_P + 4096;
constexpr int L_OW = L_QS + 1024;
constexpr int L_MW = L_OW + 2304;
constexpr int L_MX = L_MW + 64;
constexpr int L_HH = L_MX + 512;
constexpr int L_TAG = L_HH + 64;
constexpr int L_JUNK = L_TAG + 64;
constexpr int L_RED = L_JUNK + 256;
constexpr int L_XN = 28672;
constexpr int L_R0 = 32768, L_R1 = 65536, L_R2 = 98304, L_R3 = 131072;
constexpr int L_END = 163840;
static_assert(L_RED + 1024 <= L_XN && L_XN + 2048 <= L_R0, "lds");
constexpr int SPIN_CAP = 1 << 16;

DEVI unsigned pk(float lo, float hi) { bf16x2 r = {(__bf16)lo, (__bf16)hi}; return __builtin_bit_cast(unsigned, r); }
DEVI float rbf(float f) { return (float)(__bf16)f; }
DEVI float lo16(unsigned w) { return __builtin_bit_cast(float, w << 16); }
DEVI float hi16(unsigned w) { return __builtin_bit_cast(float, w & 0xffff0000u); }
DEVI float asf(unsigned w) { return __builtin_bit_cast(float, w); }
DEVI unsigned asu(float w) { return __builtin_bit_cast(unsigned, w); }
DEVI float mx(float a, float b) { float r; asm("v_max_f32 %0, %1, %2" : "=v"(r) : "v"(a), "v"(b)); return r; }
DEVI float mx3(float a, float b, float c) { float r; asm("v_max3_f32 %0, %1, %2, %3" : "=v"(r) : "v"(a), "v"(b), "v"(c)); return r; }
DEVI f32x4 mfma32(v4u a, v4u b, f32x4 c) { return __builtin_amdgcn_mfma_f32_16x16x32_bf16(__builtin_bit_cast(bf16x8, a), __builtin_bit_cast(bf16x8, b), c, 0, 0, 0); }
DEVI f32x4 mfma16(v2u a, v2u b, f32x4 c) { return __builtin_amdgcn_mfma_f32_16x16x16bf16_1k(__builtin_bit_cast(s16x4, a), __builtin_bit_cast(s16x4, b), c, 0, 0, 0); }
template <class T> DEVI T lrd(unsigned off) { return *(LAS T*)(size_t)off; }
template <class T> DEVI void lwr(unsigned off, T v) { *(LAS T*)(size_t)off = v; }
DEVI v2u trd(unsigned off) { return __builtin_bit_cast(v2u, __builtin_amdgcn_ds_read_tr16_b64_v4i16((LAS s16x4*)(size_t)off)); }
DEVI v4u trd2(unsigned o0, unsigned o1) { v2u a = trd(o0), b = trd(o1); return v4u{a.x, a.y, b.x, b.y}; }
DEVI float sum16(float x) { auto r = __builtin_amdgcn_permlane16_swap(asu(x), asu(x), false, false); return asf(r[0]) + asf(r[1]); }
DEVI float sum32(float x) { auto r = __builtin_amdgcn_permlane32_swap(asu(x), asu(x), false, false); return asf(r[0]) + asf(r[1]); }
DEVI float max16(float x) { auto r = __builtin_amdgcn_permlane16_swap(asu(x), asu(x), false, false); return mx(asf(r[0]), asf(r[1])); }
DEVI float max32(float x) { auto r = __builtin_amdgcn_permlane32_swap(asu(x), asu(x), false, false); return mx(asf(r[0]), asf(r[1])); }
DEVI float odd16(float x) { auto r = __builtin_amdgcn_permlane16_swap(asu(x), asu(x), false, false); return asf(r[1]); }
DEVI float sum16x2(float x, float y) { auto r = __builtin_amdgcn_permlane16_swap(asu(x), asu(y), false, false); return asf(r[0]) + asf(r[1]); }
DEVI float sum32x2(float x, float y) { auto r = __builtin_amdgcn_permlane32_swap(asu(x), asu(y), false, false); return asf(r[0]) + asf(r[1]); }
template <int CTRL> DEVI float dpp(float x) { return asf((unsigned)__builtin_amdgcn_update_dpp(0, (int)asu(x), CTRL, 0xf, 0xf, true)); }
DEVI unsigned rowxor(unsigned x) {
    x ^= (unsigned)__builtin_amdgcn_update_dpp(0, (int)x, 0xB1, 0xf, 0xf, true); x ^= (unsigned)__builtin_amdgcn_update_dpp(0, (int)x, 0x4E, 0xf, 0xf, true);
    x ^= (unsigned)__builtin_amdgcn_update_dpp(0, (int)x, 0x141, 0xf, 0xf, true); x ^= (unsigned)__builtin_amdgcn_update_dpp(0, (int)x, 0x140, 0xf, 0xf, true);
    return x;
}
DEVI float wavesum(float x) {
    x += dpp<0xB1>(x); x += dpp<0x4E>(x); x += dpp<0x141>(x); x += dpp<0x140>(x);
    return sum32(sum16(x));
}
DEVI float dot2one(unsigned p, float c) { const unsigned one = 0x3f803f80u; return __builtin_amdgcn_fdot2_f32_bf16(__builtin_bit_cast(bf16x2, p), __builtin_bit_cast(bf16x2, one), c, false); }
#define STR_(x) #x
#define STR(x) STR_(x)
#define WAITVM(n) asm volatile("s_waitcnt vmcnt(" STR(n) ")" ::: "memory")
#define PIN(x) asm volatile("" : "+v"(x))
#define MFMA_SETTLE(x) asm volatile("s_nop 7" : "+v"(x))
#define UNDEF(x) asm volatile("" : "=v"(x))
#define FENCE() __builtin_amdgcn_sched_barrier(0)
#define SPIN for (int it_ = SPIN_CAP; it_ > 0; --it_)
DEVI void bar() { asm volatile("s_waitcnt lgkmcnt(0)\n\ts_barrier" ::: "memory"); }
DEVI void bar0() { asm volatile("s_barrier" ::: "memory"); }
#define USE_NT 0
template <bool NT = false> DEVI void glds(unsigned voff, const void* sbase, unsigned lds) {
    unsigned keep;
    if (NT && USE_NT) asm volatile("s_mov_b32 %0, m0\n\ts_mov_b32 m0, %3\n\ts_nop 0\n\tglobal_load_lds_dwordx4 %1, %2 nt\n\ts_mov_b32 m0, %0" : "=&s"(keep) : "v"(voff), "s"(sbase), "s"(lds) : "memory");
    else asm volatile("s_mov_b32 %0, m0\n\ts_mov_b32 m0, %3\n\ts_nop 0\n\tglobal_load_lds_dwordx4 %1, %2\n\ts_mov_b32 m0, %0" : "=&s"(keep) : "v"(voff), "s"(sbase), "s"(lds) : "memory");
}
typedef __amdgpu_buffer_rsrc_t rsrc_t;
constexpr unsigned OOB = 0x80000000u;
DEVI void stx(rsrc_t rs, unsigned off, v4u v) { __builtin_amdgcn_raw_buffer_store_b128(v, rs, off, 0, 16); }
DEVI bool none(bool bad) { asm volatile("" ::: "memory"); return __builtin_amdgcn_ballot_w64(bad) == 0ull; }
DEVI unsigned tagx(v4u a, unsigned tag) { unsigned r = (a[1] ^ tag) | (a[3] ^ tag); asm("" : "+v"(r)); return r; }
DEVI unsigned tagy(unsigned a, unsigned b, unsigned tag) { unsigned r = (a ^ tag) | (b ^ tag); asm("" : "+v"(r)); return r; }
DEVI unsigned tagx4(const v4u (&a)[4], unsigned tag) { return (tagx(a[0], tag) | tagx(a[1], tag)) | (tagx(a[2], tag) | tagx(a[3], tag)); }
DEVI void get1(v4u& a, unsigned off, v4u rq) { asm volatile("buffer_load_dwordx4 %0, %1, %2, 0 offen sc1\n\ts_waitcnt vmcnt(0)" : "+&v"(a) : "v"(off), "s"(rq) : "memory"); }
DEVI void get2(v4u& a, v4u& b, unsigned oa, unsigned ob, v4u rq) {
    asm volatile("buffer_load_dwordx4 %0, %2, %4, 0 offen sc1\n\tbuffer_load_dwordx4 %1, %3, %4, 0 offen sc1\n\ts_waitcnt vmcnt(0)" : "+&v"(a), "+&v"(b) : "v"(oa), "v"(ob), "s"(rq) : "memory");
}
template <int ST> DEVI void get4(v4u (&a)[4], unsigned off, v4u rq) {
    asm volatile("buffer_load_dwordx4 %0, %4, %5, 0 offen sc1\n\tbuffer_load_dwordx4 %1, %4, %5, 0 offen offset:%6 sc1\n\t"
                 "buffer_load_dwordx4 %2, %4, %5, 0 offen offset:%7 sc1\n\tbuffer_load_dwordx4 %3, %4, %5, 0 offen offset:%8 sc1\n\ts_waitcnt vmcnt(0)"
                 : "+&v"(a[0]), "+&v"(a[1]), "+&v"(a[2]), "+&v"(a[3]) : "v"(off), "s"(rq), "i"(ST), "i"(2 * ST), "i"(3 * ST) : "memory");
}
DEVI void get4s(v4u (&a)[4], unsigned off, v4u rq, unsigned s1, unsigned s2, unsigned s3) {
    asm volatile("buffer_load_dwordx4 %0, %4, %5, 0 offen sc1\n\tbuffer_load_dwordx4 %1, %4, %5, %6 offen sc1\n\t"
                 "buffer_load_dwordx4 %2, %4, %5, %7 offen sc1\n\tbuffer_load_dwordx4 %3, %4, %5, %8 offen sc1\n\ts_waitcnt vmcnt(0)"
                 : "+&v"(a[0]), "+&v"(a[1]), "+&v"(a[2]), "+&v"(a[3]) : "v"(off), "s"(rq), "s"(s1), "s"(s2), "s"(s3) : "memory");
}
DEVI unsigned now() { return (unsigned)__builtin_amdgcn_s_memrealtime(); }
DEVI void hint_put(rsrc_t rs, unsigned off, unsigned tag, unsigned t) { __builtin_amdgcn_raw_buffer_store_b64(v2u{tag, t}, rs, off, 0, 16); }
DEVI void hint_put(rsrc_t rs, unsigned off, unsigned tag) { hint_put(rs, off, tag, now()); }
DEVI v2u hint_get(v4u rq, unsigned off) { v2u v; asm volatile("buffer_load_dwordx2 %0, %1, %2, 0 offen sc1\n\ts_waitcnt vmcnt(0)" : "=&v"(v) : "v"(off), "s"(rq) : "memory"); return v; }
DEVI void pace(unsigned t, unsigned lead) {
#pragma nounroll
    for (int it = 256; it > 0; --it) { if (none(now() - t < lead)) break; }
}
DEVI unsigned hint_wait(v4u rq, unsigned off, unsigned tag) {
    v2u v;
#pragma nounroll
    SPIN { v = hint_get(rq, off); if (none(v[0] != tag)) break; __builtin_amdgcn_s_sleep(1); }
    return v[1];
}
DEVI unsigned mktag(unsigned ep) { return ep + 1u; }

#define NST 16
#define STAMP(i) do { if (!((SMASK >> (i)) & 1)) break; FENCE(); asm volatile("s_memrealtime %0\n\ts_waitcnt lgkmcnt(0)" : "=s"(st[i]) :: "memory"); FENCE(); } while (0)

#define CNT0(i) st[i] = st[0]
#define CNT(i) st[i] += 100
#define STAMPC(i) STAMP(i)
#define STAMP2(i)
DEVI void frags128(unsigned a0, unsigned a1, v4u (&f)[4]) {
#pragma unroll
    for (int s = 0; s < 4; ++s) f[s] = trd2(a0 + s * 8192, a1 + s * 8192);
}
DEVI f32x4 gemv4(const v4u (&a)[4], const v4u (&b)[4]) {
    f32x4 c = {0.f, 0.f, 0.f, 0.f};
#pragma unroll
    for (int s = 0; s < 4; ++s) c = mfma32(a[s], b[s], c);
    return c;
}
DEVI void layernorm(v4u xv, int w, int l) {
    float v[8];
#pragma unroll
    for (int j = 0; j < 4; ++j) { v[2 * j] = lo16(xv[j]); v[2 * j + 1] = hi16(xv[j]); }
    const float mean = wavesum(((v[0] + v[1]) + (v[2] + v[3])) + ((v[4] + v[5]) + (v[6] + v[7]))) * (1.f / 512.f);
    float sq = 0.f;
#pragma unroll
    for (int j = 0; j < 8; ++j) { const float d = v[j] - mean; sq += d * d; }
    const float var = wavesum(sq) * (1.f / 512.f);
    const float mb = rbf(mean), ri = __builtin_amdgcn_rcpf(rbf(__builtin_amdgcn_sqrtf(rbf(rbf(var) + 1e-5f))));
    v4u o;
#pragma unroll
    for (int j = 0; j < 4; ++j) o[j] = pk(rbf(v[2 * j] - mb) * ri, rbf(v[2 * j + 1] - mb) * ri);
    lwr<v4u>(L_XN + w * 1024 + l * 16, o);
}

__global__ void __launch_bounds__(512, 2)
mla_glu_decode(const u16* __restrict__ x, const u16* __restrict__ ckv, const u16* __restrict__ wq, const u16* __restrict__ wuk,
               const u16* __restrict__ wuv, const u16* __restrict__ wo, const u16* __restrict__ wg, const u16* __restrict__ wu,
               const u16* __restrict__ wd, u16* __restrict__ out, unsigned char* __restrict__ ws) {
    unsigned long long st[NST];
#pragma unroll
    for (int i = 0; i < NST; ++i) st[i] = 0;
    STAMP(0);
    const int tid = threadIdx.x, l = tid & 63, w = __builtin_amdgcn_readfirstlane(tid >> 6);
    const int id = blockIdx.x, ai = (id & 3) * 32 + (id >> 3);
    const int xc = XMAP ? ai >> 4 : id & 7, yy = XMAP ? ai & 15 : (id >> 3) & 15, y = XMAP ? (id & 4) * 4 : id >> 3;
    const int un = yy * 8 + xc;
    const unsigned g = l >> 4, l15 = l & 15;
    unsigned cp0 = 0;
    if (w == 7) {
        unsigned long long pc;
        asm volatile("s_getpc_b64 %0" : "=s"(pc));
        pc &= ~255ull;
        asm volatile("global_load_dword %0, %1, %2" : "=&v"(cp0) : "v"(min(((id >> 3) + 32 * (l & 3)) * 128, CODEB - 128)), "s"(pc) : "memory");
    }
    unsigned vb = (128 * (yy & 3) + 16 * w + g) * 8192 + (4 * xc + (yy >> 2)) * 256, sw[4], o_q[4];
#pragma unroll
    for (int i = 0; i < 4; ++i) { sw[i] = (l15 ^ ((g << 2) | i)) << 4; o_q[i] = vb + i * 32768 + sw[i]; PIN(o_q[i]); }
    asm volatile("" :: "s"(x), "s"(ckv), "s"(wq), "s"(wuk), "s"(wuv), "s"(wo), "s"(wg), "s"(wu), "s"(wd), "s"(out), "s"(ws));
    const rsrc_t rs = __builtin_amdgcn_make_buffer_rsrc(ws, 0, WS_END, 0x00020000);
    const v4u rsq = {(unsigned)(size_t)ws, (unsigned)((size_t)ws >> 32) & 0xffffu, (unsigned)WS_END, 0x00020000u};
    const f32x4 ZF = {0.f, 0.f, 0.f, 0.f};

    if (__builtin_expect(y < 16, 1)) {
        const int h = 4 * xc + (yy >> 2), cq = yy & 3;
        v4u xv; unsigned ep;
        if (w < 2) {
            asm volatile("global_load_dwordx4 %0, %1, %2" : "=v"(xv) : "v"(w * 1024 + l * 16), "s"(x) : "memory");
            asm volatile("global_load_dword %0, %1, %2 sc1" : "=v"(ep) : "v"(0), "s"(ws) : "memory");
        }
#pragma unroll
        for (int i = 0; i < 4; ++i) glds<true>(o_q[i], wq, L_R0 + (16 * w + 4 * i) * 256);
        bar0();
#pragma unroll
        for (int i = 0; i < 4; ++i) glds<true>(vb + i * 32768 + ((l15 ^ (4 * i + g)) << 4), wuk, L_R1 + (16 * w + 4 * i) * 256);
        STAMP(1);
        if (w < 2) {
            asm volatile("s_waitcnt vmcnt(8)" : "+v"(xv), "+v"(ep) :: "memory");
            layernorm(xv, w, l);
            lwr<v4u>(L_TAG, v4u{mktag(ep), 0u, 0u, 0u});
        }
        bar();
        const unsigned tag = lrd<unsigned>(L_TAG);
        unsigned o_p1 = l < 16 ? WS_H1 + ((h * 4 + cq) * 128 + 16 * w + l) * 16 : OOB;
        unsigned o_g1 = WS_H1 + ((h * 4 + g) * 128 + 16 * w + l15) * 16;
        unsigned o_h2 = WS_H2 + ((((w & (NCOPY - 1)) * 2 + (l >> 5)) * 32 + h) * 128 + 32 * cq + (l & 31)) * 16;
        unsigned a_2w = l15 < 2 ? L_RED + l15 * 256 + (4 * w + g) * 8 : L_JUNK + (l & 31) * 8, a_2r = L_RED + l * 8;
        PIN(a_2w); PIN(a_2r);
        unsigned o_t2 = l < 8 ? WS_T2 + (l * 128 + h * 4 + cq) * 128 : OOB;
        unsigned a0, a1;
        {
            const unsigned q = l15 >> 2, p = l & 3, ch = 2 * w + (p >> 1);
            a0 = L_R0 + (8 * g + q) * 256 + 16 * (ch ^ ((q << 2) | ((2 * g) & 3))) + 8 * (p & 1);
            a1 = L_R0 + (8 * g + q + 4) * 256 + 16 * (ch ^ ((q << 2) | ((2 * g + 1) & 3))) + 8 * (p & 1);
        }
        unsigned a_qw = L_QS + (g & 1) * 272 + (16 * w + l15) * 2;
        unsigned a_qr = L_QS + (l & 1) * 272 + 16 * g;
        unsigned a_uk[4], o_gu[3], a_cnt = l == 0 ? L_TAG + 8 : L_JUNK + l * 4, one = 1u;
        v4u xf[4];
#pragma unroll
        for (int s = 0; s < 4; ++s) {
            xf[s] = lrd<v4u>(L_XN + (l & 1) * 1024 + (128 * cq + 32 * s + 8 * g) * 2);
            a_uk[s] = L_R1 + (16 * w + l15) * 256 + (((4 * s + g) ^ l15) << 4);
            PIN(xf[s]); PIN(a_uk[s]);
        }
        const int mat = un >> 6, mp = (un >> 2) & 15, fp = un & 3;
#pragma unroll
        for (int i = 0; i < 3; ++i) {
            const unsigned pc = min(8 * i + w, 21), idx = min(64 * pc + l, 1375u), row = idx / 43u, ch = idx - row * 43u;
            o_gu[i] = (32 * mp + row) * 2752 + fp * 688 + ch * 16; PIN(o_gu[i]);
        }
        v4u pb = {0u, tag, 0u, tag}, ga;
        PIN(a0); PIN(a1); PIN(o_p1); PIN(o_g1); PIN(o_h2); PIN(o_t2); PIN(a_qw); PIN(a_qr); PIN(pb); PIN(a_cnt); PIN(one);
        asm volatile("s_waitcnt vmcnt(4)" : "+v"(cp0) :: "memory");
        bar0();
        STAMP(2);
        {
            v4u f[4]; frags128(a0, a1, f);
            FENCE();
            const f32x4 acc = gemv4(xf, f);
            pb[0] = asu(acc[0]); pb[2] = asu(acc[1]);
            stx(rs, o_p1, pb);
        }
        STAMP(3);
        if (STOPAT <= 3) { WAITVM(0); STAMP(14); goto done; }
        __builtin_amdgcn_s_sleep(10);
        WAITVM(3);
        v4u fu[4];
        {
            unsigned arr;
            asm volatile("buffer_load_dwordx4 %1, %4, %5, 0 offen sc1\n\tds_add_rtn_u32 %0, %2, %3\n\ts_waitcnt lgkmcnt(0)" : "=&v"(arr), "=&v"(ga) : "v"(a_cnt), "v"(one), "v"(o_g1), "s"(rsq) : "memory");
            hint_put(rs, __builtin_amdgcn_readfirstlane(arr) == 7u ? o_t2 : OOB, tag);
            asm volatile("s_waitcnt vmcnt(1)" : "+v"(ga) :: "memory");
            STAMP(14);
#pragma unroll
            for (int s = 0; s < 4; ++s) fu[s] = lrd<v4u>(a_uk[s]);
            if (__builtin_expect(!none(tagx(ga, tag) != 0u), 0)) {
#pragma nounroll
                SPIN { __builtin_amdgcn_s_sleep(4); get1(ga, o_g1, rsq); if (none(tagx(ga, tag) != 0u)) break; }
            }
            lwr<u16>(a_qw, (u16)pk(sum32(sum16x2(asf(ga[0]), asf(ga[2]))), 0.f));
        }
        STAMP(4);
        bar();
        {
            v4u qf[4];
#pragma unroll
            for (int s = 0; s < 4; ++s) qf[s] = lrd<v4u>(a_qr + 64 * s);
            FENCE();
            const f32x4 acc = gemv4(fu, qf);
            constexpr float SC = 0.08838834764831845f * 1.4426950408889634f;
            lwr<v2u>(a_2w, v2u{pk(acc[0] * SC, acc[1] * SC), pk(acc[2] * SC, acc[3] * SC)});
            bar();
            if (w < NCOPY) {
                const v2u t = lrd<v2u>(a_2r);
                pb[0] = t[0]; pb[2] = t[1];
                stx(rs, o_h2, pb);
            }
        }
        STAMP(5);
        if (STOPAT <= 5) goto done;
        const int r = xc * 8 + (yy >> 1);
        unsigned o_t3 = WS_T3 + ((xc * 4 + cq) * 64 + 8 * w + (l & 7)) * 128, o_t5 = WS_T5 + (r * 32 + (l & 31)) * 128, j0, j1;
        asm volatile("buffer_load_dword %0, %2, %4, 0 offen sc1\n\tbuffer_load_dword %1, %3, %4, 0 offen sc1"
                     : "=&v"(j0), "=&v"(j1) : "v"(o_t3), "v"((w == 0 && !(yy & 1)) ? o_t5 : o_t3), "s"(rsq) : "memory");
#pragma unroll
        for (int i = 0; i < 3; ++i) glds(o_gu[i], mat ? wu : wg, L_R2 + min(8 * i + w, 21) * 1024);
#define V_ISSUE(i) glds<true>(vb + (i) * 32768 + sw[i], wuv, L_R1 + (16 * w + 4 * (i)) * 256)
        if (VDEF == 3) { V_ISSUE(0); V_ISSUE(1); }
        {
            const v4u xg = lrd<v4u>(L_XN + (l & 1) * 1024 + (32 * mp + 8 * g) * 2);
            const unsigned ag = L_R2 + (8 * g + (l15 >> 2)) * 688 + (l & 3) * 8;
            const unsigned f = 16 * (8 * g + w) + l15;
            const unsigned o_hg = (g < 3 && f < 344) ? WS_HG + ((mat * 16 + mp) * 1376 + 344 * fp + f) * 16 : OOB;
            unsigned o_rep = WS_T2 + (xc * 128 + (l & 7) * 16 + 5) * 128, rep;
            if (VDEF == 3) asm volatile("s_waitcnt vmcnt(2)" : "+v"(j0), "+v"(j1) :: "memory"); else asm volatile("s_waitcnt vmcnt(0)" : "+v"(j0), "+v"(j1) :: "memory");
            asm volatile("buffer_load_dword %0, %1, %2, 0 offen sc1" : "=&v"(rep) : "v"(o_rep), "s"(rsq) : "memory");
            bar();
            f32x4 o[3];
#pragma unroll
            for (int t = 0; t < 3; ++t) {
                const unsigned tl = min(8 * t + w, 21);
                o[t] = mfma32(xg, trd2(ag + tl * 32, ag + tl * 32 + 4 * 688), ZF);
            }
            const float o0 = g == 0 ? o[0][0] : g == 1 ? o[1][0] : o[2][0], o1 = g == 0 ? o[0][1] : g == 1 ? o[1][1] : o[2][1];
            stx(rs, o_hg, v4u{asu(o0), tag, asu(o1), tag});
            asm volatile("s_waitcnt vmcnt(1)" : "+v"(rep) :: "memory");
            if (!none(rep != tag)) (void)hint_wait(rsq, o_rep, tag);
        }
        if (VDEF != 3) { V_ISSUE(0); V_ISSUE(1); }
        V_ISSUE(2); V_ISSUE(3);
        STAMP(6);
        unsigned o_h3 = WS_H3 + (((8 * w + g) * 2 + (h >> 4)) * 4 + cq) * 4096 + (l15 >> 3) * 2048 + (h & 15) * 128 + (l & 7) * 16;
        unsigned o_s3 = WS_T3B + ((xc * 4 + cq) * 64 + 8 * w + g) * 128 + (h & 3) * 16;
        unsigned o_t4 = l < 4 ? WS_T4 + ((h * 4 + l) * 4 + cq) * 128 : OOB;
        unsigned o_p4 = l < 16 ? WS_H4 + ((h * 4 + cq) * 128 + 16 * w + l) * 16 : OOB;
        unsigned a_ow, a_ms = l == 0 ? L_MW + w * 4 : l == 32 ? L_MW + 32 + w * 4 : L_JUNK + l * 4;
        {
            const unsigned gg = l15 & 3, t = 2 * (l15 >> 2) + (g & 1);
            a_ow = L_OW + w * 272 + (32 * gg + 4 * (t ^ (gg >> 1)) + 2 * (g >> 1)) * 2;
        }
        unsigned a_or = L_OW + (l & 7) * 272 + 16 * g, a_mw = L_MW + (g & 1) * 16;
        v4u a[4];
        UNDEF(a[0]); UNDEF(a[1]); UNDEF(a[2]); UNDEF(a[3]);
        a_cnt += l == 0 ? 4 : 0;
        PIN(o_h3); PIN(o_t3); PIN(o_t4); PIN(o_p4); PIN(a_ow); PIN(o_s3); PIN(a_or); PIN(a_mw); PIN(a_ms); PIN(a_cnt);
        WAITVM(0);
        bar();
        v4u fv[4];
        frags128(a0 + (L_R1 - L_R0), a1 + (L_R1 - L_R0), fv);
        PIN(fv[0]); PIN(fv[1]); PIN(fv[2]); PIN(fv[3]);
        STAMP(7);
        if (STOPAT <= 7) goto done;
        CNT0(2); CNT0(3);
        const unsigned t3 = hint_wait(rsq, o_t3, tag);
        STAMPC(1);
        pace(t3, P3);
        STAMP(15);
        {
#define ROWBAD(d, s_) (rowxor(((d)[0] ^ (d)[1]) ^ ((d)[2] ^ (d)[3])) ^ (s_)[2])
            if (SMODE == 1) {
#pragma nounroll
                SPIN { get2(a[2], a[3], o_s3, o_s3 + 512, rsq); if (none(tagy(a[2][3], a[3][3], tag) != 0u)) break; }
#pragma nounroll
                SPIN { CNT(2); get2(a[0], a[1], o_h3, o_h3 + 131072, rsq); if (none((ROWBAD(a[0], a[2]) | ROWBAD(a[1], a[3])) != 0u)) break; __builtin_amdgcn_s_sleep(2); }
            } else {
#pragma nounroll
                SPIN {
                    CNT(2);
                    asm volatile("buffer_load_dwordx4 %0, %4, %6, 0 offen sc1\n\tbuffer_load_dwordx4 %1, %4, %6, %7 offen sc1\n\t"
                                 "buffer_load_dwordx4 %2, %5, %6, 0 offen sc1\n\tbuffer_load_dwordx4 %3, %5, %6, 0 offen offset:512 sc1\n\ts_waitcnt vmcnt(0)"
                                 : "+&v"(a[0]), "+&v"(a[1]), "+&v"(a[2]), "+&v"(a[3]) : "v"(o_h3), "v"(o_s3), "s"(rsq), "s"(131072) : "memory");
                    if (none((tagy(a[2][3], a[3][3], tag) | ROWBAD(a[0], a[2]) | ROWBAD(a[1], a[3])) != 0u)) break;
                    __builtin_amdgcn_s_sleep(4);
                }
            }
            STAMP(8);
            unsigned arr;
            unsigned long long tm;
            asm volatile("ds_add_rtn_u32 %0, %2, %3\n\ts_memrealtime %1" : "=&v"(arr), "=&s"(tm) : "v"(a_cnt), "v"(one) : "memory");
            const float m0 = asf(a[2][0]), m1 = asf(a[3][0]);
            const float M = max32(max16(mx(m0, m1)));
            const float a0_ = __builtin_amdgcn_exp2f(m0 - M), a1_ = __builtin_amdgcn_exp2f(m1 - M);
            FENCE();
            asm volatile("s_waitcnt lgkmcnt(0)" : "+v"(arr), "+s"(tm) :: "memory");
            hint_put(rs, __builtin_amdgcn_readfirstlane(arr) == 7u ? o_t4 : OOB, tag, (unsigned)tm);
            const f32x2 aa0 = {a0_, a0_}, aa1 = {a1_, a1_};
            f32x2 c[4];
#pragma unroll
            for (int j = 0; j < 4; ++j) {
                const unsigned p = a[0][j], q = a[1][j];
                c[j] = __builtin_elementwise_fma(aa1, f32x2{lo16(q), hi16(q)}, aa0 * f32x2{lo16(p), hi16(p)});
            }
            const float v0 = sum16x2(c[0][0], c[2][0]), v1 = sum16x2(c[0][1], c[2][1]), v2 = sum16x2(c[1][0], c[3][0]), v3 = sum16x2(c[1][1], c[3][1]);
            lwr<unsigned>(a_ow, pk(sum32x2(v0, v2), sum32x2(v1, v3)));
            const float Ls = sum32(sum16(__builtin_fmaf(a1_, asf(a[3][1]), a0_ * asf(a[2][1]))));
            lwr<float>(a_ms, l < 32 ? M : Ls);
        }
        bar();
        {
            v4u of[4];
#pragma unroll
            for (int s = 0; s < 4; ++s) of[s] = lrd<v4u>(a_or + 64 * s);
            const f32x4 mw = lrd<f32x4>(a_mw), lw = lrd<f32x4>(a_mw + 32);
            FENCE();
            const f32x4 c = gemv4(of, fv);
            const float M = mx(mx3(mw[0], mw[1], mw[2]), mw[3]);
            float be[4];
#pragma unroll
            for (int r = 0; r < 4; ++r) be[r] = __builtin_amdgcn_exp2f(mw[r] - M);
            const float rd = __builtin_amdgcn_rcpf((be[0] * lw[0] + be[1] * lw[1]) + (be[2] * lw[2] + be[3] * lw[3]));
            const float v = ((be[0] * c[0] + be[1] * c[1]) + (be[2] * c[2] + be[3] * c[3])) * rd;
            auto sv = __builtin_amdgcn_permlane16_swap(asu(v), asu(v), false, false);
            pb[0] = sv[0]; pb[2] = sv[1];
            stx(rs, o_p4, pb);
        }
        STAMP(9);
        if ((yy & 1) == 0) {
            const unsigned o1 = WS_H5 + ((32 + (tid >> 3)) * 512 + 8 * r + (tid & 7)) * 16;
            const bool two = tid < 176;
            const unsigned o2 = two ? o1 + 64 * 512 * 16 : o1;
            pace(t3, PG);
#pragma nounroll
            SPIN { get2(a[0], a[1], o1, o2, rsq); if (none((tagx(a[0], tag) | tagx(a[1], tag)) != 0u)) break; __builtin_amdgcn_s_sleep(8); }
            {
                float t = sum32(sum16x2(asf(a[0][0]) + (two ? asf(a[1][0]) : 0.f), asf(a[0][2]) + (two ? asf(a[1][2]) : 0.f)));
                t += dpp<0x128>(t);
                lwr<float>(L_RED + ((g & 1) * 8 + (l & 7)) * 32 + w * 4, t);
            }
            bar();
            STAMP(10);
            if (w == 0) {
                const f32x4 t2 = lrd<f32x4>(L_RED + ((g & 1) * 8 + (l & 7)) * 32), t4 = lrd<f32x4>(L_RED + ((g & 1) * 8 + (l & 7)) * 32 + 16);
                float glu = rbf(((t2[0] + t2[1]) + (t2[2] + t2[3])) + ((t4[0] + t4[1]) + (t4[2] + t4[3])));
                float xin = lo16(x[(g & 1) * 512 + 8 * r + (l & 7)]);
                unsigned o0 = WS_H5 + ((l >> 3) * 512 + 8 * r + (l & 7)) * 16;
                unsigned o_out = ((g & 1) * 512 + 8 * r + (l & 7)) * 2;
                PIN(glu); PIN(xin); PIN(o0); PIN(o_out);
                pace(hint_wait(rsq, o_t5, tag), P5);
                STAMP(11);
                asm volatile("buffer_load_dwordx4 %0, %4, %5, 0 offen sc1\n\tbuffer_load_dwordx4 %1, %4, %5, %6 offen sc1\n\t"
                             "buffer_load_dwordx4 %2, %4, %5, %7 offen sc1\n\tbuffer_load_dwordx4 %3, %4, %5, %8 offen sc1"
                             : "=&v"(a[0]), "=&v"(a[1]), "=&v"(a[2]), "=&v"(a[3]) : "v"(o0), "s"(rsq), "s"(65536), "s"(131072), "s"(196608) : "memory");
                __builtin_amdgcn_raw_buffer_store_b32(tag, rs, (r == 0 && l == 0) ? 0u : OOB, 0, 16);
                asm volatile("s_waitcnt vmcnt(1)" : "+v"(a[0]), "+v"(a[1]), "+v"(a[2]), "+v"(a[3]) :: "memory");
                if (__builtin_expect(!none(tagx4(a, tag) != 0u), 0)) {
#pragma nounroll
                    SPIN { CNT(3); get4s(a, o0, rsq, 65536, 131072, 196608); if (none(tagx4(a, tag) != 0u)) break; }
                }
                STAMP(12);
                float t = sum32(sum16x2((asf(a[0][0]) + asf(a[1][0])) + (asf(a[2][0]) + asf(a[3][0])), (asf(a[0][2]) + asf(a[1][2])) + (asf(a[2][2]) + asf(a[3][2]))));
                t += dpp<0x128>(t);
                {
                    const unsigned v = pk(rbf(xin + rbf(t)) + glu, 0.f);
                    const unsigned p = __builtin_amdgcn_perm((unsigned)__builtin_amdgcn_update_dpp(0, (int)v, 0x101, 0xf, 0xf, true), v, 0x05040100u);
                    const v4u o = {p, (unsigned)__builtin_amdgcn_update_dpp(0, (int)p, 0x102, 0xf, 0xf, true), (unsigned)__builtin_amdgcn_update_dpp(0, (int)p, 0x104, 0xf, 0xf, true),
                                   (unsigned)__builtin_amdgcn_update_dpp(0, (int)p, 0x106, 0xf, 0xf, true)};
                    __builtin_amdgcn_raw_buffer_store_b128(o, __builtin_amdgcn_make_buffer_rsrc(out, 0, 2048, 0x00020000), (l == 0 || l == 16) ? o_out : OOB, 0, 0);
                }
            }
        }
    } else {
        const int pr = xc * 8 + (yy >> 1), hg = yy & 1, b = pr >> 5, sp = pr & 31;
        const unsigned t_in = now();
        unsigned ep, j0;
        unsigned o_t2 = WS_T2 + (xc * 128 + (16 * hg + 2 * w) * 4 + (l & 7)) * 128;
        asm volatile("global_load_dword %0, %1, %2 sc1" : "=v"(ep) : "v"(0), "s"(ws) : "memory");
        asm volatile("buffer_load_dword %0, %1, %2, 0 offen sc1" : "=v"(j0) : "v"(o_t2), "s"(rsq) : "memory");
        unsigned qoff = WS_H2 + ((((xc & (NCOPY - 1)) * 2 + b) * 32 + 16 * hg + 2 * w) * 128 + l) * 16;
        unsigned a_q0 = L_Q + 2 * w * 1024 + (((l >> 1) ^ (2 * w)) << 4) + (l & 1) * 8, a_q1 = L_Q + (2 * w + 1) * 1024 + (((l >> 1) ^ (2 * w + 1)) << 4) + (l & 1) * 8;
        unsigned o_kv[8];
#pragma unroll
        for (int i = 0; i < 8; ++i) { o_kv[i] = (pr * 128 + 64 * hg + 8 * w + i) * 1024 + ((l ^ (8 * (w & 1) + i)) << 4); PIN(o_kv[i]); }
        PIN(qoff); PIN(a_q0); PIN(a_q1);
        asm volatile("s_waitcnt vmcnt(0)" : "+v"(ep), "+v"(j0), "+v"(cp0) :: "memory");
        const unsigned tag = mktag(ep);
        pace(t_in, PB);
#pragma unroll
        for (int i = 0; i < 8; ++i) glds(o_kv[i], ckv, L_R0 + (8 * w + i) * 1024);
        STAMP(1);
        bar0();
#pragma unroll
        for (int i = 0; i < 8; ++i) glds(o_kv[i] ^ 65536, ckv, L_R0 + (64 + 8 * w + i) * 1024);
        WAITVM(8);
        bar0();
        STAMP(2);
#define Q_ISSUE() \
            FENCE(); \
            WAITVM(2); \
            const v2u hv = hint_get(rsq, o_t2);        \
            STAMP(14); \
            pace(none(hv[0] != tag) ? hv[1] : hint_wait(rsq, o_t2, tag), P2); \
            STAMP(15); \
            v4u qa[4]; \
            asm volatile("buffer_load_dwordx4 %0, %4, %5, 0 offen sc1\n\tbuffer_load_dwordx4 %1, %4, %5, 0 offen offset:1024 sc1\n\t" \
                         "buffer_load_dwordx4 %2, %4, %5, 0 offen offset:2048 sc1\n\tbuffer_load_dwordx4 %3, %4, %5, 0 offen offset:3072 sc1" \
                         : "=&v"(qa[0]), "=&v"(qa[1]), "=&v"(qa[2]), "=&v"(qa[3]) : "v"(qoff), "s"(rsq) : "memory"); \
            bar0()
#define Q_LAND(n) do { \
            asm volatile("s_waitcnt vmcnt(" STR(n) ")" : "+v"(qa[0]), "+v"(qa[1]), "+v"(qa[2]), "+v"(qa[3]) :: "memory"); \
            if (__builtin_expect(!none(tagx4(qa, tag) != 0u), 0)) { _Pragma("nounroll") SPIN { __builtin_amdgcn_s_sleep(4); get4<1024>(qa, qoff, rsq); if (none(tagx4(qa, tag) != 0u)) break; } } \
            asm volatile("ds_write2_b32 %0, %2, %3 offset1:1\n\tds_write2_b32 %0, %4, %5 offset0:128 offset1:129\n\tds_write2_b32 %1, %6, %7 offset1:1\n\tds_write2_b32 %1, %8, %9 offset0:128 offset1:129" \
                         :: "v"(a_q0), "v"(a_q1), "v"(qa[0][0]), "v"(qa[0][2]), "v"(qa[1][0]), "v"(qa[1][2]), "v"(qa[2][0]), "v"(qa[2][2]), "v"(qa[3][0]), "v"(qa[3][2]) : "memory"); \
            STAMP(3); bar();   } while (0)
        unsigned t5 = 0;
        const int h = 4 * xc + (yy >> 2), cq = yy & 3;
        unsigned o_t4 = WS_T4 + ((h * 4 + cq) * 4 + (l & 3)) * 128;
        unsigned o_hg = (EXP & 2) ? 0 : WS_HG + ((4 * (w & 3) + g) * 1376 + 16 * min(un, 85) + l15) * 16;
        if (w >= 4) {
            unsigned a_r[4];
#pragma unroll
            for (int j = 0; j < 4; ++j) a_r[j] = l15 * 1024 + (((4 * j + g) ^ l15) << 4);
            const unsigned kb = L_R0 + (w - 4) * 16384;
            v4u cr[32], qf[16];
#pragma unroll
            for (int s = 0; s < 16; ++s) cr[s] = lrd<v4u>(a_r[s & 3] + kb + (s >> 2) * 256);
            unsigned a_mxw = L_MX + l15 * 16 + (w - 4) * 4, a_mxr = L_MX + l15 * 16;
            unsigned a_pw = L_P + l15 * 256 + (((2 * (w - 4) + (g >> 1)) ^ l15) << 4) + (g & 1) * 8;
            __builtin_amdgcn_s_setprio(1);
            PIN(a_mxw); PIN(a_mxr); PIN(a_pw); PIN(o_hg);
            Q_ISSUE();
#pragma unroll
            for (int s = 0; s < 16; ++s) cr[16 + s] = lrd<v4u>(a_r[s & 3] + kb + 65536 + (s >> 2) * 256);
            FENCE();
            Q_LAND(0);
            STAMP(4);
#pragma unroll
            for (int s = 0; s < 16; ++s) qf[s] = lrd<v4u>(a_r[s & 3] + L_Q + (s >> 2) * 256);
            FENCE();
            f32x4 s1 = ZF, s2 = ZF;
#pragma unroll
            for (int s = 0; s < 16; ++s) { s1 = mfma32(cr[s], qf[s], s1); s2 = mfma32(cr[16 + s], qf[s], s2); }
            asm volatile("s_nop 7" : "+v"(s1), "+v"(s2));
            lwr<float>(a_mxw, max32(max16(mx(mx3(mx3(s1[0], s1[1], s1[2]), s1[3], s2[0]), mx3(s2[1], s2[2], s2[3])))));
            bar();
            STAMP(12);
            const f32x4 m0 = lrd<f32x4>(a_mxr);
            const float mr = mx(mx3(m0[0], m0[1], m0[2]), m0[3]);
            v2u p1 = {pk(__builtin_amdgcn_exp2f(s1[0] - mr), __builtin_amdgcn_exp2f(s1[1] - mr)), pk(__builtin_amdgcn_exp2f(s1[2] - mr), __builtin_amdgcn_exp2f(s1[3] - mr))};
            lwr<v2u>(a_pw, p1);
            v2u p2 = {pk(__builtin_amdgcn_exp2f(s2[0] - mr), __builtin_amdgcn_exp2f(s2[1] - mr)), pk(__builtin_amdgcn_exp2f(s2[2] - mr), __builtin_amdgcn_exp2f(s2[3] - mr))};
            lwr<v2u>(a_pw ^ 128, p2);
            bar();
            STAMP(5);
            PIN(p1); PIN(p2);
            {
                const unsigned q0 = p1[0], q1 = p1[1], q2 = p2[0], q3 = p2[1];
                lwr<float>(a_mxw + 256, sum32(sum16(((lo16(q0) + hi16(q0)) + (lo16(q1) + hi16(q1))) + ((lo16(q2) + hi16(q2)) + (lo16(q3) + hi16(q3))))));
            }
            bar();
            __builtin_amdgcn_s_setprio(0);
            t5 = now();
            if (PH) pace(t5, PH);
            v4u ga, gb;
            asm volatile("buffer_load_dwordx4 %0, %3, %6, 0 offen sc1\n\tbuffer_load_dwordx4 %1, %4, %6, 0 offen sc1\n\tbuffer_load_dword %2, %5, %6, 0 offen sc1\n\ts_waitcnt vmcnt(0)"
                         : "=&v"(ga), "=&v"(gb), "=&v"(j0), "+v"(o_hg) : "v"(o_hg + 16 * 1376 * 16), "v"(o_t4), "s"(rsq) : "memory");
            if (un < 86) {
                if (!(EXP & 2) && __builtin_expect(!none((tagx(ga, tag) | tagx(gb, tag)) != 0u), 0)) {
#pragma nounroll
                    SPIN { __builtin_amdgcn_s_sleep(16); get2(ga, gb, o_hg, o_hg + 16 * 1376 * 16, rsq); if (none((tagx(ga, tag) | tagx(gb, tag)) != 0u)) break; }
                }
                lwr<float>(L_RED + (l & 31) * 32 + (w - 4) * 4, sum32(sum16x2(asf(ga[0]), asf(ga[2]))));
                lwr<float>(L_RED + (l & 31) * 32 + w * 4, sum32(sum16x2(asf(gb[0]), asf(gb[2]))));
            }
            pace(t5, PW);
#pragma unroll
            for (int i = 0; i < 8; ++i) {
                const unsigned ch = l15 ^ ((g << 2) | (i & 3));
                glds((h * 128 + 32 * (w - 4) + 4 * i + g) * 1024 + cq * 128 + (ch >> 3) * 512 + (ch & 7) * 16, wo, L_R1 + (32 * (w - 4) + 4 * i) * 256);
            }
        } else {
            unsigned a_t[2][4];
            {
                const unsigned q = l15 >> 2, p = l & 3;
#pragma unroll
                for (int j = 0; j < 8; ++j) {
                    const unsigned r0 = 8 * g + 4 * (j >> 2) + q, ch = 16 * w + 4 * p + (j & 3);
                    a_t[j >> 2][j & 3] = L_R0 + r0 * 1024 + ((ch ^ (r0 & 15)) << 4) + (p >> 1) * 8;
                }
            }
            v4u ct[8][4];
#pragma unroll
            for (int t = 0; t < 8; ++t)
#pragma unroll
                for (int s = 0; s < 2; ++s) ct[t][s] = trd2((a_t[0][t >> 1] ^ ((t & 1) * 8)) + s * 32768, (a_t[1][t >> 1] ^ ((t & 1) * 8)) + s * 32768);
            unsigned a_pr[4], a_mxr = L_MX + l15 * 16;
#pragma unroll
            for (int j = 0; j < 4; ++j) { a_pr[j] = L_P + l15 * 256 + (((4 * j + g) ^ l15) << 4); PIN(a_pr[j]); }
            unsigned o_t3 = l < 4 ? WS_T3 + (((4 * hg + l) * 4 + w) * 64 + pr) * 128 : OOB;
            unsigned o_t3b = l < 32 ? WS_T3B + (((4 * hg + (l >> 3)) * 4 + w) * 64 + pr) * 128 + (l & 7) * 16 : OOB;
            PIN(o_t3b);
            unsigned o_h3 = WS_H3 + ((pr * 2 + hg) * 4 + w) * 4096 + l * 16;
            unsigned a_tw = L_R2 + w * 8192 + 4608 + l15 * 16, a_tr = L_R2 + w * 8192 + 4608 + (4 * ((l >> 3) & 3) + (l & 3)) * 16;
            PIN(a_tw); PIN(a_tr);
            unsigned a_sw = L_R2 + w * 8192 + l15 * 144 + g * 16, a_sr = L_R2 + w * 8192 + (l >> 3) * 144 + (l & 7) * 16;
            PIN(a_sw); PIN(a_sr);
            unsigned o_wd[4], a_lr = L_MX + 256 + l15 * 16;
#pragma unroll
            for (int i = 0; i < 4; ++i) { const unsigned row = 4 * w + i; o_wd[i] = (EXP & 1) ? 0 : (16 * min(un, 85) + row) * 1024 + ((l ^ ((row & 7) << 1)) << 4); PIN(o_wd[i]); }
            PIN(a_mxr); PIN(o_t3); PIN(o_h3); PIN(a_lr);
            Q_ISSUE();
            v4u wst[4];
            asm volatile("global_load_dwordx4 %0, %4, %8\n\tglobal_load_dwordx4 %1, %5, %8\n\tglobal_load_dwordx4 %2, %6, %8\n\tglobal_load_dwordx4 %3, %7, %8"
                         : "=&v"(wst[0]), "=&v"(wst[1]), "=&v"(wst[2]), "=&v"(wst[3]) : "v"(o_wd[0]), "v"(o_wd[1]), "v"(o_wd[2]), "v"(o_wd[3]), "s"(wd) : "memory");
#pragma unroll
            for (int t = 0; t < 8; ++t)
#pragma unroll
                for (int s = 2; s < 4; ++s) ct[t][s] = trd2((a_t[0][t >> 1] ^ ((t & 1) * 8)) + s * 32768, (a_t[1][t >> 1] ^ ((t & 1) * 8)) + s * 32768);
            FENCE();
            Q_LAND(4);
            STAMP(4);
            if (HP == 4) hint_put(rs, o_t3, tag);
            bar0();
            if (HP == 5) hint_put(rs, o_t3, tag);
            const f32x4 m0 = lrd<f32x4>(a_mxr);
            float mr = mx(mx3(m0[0], m0[1], m0[2]), m0[3]);
            PIN(mr);
            bar();
            STAMP(5);
            v4u pf[4];
#pragma unroll
            for (int s = 0; s < 4; ++s) pf[s] = lrd<v4u>(a_pr[s]);
            if (HP == 0) hint_put(rs, o_t3, tag);
            FENCE();
            f32x4 o[8];
#define M0_(t) asm volatile("v_mfma_f32_16x16x32_bf16 %0, %1, %2, 0" : "=&v"(o[t]) : "v"(ct[t][0]), "v"(pf[0]))
#define MM(t, s) asm volatile("v_mfma_f32_16x16x32_bf16 %0, %1, %2, %0" : "+v"(o[t]) : "v"(ct[t][s]), "v"(pf[s]))
#define RDY(t) PIN(o[t])
#define SEQ() asm volatile("" ::: "memory")
#define CV(d, t, i) do { d = pk(o[t][i], o[t][i + 1]); PIN(d); } while (0)
#define PUT(n, k, A, B, C, D) lwr<v4u>(a_sw + (n) * 2304 + (k) * 64, v4u{A, B, C, D})
#define GETL(n) do { x0 = lrd<v4u>(a_sr + (n) * 2304); x1 = lrd<v4u>(a_sr + (n) * 2304 + 8 * 144); } while (0)
#define OUTL(n) do { stx(rs, o_h3 + (n) * 2048, x0); stx(rs, o_h3 + (n) * 2048 + 1024, x1); } while (0)
#define CS(A, B) do { cs = cs ^ (A) ^ (B); PIN(cs); } while (0)
            unsigned d0, d1, d2, d3, e0, e1, e2, e3, cs = 0;
            v4u x0, x1;
            M0_(0); M0_(1); MM(0, 1); MM(1, 1); MM(0, 2); MM(1, 2); MM(0, 3); MM(1, 3);
            M0_(2); M0_(3); MM(2, 1); MM(3, 1); MM(2, 2); MM(3, 2); MM(2, 3);
            RDY(0); CV(d0, 0, 0); CV(d1, 0, 2);
            MM(3, 3); RDY(1); CV(d2, 1, 0); CV(d3, 1, 2);
            M0_(4); PUT(0, 0, d0, d1, d2, d3); SEQ();
            MM(4, 1); CS(d0, d1); CS(d2, d3);
            MM(4, 2); RDY(2); CV(e0, 2, 0); CV(e1, 2, 2);
            MM(4, 3);
            bar0();
            const f32x4 l0 = lrd<f32x4>(a_lr);
            M0_(5); RDY(3); CV(e2, 3, 0); CV(e3, 3, 2);
            MM(5, 1); PUT(0, 1, e0, e1, e2, e3); GETL(0); SEQ();
            MM(5, 2); CS(e0, e1); CS(e2, e3);
            MM(5, 3); RDY(4); CV(d0, 4, 0); CV(d1, 4, 2);
            M0_(6);
            float lsum = (l0[0] + l0[1]) + (l0[2] + l0[3]);
            PIN(lsum);
            MM(6, 1); OUTL(0); SEQ();
            MM(6, 2); MM(6, 3);
            M0_(7); RDY(5); CV(d2, 5, 0); CV(d3, 5, 2);
            MM(7, 1); PUT(1, 0, d0, d1, d2, d3); SEQ();
            MM(7, 2); CS(d0, d1); CS(d2, d3);
            MM(7, 3);
            asm volatile("s_nop 1" : "+v"(o[6]));
            CV(e0, 6, 0); CV(e1, 6, 2);
            asm volatile("s_nop 4" : "+v"(o[7]));
            CV(e2, 7, 0); CV(e3, 7, 2);
            PUT(1, 1, e0, e1, e2, e3); GETL(1); SEQ();
            CS(e0, e1); CS(e2, e3);
            { auto r1 = __builtin_amdgcn_permlane16_swap(cs, cs, false, false); cs = r1[0] ^ r1[1]; auto r2 = __builtin_amdgcn_permlane32_swap(cs, cs, false, false); cs = r2[0] ^ r2[1]; }
            lwr<v4u>(a_tw, v4u{asu(mr), asu(lsum), cs, tag});
            const v4u x2 = lrd<v4u>(a_tr);
            OUTL(1);
            stx(rs, o_t3b, x2);
            FENCE();
            STAMP(6);
            asm volatile("s_waitcnt vmcnt(6)" : "+v"(wst[0]), "+v"(wst[1]), "+v"(wst[2]), "+v"(wst[3]) :: "memory");
#pragma unroll
            for (int i = 0; i < 4; ++i) lwr<v4u>(L_R0 + (4 * w + i) * 1024 + l * 16, wst[i]);
        }
        if (STOPAT <= 7) goto done;
        bar();
        if (un < 86) {
            if (w == 4) {
                const f32x4 gp = lrd<f32x4>(L_RED + (l & 31) * 32), up = lrd<f32x4>(L_RED + (l & 31) * 32 + 16);
                const float gg = rbf((gp[0] + gp[1]) + (gp[2] + gp[3])), uu = rbf((up[0] + up[1]) + (up[2] + up[3]));
                const float sg = rbf(__builtin_amdgcn_rcpf(1.f + __builtin_amdgcn_exp2f(gg * -1.4426950408889634f)));
                lwr<u16>(L_HH + (l & 31) * 2, (u16)pk(sg * uu, 0.f));
            }
            bar();
            const v2u ha = lrd<v2u>(L_HH + (l & 1) * 32 + g * 8);
            const unsigned q = l15 >> 2, p = l & 3, row = 4 * g + q;
            f32x4 o[4];
#pragma unroll
            for (int t = 0; t < 4; ++t) {
                const unsigned ch = 8 * w + 2 * t + (p >> 1);
                o[t] = mfma16(ha, trd(L_R0 + row * 1024 + ((ch ^ ((row & 7) << 1)) << 4) + (p & 1) * 8), ZF);
            }
            const float o0 = g == 0 ? o[0][0] : g == 1 ? o[1][0] : g == 2 ? o[2][0] : o[3][0];
            const float o1 = g == 0 ? o[0][1] : g == 1 ? o[1][1] : g == 2 ? o[2][1] : o[3][1];
            stx(rs, WS_H5 + ((32 + un) * 512 + 64 * w + l) * 16, v4u{asu(o0), tag, asu(o1), tag});
        }
        STAMP(7);
        {
            unsigned a0, a1;
            {
                const unsigned q = l15 >> 2, p = l & 3, ch = 2 * w + (p >> 1);
                a0 = L_R1 + (8 * g + q) * 256 + 16 * (ch ^ ((q << 2) | ((2 * g) & 3))) + 8 * (p & 1);
                a1 = L_R1 + (8 * g + q + 4) * 256 + 16 * (ch ^ ((q << 2) | ((2 * g + 1) & 3))) + 8 * (p & 1);
            }
            unsigned o_g4 = WS_H4 + ((h * 4 + g) * 128 + 16 * w + l15) * 16;
            unsigned o_t5 = tid < 16 ? WS_T5 + ((8 * (cq + 4 * (tid >> 3)) + (tid & 7)) * 32 + h) * 128 : OOB;
            unsigned o_h5 = l < 16 ? WS_H5 + (h * 512 + 64 * (cq + 4 * (w >> 2)) + 16 * (w & 3) + l) * 16 : OOB;
            unsigned a_qw = L_QS + (g & 1) * 272 + (16 * w + l15) * 2, a_qr = L_QS + (l & 1) * 272 + 16 * g;
            v4u pb = {0u, tag, 0u, tag}, fw[4], ga;
            WAITVM(0);
            bar();
            frags128(a0, a1, fw);
            PIN(fw[0]); PIN(fw[1]); PIN(fw[2]); PIN(fw[3]); PIN(o_g4); PIN(o_t5); PIN(o_h5); PIN(a_qw); PIN(a_qr); PIN(pb);
            STAMP(10);
            if (w == 4) pace(hint_wait(rsq, o_t4, tag), P4);
            bar0();
            STAMP(11);
            asm volatile("buffer_load_dwordx4 %0, %1, %2, 0 offen sc1" : "=&v"(ga) : "v"(o_g4), "s"(rsq) : "memory");
            hint_put(rs, o_t5, tag);
            asm volatile("s_waitcnt vmcnt(1)" : "+v"(ga) :: "memory");
            if (__builtin_expect(!none(tagx(ga, tag) != 0u), 0)) {
#pragma nounroll
                SPIN { __builtin_amdgcn_s_sleep(2); get1(ga, o_g4, rsq); if (none(tagx(ga, tag) != 0u)) break; }
            }
            STAMP(8);
            lwr<u16>(a_qw, (u16)pk(sum32(sum16x2(asf(ga[0]), asf(ga[2]))), 0.f));
            bar();
            v4u cf[4];
#pragma unroll
            for (int s = 0; s < 4; ++s) cf[s] = lrd<v4u>(a_qr + 64 * s);
            FENCE();
            const f32x4 acc = gemv4(cf, fw);
            pb[0] = asu(acc[0]); pb[2] = asu(acc[1]);
            stx(rs, o_h5, pb);
        }
        STAMP(9);
    }
done:
    STAMP(13);
    if (l == 0) {
        GAS unsigned long long* so = (GAS unsigned long long*)(ws + WS_STAMP) + ((size_t)id * 8 + w) * NST;
#pragma unroll
        for (int i = 0; i < NST; ++i) so[i] = st[i];
    }
}

__global__ void __launch_bounds__(256) k_report(const unsigned long long* stp, int cls, int k, int mode, int scale) {
    const unsigned long long tin = __builtin_amdgcn_s_memrealtime();
    __shared__ unsigned long long r0[256], r1[256], r2[256]; __shared__ unsigned cn[256];
    const int id = threadIdx.x; unsigned long long mn = ~0ull, mx = 0, sm = 0; unsigned c = 0;
    for (int w = 0; w < 8; ++w) { const unsigned long long a = stp[((size_t)id * 8 + w) * NST], v = stp[((size_t)id * 8 + w) * NST + k]; mn = a < mn ? a : mn;
        if (v && (cls == 2 || (cls == 0) == ((id >> 3) < 16))) { mx = v > mx ? v : mx; sm += mode == 3 ? v - a : v & 0xffffffffull; ++c; } }
    r0[id] = mn; r1[id] = mx; r2[id] = sm; cn[id] = c; __syncthreads();
    if (id == 0) { for (int i = 1; i < 256; ++i) { mn = r0[i] < mn ? r0[i] : mn; mx = r1[i] > mx ? r1[i] : mx; sm += r2[i]; c += cn[i]; }
        unsigned long long q = mode == 3 ? sm / c : mode == 2 ? tin - mx : mode == 0 ? mx - mn : (sm / c - (mn & 0xffffffffull)); if (q > 20000) q = 20000;
        const unsigned long long t = __builtin_amdgcn_s_memrealtime() + q * scale; while (__builtin_amdgcn_s_memrealtime() < t) __builtin_amdgcn_s_sleep(1); }
}
extern "C" void kernel_launch(void* const* d_in, const int* in_sizes, int n_in, void* d_out, int out_size, void* d_ws, size_t ws_size,
                              hipStream_t stream) {
    (void)in_sizes; (void)n_in; (void)out_size; (void)ws_size;
    (void)hipFuncSetAttribute((const void*)mla_glu_decode, hipFuncAttributeMaxDynamicSharedMemorySize, L_END);
    mla_glu_decode<<<256, 512, L_END, stream>>>((const u16*)d_in[0], (const u16*)d_in[1], (const u16*)d_in[2], (const u16*)d_in[3], (const u16*)d_in[4],
                                               (const u16*)d_in[5], (const u16*)d_in[7], (const u16*)d_in[8], (const u16*)d_in[9], (u16*)d_out,
                                               (unsigned char*)d_ws);
    k_report<<<1, 256, 0, stream>>>((const unsigned long long*)((char*)d_ws + WS_STAMP), 0, 2, 3, 100);
}
```

```cpp
#include <hip/hip_runtime.h>
#include <hip/hip_bf16.h>

typedef unsigned short u16;
typedef unsigned v4u __attribute__((ext_vector_type(4)));
typedef unsigned v2u __attribute__((ext_vector_type(2)));
typedef float f32x4 __attribute__((ext_vector_type(4)));
typedef float f32x2 __attribute__((ext_vector_type(2)));
typedef short s16x4 __attribute__((ext_vector_type(4)));
typedef __bf16 bf16x8 __attribute__((ext_vector_type(8)));
typedef __bf16 bf16x2 __attribute__((ext_vector_type(2)));
#define LAS __attribute__((address_space(3)))
#define GAS __attribute__((address_space(1)))
#define DEVI __device__ __forceinline__

#define PB 85
#define P2 110
#define P3 155
#define P4 62
#define P5 58
#define PG 220
#define PW 70
#define PA 0
#define CODEB 14080
#define EXP 0
#define PH 0
#define H3S 12288
#define XMAP 0
#define VDEF 1
#define HP 5
#define PREH3 0
#define NCOPY 1
#define P3A 75
#define SMODE 0
#define STOPAT 99

constexpr int WS_T2 = 4096;
constexpr int WS_T3 = WS_T2 + 8 * 128 * 128;
constexpr int WS_T3B = WS_T3 + 8 * 64 * 4 * 128;
constexpr int WS_T5 = WS_T3B + 8 * 64 * 4 * 128;
constexpr int WS_T4 = WS_T5 + 64 * 32 * 128;
constexpr int WS_H1 = WS_T4 + 32 * 16 * 128;
constexpr int WS_H4 = WS_H1 + 32 * 4 * 128 * 16;
constexpr int WS_H2 = WS_H4 + 32 * 4 * 128 * 16;
constexpr int WS_HG = WS_H2 + 4 * 2 * 32 * 128 * 16;
constexpr int WS_H5 = WS_HG + 2 * 16 * 1376 * 16;
constexpr int WS_H3 = WS_H5 + 118 * 512 * 16;
constexpr int WS_END = WS_H3 + 128 * 4 * 4096;
constexpr int WS_STAMP = 16 << 20;
constexpr int L_Q = 0;
constexpr int L_P = 16384;
constexpr int L_QS = L_P + 4096;
constexpr int L_OW = L_QS + 1024;
constexpr int L_MW = L_OW + 2304;
constexpr int L_MX = L_MW + 64;
constexpr int L_HH = L_MX + 512;
constexpr int L_TAG = L_HH + 64;
constexpr int L_JUNK = L_TAG + 64;
constexpr int L_RED = L_JUNK + 256;
constexpr int L_XN = 28672;
constexpr int L_R0 = 32768, L_R1 = 65536, L_R2 = 98304, L_R3 = 131072;
constexpr int L_END = 163840;
static_assert(L_RED + 1024 <= L_XN && L_XN + 2048 <= L_R0, "lds");
constexpr int SPIN_CAP = 1 << 16;

DEVI unsigned pk(float lo, float hi) { bf16x2 r = {(__bf16)lo, (__bf16)hi}; return __builtin_bit_cast(unsigned, r); }
DEVI float rbf(float f) { return (float)(__bf16)f; }
DEVI float lo16(unsigned w) { return __builtin_bit_cast(float, w << 16); }
DEVI float hi16(unsigned w) { return __builtin_bit_cast(float, w & 0xffff0000u); }
DEVI float asf(unsigned w) { return __builtin_bit_cast(float, w); }
DEVI unsigned asu(float w) { return __builtin_bit_cast(unsigned, w); }
DEVI float mx(float a, float b) { float r; asm("v_max_f32 %0, %1, %2" : "=v"(r) : "v"(a), "v"(b)); return r; }
DEVI float mx3(float a, float b, float c) { float r; asm("v_max3_f32 %0, %1, %2, %3" : "=v"(r) : "v"(a), "v"(b), "v"(c)); return r; }
DEVI f32x4 mfma32(v4u a, v4u b, f32x4 c) { return __builtin_amdgcn_mfma_f32_16x16x32_bf16(__builtin_bit_cast(bf16x8, a), __builtin_bit_cast(bf16x8, b), c, 0, 0, 0); }
DEVI f32x4 mfma16(v2u a, v2u b, f32x4 c) { return __builtin_amdgcn_mfma_f32_16x16x16bf16_1k(__builtin_bit_cast(s16x4, a), __builtin_bit_cast(s16x4, b), c, 0, 0, 0); }
template <class T> DEVI T lrd(unsigned off) { return *(LAS T*)(size_t)off; }
template <class T> DEVI void lwr(unsigned off, T v) { *(LAS T*)(size_t)off = v; }
DEVI v2u trd(unsigned off) { return __builtin_bit_cast(v2u, __builtin_amdgcn_ds_read_tr16_b64_v4i16((LAS s16x4*)(size_t)off)); }
DEVI v4u trd2(unsigned o0, unsigned o1) { v2u a = trd(o0), b = trd(o1); return v4u{a.x, a.y, b.x, b.y}; }
DEVI float sum16(float x) { auto r = __builtin_amdgcn_permlane16_swap(asu(x), asu(x), false, false); return asf(r[0]) + asf(r[1]); }
DEVI float sum32(float x) { auto r = __builtin_amdgcn_permlane32_swap(asu(x), asu(x), false, false); return asf(r[0]) + asf(r[1]); }
DEVI float max16(float x) { auto r = __builtin_amdgcn_permlane16_swap(asu(x), asu(x), false, false); return mx(asf(r[0]), asf(r[1])); }
DEVI float max32(float x) { auto r = __builtin_amdgcn_permlane32_swap(asu(x), asu(x), false, false); return mx(asf(r[0]), asf(r[1])); }
DEVI float odd16(float x) { auto r = __builtin_amdgcn_permlane16_swap(asu(x), asu(x), false, false); return asf(r[1]); }
DEVI float sum16x2(float x, float y) { auto r = __builtin_amdgcn_permlane16_swap(asu(x), asu(y), false, false); return asf(r[0]) + asf(r[1]); }
DEVI float sum32x2(float x, float y) { auto r = __builtin_amdgcn_permlane32_swap(asu(x), asu(y), false, false); return asf(r[0]) + asf(r[1]); }
template <int CTRL> DEVI float dpp(float x) { return asf((unsigned)__builtin_amdgcn_update_dpp(0, (int)asu(x), CTRL, 0xf, 0xf, true)); }
DEVI unsigned rowxor(unsigned x) {
    x ^= (unsigned)__builtin_amdgcn_update_dpp(0, (int)x, 0xB1, 0xf, 0xf, true); x ^= (unsigned)__builtin_amdgcn_update_dpp(0, (int)x, 0x4E, 0xf, 0xf, true);
    x ^= (unsigned)__builtin_amdgcn_update_dpp(0, (int)x, 0x141, 0xf, 0xf, true); x ^= (unsigned)__builtin_amdgcn_update_dpp(0, (int)x, 0x140, 0xf, 0xf, true);
    return x;
}
DEVI float wavesum(float x) {
    x += dpp<0xB1>(x); x += dpp<0x4E>(x); x += dpp<0x141>(x); x += dpp<0x140>(x);
    return sum32(sum16(x));
}
DEVI float dot2one(unsigned p, float c) { const unsigned one = 0x3f803f80u; return __builtin_amdgcn_fdot2_f32_bf16(__builtin_bit_cast(bf16x2, p), __builtin_bit_cast(bf16x2, one), c, false); }
#define STR_(x) #x
#define STR(x) STR_(x)
#define WAITVM(n) asm volatile("s_waitcnt vmcnt(" STR(n) ")" ::: "memory")
#define PIN(x) asm volatile("" : "+v"(x))
#define MFMA_SETTLE(x) asm volatile("s_nop 7" : "+v"(x))
#define UNDEF(x) asm volatile("" : "=v"(x))
#define FENCE() __builtin_amdgcn_sched_barrier(0)
#define SPIN for (int it_ = SPIN_CAP; it_ > 0; --it_)
DEVI void bar() { asm volatile("s_waitcnt lgkmcnt(0)\n\ts_barrier" ::: "memory"); }
DEVI void bar0() { asm volatile("s_barrier" ::: "memory"); }
#define USE_NT 0
template <bool NT = false> DEVI void glds(unsigned voff, const void* sbase, unsigned lds) {
    unsigned keep;
    if (NT && USE_NT) asm volatile("s_mov_b32 %0, m0\n\ts_mov_b32 m0, %3\n\ts_nop 0\n\tglobal_load_lds_dwordx4 %1, %2 nt\n\ts_mov_b32 m0, %0" : "=&s"(keep) : "v"(voff), "s"(sbase), "s"(lds) : "memory");
    else asm volatile("s_mov_b32 %0, m0\n\ts_mov_b32 m0, %3\n\ts_nop 0\n\tglobal_load_lds_dwordx4 %1, %2\n\ts_mov_b32 m0, %0" : "=&s"(keep) : "v"(voff), "s"(sbase), "s"(lds) : "memory");
}
typedef __amdgpu_buffer_rsrc_t rsrc_t;
constexpr unsigned OOB = 0x80000000u;
DEVI void stx(rsrc_t rs, unsigned off, v4u v) { __builtin_amdgcn_raw_buffer_store_b128(v, rs, off, 0, 16); }
DEVI bool none(bool bad) { asm volatile("" ::: "memory"); return __builtin_amdgcn_ballot_w64(bad) == 0ull; }
DEVI unsigned tagx(v4u a, unsigned tag) { unsigned r = (a[1] ^ tag) | (a[3] ^ tag); asm("" : "+v"(r)); return r; }
DEVI unsigned tagy(unsigned a, unsigned b, unsigned tag) { unsigned r = (a ^ tag) | (b ^ tag); asm("" : "+v"(r)); return r; }
DEVI unsigned tagx4(const v4u (&a)[4], unsigned tag) { return (tagx(a[0], tag) | tagx(a[1], tag)) | (tagx(a[2], tag) | tagx(a[3], tag)); }
DEVI void get1(v4u& a, unsigned off, v4u rq) { asm volatile("buffer_load_dwordx4 %0, %1, %2, 0 offen sc1\n\ts_waitcnt vmcnt(0)" : "+&v"(a) : "v"(off), "s"(rq) : "memory"); }
DEVI void get2(v4u& a, v4u& b, unsigned oa, unsigned ob, v4u rq) {
    asm volatile("buffer_load_dwordx4 %0, %2, %4, 0 offen sc1\n\tbuffer_load_dwordx4 %1, %3, %4, 0 offen sc1\n\ts_waitcnt vmcnt(0)" : "+&v"(a), "+&v"(b) : "v"(oa), "v"(ob), "s"(rq) : "memory");
}
template <int ST> DEVI void get4(v4u (&a)[4], unsigned off, v4u rq) {
    asm volatile("buffer_load_dwordx4 %0, %4, %5, 0 offen sc1\n\tbuffer_load_dwordx4 %1, %4, %5, 0 offen offset:%6 sc1\n\t"
                 "buffer_load_dwordx4 %2, %4, %5, 0 offen offset:%7 sc1\n\tbuffer_load_dwordx4 %3, %4, %5, 0 offen offset:%8 sc1\n\ts_waitcnt vmcnt(0)"
                 : "+&v"(a[0]), "+&v"(a[1]), "+&v"(a[2]), "+&v"(a[3]) : "v"(off), "s"(rq), "i"(ST), "i"(2 * ST), "i"(3 * ST) : "memory");
}
DEVI void get4s(v4u (&a)[4], unsigned off, v4u rq, unsigned s1, unsigned s2, unsigned s3) {
    asm volatile("buffer_load_dwordx4 %0, %4, %5, 0 offen sc1\n\tbuffer_load_dwordx4 %1, %4, %5, %6 offen sc1\n\t"
                 "buffer_load_dwordx4 %2, %4, %5, %7 offen sc1\n\tbuffer_load_dwordx4 %3, %4, %5, %8 offen sc1\n\ts_waitcnt vmcnt(0)"
                 : "+&v"(a[0]), "+&v"(a[1]), "+&v"(a[2]), "+&v"(a[3]) : "v"(off), "s"(rq), "s"(s1), "s"(s2), "s"(s3) : "memory");
}
DEVI unsigned now() { return (unsigned)__builtin_amdgcn_s_memrealtime(); }
DEVI void hint_put(rsrc_t rs, unsigned off, unsigned tag, unsigned t) { __builtin_amdgcn_raw_buffer_store_b64(v2u{tag, t}, rs, off, 0, 16); }
DEVI void hint_put(rsrc_t rs, unsigned off, unsigned tag) { hint_put(rs, off, tag, now()); }
DEVI v2u hint_get(v4u rq, unsigned off) { v2u v; asm volatile("buffer_load_dwordx2 %0, %1, %2, 0 offen sc1\n\ts_waitcnt vmcnt(0)" : "=&v"(v) : "v"(off), "s"(rq) : "memory"); return v; }
DEVI void pace(unsigned t, unsigned lead) {
#pragma nounroll
    for (int it = 256; it > 0; --it) { if (none(now() - t < lead)) break; }
}
DEVI unsigned hint_wait(v4u rq, unsigned off, unsigned tag) {
    v2u v;
#pragma nounroll
    SPIN { v = hint_get(rq, off); if (none(v[0] != tag)) break; __builtin_amdgcn_s_sleep(1); }
    return v[1];
}
DEVI unsigned mktag(unsigned ep) { return ep + 1u; }

#define STAMP(i)

#define CNT0(i)
#define CNT(i)
#define STAMPC(i)
#define STAMP2(i)
DEVI void frags128(unsigned a0, unsigned a1, v4u (&f)[4]) {
#pragma unroll
    for (int s = 0; s < 4; ++s) f[s] = trd2(a0 + s * 8192, a1 + s * 8192);
}
DEVI f32x4 gemv4(const v4u (&a)[4], const v4u (&b)[4]) {
    f32x4 c = {0.f, 0.f, 0.f, 0.f};
#pragma unroll
    for (int s = 0; s < 4; ++s) c = mfma32(a[s], b[s], c);
    return c;
}
DEVI void layernorm(v4u xv, int w, int l) {
    float v[8];
#pragma unroll
    for (int j = 0; j < 4; ++j) { v[2 * j] = lo16(xv[j]); v[2 * j + 1] = hi16(xv[j]); }
    const float mean = wavesum(((v[0] + v[1]) + (v[2] + v[3])) + ((v[4] + v[5]) + (v[6] + v[7]))) * (1.f / 512.f);
    float sq = 0.f;
#pragma unroll
    for (int j = 0; j < 8; ++j) { const float d = v[j] - mean; sq += d * d; }
    const float var = wavesum(sq) * (1.f / 512.f);
    const float mb = rbf(mean), ri = __builtin_amdgcn_rcpf(rbf(__builtin_amdgcn_sqrtf(rbf(rbf(var) + 1e-5f))));
    v4u o;
#pragma unroll
    for (int j = 0; j < 4; ++j) o[j] = pk(rbf(v[2 * j] - mb) * ri, rbf(v[2 * j + 1] - mb) * ri);
    lwr<v4u>(L_XN + w * 1024 + l * 16, o);
}

__global__ void __launch_bounds__(512, 2)
mla_glu_decode(const u16* __restrict__ x, const u16* __restrict__ ckv, const u16* __restrict__ wq, const u16* __restrict__ wuk,
               const u16* __restrict__ wuv, const u16* __restrict__ wo, const u16* __restrict__ wg, const u16* __restrict__ wu,
               const u16* __restrict__ wd, u16* __restrict__ out, unsigned char* __restrict__ ws) {
    STAMP(0);
    const int tid = threadIdx.x, l = tid & 63, w = __builtin_amdgcn_readfirstlane(tid >> 6);
    const int id = blockIdx.x, ai = (id & 3) * 32 + (id >> 3);
    const int xc = XMAP ? ai >> 4 : id & 7, yy = XMAP ? ai & 15 : (id >> 3) & 15, y = XMAP ? (id & 4) * 4 : id >> 3;
    const int un = yy * 8 + xc;
    const unsigned g = l >> 4, l15 = l & 15;
    unsigned cp0 = 0;
    if (w == 7) {
        unsigned long long pc;
        asm volatile("s_getpc_b64 %0" : "=s"(pc));
        pc &= ~255ull;
        asm volatile("global_load_dword %0, %1, %2" : "=&v"(cp0) : "v"(min(((id >> 3) + 32 * (l & 3)) * 128, CODEB - 128)), "s"(pc) : "memory");
    }
    unsigned vb = (128 * (yy & 3) + 16 * w + g) * 8192 + (4 * xc + (yy >> 2)) * 256, sw[4], o_q[4];
#pragma unroll
    for (int i = 0; i < 4; ++i) { sw[i] = (l15 ^ ((g << 2) | i)) << 4; o_q[i] = vb + i * 32768 + sw[i]; PIN(o_q[i]); }
    asm volatile("" :: "s"(x), "s"(ckv), "s"(wq), "s"(wuk), "s"(wuv), "s"(wo), "s"(wg), "s"(wu), "s"(wd), "s"(out), "s"(ws));
    const rsrc_t rs = __builtin_amdgcn_make_buffer_rsrc(ws, 0, WS_END, 0x00020000);
    const v4u rsq = {(unsigned)(size_t)ws, (unsigned)((size_t)ws >> 32) & 0xffffu, (unsigned)WS_END, 0x00020000u};
    const f32x4 ZF = {0.f, 0.f, 0.f, 0.f};

    if (__builtin_expect(y < 16, 1)) {
        const int h = 4 * xc + (yy >> 2), cq = yy & 3;
        v4u xv; unsigned ep;
        if (w < 2) {
            asm volatile("global_load_dwordx4 %0, %1, %2" : "=v"(xv) : "v"(w * 1024 + l * 16), "s"(x) : "memory");
            asm volatile("global_load_dword %0, %1, %2 sc1" : "=v"(ep) : "v"(0), "s"(ws) : "memory");
        }
#pragma unroll
        for (int i = 0; i < 4; ++i) glds<true>(o_q[i], wq, L_R0 + (16 * w + 4 * i) * 256);
        bar0();
#pragma unroll
        for (int i = 0; i < 4; ++i) glds<true>(vb + i * 32768 + ((l15 ^ (4 * i + g)) << 4), wuk, L_R1 + (16 * w + 4 * i) * 256);
        STAMP(1);
        if (w < 2) {
            asm volatile("s_waitcnt vmcnt(8)" : "+v"(xv), "+v"(ep) :: "memory");
            layernorm(xv, w, l);
            lwr<v4u>(L_TAG, v4u{mktag(ep), 0u, 0u, 0u});
        }
        bar();
        const unsigned tag = lrd<unsigned>(L_TAG);
        unsigned o_p1 = l < 16 ? WS_H1 + ((h * 4 + cq) * 128 + 16 * w + l) * 16 : OOB;
        unsigned o_g1 = WS_H1 + ((h * 4 + g) * 128 + 16 * w + l15) * 16;
        unsigned o_h2 = WS_H2 + ((((w & (NCOPY - 1)) * 2 + (l >> 5)) * 32 + h) * 128 + 32 * cq + (l & 31)) * 16;
        unsigned a_2w = l15 < 2 ? L_RED + l15 * 256 + (4 * w + g) * 8 : L_JUNK + (l & 31) * 8, a_2r = L_RED + l * 8;
        PIN(a_2w); PIN(a_2r);
        unsigned o_t2 = l < 8 ? WS_T2 + (l * 128 + h * 4 + cq) * 128 : OOB;
        unsigned a0, a1;
        {
            const unsigned q = l15 >> 2, p = l & 3, ch = 2 * w + (p >> 1);
            a0 = L_R0 + (8 * g + q) * 256 + 16 * (ch ^ ((q << 2) | ((2 * g) & 3))) + 8 * (p & 1);
            a1 = L_R0 + (8 * g + q + 4) * 256 + 16 * (ch ^ ((q << 2) | ((2 * g + 1) & 3))) + 8 * (p & 1);
        }
        unsigned a_qw = L_QS + (g & 1) * 272 + (16 * w + l15) * 2;
        unsigned a_qr = L_QS + (l & 1) * 272 + 16 * g;
        unsigned a_uk[4], o_gu[3], a_cnt = l == 0 ? L_TAG + 8 : L_JUNK + l * 4, one = 1u;
        v4u xf[4];
#pragma unroll
        for (int s = 0; s < 4; ++s) {
            xf[s] = lrd<v4u>(L_XN + (l & 1) * 1024 + (128 * cq + 32 * s + 8 * g) * 2);
            a_uk[s] = L_R1 + (16 * w + l15) * 256 + (((4 * s + g) ^ l15) << 4);
            PIN(xf[s]); PIN(a_uk[s]);
        }
        const int mat = un >> 6, mp = (un >> 2) & 15, fp = un & 3;
#pragma unroll
        for (int i = 0; i < 3; ++i) {
            const unsigned pc = min(8 * i + w, 21), idx = min(64 * pc + l, 1375u), row = idx / 43u, ch = idx - row * 43u;
            o_gu[i] = (32 * mp + row) * 2752 + fp * 688 + ch * 16; PIN(o_gu[i]);
        }
        v4u pb = {0u, tag, 0u, tag}, ga;
        PIN(a0); PIN(a1); PIN(o_p1); PIN(o_g1); PIN(o_h2); PIN(o_t2); PIN(a_qw); PIN(a_qr); PIN(pb); PIN(a_cnt); PIN(one);
        asm volatile("s_waitcnt vmcnt(4)" : "+v"(cp0) :: "memory");
        bar0();
        STAMP(2);
        {
            v4u f[4]; frags128(a0, a1, f);
            FENCE();
            const f32x4 acc = gemv4(xf, f);
            pb[0] = asu(acc[0]); pb[2] = asu(acc[1]);
            stx(rs, o_p1, pb);
        }
        STAMP(3);
        if (STOPAT <= 3) { WAITVM(0); STAMP(14); goto done; }
        __builtin_amdgcn_s_sleep(10);
        WAITVM(3);
        v4u fu[4];
        {
            unsigned arr;
            asm volatile("buffer_load_dwordx4 %1, %4, %5, 0 offen sc1\n\tds_add_rtn_u32 %0, %2, %3\n\ts_waitcnt lgkmcnt(0)" : "=&v"(arr), "=&v"(ga) : "v"(a_cnt), "v"(one), "v"(o_g1), "s"(rsq) : "memory");
            hint_put(rs, __builtin_amdgcn_readfirstlane(arr) == 7u ? o_t2 : OOB, tag);
            asm volatile("s_waitcnt vmcnt(1)" : "+v"(ga) :: "memory");
            STAMP(14);
#pragma unroll
            for (int s = 0; s < 4; ++s) fu[s] = lrd<v4u>(a_uk[s]);
            if (__builtin_expect(!none(tagx(ga, tag) != 0u), 0)) {
#pragma nounroll
                SPIN { __builtin_amdgcn_s_sleep(4); get1(ga, o_g1, rsq); if (none(tagx(ga, tag) != 0u)) break; }
            }
            lwr<u16>(a_qw, (u16)pk(sum32(sum16x2(asf(ga[0]), asf(ga[2]))), 0.f));
        }
        STAMP(4);
        bar();
        {
            v4u qf[4];
#pragma unroll
            for (int s = 0; s < 4; ++s) qf[s] = lrd<v4u>(a_qr + 64 * s);
            FENCE();
            const f32x4 acc = gemv4(fu, qf);
            constexpr float SC = 0.08838834764831845f * 1.4426950408889634f;
            lwr<v2u>(a_2w, v2u{pk(acc[0] * SC, acc[1] * SC), pk(acc[2] * SC, acc[3] * SC)});
            bar();
            if (w < NCOPY) {
                const v2u t = lrd<v2u>(a_2r);
                pb[0] = t[0]; pb[2] = t[1];
                stx(rs, o_h2, pb);
            }
        }
        STAMP(5);
        if (STOPAT <= 5) goto done;
        const int r = xc * 8 + (yy >> 1);
        unsigned o_t3 = WS_T3 + ((xc * 4 + cq) * 64 + 8 * w + (l & 7)) * 128, o_t5 = WS_T5 + (r * 32 + (l & 31)) * 128, j0, j1;
        asm volatile("buffer_load_dword %0, %2, %4, 0 offen sc1\n\tbuffer_load_dword %1, %3, %4, 0 offen sc1"
                     : "=&v"(j0), "=&v"(j1) : "v"(o_t3), "v"((w == 0 && !(yy & 1)) ? o_t5 : o_t3), "s"(rsq) : "memory");
#pragma unroll
        for (int i = 0; i < 3; ++i) glds(o_gu[i], mat ? wu : wg, L_R2 + min(8 * i + w, 21) * 1024);
#define V_ISSUE(i) glds<true>(vb + (i) * 32768 + sw[i], wuv, L_R1 + (16 * w + 4 * (i)) * 256)
        if (VDEF == 3) { V_ISSUE(0); V_ISSUE(1); }
        {
            const v4u xg = lrd<v4u>(L_XN + (l & 1) * 1024 + (32 * mp + 8 * g) * 2);
            const unsigned ag = L_R2 + (8 * g + (l15 >> 2)) * 688 + (l & 3) * 8;
            const unsigned f = 16 * (8 * g + w) + l15;
            const unsigned o_hg = (g < 3 && f < 344) ? WS_HG + ((mat * 16 + mp) * 1376 + 344 * fp + f) * 16 : OOB;
            unsigned o_rep = WS_T2 + (xc * 128 + (l & 7) * 16 + 5) * 128, rep;
            if (VDEF == 3) asm volatile("s_waitcnt vmcnt(2)" : "+v"(j0), "+v"(j1) :: "memory"); else asm volatile("s_waitcnt vmcnt(0)" : "+v"(j0), "+v"(j1) :: "memory");
            asm volatile("buffer_load_dword %0, %1, %2, 0 offen sc1" : "=&v"(rep) : "v"(o_rep), "s"(rsq) : "memory");
            bar();
            f32x4 o[3];
#pragma unroll
            for (int t = 0; t < 3; ++t) {
                const unsigned tl = min(8 * t + w, 21);
                o[t] = mfma32(xg, trd2(ag + tl * 32, ag + tl * 32 + 4 * 688), ZF);
            }
            const float o0 = g == 0 ? o[0][0] : g == 1 ? o[1][0] : o[2][0], o1 = g == 0 ? o[0][1] : g == 1 ? o[1][1] : o[2][1];
            stx(rs, o_hg, v4u{asu(o0), tag, asu(o1), tag});
            asm volatile("s_waitcnt vmcnt(1)" : "+v"(rep) :: "memory");
            if (!none(rep != tag)) (void)hint_wait(rsq, o_rep, tag);
        }
        if (VDEF != 3) { V_ISSUE(0); V_ISSUE(1); }
        V_ISSUE(2); V_ISSUE(3);
        STAMP(6);
        unsigned o_h3 = WS_H3 + (((8 * w + g) * 2 + (h >> 4)) * 4 + cq) * 4096 + (l15 >> 3) * 2048 + (h & 15) * 128 + (l & 7) * 16;
        unsigned o_s3 = WS_T3B + ((xc * 4 + cq) * 64 + 8 * w + g) * 128 + (h & 3) * 16;
        unsigned o_t4 = l < 4 ? WS_T4 + ((h * 4 + l) * 4 + cq) * 128 : OOB;
        unsigned o_p4 = l < 16 ? WS_H4 + ((h * 4 + cq) * 128 + 16 * w + l) * 16 : OOB;
        unsigned a_ow, a_ms = l == 0 ? L_MW + w * 4 : l == 32 ? L_MW + 32 + w * 4 : L_JUNK + l * 4;
        {
            const unsigned gg = l15 & 3, t = 2 * (l15 >> 2) + (g & 1);
            a_ow = L_OW + w * 272 + (32 * gg + 4 * (t ^ (gg >> 1)) + 2 * (g >> 1)) * 2;
        }
        unsigned a_or = L_OW + (l & 7) * 272 + 16 * g, a_mw = L_MW + (g & 1) * 16;
        v4u a[4];
        UNDEF(a[0]); UNDEF(a[1]); UNDEF(a[2]); UNDEF(a[3]);
        a_cnt += l == 0 ? 4 : 0;
        PIN(o_h3); PIN(o_t3); PIN(o_t4); PIN(o_p4); PIN(a_ow); PIN(o_s3); PIN(a_or); PIN(a_mw); PIN(a_ms); PIN(a_cnt);
        WAITVM(0);
        bar();
        v4u fv[4];
        frags128(a0 + (L_R1 - L_R0), a1 + (L_R1 - L_R0), fv);
        PIN(fv[0]); PIN(fv[1]); PIN(fv[2]); PIN(fv[3]);
        STAMP(7);
        if (STOPAT <= 7) goto done;
        CNT0(2); CNT0(3);
        const unsigned t3 = hint_wait(rsq, o_t3, tag);
        STAMPC(1);
        pace(t3, P3);
        STAMP(15);
        {
#define ROWBAD(d, s_) (rowxor(((d)[0] ^ (d)[1]) ^ ((d)[2] ^ (d)[3])) ^ (s_)[2])
            if (SMODE == 1) {
#pragma nounroll
                SPIN { get2(a[2], a[3], o_s3, o_s3 + 512, rsq); if (none(tagy(a[2][3], a[3][3], tag) != 0u)) break; }
#pragma nounroll
                SPIN { CNT(2); get2(a[0], a[1], o_h3, o_h3 + 131072, rsq); if (none((ROWBAD(a[0], a[2]) | ROWBAD(a[1], a[3])) != 0u)) break; __builtin_amdgcn_s_sleep(2); }
            } else {
#pragma nounroll
                SPIN {
                    CNT(2);
                    asm volatile("buffer_load_dwordx4 %0, %4, %6, 0 offen sc1\n\tbuffer_load_dwordx4 %1, %4, %6, %7 offen sc1\n\t"
                                 "buffer_load_dwordx4 %2, %5, %6, 0 offen sc1\n\tbuffer_load_dwordx4 %3, %5, %6, 0 offen offset:512 sc1\n\ts_waitcnt vmcnt(0)"
                                 : "+&v"(a[0]), "+&v"(a[1]), "+&v"(a[2]), "+&v"(a[3]) : "v"(o_h3), "v"(o_s3), "s"(rsq), "s"(131072) : "memory");
                    if (none((tagy(a[2][3], a[3][3], tag) | ROWBAD(a[0], a[2]) | ROWBAD(a[1], a[3])) != 0u)) break;
                    __builtin_amdgcn_s_sleep(4);
                }
            }
            STAMP(8);
            unsigned arr;
            unsigned long long tm;
            asm volatile("ds_add_rtn_u32 %0, %2, %3\n\ts_memrealtime %1" : "=&v"(arr), "=&s"(tm) : "v"(a_cnt), "v"(one) : "memory");
            const float m0 = asf(a[2][0]), m1 = asf(a[3][0]);
            const float M = max32(max16(mx(m0, m1)));
            const float a0_ = __builtin_amdgcn_exp2f(m0 - M), a1_ = __builtin_amdgcn_exp2f(m1 - M);
            FENCE();
            asm volatile("s_waitcnt lgkmcnt(0)" : "+v"(arr), "+s"(tm) :: "memory");
            hint_put(rs, __builtin_amdgcn_readfirstlane(arr) == 7u ? o_t4 : OOB, tag, (unsigned)tm);
            const f32x2 aa0 = {a0_, a0_}, aa1 = {a1_, a1_};
            f32x2 c[4];
#pragma unroll
            for (int j = 0; j < 4; ++j) {
                const unsigned p = a[0][j], q = a[1][j];
                c[j] = __builtin_elementwise_fma(aa1, f32x2{lo16(q), hi16(q)}, aa0 * f32x2{lo16(p), hi16(p)});
            }
            const float v0 = sum16x2(c[0][0], c[2][0]), v1 = sum16x2(c[0][1], c[2][1]), v2 = sum16x2(c[1][0], c[3][0]), v3 = sum16x2(c[1][1], c[3][1]);
            lwr<unsigned>(a_ow, pk(sum32x2(v0, v2), sum32x2(v1, v3)));
            const float Ls = sum32(sum16(__builtin_fmaf(a1_, asf(a[3][1]), a0_ * asf(a[2][1]))));
            lwr<float>(a_ms, l < 32 ? M : Ls);
        }
        bar();
        {
            v4u of[4];
#pragma unroll
            for (int s = 0; s < 4; ++s) of[s] = lrd<v4u>(a_or + 64 * s);
            const f32x4 mw = lrd<f32x4>(a_mw), lw = lrd<f32x4>(a_mw + 32);
            FENCE();
            const f32x4 c = gemv4(of, fv);
            const float M = mx(mx3(mw[0], mw[1], mw[2]), mw[3]);
            float be[4];
#pragma unroll
            for (int r = 0; r < 4; ++r) be[r] = __builtin_amdgcn_exp2f(mw[r] - M);
            const float rd = __builtin_amdgcn_rcpf((be[0] * lw[0] + be[1] * lw[1]) + (be[2] * lw[2] + be[3] * lw[3]));
            const float v = ((be[0] * c[0] + be[1] * c[1]) + (be[2] * c[2] + be[3] * c[3])) * rd;
            auto sv = __builtin_amdgcn_permlane16_swap(asu(v), asu(v), false, false);
            pb[0] = sv[0]; pb[2] = sv[1];
            stx(rs, o_p4, pb);
        }
        STAMP(9);
        if ((yy & 1) == 0) {
            const unsigned o1 = WS_H5 + ((32 + (tid >> 3)) * 512 + 8 * r + (tid & 7)) * 16;
            const bool two = tid < 176;
            const unsigned o2 = two ? o1 + 64 * 512 * 16 : o1;
            pace(t3, PG);
#pragma nounroll
            SPIN { get2(a[0], a[1], o1, o2, rsq); if (none((tagx(a[0], tag) | tagx(a[1], tag)) != 0u)) break; __builtin_amdgcn_s_sleep(8); }
            {
                float t = sum32(sum16x2(asf(a[0][0]) + (two ? asf(a[1][0]) : 0.f), asf(a[0][2]) + (two ? asf(a[1][2]) : 0.f)));
                t += dpp<0x128>(t);
                lwr<float>(L_RED + ((g & 1) * 8 + (l & 7)) * 32 + w * 4, t);
            }
            bar();
            STAMP(10);
            if (w == 0) {
                const f32x4 t2 = lrd<f32x4>(L_RED + ((g & 1) * 8 + (l & 7)) * 32), t4 = lrd<f32x4>(L_RED + ((g & 1) * 8 + (l & 7)) * 32 + 16);
                float glu = rbf(((t2[0] + t2[1]) + (t2[2] + t2[3])) + ((t4[0] + t4[1]) + (t4[2] + t4[3])));
                float xin = lo16(x[(g & 1) * 512 + 8 * r + (l & 7)]);
                unsigned o0 = WS_H5 + ((l >> 3) * 512 + 8 * r + (l & 7)) * 16;
                unsigned o_out = ((g & 1) * 512 + 8 * r + (l & 7)) * 2;
                PIN(glu); PIN(xin); PIN(o0); PIN(o_out);
                pace(hint_wait(rsq, o_t5, tag), P5);
                STAMP(11);
                asm volatile("buffer_load_dwordx4 %0, %4, %5, 0 offen sc1\n\tbuffer_load_dwordx4 %1, %4, %5, %6 offen sc1\n\t"
                             "buffer_load_dwordx4 %2, %4, %5, %7 offen sc1\n\tbuffer_load_dwordx4 %3, %4, %5, %8 offen sc1"
                             : "=&v"(a[0]), "=&v"(a[1]), "=&v"(a[2]), "=&v"(a[3]) : "v"(o0), "s"(rsq), "s"(65536), "s"(131072), "s"(196608) : "memory");
                __builtin_amdgcn_raw_buffer_store_b32(tag, rs, (r == 0 && l == 0) ? 0u : OOB, 0, 16);
                asm volatile("s_waitcnt vmcnt(1)" : "+v"(a[0]), "+v"(a[1]), "+v"(a[2]), "+v"(a[3]) :: "memory");
                if (__builtin_expect(!none(tagx4(a, tag) != 0u), 0)) {
#pragma nounroll
                    SPIN { CNT(3); get4s(a, o0, rsq, 65536, 131072, 196608); if (none(tagx4(a, tag) != 0u)) break; }
                }
                STAMP(12);
                float t = sum32(sum16x2((asf(a[0][0]) + asf(a[1][0])) + (asf(a[2][0]) + asf(a[3][0])), (asf(a[0][2]) + asf(a[1][2])) + (asf(a[2][2]) + asf(a[3][2]))));
                t += dpp<0x128>(t);
                {
                    const unsigned v = pk(rbf(xin + rbf(t)) + glu, 0.f);
                    const unsigned p = __builtin_amdgcn_perm((unsigned)__builtin_amdgcn_update_dpp(0, (int)v, 0x101, 0xf, 0xf, true), v, 0x05040100u);
                    const v4u o = {p, (unsigned)__builtin_amdgcn_update_dpp(0, (int)p, 0x102, 0xf, 0xf, true), (unsigned)__builtin_amdgcn_update_dpp(0, (int)p, 0x104, 0xf, 0xf, true),
                                   (unsigned)__builtin_amdgcn_update_dpp(0, (int)p, 0x106, 0xf, 0xf, true)};
                    __builtin_amdgcn_raw_buffer_store_b128(o, __builtin_amdgcn_make_buffer_rsrc(out, 0, 2048, 0x00020000), (l == 0 || l == 16) ? o_out : OOB, 0, 0);
                }
            }
        }
    } else {
        const int pr = xc * 8 + (yy >> 1), hg = yy & 1, b = pr >> 5, sp = pr & 31;
        const unsigned t_in = now();
        unsigned ep, j0;
        unsigned o_t2 = WS_T2 + (xc * 128 + (16 * hg + 2 * w) * 4 + (l & 7)) * 128;
        asm volatile("global_load_dword %0, %1, %2 sc1" : "=v"(ep) : "v"(0), "s"(ws) : "memory");
        asm volatile("buffer_load_dword %0, %1, %2, 0 offen sc1" : "=v"(j0) : "v"(o_t2), "s"(rsq) : "memory");
        unsigned qoff = WS_H2 + ((((xc & (NCOPY - 1)) * 2 + b) * 32 + 16 * hg + 2 * w) * 128 + l) * 16;
        unsigned a_q0 = L_Q + 2 * w * 1024 + (((l >> 1) ^ (2 * w)) << 4) + (l & 1) * 8, a_q1 = L_Q + (2 * w + 1) * 1024 + (((l >> 1) ^ (2 * w + 1)) << 4) + (l & 1) * 8;
        unsigned o_kv[8];
#pragma unroll
        for (int i = 0; i < 8; ++i) { o_kv[i] = (pr * 128 + 64 * hg + 8 * w + i) * 1024 + ((l ^ (8 * (w & 1) + i)) << 4); PIN(o_kv[i]); }
        PIN(qoff); PIN(a_q0); PIN(a_q1);
        asm volatile("s_waitcnt vmcnt(0)" : "+v"(ep), "+v"(j0), "+v"(cp0) :: "memory");
        const unsigned tag = mktag(ep);
        pace(t_in, PB);
#pragma unroll
        for (int i = 0; i < 8; ++i) glds(o_kv[i], ckv, L_R0 + (8 * w + i) * 1024);
        STAMP(1);
        bar0();
#pragma unroll
        for (int i = 0; i < 8; ++i) glds(o_kv[i] ^ 65536, ckv, L_R0 + (64 + 8 * w + i) * 1024);
        WAITVM(8);
        bar0();
        STAMP(2);
#define Q_ISSUE() \
            FENCE(); \
            WAITVM(2); \
            const v2u hv = hint_get(rsq, o_t2);        \
            STAMP(14); \
            pace(none(hv[0] != tag) ? hv[1] : hint_wait(rsq, o_t2, tag), P2); \
            STAMP(15); \
            v4u qa[4]; \
            asm volatile("buffer_load_dwordx4 %0, %4, %5, 0 offen sc1\n\tbuffer_load_dwordx4 %1, %4, %5, 0 offen offset:1024 sc1\n\t" \
                         "buffer_load_dwordx4 %2, %4, %5, 0 offen offset:2048 sc1\n\tbuffer_load_dwordx4 %3, %4, %5, 0 offen offset:3072 sc1" \
                         : "=&v"(qa[0]), "=&v"(qa[1]), "=&v"(qa[2]), "=&v"(qa[3]) : "v"(qoff), "s"(rsq) : "memory"); \
            bar0()
#define Q_LAND(n) do { \
            asm volatile("s_waitcnt vmcnt(" STR(n) ")" : "+v"(qa[0]), "+v"(qa[1]), "+v"(qa[2]), "+v"(qa[3]) :: "memory"); \
            if (__builtin_expect(!none(tagx4(qa, tag) != 0u), 0)) { _Pragma("nounroll") SPIN { __builtin_amdgcn_s_sleep(4); get4<1024>(qa, qoff, rsq); if (none(tagx4(qa, tag) != 0u)) break; } } \
            asm volatile("ds_write2_b32 %0, %2, %3 offset1:1\n\tds_write2_b32 %0, %4, %5 offset0:128 offset1:129\n\tds_write2_b32 %1, %6, %7 offset1:1\n\tds_write2_b32 %1, %8, %9 offset0:128 offset1:129" \
                         :: "v"(a_q0), "v"(a_q1), "v"(qa[0][0]), "v"(qa[0][2]), "v"(qa[1][0]), "v"(qa[1][2]), "v"(qa[2][0]), "v"(qa[2][2]), "v"(qa[3][0]), "v"(qa[3][2]) : "memory"); \
            STAMP(3); bar();   } while (0)
        unsigned t5 = 0;
        const int h = 4 * xc + (yy >> 2), cq = yy & 3;
        unsigned o_t4 = WS_T4 + ((h * 4 + cq) * 4 + (l & 3)) * 128;
        unsigned o_hg = (EXP & 2) ? 0 : WS_HG + ((4 * (w & 3) + g) * 1376 + 16 * min(un, 85) + l15) * 16;
        if (w >= 4) {
            unsigned a_r[4];
#pragma unroll
            for (int j = 0; j < 4; ++j) a_r[j] = l15 * 1024 + (((4 * j + g) ^ l15) << 4);
            const unsigned kb = L_R0 + (w - 4) * 16384;
            v4u cr[32], qf[16];
#pragma unroll
            for (int s = 0; s < 16; ++s) cr[s] = lrd<v4u>(a_r[s & 3] + kb + (s >> 2) * 256);
            unsigned a_mxw = L_MX + l15 * 16 + (w - 4) * 4, a_mxr = L_MX + l15 * 16;
            unsigned a_pw = L_P + l15 * 256 + (((2 * (w - 4) + (g >> 1)) ^ l15) << 4) + (g & 1) * 8;
            __builtin_amdgcn_s_setprio(1);
            PIN(a_mxw); PIN(a_mxr); PIN(a_pw); PIN(o_hg);
            Q_ISSUE();
#pragma unroll
            for (int s = 0; s < 16; ++s) cr[16 + s] = lrd<v4u>(a_r[s & 3] + kb + 65536 + (s >> 2) * 256);
            FENCE();
            Q_LAND(0);
            STAMP(4);
#pragma unroll
            for (int s = 0; s < 16; ++s) qf[s] = lrd<v4u>(a_r[s & 3] + L_Q + (s >> 2) * 256);
            FENCE();
            f32x4 s1 = ZF, s2 = ZF;
#pragma unroll
            for (int s = 0; s < 16; ++s) { s1 = mfma32(cr[s], qf[s], s1); s2 = mfma32(cr[16 + s], qf[s], s2); }
            asm volatile("s_nop 7" : "+v"(s1), "+v"(s2));
            lwr<float>(a_mxw, max32(max16(mx(mx3(mx3(s1[0], s1[1], s1[2]), s1[3], s2[0]), mx3(s2[1], s2[2], s2[3])))));
            bar();
            STAMP(12);
            const f32x4 m0 = lrd<f32x4>(a_mxr);
            const float mr = mx(mx3(m0[0], m0[1], m0[2]), m0[3]);
            v2u p1 = {pk(__builtin_amdgcn_exp2f(s1[0] - mr), __builtin_amdgcn_exp2f(s1[1] - mr)), pk(__builtin_amdgcn_exp2f(s1[2] - mr), __builtin_amdgcn_exp2f(s1[3] - mr))};
            lwr<v2u>(a_pw, p1);
            v2u p2 = {pk(__builtin_amdgcn_exp2f(s2[0] - mr), __builtin_amdgcn_exp2f(s2[1] - mr)), pk(__builtin_amdgcn_exp2f(s2[2] - mr), __builtin_amdgcn_exp2f(s2[3] - mr))};
            lwr<v2u>(a_pw ^ 128, p2);
            bar();
            STAMP(5);
            PIN(p1); PIN(p2);
            {
                const unsigned q0 = p1[0], q1 = p1[1], q2 = p2[0], q3 = p2[1];
                lwr<float>(a_mxw + 256, sum32(sum16(((lo16(q0) + hi16(q0)) + (lo16(q1) + hi16(q1))) + ((lo16(q2) + hi16(q2)) + (lo16(q3) + hi16(q3))))));
            }
            bar();
            __builtin_amdgcn_s_setprio(0);
            t5 = now();
            if (PH) pace(t5, PH);
            v4u ga, gb;
            asm volatile("buffer_load_dwordx4 %0, %3, %6, 0 offen sc1\n\tbuffer_load_dwordx4 %1, %4, %6, 0 offen sc1\n\tbuffer_load_dword %2, %5, %6, 0 offen sc1\n\ts_waitcnt vmcnt(0)"
                         : "=&v"(ga), "=&v"(gb), "=&v"(j0), "+v"(o_hg) : "v"(o_hg + 16 * 1376 * 16), "v"(o_t4), "s"(rsq) : "memory");
            if (un < 86) {
                if (!(EXP & 2) && __builtin_expect(!none((tagx(ga, tag) | tagx(gb, tag)) != 0u), 0)) {
#pragma nounroll
                    SPIN { __builtin_amdgcn_s_sleep(16); get2(ga, gb, o_hg, o_hg + 16 * 1376 * 16, rsq); if (none((tagx(ga, tag) | tagx(gb, tag)) != 0u)) break; }
                }
                lwr<float>(L_RED + (l & 31) * 32 + (w - 4) * 4, sum32(sum16x2(asf(ga[0]), asf(ga[2]))));
                lwr<float>(L_RED + (l & 31) * 32 + w * 4, sum32(sum16x2(asf(gb[0]), asf(gb[2]))));
            }
            pace(t5, PW);
#pragma unroll
            for (int i = 0; i < 8; ++i) {
                const unsigned ch = l15 ^ ((g << 2) | (i & 3));
                glds((h * 128 + 32 * (w - 4) + 4 * i + g) * 1024 + cq * 128 + (ch >> 3) * 512 + (ch & 7) * 16, wo, L_R1 + (32 * (w - 4) + 4 * i) * 256);
            }
        } else {
            unsigned a_t[2][4];
            {
                const unsigned q = l15 >> 2, p = l & 3;
#pragma unroll
                for (int j = 0; j < 8; ++j) {
                    const unsigned r0 = 8 * g + 4 * (j >> 2) + q, ch = 16 * w + 4 * p + (j & 3);
                    a_t[j >> 2][j & 3] = L_R0 + r0 * 1024 + ((ch ^ (r0 & 15)) << 4) + (p >> 1) * 8;
                }
            }
            v4u ct[8][4];
#pragma unroll
            for (int t = 0; t < 8; ++t)
#pragma unroll
                for (int s = 0; s < 2; ++s) ct[t][s] = trd2((a_t[0][t >> 1] ^ ((t & 1) * 8)) + s * 32768, (a_t[1][t >> 1] ^ ((t & 1) * 8)) + s * 32768);
            unsigned a_pr[4], a_mxr = L_MX + l15 * 16;
#pragma unroll
            for (int j = 0; j < 4; ++j) { a_pr[j] = L_P + l15 * 256 + (((4 * j + g) ^ l15) << 4); PIN(a_pr[j]); }
            unsigned o_t3 = l < 4 ? WS_T3 + (((4 * hg + l) * 4 + w) * 64 + pr) * 128 : OOB;
            unsigned o_t3b = l < 32 ? WS_T3B + (((4 * hg + (l >> 3)) * 4 + w) * 64 + pr) * 128 + (l & 7) * 16 : OOB;
            PIN(o_t3b);
            unsigned o_h3 = WS_H3 + ((pr * 2 + hg) * 4 + w) * 4096 + l * 16;
            unsigned a_tw = L_R2 + w * 8192 + 4608 + l15 * 16, a_tr = L_R2 + w * 8192 + 4608 + (4 * ((l >> 3) & 3) + (l & 3)) * 16;
            PIN(a_tw); PIN(a_tr);
            unsigned a_sw = L_R2 + w * 8192 + l15 * 144 + g * 16, a_sr = L_R2 + w * 8192 + (l >> 3) * 144 + (l & 7) * 16;
            PIN(a_sw); PIN(a_sr);
            unsigned o_wd[4], a_lr = L_MX + 256 + l15 * 16;
#pragma unroll
            for (int i = 0; i < 4; ++i) { const unsigned row = 4 * w + i; o_wd[i] = (EXP & 1) ? 0 : (16 * min(un, 85) + row) * 1024 + ((l ^ ((row & 7) << 1)) << 4); PIN(o_wd[i]); }
            PIN(a_mxr); PIN(o_t3); PIN(o_h3); PIN(a_lr);
            Q_ISSUE();
            v4u wst[4];
            asm volatile("global_load_dwordx4 %0, %4, %8\n\tglobal_load_dwordx4 %1, %5, %8\n\tglobal_load_dwordx4 %2, %6, %8\n\tglobal_load_dwordx4 %3, %7, %8"
                         : "=&v"(wst[0]), "=&v"(wst[1]), "=&v"(wst[2]), "=&v"(wst[3]) : "v"(o_wd[0]), "v"(o_wd[1]), "v"(o_wd[2]), "v"(o_wd[3]), "s"(wd) : "memory");
#pragma unroll
            for (int t = 0; t < 8; ++t)
#pragma unroll
                for (int s = 2; s < 4; ++s) ct[t][s] = trd2((a_t[0][t >> 1] ^ ((t & 1) * 8)) + s * 32768, (a_t[1][t >> 1] ^ ((t & 1) * 8)) + s * 32768);
            FENCE();
            Q_LAND(4);
            STAMP(4);
            if (HP == 4) hint_put(rs, o_t3, tag);
            bar0();
            if (HP == 5) hint_put(rs, o_t3, tag);
            const f32x4 m0 = lrd<f32x4>(a_mxr);
            float mr = mx(mx3(m0[0], m0[1], m0[2]), m0[3]);
            PIN(mr);
            bar();
            STAMP(5);
            v4u pf[4];
#pragma unroll
            for (int s = 0; s < 4; ++s) pf[s] = lrd<v4u>(a_pr[s]);
            if (HP == 0) hint_put(rs, o_t3, tag);
            FENCE();
            f32x4 o[8];
#define M0_(t) asm volatile("v_mfma_f32_16x16x32_bf16 %0, %1, %2, 0" : "=&v"(o[t]) : "v"(ct[t][0]), "v"(pf[0]))
#define MM(t, s) asm volatile("v_mfma_f32_16x16x32_bf16 %0, %1, %2, %0" : "+v"(o[t]) : "v"(ct[t][s]), "v"(pf[s]))
#define RDY(t) PIN(o[t])
#define SEQ() asm volatile("" ::: "memory")
#define CV(d, t, i) do { d = pk(o[t][i], o[t][i + 1]); PIN(d); } while (0)
#define PUT(n, k, A, B, C, D) lwr<v4u>(a_sw + (n) * 2304 + (k) * 64, v4u{A, B, C, D})
#define GETL(n) do { x0 = lrd<v4u>(a_sr + (n) * 2304); x1 = lrd<v4u>(a_sr + (n) * 2304 + 8 * 144); } while (0)
#define OUTL(n) do { stx(rs, o_h3 + (n) * 2048, x0); stx(rs, o_h3 + (n) * 2048 + 1024, x1); } while (0)
#define CS(A, B) do { cs = cs ^ (A) ^ (B); PIN(cs); } while (0)
            unsigned d0, d1, d2, d3, e0, e1, e2, e3, cs = 0;
            v4u x0, x1;
            M0_(0); M0_(1); MM(0, 1); MM(1, 1); MM(0, 2); MM(1, 2); MM(0, 3); MM(1, 3);
            M0_(2); M0_(3); MM(2, 1); MM(3, 1); MM(2, 2); MM(3, 2); MM(2, 3);
            RDY(0); CV(d0, 0, 0); CV(d1, 0, 2);
            MM(3, 3); RDY(1); CV(d2, 1, 0); CV(d3, 1, 2);
            M0_(4); PUT(0, 0, d0, d1, d2, d3); SEQ();
            MM(4, 1); CS(d0, d1); CS(d2, d3);
            MM(4, 2); RDY(2); CV(e0, 2, 0); CV(e1, 2, 2);
            MM(4, 3);
            bar0();
            const f32x4 l0 = lrd<f32x4>(a_lr);
            M0_(5); RDY(3); CV(e2, 3, 0); CV(e3, 3, 2);
            MM(5, 1); PUT(0, 1, e0, e1, e2, e3); GETL(0); SEQ();
            MM(5, 2); CS(e0, e1); CS(e2, e3);
            MM(5, 3); RDY(4); CV(d0, 4, 0); CV(d1, 4, 2);
            M0_(6);
            float lsum = (l0[0] + l0[1]) + (l0[2] + l0[3]);
            PIN(lsum);
            MM(6, 1); OUTL(0); SEQ();
            MM(6, 2); MM(6, 3);
            M0_(7); RDY(5); CV(d2, 5, 0); CV(d3, 5, 2);
            MM(7, 1); PUT(1, 0, d0, d1, d2, d3); SEQ();
            MM(7, 2); CS(d0, d1); CS(d2, d3);
            MM(7, 3);
            asm volatile("s_nop 1" : "+v"(o[6]));
            CV(e0, 6, 0); CV(e1, 6, 2);
            asm volatile("s_nop 4" : "+v"(o[7]));
            CV(e2, 7, 0); CV(e3, 7, 2);
            PUT(1, 1, e0, e1, e2, e3); GETL(1); SEQ();
            CS(e0, e1); CS(e2, e3);
            { auto r1 = __builtin_amdgcn_permlane16_swap(cs, cs, false, false); cs = r1[0] ^ r1[1]; auto r2 = __builtin_amdgcn_permlane32_swap(cs, cs, false, false); cs = r2[0] ^ r2[1]; }
            lwr<v4u>(a_tw, v4u{asu(mr), asu(lsum), cs, tag});
            const v4u x2 = lrd<v4u>(a_tr);
            OUTL(1);
            stx(rs, o_t3b, x2);
            FENCE();
            STAMP(6);
            asm volatile("s_waitcnt vmcnt(6)" : "+v"(wst[0]), "+v"(wst[1]), "+v"(wst[2]), "+v"(wst[3]) :: "memory");
#pragma unroll
            for (int i = 0; i < 4; ++i) lwr<v4u>(L_R0 + (4 * w + i) * 1024 + l * 16, wst[i]);
        }
        if (STOPAT <= 7) goto done;
        bar();
        if (un < 86) {
            if (w == 4) {
                const f32x4 gp = lrd<f32x4>(L_RED + (l & 31) * 32), up = lrd<f32x4>(L_RED + (l & 31) * 32 + 16);
                const float gg = rbf((gp[0] + gp[1]) + (gp[2] + gp[3])), uu = rbf((up[0] + up[1]) + (up[2] + up[3]));
                const float sg = rbf(__builtin_amdgcn_rcpf(1.f + __builtin_amdgcn_exp2f(gg * -1.4426950408889634f)));
                lwr<u16>(L_HH + (l & 31) * 2, (u16)pk(sg * uu, 0.f));
            }
            bar();
            const v2u ha = lrd<v2u>(L_HH + (l & 1) * 32 + g * 8);
            const unsigned q = l15 >> 2, p = l & 3, row = 4 * g + q;
            f32x4 o[4];
#pragma unroll
            for (int t = 0; t < 4; ++t) {
                const unsigned ch = 8 * w + 2 * t + (p >> 1);
                o[t] = mfma16(ha, trd(L_R0 + row * 1024 + ((ch ^ ((row & 7) << 1)) << 4) + (p & 1) * 8), ZF);
            }
            const float o0 = g == 0 ? o[0][0] : g == 1 ? o[1][0] : g == 2 ? o[2][0] : o[3][0];
            const float o1 = g == 0 ? o[0][1] : g == 1 ? o[1][1] : g == 2 ? o[2][1] : o[3][1];
            stx(rs, WS_H5 + ((32 + un) * 512 + 64 * w + l) * 16, v4u{asu(o0), tag, asu(o1), tag});
        }
        STAMP(7);
        {
            unsigned a0, a1;
            {
                const unsigned q = l15 >> 2, p = l & 3, ch = 2 * w + (p >> 1);
                a0 = L_R1 + (8 * g + q) * 256 + 16 * (ch ^ ((q << 2) | ((2 * g) & 3))) + 8 * (p & 1);
                a1 = L_R1 + (8 * g + q + 4) * 256 + 16 * (ch ^ ((q << 2) | ((2 * g + 1) & 3))) + 8 * (p & 1);
            }
            unsigned o_g4 = WS_H4 + ((h * 4 + g) * 128 + 16 * w + l15) * 16;
            unsigned o_t5 = tid < 16 ? WS_T5 + ((8 * (cq + 4 * (tid >> 3)) + (tid & 7)) * 32 + h) * 128 : OOB;
            unsigned o_h5 = l < 16 ? WS_H5 + (h * 512 + 64 * (cq + 4 * (w >> 2)) + 16 * (w & 3) + l) * 16 : OOB;
            unsigned a_qw = L_QS + (g & 1) * 272 + (16 * w + l15) * 2, a_qr = L_QS + (l & 1) * 272 + 16 * g;
            v4u pb = {0u, tag, 0u, tag}, fw[4], ga;
            WAITVM(0);
            bar();
            frags128(a0, a1, fw);
            PIN(fw[0]); PIN(fw[1]); PIN(fw[2]); PIN(fw[3]); PIN(o_g4); PIN(o_t5); PIN(o_h5); PIN(a_qw); PIN(a_qr); PIN(pb);
            STAMP(10);
            if (w == 4) pace(hint_wait(rsq, o_t4, tag), P4);
            bar0();
            STAMP(11);
            asm volatile("buffer_load_dwordx4 %0, %1, %2, 0 offen sc1" : "=&v"(ga) : "v"(o_g4), "s"(rsq) : "memory");
            hint_put(rs, o_t5, tag);
            asm volatile("s_waitcnt vmcnt(1)" : "+v"(ga) :: "memory");
            if (__builtin_expect(!none(tagx(ga, tag) != 0u), 0)) {
#pragma nounroll
                SPIN { __builtin_amdgcn_s_sleep(2); get1(ga, o_g4, rsq); if (none(tagx(ga, tag) != 0u)) break; }
            }
            STAMP(8);
            lwr<u16>(a_qw, (u16)pk(sum32(sum16x2(asf(ga[0]), asf(ga[2]))), 0.f));
            bar();
            v4u cf[4];
#pragma unroll
            for (int s = 0; s < 4; ++s) cf[s] = lrd<v4u>(a_qr + 64 * s);
            FENCE();
            const f32x4 acc = gemv4(cf, fw);
            pb[0] = asu(acc[0]); pb[2] = asu(acc[1]);
            stx(rs, o_h5, pb);
        }
        STAMP(9);
    }
done:
    STAMP(13);
}

extern "C" void kernel_launch(void* const* d_in, const int* in_sizes, int n_in, void* d_out, int out_size, void* d_ws, size_t ws_size,
                              hipStream_t stream) {
    (void)in_sizes; (void)n_in; (void)out_size; (void)ws_size;
    (void)hipFuncSetAttribute((const void*)mla_glu_decode, hipFuncAttributeMaxDynamicSharedMemorySize, L_END);
    mla_glu_decode<<<256, 512, L_END, stream>>>((const u16*)d_in[0], (const u16*)d_in[1], (const u16*)d_in[2], (const u16*)d_in[3], (const u16*)d_in[4],
                                               (const u16*)d_in[5], (const u16*)d_in[7], (const u16*)d_in[8], (const u16*)d_in[9], (u16*)d_out,
                                               (unsigned char*)d_ws);
}
```

```cpp
#include <hip/hip_runtime.h>
#include <hip/hip_bf16.h>

typedef unsigned short u16;
typedef unsigned v4u __attribute__((ext_vector_type(4)));
typedef unsigned v2u __attribute__((ext_vector_type(2)));
typedef float f32x4 __attribute__((ext_vector_type(4)));
typedef float f32x2 __attribute__((ext_vector_type(2)));
typedef short s16x4 __attribute__((ext_vector_type(4)));
typedef __bf16 bf16x8 __attribute__((ext_vector_type(8)));
typedef __bf16 bf16x2 __attribute__((ext_vector_type(2)));
#define LAS __attribute__((address_space(3)))
#define GAS __attribute__((address_space(1)))
#define DEVI __device__ __forceinline__

constexpr unsigned PB = 110;
constexpr unsigned P2 = 110;
constexpr unsigned P3 = 140;
constexpr unsigned P4 = 55;
constexpr unsigned P5 = 50;
constexpr unsigned PG = 205;
constexpr unsigned PW = 50;
constexpr int CODEB = 14208;

constexpr int WS_T2 = 4096;
constexpr int WS_T3 = WS_T2 + 8 * 128 * 128;
constexpr int WS_T5 = WS_T3 + 8 * 256 * 128;
constexpr int WS_T4 = WS_T5 + 64 * 32 * 128;
constexpr int WS_S3 = WS_T4 + 32 * 16 * 128;
constexpr int WS_H1 = WS_S3 + 8 * 256 * 128;
constexpr int WS_H4 = WS_H1 + 32 * 4 * 128 * 16;
constexpr int WS_H2 = WS_H4 + 32 * 4 * 128 * 16;
constexpr int WS_HG = WS_H2 + 2 * 32 * 128 * 16;
constexpr int WS_H5 = WS_HG + 2 * 16 * 1376 * 16;
constexpr int WS_H3 = WS_H5 + 118 * 512 * 16;
constexpr int WS_END = WS_H3 + 128 * 4 * 4096;
constexpr int L_Q = 0;
constexpr int L_P = 16384;
constexpr int L_QS = L_P + 4096;
constexpr int L_OW = L_QS + 1024;
constexpr int L_MW = L_OW + 2304;
constexpr int L_MX = L_MW + 64;
constexpr int L_HH = L_MX + 512;
constexpr int L_TAG = L_HH + 64;
constexpr int L_JUNK = L_TAG + 64;
constexpr int L_RED = L_JUNK + 256;
constexpr int L_XN = 28672;
constexpr int L_R0 = 32768, L_R1 = 65536, L_R2 = 98304;
constexpr int L_END = 163840;
static_assert(L_RED + 1024 <= L_XN && L_XN + 2048 <= L_R0, "lds");
constexpr int SPIN_CAP = 1 << 16;

DEVI unsigned pk(float lo, float hi) { bf16x2 r = {(__bf16)lo, (__bf16)hi}; return __builtin_bit_cast(unsigned, r); }
DEVI float rbf(float f) { return (float)(__bf16)f; }
DEVI float lo16(unsigned w) { return __builtin_bit_cast(float, w << 16); }
DEVI float hi16(unsigned w) { return __builtin_bit_cast(float, w & 0xffff0000u); }
DEVI float asf(unsigned w) { return __builtin_bit_cast(float, w); }
DEVI unsigned asu(float w) { return __builtin_bit_cast(unsigned, w); }
DEVI float mx(float a, float b) { float r; asm("v_max_f32 %0, %1, %2" : "=v"(r) : "v"(a), "v"(b)); return r; }
DEVI float mx3(float a, float b, float c) { float r; asm("v_max3_f32 %0, %1, %2, %3" : "=v"(r) : "v"(a), "v"(b), "v"(c)); return r; }
DEVI f32x4 mfma32(v4u a, v4u b, f32x4 c) { return __builtin_amdgcn_mfma_f32_16x16x32_bf16(__builtin_bit_cast(bf16x8, a), __builtin_bit_cast(bf16x8, b), c, 0, 0, 0); }
DEVI f32x4 mfma16(v2u a, v2u b, f32x4 c) { return __builtin_amdgcn_mfma_f32_16x16x16bf16_1k(__builtin_bit_cast(s16x4, a), __builtin_bit_cast(s16x4, b), c, 0, 0, 0); }
template <class T> DEVI T lrd(unsigned off) { return *(LAS T*)(size_t)off; }
template <class T> DEVI void lwr(unsigned off, T v) { *(LAS T*)(size_t)off = v; }
DEVI v2u trd(unsigned off) { return __builtin_bit_cast(v2u, __builtin_amdgcn_ds_read_tr16_b64_v4i16((LAS s16x4*)(size_t)off)); }
DEVI v4u trd2(unsigned o0, unsigned o1) { v2u a = trd(o0), b = trd(o1); return v4u{a.x, a.y, b.x, b.y}; }
DEVI float sum16(float x) { auto r = __builtin_amdgcn_permlane16_swap(asu(x), asu(x), false, false); return asf(r[0]) + asf(r[1]); }
DEVI float sum32(float x) { auto r = __builtin_amdgcn_permlane32_swap(asu(x), asu(x), false, false); return asf(r[0]) + asf(r[1]); }
DEVI float max16(float x) { auto r = __builtin_amdgcn_permlane16_swap(asu(x), asu(x), false, false); return mx(asf(r[0]), asf(r[1])); }
DEVI float max32(float x) { auto r = __builtin_amdgcn_permlane32_swap(asu(x), asu(x), false, false); return mx(asf(r[0]), asf(r[1])); }
DEVI float sum16x2(float x, float y) { auto r = __builtin_amdgcn_permlane16_swap(asu(x), asu(y), false, false); return asf(r[0]) + asf(r[1]); }
DEVI float sum32x2(float x, float y) { auto r = __builtin_amdgcn_permlane32_swap(asu(x), asu(y), false, false); return asf(r[0]) + asf(r[1]); }
template <int CTRL> DEVI float dpp(float x) { return asf((unsigned)__builtin_amdgcn_update_dpp(0, (int)asu(x), CTRL, 0xf, 0xf, true)); }
DEVI unsigned rowxor(unsigned x) {
    x ^= (unsigned)__builtin_amdgcn_update_dpp(0, (int)x, 0xB1, 0xf, 0xf, true); x ^= (unsigned)__builtin_amdgcn_update_dpp(0, (int)x, 0x4E, 0xf, 0xf, true);
    x ^= (unsigned)__builtin_amdgcn_update_dpp(0, (int)x, 0x141, 0xf, 0xf, true); x ^= (unsigned)__builtin_amdgcn_update_dpp(0, (int)x, 0x140, 0xf, 0xf, true);
    return x;
}
DEVI float sel4(unsigned g, float a, float b, float c, float d) { const float x = (g & 1) ? b : a, y = (g & 1) ? d : c; return (g & 2) ? y : x; }
DEVI float wavesum(float x) {
    x += dpp<0xB1>(x); x += dpp<0x4E>(x); x += dpp<0x141>(x); x += dpp<0x140>(x);
    return sum32(sum16(x));
}
#define STR_(x) #x
#define STR(x) STR_(x)
#define WAITVM(n) asm volatile("s_waitcnt vmcnt(" STR(n) ")" ::: "memory")
#define PIN(x) asm volatile("" : "+v"(x))
#define UNDEF(x) asm volatile("" : "=v"(x))
#define FENCE() __builtin_amdgcn_sched_barrier(0)
#define SPIN for (int it_ = SPIN_CAP; it_ > 0; --it_)
DEVI void bar() { asm volatile("s_waitcnt lgkmcnt(0)\n\ts_barrier" ::: "memory"); }
DEVI void bar0() { asm volatile("s_barrier" ::: "memory"); }
DEVI void glds(unsigned voff, const void* sbase, unsigned lds) {
    unsigned keep;
    asm volatile("s_mov_b32 %0, m0\n\ts_mov_b32 m0, %3\n\ts_nop 0\n\tglobal_load_lds_dwordx4 %1, %2\n\ts_mov_b32 m0, %0" : "=&s"(keep) : "v"(voff), "s"(sbase), "s"(lds) : "memory");
}
typedef __amdgpu_buffer_rsrc_t rsrc_t;
constexpr unsigned OOB = 0x80000000u;
DEVI void stx(rsrc_t rs, unsigned off, v4u v) { __builtin_amdgcn_raw_buffer_store_b128(v, rs, off, 0, 16); }
DEVI bool none(bool bad) { asm volatile("" ::: "memory"); return __builtin_amdgcn_ballot_w64(bad) == 0ull; }
DEVI unsigned tagx(v4u a, unsigned tag) { unsigned r = (a[1] ^ tag) | (a[3] ^ tag); asm("" : "+v"(r)); return r; }
DEVI unsigned tagy(unsigned a, unsigned b, unsigned tag) { unsigned r = (a ^ tag) | (b ^ tag); asm("" : "+v"(r)); return r; }
DEVI unsigned tagx4(const v4u (&a)[4], unsigned tag) { return (tagx(a[0], tag) | tagx(a[1], tag)) | (tagx(a[2], tag) | tagx(a[3], tag)); }
DEVI void get1(v4u& a, unsigned off, v4u rq) { asm volatile("buffer_load_dwordx4 %0, %1, %2, 0 offen sc1\n\ts_waitcnt vmcnt(0)" : "+&v"(a) : "v"(off), "s"(rq) : "memory"); }
DEVI void get2(v4u& a, v4u& b, unsigned oa, unsigned ob, v4u rq) {
    asm volatile("buffer_load_dwordx4 %0, %2, %4, 0 offen sc1\n\tbuffer_load_dwordx4 %1, %3, %4, 0 offen sc1\n\ts_waitcnt vmcnt(0)" : "+&v"(a), "+&v"(b) : "v"(oa), "v"(ob), "s"(rq) : "memory");
}
template <int ST> DEVI void get4(v4u (&a)[4], unsigned off, v4u rq) {
    asm volatile("buffer_load_dwordx4 %0, %4, %5, 0 offen sc1\n\tbuffer_load_dwordx4 %1, %4, %5, 0 offen offset:%6 sc1\n\t"
                 "buffer_load_dwordx4 %2, %4, %5, 0 offen offset:%7 sc1\n\tbuffer_load_dwordx4 %3, %4, %5, 0 offen offset:%8 sc1\n\ts_waitcnt vmcnt(0)"
                 : "+&v"(a[0]), "+&v"(a[1]), "+&v"(a[2]), "+&v"(a[3]) : "v"(off), "s"(rq), "i"(ST), "i"(2 * ST), "i"(3 * ST) : "memory");
}
DEVI void get4s(v4u (&a)[4], unsigned off, v4u rq, unsigned s1, unsigned s2, unsigned s3) {
    asm volatile("buffer_load_dwordx4 %0, %4, %5, 0 offen sc1\n\tbuffer_load_dwordx4 %1, %4, %5, %6 offen sc1\n\t"
                 "buffer_load_dwordx4 %2, %4, %5, %7 offen sc1\n\tbuffer_load_dwordx4 %3, %4, %5, %8 offen sc1\n\ts_waitcnt vmcnt(0)"
                 : "+&v"(a[0]), "+&v"(a[1]), "+&v"(a[2]), "+&v"(a[3]) : "v"(off), "s"(rq), "s"(s1), "s"(s2), "s"(s3) : "memory");
}
DEVI unsigned now() { return (unsigned)__builtin_amdgcn_s_memrealtime(); }
DEVI void hint_put(rsrc_t rs, unsigned off, unsigned tag, unsigned t) { __builtin_amdgcn_raw_buffer_store_b64(v2u{tag, t}, rs, off, 0, 16); }
DEVI void hint_put(rsrc_t rs, unsigned off, unsigned tag) { hint_put(rs, off, tag, now()); }
DEVI v2u hint_get(v4u rq, unsigned off) { v2u v; asm volatile("buffer_load_dwordx2 %0, %1, %2, 0 offen sc1\n\ts_waitcnt vmcnt(0)" : "=&v"(v) : "v"(off), "s"(rq) : "memory"); return v; }
DEVI void pace(unsigned t, unsigned lead) {
#pragma nounroll
    for (int it = 256; it > 0; --it) { if (none(now() - t < lead)) break; }
}
DEVI unsigned hint_wait(v4u rq, unsigned off, unsigned tag) {
    v2u v;
#pragma nounroll
    SPIN { v = hint_get(rq, off); if (none(v[0] != tag)) break; __builtin_amdgcn_s_sleep(1); }
    return v[1];
}
DEVI unsigned mktag(unsigned ep) { return ep + 1u; }

DEVI void frags128(unsigned a0, unsigned a1, v4u (&f)[4]) {
#pragma unroll
    for (int s = 0; s < 4; ++s) f[s] = trd2(a0 + s * 8192, a1 + s * 8192);
}
DEVI f32x4 gemv4(const v4u (&a)[4], const v4u (&b)[4]) {
    f32x4 c = {0.f, 0.f, 0.f, 0.f};
#pragma unroll
    for (int s = 0; s < 4; ++s) c = mfma32(a[s], b[s], c);
    return c;
}
DEVI void layernorm(v4u xv, int w, int l) {
    float v[8];
#pragma unroll
    for (int j = 0; j < 4; ++j) { v[2 * j] = lo16(xv[j]); v[2 * j + 1] = hi16(xv[j]); }
    const float mean = wavesum(((v[0] + v[1]) + (v[2] + v[3])) + ((v[4] + v[5]) + (v[6] + v[7]))) * (1.f / 512.f);
    float sq = 0.f;
#pragma unroll
    for (int j = 0; j < 8; ++j) { const float d = v[j] - mean; sq += d * d; }
    const float var = wavesum(sq) * (1.f / 512.f);
    const float mb = rbf(mean), ri = __builtin_amdgcn_rcpf(rbf(__builtin_amdgcn_sqrtf(rbf(rbf(var) + 1e-5f))));
    v4u o;
#pragma unroll
    for (int j = 0; j < 4; ++j) o[j] = pk(rbf(v[2 * j] - mb) * ri, rbf(v[2 * j + 1] - mb) * ri);
    lwr<v4u>(L_XN + w * 1024 + l * 16, o);
}

__global__ void __launch_bounds__(512, 2)
mla_glu_decode(const u16* __restrict__ x, const u16* __restrict__ ckv, const u16* __restrict__ wq, const u16* __restrict__ wuk,
               const u16* __restrict__ wuv, const u16* __restrict__ wo, const u16* __restrict__ wg, const u16* __restrict__ wu,
               const u16* __restrict__ wd, u16* __restrict__ out, unsigned char* __restrict__ ws) {
    const int tid = threadIdx.x, l = tid & 63, w = __builtin_amdgcn_readfirstlane(tid >> 6);
    const int id = blockIdx.x, xc = id & 7, yy = (id >> 3) & 15;
    const int un = yy * 8 + xc;
    const unsigned g = l >> 4, l15 = l & 15;
    unsigned cp0 = 0;
    if (w == 7) {
        unsigned long long pc;
        asm volatile("s_getpc_b64 %0" : "=s"(pc));
        pc &= ~255ull;
        asm volatile("global_load_dword %0, %1, %2" : "=&v"(cp0) : "v"(min(((id >> 3) + 32 * (l & 3)) * 128, CODEB - 128)), "s"(pc) : "memory");
    }
    unsigned vb = (128 * (yy & 3) + 16 * w + g) * 8192 + (4 * xc + (yy >> 2)) * 256, sw[4], o_q[4];
#pragma unroll
    for (int i = 0; i < 4; ++i) { sw[i] = (l15 ^ ((g << 2) | i)) << 4; o_q[i] = vb + i * 32768 + sw[i]; PIN(o_q[i]); }
    asm volatile("" :: "s"(x), "s"(ckv), "s"(wq), "s"(wuk), "s"(wuv), "s"(wo), "s"(wg), "s"(wu), "s"(wd), "s"(out), "s"(ws));
    const rsrc_t rs = __builtin_amdgcn_make_buffer_rsrc(ws, 0, WS_END, 0x00020000);
    const v4u rsq = {(unsigned)(size_t)ws, (unsigned)((size_t)ws >> 32) & 0xffffu, (unsigned)WS_END, 0x00020000u};
    const f32x4 ZF = {0.f, 0.f, 0.f, 0.f};
    const unsigned t_in = now();

    if (__builtin_expect(id < 128, 1)) {
        const int h = 4 * xc + (yy >> 2), cq = yy & 3;
        v4u xv; unsigned ep;
        if (w < 2) {
            asm volatile("global_load_dwordx4 %0, %1, %2" : "=v"(xv) : "v"(w * 1024 + l * 16), "s"(x) : "memory");
            asm volatile("global_load_dword %0, %1, %2 sc1" : "=v"(ep) : "v"(0), "s"(ws) : "memory");
        }
#pragma unroll
        for (int i = 0; i < 4; ++i) glds(o_q[i], wq, L_R0 + (16 * w + 4 * i) * 256);
        bar0();
#pragma unroll
        for (int i = 0; i < 4; ++i) glds(vb + i * 32768 + ((l15 ^ (4 * i + g)) << 4), wuk, L_R1 + (16 * w + 4 * i) * 256);
        if (w < 2) {
            asm volatile("s_waitcnt vmcnt(8)" : "+v"(xv), "+v"(ep) :: "memory");
            layernorm(xv, w, l);
            lwr<v4u>(L_TAG, v4u{mktag(ep), 0u, 0u, 0u});
        }
        bar();
        const unsigned tag = lrd<unsigned>(L_TAG);
        unsigned o_p1 = l < 16 ? WS_H1 + ((h * 4 + cq) * 128 + 16 * w + l) * 16 : OOB;
        unsigned o_g1 = WS_H1 + ((h * 4 + g) * 128 + 16 * w + l15) * 16;
        unsigned o_h2 = WS_H2 + (((l >> 5) * 32 + h) * 128 + 32 * cq + (l & 31)) * 16;
        unsigned a_2w = l15 < 2 ? L_RED + l15 * 256 + (4 * w + g) * 8 : L_JUNK + (l & 31) * 8, a_2r = L_RED + l * 8;
        PIN(a_2w); PIN(a_2r);
        unsigned o_t2 = l < 8 ? WS_T2 + (l * 128 + h * 4 + cq) * 128 : OOB;
        unsigned a0, a1;
        {
            const unsigned q = l15 >> 2, p = l & 3, ch = 2 * w + (p >> 1);
            a0 = L_R0 + (8 * g + q) * 256 + 16 * (ch ^ ((q << 2) | ((2 * g) & 3))) + 8 * (p & 1);
            a1 = L_R0 + (8 * g + q + 4) * 256 + 16 * (ch ^ ((q << 2) | ((2 * g + 1) & 3))) + 8 * (p & 1);
        }
        unsigned a_qw = L_QS + (g & 1) * 272 + (16 * w + l15) * 2;
        unsigned a_qr = L_QS + (l & 1) * 272 + 16 * g;
        unsigned a_uk[4], o_gu[3], a_cnt = l == 0 ? L_TAG + 8 : L_JUNK + l * 4, one = 1u;
        v4u xf[4];
#pragma unroll
        for (int s = 0; s < 4; ++s) {
            xf[s] = lrd<v4u>(L_XN + (l & 1) * 1024 + (128 * cq + 32 * s + 8 * g) * 2);
            a_uk[s] = L_R1 + (16 * w + l15) * 256 + (((4 * s + g) ^ l15) << 4);
            PIN(xf[s]); PIN(a_uk[s]);
        }
        const int mat = un >> 6, mp = (un >> 2) & 15, fp = un & 3;
#pragma unroll
        for (int i = 0; i < 3; ++i) {
            const unsigned pc = min(8 * i + w, 21), idx = min(64 * pc + l, 1375u), row = idx / 43u, ch = idx - row * 43u;
            o_gu[i] = (32 * mp + row) * 2752 + fp * 688 + ch * 16; PIN(o_gu[i]);
        }
        v4u pb = {0u, tag, 0u, tag}, ga;
        PIN(a0); PIN(a1); PIN(o_p1); PIN(o_g1); PIN(o_h2); PIN(o_t2); PIN(a_qw); PIN(a_qr); PIN(pb); PIN(a_cnt); PIN(one);
        asm volatile("s_waitcnt vmcnt(4)" : "+v"(cp0) :: "memory");
        bar0();
        {
            v4u f[4]; frags128(a0, a1, f);
            FENCE();
            const f32x4 acc = gemv4(xf, f);
            pb[0] = asu(acc[0]); pb[2] = asu(acc[1]);
            stx(rs, o_p1, pb);
        }
        __builtin_amdgcn_s_sleep(10);
        WAITVM(3);
        v4u fu[4];
        {
            unsigned arr;
            asm volatile("buffer_load_dwordx4 %1, %4, %5, 0 offen sc1\n\tds_add_rtn_u32 %0, %2, %3\n\ts_waitcnt lgkmcnt(0)" : "=&v"(arr), "=&v"(ga) : "v"(a_cnt), "v"(one), "v"(o_g1), "s"(rsq) : "memory");
            hint_put(rs, __builtin_amdgcn_readfirstlane(arr) == 7u ? o_t2 : OOB, tag);
            asm volatile("s_waitcnt vmcnt(1)" : "+v"(ga) :: "memory");
#pragma unroll
            for (int s = 0; s < 4; ++s) fu[s] = lrd<v4u>(a_uk[s]);
            if (__builtin_expect(!none(tagx(ga, tag) != 0u), 0)) {
#pragma nounroll
                SPIN { __builtin_amdgcn_s_sleep(4); get1(ga, o_g1, rsq); if (none(tagx(ga, tag) != 0u)) break; }
            }
            lwr<u16>(a_qw, (u16)pk(sum32(sum16x2(asf(ga[0]), asf(ga[2]))), 0.f));
        }
        bar();
        {
            v4u qf[4];
#pragma unroll
            for (int s = 0; s < 4; ++s) qf[s] = lrd<v4u>(a_qr + 64 * s);
            FENCE();
            const f32x4 acc = gemv4(fu, qf);
            constexpr float SC = 0.08838834764831845f * 1.4426950408889634f;
            lwr<v2u>(a_2w, v2u{pk(acc[0] * SC, acc[1] * SC), pk(acc[2] * SC, acc[3] * SC)});
            bar();
            if (w == 0) {
                const v2u t = lrd<v2u>(a_2r);
                pb[0] = t[0]; pb[2] = t[1];
                stx(rs, o_h2, pb);
            }
        }
        const int r = xc * 8 + (yy >> 1);
        unsigned o_t3 = WS_T3 + ((xc * 4 + cq) * 64 + 8 * w + (l & 7)) * 128, o_t5 = WS_T5 + (r * 32 + (l & 31)) * 128, j0, j1;
        asm volatile("buffer_load_dword %0, %2, %4, 0 offen sc1\n\tbuffer_load_dword %1, %3, %4, 0 offen sc1"
                     : "=&v"(j0), "=&v"(j1) : "v"(o_t3), "v"((w == 0 && !(yy & 1)) ? o_t5 : o_t3), "s"(rsq) : "memory");
#pragma unroll
        for (int i = 0; i < 3; ++i) glds(o_gu[i], mat ? wu : wg, L_R2 + min(8 * i + w, 21) * 1024);
        {
            const v4u xg = lrd<v4u>(L_XN + (l & 1) * 1024 + (32 * mp + 8 * g) * 2);
            const unsigned ag = L_R2 + (8 * g + (l15 >> 2)) * 688 + (l & 3) * 8;
            const unsigned f = 16 * (8 * g + w) + l15;
            const unsigned o_hg = (g < 3 && f < 344) ? WS_HG + ((mat * 16 + mp) * 1376 + 344 * fp + f) * 16 : OOB;
            unsigned o_rep = WS_T2 + (xc * 128 + (l & 7) * 16 + 5) * 128, rep;
            asm volatile("s_waitcnt vmcnt(0)" : "+v"(j0), "+v"(j1) :: "memory");
            asm volatile("buffer_load_dword %0, %1, %2, 0 offen sc1" : "=&v"(rep) : "v"(o_rep), "s"(rsq) : "memory");
            bar();
            f32x4 o[3];
#pragma unroll
            for (int t = 0; t < 3; ++t) {
                const unsigned tl = min(8 * t + w, 21);
                o[t] = mfma32(xg, trd2(ag + tl * 32, ag + tl * 32 + 4 * 688), ZF);
            }
            const float o0 = sel4(g, o[0][0], o[1][0], o[2][0], o[2][0]), o1 = sel4(g, o[0][1], o[1][1], o[2][1], o[2][1]);
            stx(rs, o_hg, v4u{asu(o0), tag, asu(o1), tag});
            asm volatile("s_waitcnt vmcnt(1)" : "+v"(rep) :: "memory");
            if (!none(rep != tag)) (void)hint_wait(rsq, o_rep, tag);
        }
#pragma unroll
        for (int i = 0; i < 4; ++i) glds(vb + i * 32768 + sw[i], wuv, L_R1 + (16 * w + 4 * i) * 256);
        unsigned o_h3 = WS_H3 + (((8 * w + g) * 2 + (h >> 4)) * 4 + cq) * 4096 + (l15 >> 3) * 2048 + (h & 15) * 128 + (l & 7) * 16;
        unsigned o_s3 = WS_S3 + ((xc * 4 + cq) * 64 + 8 * w + g) * 128 + (h & 3) * 16;
        unsigned salt = (l & 7) == 0 ? (l15 ? tag * 3u : tag) : 0u;
        PIN(salt);
        unsigned o_t4 = l < 4 ? WS_T4 + ((h * 4 + l) * 4 + cq) * 128 : OOB;
        unsigned o_p4 = l < 16 ? WS_H4 + ((h * 4 + cq) * 128 + 16 * w + l) * 16 : OOB;
        unsigned a_ow, a_ms = l == 0 ? L_MW + w * 4 : l == 32 ? L_MW + 32 + w * 4 : L_JUNK + l * 4;
        {
            const unsigned gg = l15 & 3, t = 2 * (l15 >> 2) + (g & 1);
            a_ow = L_OW + w * 272 + (32 * gg + 4 * (t ^ (gg >> 1)) + 2 * (g >> 1)) * 2;
        }
        unsigned a_or = L_OW + (l & 7) * 272 + 16 * g, a_mw = L_MW + (g & 1) * 16;
        v4u a[4];
        UNDEF(a[0]); UNDEF(a[1]); UNDEF(a[2]); UNDEF(a[3]);
        a_cnt += l == 0 ? 4 : 0;
        PIN(o_h3); PIN(o_t3); PIN(o_t4); PIN(o_p4); PIN(a_ow); PIN(o_s3); PIN(a_or); PIN(a_mw); PIN(a_ms); PIN(a_cnt);
        WAITVM(0);
        bar();
        v4u fv[4];
        frags128(a0 + (L_R1 - L_R0), a1 + (L_R1 - L_R0), fv);
        PIN(fv[0]); PIN(fv[1]); PIN(fv[2]); PIN(fv[3]);
        const unsigned t3 = hint_wait(rsq, o_t3, tag);
        pace(t3, P3);
        {
#define ROWBAD(d, s_) (rowxor(((d)[0] ^ (d)[1]) ^ ((d)[2] ^ (d)[3])) ^ (s_)[2])
            {
#pragma nounroll
                SPIN {
                    asm volatile("buffer_load_dwordx4 %0, %4, %6, 0 offen sc1\n\tbuffer_load_dwordx4 %1, %4, %6, %7 offen sc1\n\t"
                                 "buffer_load_dwordx4 %2, %5, %6, 0 offen sc1\n\tbuffer_load_dwordx4 %3, %5, %6, 0 offen offset:512 sc1\n\ts_waitcnt vmcnt(0)"
                                 : "+&v"(a[0]), "+&v"(a[1]), "+&v"(a[2]), "+&v"(a[3]) : "v"(o_h3), "v"(o_s3), "s"(rsq), "s"(131072) : "memory");
                    if (none((tagy(a[2][3], a[3][3], tag) | ROWBAD(a[0], a[2]) | ROWBAD(a[1], a[3])) != 0u)) break;
                    __builtin_amdgcn_s_sleep(4);
                }
            }
            unsigned arr;
            unsigned long long tm;
            asm volatile("ds_add_rtn_u32 %0, %2, %3\n\ts_memrealtime %1" : "=&v"(arr), "=&s"(tm) : "v"(a_cnt), "v"(one) : "memory");
            const float m0 = asf(a[2][0]), m1 = asf(a[3][0]);
            const float M = max32(max16(mx(m0, m1)));
            const float a0_ = __builtin_amdgcn_exp2f(m0 - M), a1_ = __builtin_amdgcn_exp2f(m1 - M);
            const f32x2 aa0 = {a0_, a0_}, aa1 = {a1_, a1_};
            f32x2 c[4];
#pragma unroll
            for (int j = 0; j < 4; ++j) {
                const unsigned p = j ? a[0][j] : a[0][0] ^ salt, q = j ? a[1][j] : a[1][0] ^ salt;
                c[j] = __builtin_elementwise_fma(aa1, f32x2{lo16(q), hi16(q)}, aa0 * f32x2{lo16(p), hi16(p)});
            }
            PIN(c[0]); PIN(c[1]); PIN(c[2]); PIN(c[3]);
            asm volatile("s_waitcnt lgkmcnt(0)" : "+v"(arr), "+s"(tm) :: "memory");
            hint_put(rs, __builtin_amdgcn_readfirstlane(arr) == 7u ? o_t4 : OOB, tag, (unsigned)tm);
            const float v0 = sum16x2(c[0][0], c[2][0]), v1 = sum16x2(c[0][1], c[2][1]), v2 = sum16x2(c[1][0], c[3][0]), v3 = sum16x2(c[1][1], c[3][1]);
            lwr<unsigned>(a_ow, pk(sum32x2(v0, v2), sum32x2(v1, v3)));
            const float Ls = sum32(sum16(__builtin_fmaf(a1_, asf(a[3][1]), a0_ * asf(a[2][1]))));
            lwr<float>(a_ms, l < 32 ? M : Ls);
        }
        bar();
        {
            v4u of[4];
#pragma unroll
            for (int s = 0; s < 4; ++s) of[s] = lrd<v4u>(a_or + 64 * s);
            const f32x4 mw = lrd<f32x4>(a_mw), lw = lrd<f32x4>(a_mw + 32);
            FENCE();
            const f32x4 c = gemv4(of, fv);
            const float M = mx(mx3(mw[0], mw[1], mw[2]), mw[3]);
            float be[4];
#pragma unroll
            for (int r = 0; r < 4; ++r) be[r] = __builtin_amdgcn_exp2f(mw[r] - M);
            const float rd = __builtin_amdgcn_rcpf((be[0] * lw[0] + be[1] * lw[1]) + (be[2] * lw[2] + be[3] * lw[3]));
            const float v = ((be[0] * c[0] + be[1] * c[1]) + (be[2] * c[2] + be[3] * c[3])) * rd;
            auto sv = __builtin_amdgcn_permlane16_swap(asu(v), asu(v), false, false);
            pb[0] = sv[0]; pb[2] = sv[1];
            stx(rs, o_p4, pb);
        }
        if ((yy & 1) == 0) {
            const unsigned o1 = WS_H5 + ((32 + (tid >> 3)) * 512 + 8 * r + (tid & 7)) * 16;
            const bool two = tid < 176;
            const unsigned o2 = two ? o1 + 64 * 512 * 16 : o1;
            pace(t3, PG);
#pragma nounroll
            SPIN { get2(a[0], a[1], o1, o2, rsq); if (none((tagx(a[0], tag) | tagx(a[1], tag)) != 0u)) break; __builtin_amdgcn_s_sleep(8); }
            {
                float t = sum32(sum16x2(asf(a[0][0]) + (two ? asf(a[1][0]) : 0.f), asf(a[0][2]) + (two ? asf(a[1][2]) : 0.f)));
                t += dpp<0x128>(t);
                lwr<float>(L_RED + ((g & 1) * 8 + (l & 7)) * 32 + w * 4, t);
            }
            bar();
            if (w == 0) {
                const f32x4 t2 = lrd<f32x4>(L_RED + ((g & 1) * 8 + (l & 7)) * 32), t4 = lrd<f32x4>(L_RED + ((g & 1) * 8 + (l & 7)) * 32 + 16);
                float glu = rbf(((t2[0] + t2[1]) + (t2[2] + t2[3])) + ((t4[0] + t4[1]) + (t4[2] + t4[3])));
                float xin = lo16(x[(g & 1) * 512 + 8 * r + (l & 7)]);
                unsigned o0 = WS_H5 + ((l >> 3) * 512 + 8 * r + (l & 7)) * 16;
                unsigned o_out = ((g & 1) * 512 + 8 * r + (l & 7)) * 2;
                PIN(glu); PIN(xin); PIN(o0); PIN(o_out);
                pace(hint_wait(rsq, o_t5, tag), P5);
                asm volatile("buffer_load_dwordx4 %0, %4, %5, 0 offen sc1\n\tbuffer_load_dwordx4 %1, %4, %5, %6 offen sc1\n\t"
                             "buffer_load_dwordx4 %2, %4, %5, %7 offen sc1\n\tbuffer_load_dwordx4 %3, %4, %5, %8 offen sc1"
                             : "=&v"(a[0]), "=&v"(a[1]), "=&v"(a[2]), "=&v"(a[3]) : "v"(o0), "s"(rsq), "s"(65536), "s"(131072), "s"(196608) : "memory");
                __builtin_amdgcn_raw_buffer_store_b32(tag, rs, (r == 0 && l == 0) ? 0u : OOB, 0, 16);
                asm volatile("s_waitcnt vmcnt(1)" : "+v"(a[0]), "+v"(a[1]), "+v"(a[2]), "+v"(a[3]) :: "memory");
                if (__builtin_expect(!none(tagx4(a, tag) != 0u), 0)) {
#pragma nounroll
                    SPIN { get4s(a, o0, rsq, 65536, 131072, 196608); if (none(tagx4(a, tag) != 0u)) break; }
                }
                float t = sum32(sum16x2((asf(a[0][0]) + asf(a[1][0])) + (asf(a[2][0]) + asf(a[3][0])), (asf(a[0][2]) + asf(a[1][2])) + (asf(a[2][2]) + asf(a[3][2]))));
                t += dpp<0x128>(t);
                {
                    const unsigned v = pk(rbf(xin + rbf(t)) + glu, 0.f);
                    const unsigned p = __builtin_amdgcn_perm((unsigned)__builtin_amdgcn_update_dpp(0, (int)v, 0x101, 0xf, 0xf, true), v, 0x05040100u);
                    const v4u o = {p, (unsigned)__builtin_amdgcn_update_dpp(0, (int)p, 0x102, 0xf, 0xf, true), (unsigned)__builtin_amdgcn_update_dpp(0, (int)p, 0x104, 0xf, 0xf, true),
                                   (unsigned)__builtin_amdgcn_update_dpp(0, (int)p, 0x106, 0xf, 0xf, true)};
                    __builtin_amdgcn_raw_buffer_store_b128(o, __builtin_amdgcn_make_buffer_rsrc(out, 0, 2048, 0x00020000), (l == 0 || l == 16) ? o_out : OOB, 0, 0);
                }
            }
        }
    } else {
        const int pr = xc * 8 + (yy >> 1), hg = yy & 1, b = pr >> 5;
        unsigned ep, j0;
        unsigned o_t2 = WS_T2 + (xc * 128 + (16 * hg + 2 * w) * 4 + (l & 7)) * 128;
        asm volatile("global_load_dword %0, %1, %2 sc1" : "=v"(ep) : "v"(0), "s"(ws) : "memory");
        asm volatile("buffer_load_dword %0, %1, %2, 0 offen sc1" : "=v"(j0) : "v"(o_t2), "s"(rsq) : "memory");
        unsigned qoff = WS_H2 + ((b * 32 + 16 * hg + 2 * w) * 128 + l) * 16;
        unsigned a_q0 = L_Q + 2 * w * 1024 + (((l >> 1) ^ (2 * w)) << 4) + (l & 1) * 8, a_q1 = L_Q + (2 * w + 1) * 1024 + (((l >> 1) ^ (2 * w + 1)) << 4) + (l & 1) * 8;
        unsigned o_kv[8];
#pragma unroll
        for (int i = 0; i < 8; ++i) { o_kv[i] = (pr * 128 + 64 * hg + 8 * w + i) * 1024 + ((l ^ (8 * (w & 1) + i)) << 4); PIN(o_kv[i]); }
        PIN(qoff); PIN(a_q0); PIN(a_q1);
        asm volatile("s_waitcnt vmcnt(0)" : "+v"(ep), "+v"(j0), "+v"(cp0) :: "memory");
        const unsigned tag = mktag(ep);
        pace(t_in, PB);
#pragma unroll
        for (int i = 0; i < 8; ++i) glds(o_kv[i], ckv, L_R0 + (8 * w + i) * 1024);
        bar0();
#pragma unroll
        for (int i = 0; i < 8; ++i) glds(o_kv[i] ^ 65536, ckv, L_R0 + (64 + 8 * w + i) * 1024);
        WAITVM(8);
        bar0();
#define Q_ISSUE() \
            FENCE(); \
            WAITVM(2); \
            const v2u hv = hint_get(rsq, o_t2);        \
            pace(none(hv[0] != tag) ? hv[1] : hint_wait(rsq, o_t2, tag), P2); \
            v4u qa[4]; \
            asm volatile("buffer_load_dwordx4 %0, %4, %5, 0 offen sc1\n\tbuffer_load_dwordx4 %1, %4, %5, 0 offen offset:1024 sc1\n\t" \
                         "buffer_load_dwordx4 %2, %4, %5, 0 offen offset:2048 sc1\n\tbuffer_load_dwordx4 %3, %4, %5, 0 offen offset:3072 sc1" \
                         : "=&v"(qa[0]), "=&v"(qa[1]), "=&v"(qa[2]), "=&v"(qa[3]) : "v"(qoff), "s"(rsq) : "memory"); \
            bar0()
#define Q_LAND(n) do { \
            asm volatile("s_waitcnt vmcnt(" STR(n) ")" : "+v"(qa[0]), "+v"(qa[1]), "+v"(qa[2]), "+v"(qa[3]) :: "memory"); \
            if (__builtin_expect(!none(tagx4(qa, tag) != 0u), 0)) { _Pragma("nounroll") SPIN { __builtin_amdgcn_s_sleep(4); get4<1024>(qa, qoff, rsq); if (none(tagx4(qa, tag) != 0u)) break; } } \
            asm volatile("ds_write2_b32 %0, %2, %3 offset1:1\n\tds_write2_b32 %0, %4, %5 offset0:128 offset1:129\n\tds_write2_b32 %1, %6, %7 offset1:1\n\tds_write2_b32 %1, %8, %9 offset0:128 offset1:129" \
                         :: "v"(a_q0), "v"(a_q1), "v"(qa[0][0]), "v"(qa[0][2]), "v"(qa[1][0]), "v"(qa[1][2]), "v"(qa[2][0]), "v"(qa[2][2]), "v"(qa[3][0]), "v"(qa[3][2]) : "memory"); \
            bar();   } while (0)
        const int h = 4 * xc + (yy >> 2), cq = yy & 3;
        unsigned o_t4 = WS_T4 + ((h * 4 + cq) * 4 + (l & 3)) * 128;
        unsigned o_hg = WS_HG + ((4 * (w & 3) + g) * 1376 + 16 * min(un, 85) + l15) * 16;
        if (w >= 4) {
            unsigned a_r[4];
#pragma unroll
            for (int j = 0; j < 4; ++j) a_r[j] = l15 * 1024 + (((4 * j + g) ^ l15) << 4);
            const unsigned kb = L_R0 + (w - 4) * 16384;
            v4u cr[32], qf[16];
#pragma unroll
            for (int s = 0; s < 16; ++s) cr[s] = lrd<v4u>(a_r[s & 3] + kb + (s >> 2) * 256);
            unsigned a_mxw = L_MX + l15 * 16 + (w - 4) * 4, a_mxr = L_MX + l15 * 16;
            unsigned a_pw = L_P + l15 * 256 + (((2 * (w - 4) + (g >> 1)) ^ l15) << 4) + (g & 1) * 8;
            __builtin_amdgcn_s_setprio(1);
            PIN(a_mxw); PIN(a_mxr); PIN(a_pw); PIN(o_hg);
            Q_ISSUE();
#pragma unroll
            for (int s = 0; s < 16; ++s) cr[16 + s] = lrd<v4u>(a_r[s & 3] + kb + 65536 + (s >> 2) * 256);
            FENCE();
            Q_LAND(0);
#pragma unroll
            for (int s = 0; s < 16; ++s) qf[s] = lrd<v4u>(a_r[s & 3] + L_Q + (s >> 2) * 256);
            FENCE();
            f32x4 s1 = ZF, s2 = ZF;
#pragma unroll
            for (int s = 0; s < 16; ++s) { s1 = mfma32(cr[s], qf[s], s1); s2 = mfma32(cr[16 + s], qf[s], s2); }
            asm volatile("s_nop 7" : "+v"(s1), "+v"(s2));
            lwr<float>(a_mxw, max32(max16(mx(mx3(mx3(s1[0], s1[1], s1[2]), s1[3], s2[0]), mx3(s2[1], s2[2], s2[3])))));
            bar();
            const f32x4 m0 = lrd<f32x4>(a_mxr);
            const float mr = mx(mx3(m0[0], m0[1], m0[2]), m0[3]);
            v2u p1 = {pk(__builtin_amdgcn_exp2f(s1[0] - mr), __builtin_amdgcn_exp2f(s1[1] - mr)), pk(__builtin_amdgcn_exp2f(s1[2] - mr), __builtin_amdgcn_exp2f(s1[3] - mr))};
            lwr<v2u>(a_pw, p1);
            v2u p2 = {pk(__builtin_amdgcn_exp2f(s2[0] - mr), __builtin_amdgcn_exp2f(s2[1] - mr)), pk(__builtin_amdgcn_exp2f(s2[2] - mr), __builtin_amdgcn_exp2f(s2[3] - mr))};
            lwr<v2u>(a_pw ^ 128, p2);
            bar();
            PIN(p1); PIN(p2);
            {
                const unsigned q0 = p1[0], q1 = p1[1], q2 = p2[0], q3 = p2[1];
                lwr<float>(a_mxw + 256, sum32(sum16(((lo16(q0) + hi16(q0)) + (lo16(q1) + hi16(q1))) + ((lo16(q2) + hi16(q2)) + (lo16(q3) + hi16(q3))))));
            }
            bar();
            __builtin_amdgcn_s_setprio(0);
            const unsigned t5 = now();
            v4u ga, gb;
            asm volatile("buffer_load_dwordx4 %0, %3, %6, 0 offen sc1\n\tbuffer_load_dwordx4 %1, %4, %6, 0 offen sc1\n\tbuffer_load_dword %2, %5, %6, 0 offen sc1\n\ts_waitcnt vmcnt(0)"
                         : "=&v"(ga), "=&v"(gb), "=&v"(j0), "+v"(o_hg) : "v"(o_hg + 16 * 1376 * 16), "v"(o_t4), "s"(rsq) : "memory");
            if (un < 86) {
                if (__builtin_expect(!none((tagx(ga, tag) | tagx(gb, tag)) != 0u), 0)) {
#pragma nounroll
                    SPIN { __builtin_amdgcn_s_sleep(16); get2(ga, gb, o_hg, o_hg + 16 * 1376 * 16, rsq); if (none((tagx(ga, tag) | tagx(gb, tag)) != 0u)) break; }
                }
                lwr<float>(L_RED + (l & 31) * 32 + (w - 4) * 4, sum32(sum16x2(asf(ga[0]), asf(ga[2]))));
                lwr<float>(L_RED + (l & 31) * 32 + w * 4, sum32(sum16x2(asf(gb[0]), asf(gb[2]))));
            }
            pace(t5, PW);
#pragma unroll
            for (int i = 0; i < 8; ++i) {
                const unsigned ch = l15 ^ ((g << 2) | (i & 3));
                glds((h * 128 + 32 * (w - 4) + 4 * i + g) * 1024 + cq * 128 + (ch >> 3) * 512 + (ch & 7) * 16, wo, L_R1 + (32 * (w - 4) + 4 * i) * 256);
            }
        } else {
            unsigned a_t[2][4];
            {
                const unsigned q = l15 >> 2, p = l & 3;
#pragma unroll
                for (int j = 0; j < 8; ++j) {
                    const unsigned r0 = 8 * g + 4 * (j >> 2) + q, ch = 16 * w + 4 * p + (j & 3);
                    a_t[j >> 2][j & 3] = L_R0 + r0 * 1024 + ((ch ^ (r0 & 15)) << 4) + (p >> 1) * 8;
                }
            }
            v4u ct[8][4];
#pragma unroll
            for (int t = 0; t < 8; ++t)
#pragma unroll
                for (int s = 0; s < 2; ++s) ct[t][s] = trd2((a_t[0][t >> 1] ^ ((t & 1) * 8)) + s * 32768, (a_t[1][t >> 1] ^ ((t & 1) * 8)) + s * 32768);
            unsigned a_pr[4], a_mxr = L_MX + l15 * 16;
#pragma unroll
            for (int j = 0; j < 4; ++j) { a_pr[j] = L_P + l15 * 256 + (((4 * j + g) ^ l15) << 4); PIN(a_pr[j]); }
            unsigned o_t3 = l < 4 ? WS_T3 + (((4 * hg + l) * 4 + w) * 64 + pr) * 128 : OOB;
            unsigned o_t3b = l < 32 ? WS_S3 + (((4 * hg + (l >> 3)) * 4 + w) * 64 + pr) * 128 + (l & 7) * 16 : OOB;
            PIN(o_t3b);
            unsigned o_h3 = WS_H3 + ((pr * 2 + hg) * 4 + w) * 4096 + l * 16;
            unsigned a_tw = L_R2 + w * 8192 + 4608 + l15 * 16, a_tr = L_R2 + w * 8192 + 4608 + (4 * ((l >> 3) & 3) + (l & 3)) * 16;
            PIN(a_tw); PIN(a_tr);
            unsigned a_sw = L_R2 + w * 8192 + l15 * 144 + g * 16, a_sr = L_R2 + w * 8192 + (l >> 3) * 144 + (l & 7) * 16;
            PIN(a_sw); PIN(a_sr);
            unsigned o_wd[4], a_lr = L_MX + 256 + l15 * 16, salt0 = g == 0 ? tag : 0u, salt1 = g == 0 ? tag * 3u : 0u;
            PIN(salt0); PIN(salt1);
#pragma unroll
            for (int i = 0; i < 4; ++i) { const unsigned row = 4 * w + i; o_wd[i] = (16 * min(un, 85) + row) * 1024 + ((l ^ ((row & 7) << 1)) << 4); PIN(o_wd[i]); }
            PIN(a_mxr); PIN(o_t3); PIN(o_h3); PIN(a_lr);
            Q_ISSUE();
            v4u wst[4];
            asm volatile("global_load_dwordx4 %0, %4, %8\n\tglobal_load_dwordx4 %1, %5, %8\n\tglobal_load_dwordx4 %2, %6, %8\n\tglobal_load_dwordx4 %3, %7, %8"
                         : "=&v"(wst[0]), "=&v"(wst[1]), "=&v"(wst[2]), "=&v"(wst[3]) : "v"(o_wd[0]), "v"(o_wd[1]), "v"(o_wd[2]), "v"(o_wd[3]), "s"(wd) : "memory");
#pragma unroll
            for (int t = 0; t < 8; ++t)
#pragma unroll
                for (int s = 2; s < 4; ++s) ct[t][s] = trd2((a_t[0][t >> 1] ^ ((t & 1) * 8)) + s * 32768, (a_t[1][t >> 1] ^ ((t & 1) * 8)) + s * 32768);
            FENCE();
            Q_LAND(4);
            bar0();
            hint_put(rs, o_t3, tag);
            const f32x4 m0 = lrd<f32x4>(a_mxr);
            float mr = mx(mx3(m0[0], m0[1], m0[2]), m0[3]);
            PIN(mr);
            bar();
            v4u pf[4];
#pragma unroll
            for (int s = 0; s < 4; ++s) pf[s] = lrd<v4u>(a_pr[s]);
            FENCE();
            f32x4 o[8];
#define M0_(t) asm volatile("v_mfma_f32_16x16x32_bf16 %0, %1, %2, 0" : "=&v"(o[t]) : "v"(ct[t][0]), "v"(pf[0]))
#define MM(t, s) asm volatile("v_mfma_f32_16x16x32_bf16 %0, %1, %2, %0" : "+v"(o[t]) : "v"(ct[t][s]), "v"(pf[s]))
#define RDY(t) PIN(o[t])
#define SEQ() asm volatile("" ::: "memory")
#define CV(d, t, i) do { d = pk(o[t][i], o[t][i + 1]); PIN(d); } while (0)
#define PUT(n, k, A, B, C, D) lwr<v4u>(a_sw + (n) * 2304 + (k) * 64, v4u{A, B, C, D})
#define GETL(n) do { x0 = lrd<v4u>(a_sr + (n) * 2304); x1 = lrd<v4u>(a_sr + (n) * 2304 + 8 * 144); } while (0)
#define OUTL(n) do { stx(rs, o_h3 + (n) * 2048, x0); stx(rs, o_h3 + (n) * 2048 + 1024, x1); } while (0)
#define CS(A, B) do { cs = cs ^ (A) ^ (B); PIN(cs); } while (0)
            unsigned d0, d1, d2, d3, e0, e1, e2, e3, cs = 0;
            v4u x0, x1;
            M0_(0); M0_(1); MM(0, 1); MM(1, 1); MM(0, 2); MM(1, 2); MM(0, 3); MM(1, 3);
            M0_(2); M0_(3); MM(2, 1); MM(3, 1); MM(2, 2); MM(3, 2); MM(2, 3);
            RDY(0); CV(d0, 0, 0); CV(d1, 0, 2); d0 ^= salt0; PIN(d0);
            MM(3, 3); RDY(1); CV(d2, 1, 0); CV(d3, 1, 2);
            M0_(4); PUT(0, 0, d0, d1, d2, d3); SEQ();
            MM(4, 1); CS(d0, d1); CS(d2, d3);
            MM(4, 2); RDY(2); CV(e0, 2, 0); CV(e1, 2, 2);
            MM(4, 3);
            bar0();
            const f32x4 l0 = lrd<f32x4>(a_lr);
            M0_(5); RDY(3); CV(e2, 3, 0); CV(e3, 3, 2);
            MM(5, 1); PUT(0, 1, e0, e1, e2, e3); GETL(0); SEQ();
            MM(5, 2); CS(e0, e1); CS(e2, e3);
            MM(5, 3); RDY(4); CV(d0, 4, 0); CV(d1, 4, 2); d0 ^= salt1; PIN(d0);
            M0_(6);
            float lsum = (l0[0] + l0[1]) + (l0[2] + l0[3]);
            PIN(lsum);
            MM(6, 1); OUTL(0); SEQ();
            MM(6, 2); MM(6, 3);
            M0_(7); RDY(5); CV(d2, 5, 0); CV(d3, 5, 2);
            MM(7, 1); PUT(1, 0, d0, d1, d2, d3); SEQ();
            MM(7, 2); CS(d0, d1); CS(d2, d3);
            MM(7, 3);
            asm volatile("s_nop 1" : "+v"(o[6]));
            CV(e0, 6, 0); CV(e1, 6, 2);
            asm volatile("s_nop 4" : "+v"(o[7]));
            CV(e2, 7, 0); CV(e3, 7, 2);
            PUT(1, 1, e0, e1, e2, e3); GETL(1); SEQ();
            CS(e0, e1); CS(e2, e3);
            { auto r1 = __builtin_amdgcn_permlane16_swap(cs, cs, false, false); cs = r1[0] ^ r1[1]; auto r2 = __builtin_amdgcn_permlane32_swap(cs, cs, false, false); cs = r2[0] ^ r2[1]; }
            lwr<v4u>(a_tw, v4u{asu(mr), asu(lsum), cs, tag});
            const v4u x2 = lrd<v4u>(a_tr);
            OUTL(1);
            stx(rs, o_t3b, x2);
            FENCE();
            asm volatile("s_waitcnt vmcnt(6)" : "+v"(wst[0]), "+v"(wst[1]), "+v"(wst[2]), "+v"(wst[3]) :: "memory");
#pragma unroll
            for (int i = 0; i < 4; ++i) lwr<v4u>(L_R0 + (4 * w + i) * 1024 + l * 16, wst[i]);
        }
        bar();
        if (un < 86) {
            if (w == 4) {
                const f32x4 gp = lrd<f32x4>(L_RED + (l & 31) * 32), up = lrd<f32x4>(L_RED + (l & 31) * 32 + 16);
                const float gg = rbf((gp[0] + gp[1]) + (gp[2] + gp[3])), uu = rbf((up[0] + up[1]) + (up[2] + up[3]));
                const float sg = rbf(__builtin_amdgcn_rcpf(1.f + __builtin_amdgcn_exp2f(gg * -1.4426950408889634f)));
                lwr<u16>(L_HH + (l & 31) * 2, (u16)pk(sg * uu, 0.f));
            }
            bar();
            const v2u ha = lrd<v2u>(L_HH + (l & 1) * 32 + g * 8);
            const unsigned q = l15 >> 2, p = l & 3, row = 4 * g + q;
            v2u fd[4];
#pragma unroll
            for (int t = 0; t < 4; ++t) fd[t] = trd(L_R0 + row * 1024 + (((8 * w + 2 * t + (p >> 1)) ^ ((row & 7) << 1)) << 4) + (p & 1) * 8);
            FENCE();
            f32x4 o[4];
#pragma unroll
            for (int t = 0; t < 4; ++t) o[t] = mfma16(ha, fd[t], ZF);
            const float o0 = sel4(g, o[0][0], o[1][0], o[2][0], o[3][0]), o1 = sel4(g, o[0][1], o[1][1], o[2][1], o[3][1]);
            stx(rs, WS_H5 + ((32 + un) * 512 + 64 * w + l) * 16, v4u{asu(o0), tag, asu(o1), tag});
        }
        {
            unsigned a0, a1;
            {
                const unsigned q = l15 >> 2, p = l & 3, ch = 2 * w + (p >> 1);
                a0 = L_R1 + (8 * g + q) * 256 + 16 * (ch ^ ((q << 2) | ((2 * g) & 3))) + 8 * (p & 1);
                a1 = L_R1 + (8 * g + q + 4) * 256 + 16 * (ch ^ ((q << 2) | ((2 * g + 1) & 3))) + 8 * (p & 1);
            }
            unsigned o_g4 = WS_H4 + ((h * 4 + g) * 128 + 16 * w + l15) * 16;
            unsigned o_t5 = tid < 16 ? WS_T5 + ((8 * (cq + 4 * (tid >> 3)) + (tid & 7)) * 32 + h) * 128 : OOB;
            unsigned o_h5 = l < 16 ? WS_H5 + (h * 512 + 64 * (cq + 4 * (w >> 2)) + 16 * (w & 3) + l) * 16 : OOB;
            unsigned a_qw = L_QS + (g & 1) * 272 + (16 * w + l15) * 2, a_qr = L_QS + (l & 1) * 272 + 16 * g;
            v4u pb = {0u, tag, 0u, tag}, fw[4], ga;
            WAITVM(0);
            bar();
            frags128(a0, a1, fw);
            PIN(fw[0]); PIN(fw[1]); PIN(fw[2]); PIN(fw[3]); PIN(o_g4); PIN(o_t5); PIN(o_h5); PIN(a_qw); PIN(a_qr); PIN(pb);
            if (w == 4) pace(hint_wait(rsq, o_t4, tag), P4);
            bar0();
            asm volatile("buffer_load_dwordx4 %0, %1, %2, 0 offen sc1" : "=&v"(ga) : "v"(o_g4), "s"(rsq) : "memory");
            hint_put(rs, o_t5, tag);
            asm volatile("s_waitcnt vmcnt(1)" : "+v"(ga) :: "memory");
            if (__builtin_expect(!none(tagx(ga, tag) != 0u), 0)) {
#pragma nounroll
                SPIN { __builtin_amdgcn_s_sleep(2); get1(ga, o_g4, rsq); if (none(tagx(ga, tag) != 0u)) break; }
            }
            lwr<u16>(a_qw, (u16)pk(sum32(sum16x2(asf(ga[0]), asf(ga[2]))), 0.f));
            bar();
            v4u cf[4];
#pragma unroll
            for (int s = 0; s < 4; ++s) cf[s] = lrd<v4u>(a_qr + 64 * s);
            FENCE();
            const f32x4 acc = gemv4(cf, fw);
            pb[0] = asu(acc[0]); pb[2] = asu(acc[1]);
            stx(rs, o_h5, pb);
        }
    }
}

extern "C" void kernel_launch(void* const* d_in, const int* in_sizes, int n_in, void* d_out, int out_size, void* d_ws, size_t ws_size,
                              hipStream_t stream) {
    (void)in_sizes; (void)n_in; (void)out_size; (void)ws_size;
    (void)hipFuncSetAttribute((const void*)mla_glu_decode, hipFuncAttributeMaxDynamicSharedMemorySize, L_END);
    mla_glu_decode<<<256, 512, L_END, stream>>>((const u16*)d_in[0], (const u16*)d_in[1], (const u16*)d_in[2], (const u16*)d_in[3], (const u16*)d_in[4],
                                               (const u16*)d_in[5], (const u16*)d_in[7], (const u16*)d_in[8], (const u16*)d_in[9], (u16*)d_out,
                                               (unsigned char*)d_ws);
}
```
